```python
import torch

def _softmax(x, axis=-1):
    return torch.softmax(x, dim=axis)

def _gelu(x):
    return torch.nn.functional.gelu(x, approximate='tanh')

def _silu(x):
    return torch.nn.functional.silu(x)

def _sigmoid(x):
    return torch.sigmoid(x)

def _layernorm(x, axis=-1, eps=1e-05):
    m = x.mean(dim=axis, keepdim=True)
    v = x.var(dim=axis, keepdim=True, unbiased=False)
    return (x - m) / torch.sqrt(v + eps)

def _scatter_add(tgt, idx, src, axis=0):
    return tgt.clone().index_add_(axis, idx, src)

def _segment_sum(src, seg, n):
    out = torch.zeros((n,) + src.shape[1:], dtype=src.dtype)
    return out.index_add_(0, seg, src)

def _pad(x, pw):
    flat = tuple((v for pair in reversed(pw) for v in pair))
    return torch.nn.functional.pad(x, flat)
kernel_name = 'concat_heads__compressed_kv_cla__memory_aug_recurrent_mem_tr__prompt'

def setup_inputs():

    _w = lambda a: (lambda t: t.to(torch.bfloat16) if t.is_floating_point() else t)(torch.as_tensor(a))

    def _A_setup():
        B, H, Sq, Sk, D, M = (1, 32, 2048, 2048, 128, 576)
        Dc = 4 * D
        return {'x': _w((lambda *s: torch.randn(*s, dtype=torch.bfloat16) * 1.6)(B, Sq, M)), 'c_kv': _w((lambda *s: torch.randn(*s, dtype=torch.bfloat16) * 0.2)(B, Sk, Dc)), 'w_q': _w((lambda *s: torch.randn(*s, dtype=torch.bfloat16) * 3.2)(M, H, D) * M ** (-0.5)), 'w_uk': _w((lambda *s: torch.randn(*s, dtype=torch.bfloat16) * 3.2)(Dc, H, D) * Dc ** (-0.5)), 'w_uv': _w((lambda *s: torch.randn(*s, dtype=torch.bfloat16) * 0.2)(Dc, H, D) * Dc ** (-0.5)), 'w_o': _w((lambda *s: torch.randn(*s, dtype=torch.bfloat16) * 0.2)(H, D, M) * (H * D) ** (-0.5))}
    _A_in = _A_setup()

    def _B_setup():
        B, S, Dm = (1, 2048, 576)
        H, Dh = (32, 128)
        M, seg = (8, 32)
        seg = min(seg, S)
        S = S // seg * seg
        return {'x': _w((lambda *s: torch.randn(*s, dtype=torch.bfloat16) * 0.05)(B, S, Dm)), 'mem0': _w((lambda *s: torch.randn(*s, dtype=torch.bfloat16) * 0.4)(B, M, Dm)), 'Wq': _w((lambda *s: torch.randn(*s, dtype=torch.bfloat16) * 6.4)(Dm, H, Dh) * Dm ** (-0.5)), 'Wk': _w((lambda *s: torch.randn(*s, dtype=torch.bfloat16) * 6.4)(Dm, H, Dh) * Dm ** (-0.5)), 'Wv': _w((lambda *s: torch.randn(*s, dtype=torch.bfloat16) * 0.4)(Dm, H, Dh) * Dm ** (-0.5)), 'Wo': _w((lambda *s: torch.randn(*s, dtype=torch.bfloat16) * 0.4)(H, Dh, Dm) * (H * Dh) ** (-0.5)), 'seg_len': seg}
    _B_in = _B_setup()
    return {'A_x': _A_in['x'], 'A_c_kv': _A_in['c_kv'], 'A_w_q': _A_in['w_q'], 'A_w_uk': _A_in['w_uk'], 'A_w_uv': _A_in['w_uv'], 'A_w_o': _A_in['w_o'], 'B_x': _B_in['x'], 'B_mem0': _B_in['mem0'], 'B_Wq': _B_in['Wq'], 'B_Wk': _B_in['Wk'], 'B_Wv': _B_in['Wv'], 'B_Wo': _B_in['Wo'], 'B_seg_len': _B_in['seg_len']}

def reference(A_x, A_c_kv, A_w_q, A_w_uk, A_w_uv, A_w_o, B_x, B_mem0, B_Wq, B_Wk, B_Wv, B_Wo, B_seg_len):

    def _A_ref(x, c_kv, w_q, w_uk, w_uv, w_o):
        B, Sq, M = (x.shape[0], x.shape[1], x.shape[2])
        Sk = c_kv.shape[1]
        H, D = (w_q.shape[1], w_q.shape[2])
        q = torch.einsum('bqm,mhd->bhqd', x, w_q)
        k = torch.einsum('bkc,chd->bhkd', c_kv, w_uk)
        v = torch.einsum('bkc,chd->bhkd', c_kv, w_uv)
        scores = torch.einsum('bhqd,bhkd->bhqk', q, k) * D ** (-0.5)
        off = Sk - Sq
        qi = torch.arange(Sq)[:, None]
        kj = torch.arange(Sk)[None, :]
        cmask = kj <= qi + off
        scores = torch.where(cmask[None, None], scores, -1000000000.0)
        p = _softmax(scores, axis=-1)
        ctx = torch.einsum('bhqk,bhkd->bhqd', p, v)
        return torch.einsum('bhqd,hdm->bqm', ctx, w_o)

    def _B_ref(x, mem0, Wq, Wk, Wv, Wo, seg_len):
        B, S, Dm = x.shape
        H, Dh = (Wq.shape[1], Wq.shape[2])
        M = mem0.shape[1]
        scale = Dh ** (-0.5)
        n_seg = S // seg_len
        L = M + seg_len
        idx = torch.arange(L)
        mask = (idx[:, None] < M) | (idx[:, None] >= idx[None, :])
        mem = mem0
        outs = []
        for s in range(n_seg):
            xs = x[:, s * seg_len:(s + 1) * seg_len, :].clone()
            h = torch.concatenate([mem, xs], axis=1)
            q = torch.einsum('btd,dhe->bhte', h, Wq)
            k = torch.einsum('btd,dhe->bhte', h, Wk)
            v = torch.einsum('btd,dhe->bhte', h, Wv)
            scores = torch.einsum('bhqe,bhke->bhqk', q, k) * scale
            scores = torch.where(mask[None, None], scores, -1000000000.0)
            p = _softmax(scores, axis=-1)
            o = torch.einsum('bhqk,bhke->bhqe', p, v)
            o = torch.einsum('bhte,hed->btd', o, Wo)
            mem = o[:, :M, :].clone()
            outs.append(o[:, M:, :])
        return torch.concatenate(outs, axis=1)
    a = _A_ref(A_x, A_c_kv, A_w_q, A_w_uk, A_w_uv, A_w_o)
    b = _B_ref(B_x, B_mem0, B_Wq, B_Wk, B_Wv, B_Wo, B_seg_len)
    return torch.concatenate([a, b], axis=-1)
```

```cpp
#include <hip/hip_runtime.h>
#include <hip/hip_bf16.h>
#include <stdint.h>
#include <type_traits>

typedef unsigned short u16;
typedef unsigned int u32;
typedef unsigned long long u64;
typedef __attribute__((ext_vector_type(8))) short bf8;
typedef __attribute__((ext_vector_type(4))) short bf4;
typedef __attribute__((ext_vector_type(4))) float f4;
typedef __attribute__((ext_vector_type(2))) float f2v;
typedef __attribute__((ext_vector_type(2))) __bf16 bf2v;
typedef __attribute__((ext_vector_type(4))) unsigned u32x4;
typedef __attribute__((ext_vector_type(2))) unsigned u32x2;
typedef float f32x16 __attribute__((ext_vector_type(16)));
typedef __attribute__((ext_vector_type(8))) unsigned u32x8;
typedef __attribute__((ext_vector_type(8))) __bf16 bh8;
typedef __attribute__((ext_vector_type(16))) __bf16 bh16;
#define LDSP __attribute__((address_space(3)))

__device__ __forceinline__ u32 pk2(float a, float b) {
  f2v x = {a, b};
  bf2v y = __builtin_convertvector(x, bf2v);
  return __builtin_bit_cast(u32, y);
}
__device__ __forceinline__ u32 umin(u32 a, u32 b) { return a < b ? a : b; }
__device__ __forceinline__ u32 umax(u32 a, u32 b) { return a > b ? a : b; }
__device__ __forceinline__ float rbf(float f) { return __uint_as_float(pk2(f, f) << 16); }
#define MFMA(a, b, c) __builtin_amdgcn_mfma_f32_16x16x32_bf16(a, b, c, 0, 0, 0)
#define MFMA16(a, b, c) __builtin_amdgcn_mfma_f32_16x16x16bf16_1k(a, b, c, 0, 0, 0)
#define SMF(a, b, c, idx) __builtin_amdgcn_smfmac_f32_16x16x64_bf16(__builtin_bit_cast(bh8, a), __builtin_bit_cast(bh16, b), c, idx, 0, 0)
__device__ __forceinline__ u32x8 ilv(bf4 a, bf4 b, bf4 c, bf4 d) {
  const u32x2 x = __builtin_bit_cast(u32x2, a), y = __builtin_bit_cast(u32x2, b), z = __builtin_bit_cast(u32x2, c), w = __builtin_bit_cast(u32x2, d);
  return (u32x8){x[0], x[1], y[0], y[1], z[0], z[1], w[0], w[1]};
}
__device__ __forceinline__ u32x8 cat16(bf8 a, bf8 b) {
  const u32x4 x = __builtin_bit_cast(u32x4, a), y = __builtin_bit_cast(u32x4, b);
  return (u32x8){x[0], x[1], x[2], x[3], y[0], y[1], y[2], y[3]};
}
__device__ __forceinline__ float swadd(float a, float b) {
  auto x = __builtin_amdgcn_permlane32_swap(__float_as_uint(a), __float_as_uint(b), false, false);
  return __uint_as_float(x[0]) + __uint_as_float(x[1]);
}
typedef __attribute__((ext_vector_type(2))) _Float16 h2v;
__device__ __forceinline__ u32x4 enc8(const float* v, u32 tag) {
  float m = fmaxf(fmaxf(fabsf(v[0]), fabsf(v[1])), fabsf(v[2]));
  m = fmaxf(fmaxf(m, fabsf(v[3])), fabsf(v[4]));
  m = fmaxf(fmaxf(m, fabsf(v[5])), fabsf(v[6]));
  m = fmaxf(m, fabsf(v[7]));
  m = m >= 0x1p-23f ? fmaxf(m, 0x1p-15f) : m;
  const u32 code = __float_as_uint(m) >> 27;
  const float inv = __uint_as_float((253u << 23) - (code << 27));
  u32x4 w;
#pragma unroll
  for (int i = 0; i < 4; i++) {
    f2v x = {v[2 * i], v[2 * i + 1]};
    x *= inv;
    w[i] = __builtin_bit_cast(u32, __builtin_convertvector(x, h2v));
  }
  const u32 t = code | (code << 15);
  w[0] = (w[0] & ~1u) | (tag & 1u);
  w[2] = (w[2] & ~1u) | (tag & 1u);
  w[1] = (w[1] & ~0x10001u) | (t & 0x10001u);
  w[3] = (w[3] & ~0x10001u) | ((t >> 2) & 0x10001u);
  return w;
}
#define FMIX_LO(acc, w, S) asm("v_fma_mix_f32 %0, %1, %2, %0 op_sel_hi:[1,0,0]" : "+v"(acc) : "v"(w), "v"(S))
#define FMIX_HI(acc, w, S) asm("v_fma_mix_f32 %0, %1, %2, %0 op_sel:[1,0,0] op_sel_hi:[1,0,0]" : "+v"(acc) : "v"(w), "v"(S))
__device__ __forceinline__ bf4 trrd(const char* p) {
  return __builtin_amdgcn_ds_read_tr16_b64_v4i16((LDSP bf4*)(p));
}
__device__ __forceinline__ bf8 cat8(bf4 a, bf4 b) { return __builtin_shufflevector(a, b, 0, 1, 2, 3, 4, 5, 6, 7); }
__device__ __forceinline__ void st8_sc1(void* p, u64 v) { __hip_atomic_store((u64*)p, v, __ATOMIC_RELAXED, __HIP_MEMORY_SCOPE_AGENT); }
__device__ __forceinline__ u32 ld4_sc1(const u32* p) { return __hip_atomic_load(p, __ATOMIC_RELAXED, __HIP_MEMORY_SCOPE_AGENT); }
__device__ __forceinline__ void st4_sc1(u32* p, u32 v) { __hip_atomic_store(p, v, __ATOMIC_RELAXED, __HIP_MEMORY_SCOPE_AGENT); }
#define VMW0() asm volatile("s_waitcnt vmcnt(0)" ::: "memory")
#define CFENCE() asm volatile("" ::: "memory")
#define VMW0C() do { __builtin_amdgcn_s_waitcnt(0x0F70); asm volatile("" ::: "memory"); } while (0)
#define SPIN_CAP (1 << 22)
#define XPS 16
#define SPS 4
#define SP2 4
#define WPS 8
__device__ __forceinline__ int tid_now(int wv) {
  int l; asm volatile("v_mbcnt_lo_u32_b32 %0, -1, 0\n\tv_mbcnt_hi_u32_b32 %0, -1, %0" : "=v"(l));
  return wv * 64 + l;
}
#define TIDX tid_now(wv)
#define CLAIM() do { if (TIDX == 0) pre = (int)__hip_atomic_fetch_add(qh, 1u, __ATOMIC_RELAXED, __HIP_MEMORY_SCOPE_AGENT); } while (0)

#define TSD 6
#define TFL 5
struct Params {
  const u16 *Ax, *Ac, *Awq, *Awk, *Awv, *Awo, *Bx, *Bm, *BWq, *BWk, *BWv, *BWo;
  u16* out;
  char* ws;
  char* big;
};
#define TG_KP(i) (i)
#define TG_CF(i) (8 + (i))
#define TG_VOTE(i) (64 + (i))
#define TG_INIT(i) (96 + (i))
#define TG_FK(i) (128 + (i))
#define TG_CL(i) (256 + (i))
#define TG_EP 512
#define SY0 2048
#define SY_QH(q) (SY0 + (q) * 32)
#define SY_CC(rb) (SY0 + 224 + (rb) * 32)
#define SY_CW (SY0 + 480)
#define SY_SEG(s, c) (SY_CS(62) + ((s) - 62) * 128 + (c))
#define SY_KMF(s) (SY0 + 704 + (s) * 32)
#define SY_CS(s) (SY0 + 2496 + ((s) < 48 ? (s) : 48 + ((s) - 48) * 8) * 32)
#define BLKS 1
#define SY_BLK(mi, cb, g) (SY0 + 8128 + ((mi) * 16 + (cb)) * 64 + (g))
#define SY_WORDS (SY0 + 56 * 256)

#define AL256(x) (((x) + 255) & ~255)
#define O_SYNC_END (SY_WORDS * 4)
#define RP 16384
#define ACB (2048 * RP)
#define ROWO(r) ((r) * RP + (((r) >> 7) & 1) * 8192)
#define S_QA 0
#define S_KA (1 * ACB)
#define S_VA (2 * ACB)
#define S_CA S_QA
#define S_QB (3 * ACB)
#define KVO 0x200000
#define S_KB (4 * ACB + KVO)
#define S_VB (5 * ACB + KVO)
#define S_OB S_QB
#define OBN 1
#define S_OL S_OB
#define OROW(r) (ROWO(r) ^ 8192)
#define S_WF(m) (6 * ACB + (8 << 20) + (m) * (12 << 20))
#define WFO(kc, f) (ROWO((kc) * 9 + ((f) >> 3)) + ((f) & 7) * 1024)
#define O_BIG (20 << 20)
#define BIG0 ((char*)((uintptr_t)p.big & ~(uintptr_t)0x1FFFFFu))
#define BIGP(kp, off) (BIG0 + (u32)((kp) * 0x200000 + (off)))
#define O_KM AL256(O_SYNC_END)
#define O_VM (O_KM + 64 * 8 * 4096 * 2)
#define O_XW (O_VM + 64 * 8 * 4096 * 2)
#define XGEO(x) ((((x) & 1) << 14) | (((x) & 2) << 11))
#define XBLK(n) ((n) * 32768 + (((n) & 16) << 10) + (((n) & 32) << 7))
#define X_M2 (56 * 32768)
#define X_ANN (62 * 32768)
#define X_W2 (1 << 21)
#define X_END (2 * X_W2)
#define WSP(T, off) ((T*)(p.ws + (off)))

#define XS0 8
#define SM0 52
#define LDS_TOTAL (131072 + 8192 + 16384)
#define LDS_ALL 163840

#define SB0() __builtin_amdgcn_sched_barrier(0)
#define RSRC(ptr, bytes) __builtin_amdgcn_make_buffer_rsrc((void*)(ptr), 0, (bytes), 0x00020000)
__device__ __forceinline__ bf8 ldsc1(__amdgpu_buffer_rsrc_t r, int off) {
  return __builtin_bit_cast(bf8, __builtin_amdgcn_raw_buffer_load_b128(r, off, 0, 16));
}
__device__ __forceinline__ u32x4 pair16(u32 a0, u32 a1, u32 b0, u32 b1) {
  auto r0 = __builtin_amdgcn_permlane16_swap(a0, b0, false, false);
  auto r1 = __builtin_amdgcn_permlane16_swap(a1, b1, false, false);
  return (u32x4){r0[0], r1[0], r0[1], r1[1]};
}
__device__ __forceinline__ int fsw(int k) { return (k & 3) | (((k >> 3) & 1) << 2); }

#define PJ_OOB 0x7F000000
#define VMW(n) asm volatile("s_waitcnt vmcnt(%0)" :: "n"(n) : "memory")
#define BARV(n) do { VMW(n); __builtin_amdgcn_s_barrier(); CFENCE(); } while (0)
#define BARL(n) do { asm volatile("s_waitcnt vmcnt(%0) lgkmcnt(0)" :: "n"(n) : "memory"); __builtin_amdgcn_s_barrier(); CFENCE(); } while (0)
#define DMA16(rs, dst, vo, so, aux) __builtin_amdgcn_raw_ptr_buffer_load_lds(rs, (LDSP void*)(smem + (dst)), 16, vo, so, 0, aux)
#define TRA(dst, a, off) asm volatile("ds_read_b64_tr_b16 %0, %1 offset:%2" : "=&v"(dst) : "v"(a), "i"(off) : "memory")
#define RDA(dst, a, off) asm volatile("ds_read_b128 %0, %1 offset:%2" : "=&v"(dst) : "v"(a), "i"(off) : "memory")
struct Start { u32x2 ep, old; u32 l, h; int slot, duty; };
#define RSS RSRC(p.ws, SY_WORDS * 4)
#define TG_OK(x) (((((x)[0] ^ st.l) >> 4) | ((x)[1] ^ st.h)) == 0u)
#define TG_LD(i) __builtin_bit_cast(u32x2, __hip_atomic_load(WSP(u64, 0) + (i), __ATOMIC_RELAXED, __HIP_MEMORY_SCOPE_AGENT))
#define TG_ST(i, v) st8_sc1(WSP(u64, 0) + (i), (u64)(st.l | (v)) | ((u64)st.h << 32))
#define TG_BEGIN() do { \
    __builtin_amdgcn_raw_buffer_store_b128((u32x4){0u, 0u, 0u, 0u}, RSS, (TIDX & 63) * 16, st.duty >= 0 ? SY0 * 4 + st.duty * 1024 : PJ_OOB, 16); \
    SB0(); asm volatile("s_nop 1" ::: "memory"); SB0(); st.ep = TG_LD(TG_EP); SB0(); } while (0)
#define TG_CLAIM(n) do { SB0(); VMW(n); asm volatile("" : "+v"(st.ep)); const int t_ = TIDX; \
    st.l = ((u32)__builtin_amdgcn_readfirstlane((int)st.ep[0]) + 16u) & ~15u; st.h = (u32)__builtin_amdgcn_readfirstlane((int)st.ep[1]); \
    st.old = (u32x2){st.l, st.h}; \
    __builtin_amdgcn_raw_buffer_store_b64(st.old, RSS, (t_ & 63) ? PJ_OOB : 0, st.duty >= 0 ? TG_CF(st.duty) * 8 : PJ_OOB, 16); \
    asm volatile("s_nop 4\n\tbuffer_atomic_swap_x2 %0, %1, %2, 0 offen sc0" : "+v"(st.old) : "v"(t_ == 448 ? TG_CL(st.slot) * 8 : PJ_OOB), "s"(RSS) : "memory"); SB0(); } while (0)
#define TG_NOTE(off) do { if (wv == 7) { asm volatile("" : "+v"(st.old)); \
    asm volatile("ds_write_b32 %0, %1\n\ts_waitcnt lgkmcnt(0)" :: "v"((int)(uintptr_t)smem + (off)), "v"(__builtin_amdgcn_readfirstlane(TG_OK(st.old) ? 1 : 0)) : "memory"); } } while (0)
#define TG_CHECK(off) do { int x_, y_; asm volatile("ds_read_b32 %0, %2\n\ts_waitcnt lgkmcnt(0)\n\tv_readfirstlane_b32 %1, %0\n\ts_cmp_eq_u32 %1, 0\n\ts_cbranch_scc1 1f\n\t" \
      "s_waitcnt vmcnt(0)\n\ts_barrier\n\ts_mov_b64 exec, -1\n\ts_mov_b64 vcc, %3\n\ts_mov_b32 m0, %4\n\ts_mov_b64 s[0:1], vcc\n\ts_and_b32 s2, m0, 0x1ff\n\ts_lshr_b32 s3, m0, 10\n\t" \
      "v_mbcnt_lo_u32_b32 v0, -1, 0\n\tv_mbcnt_hi_u32_b32 v0, -1, v0\n\tv_add_u32 v0, s3, v0\n\t" \
      "s_getpc_b64 s[4:5]\n\ts_add_u32 s4, s4, _Z4mega6Params@rel32@lo+4\n\ts_addc_u32 s5, s5, _Z4mega6Params@rel32@hi+12\n\ts_setpc_b64 s[4:5]\n1:" \
      : "=&v"(x_), "=&s"(y_) : "v"((int)(uintptr_t)smem + (off)), "s"(__builtin_amdgcn_kernarg_segment_ptr()), "s"((wv << 16) | 256 | ((st.slot + 1) & 255)) : "scc", "memory"); } while (0)
#define TG_KPW(kv, sl) do { if (!__all(TG_OK(kv))) { int it_ = 0; \
      do { __builtin_amdgcn_s_sleep(sl); kv = TG_LD(TIDX & 63); } while (!__all(TG_OK(kv)) && ++it_ < SPIN_CAP); } \
    kp = __builtin_amdgcn_readfirstlane(__popc((u32)__ballot((kv)[0] & 1u) & 255u) > 4); } while (0)
#define FKS(i)
__device__ __forceinline__ void first_kv(const Params& p, Start& st, const int mat, const int col, const int tb, int& kp, char* smem, const int wv) {
  TG_BEGIN();
  const u16* const W = (mat ? p.BWv : p.BWk) + col;
  const int co = (mat ? S_VB : S_KB) + col * 2, ff = TG_FK((col >> 7) * 4 + mat * 2 + ((col >> 6) & 1));
  u32* const fl = WSP(u32, 0) + SY_BLK(mat, col >> 8, 0);
  const int tid = TIDX, lane = tid & 63, wid = wv;
  const int l15 = lane & 15, lq = lane >> 4, q4 = l15 >> 2, p4 = lane & 3, wr = wid >> 1, wc = wid & 1;
  constexpr int Y = 73728;
  u32 fk_[12];
#undef FKS
#define FKS(i)
  FKS(0);
  const __amdgpu_buffer_rsrc_t ra = RSRC(p.Bx, 256 * 1152), rb = RSRC(W, 576 * 8192 - 8192 + 128);
  const int va = (wid * 8 + (lane >> 3)) * 1152 + (((lane & 7) ^ (lane >> 3)) << 4);
  const int vb = (wid * 8 + (lane >> 3)) * 8192 + ((((lane & 7) >> 1) ^ (((lane >> 4) & 1) | ((wid & 1) << 1))) << 5) + (lane & 1) * 16;
  __builtin_amdgcn_raw_ptr_buffer_load_lds(ra, (LDSP void*)(smem + 2 * Y + wid * 256), 4, lane < 7 ? lane * 128 : PJ_OOB, 64 * 1152 + ((tb & 31) * 8 + wid) * 896, 0, 0);
#define KBO 0
#define KB(k) (KBO && (k) < 8 ? ((k) >> 1) + ((k) & 1) * 4 : (k))
  auto grp = [&](const int g) {
#pragma unroll
    for (int kb = g * 3; kb < g * 3 + 3; kb++) { DMA16(rb, Y + kb * 8192 + wid * 1024, vb, KB(kb) * 64 * 8192, 0); DMA16(ra, kb * 8192 + wid * 1024, va, KB(kb) * 128, 0); }
    SB0();
  };
#define TQP 3
  auto tq = [&]() {
    __builtin_amdgcn_raw_ptr_buffer_load_lds(RSRC((TFL & 8) ? ((tb & 64) ? p.BWk : p.BWv) : p.BWq, 576 * 8192), (LDSP void*)(smem + 2 * Y + wid * 256), 4, lane * 128,
      (TFL & 8) ? (wid * 32 + (tb & 32) * 8 + (tb & 31)) * 8192 : (TFL & 1) ? (wid * 128 + tb) * 8192 : PJ_OOB, 0, 0);
  SB0();
  };
  if (TQP == 0) tq();
  grp(0);
  if (TQP == 1) tq();
  __builtin_amdgcn_s_barrier(); SB0();
  grp(1);
  if (TQP == 2) tq();
  TG_CLAIM(TQP < 3 ? 14 : 13);
  auto fill = [&](const int c, const int dst, const int k0, const int k1) {
#pragma unroll
    for (int kb = k0; kb < k1; kb++) DMA16(ra, dst + kb * 8192 + wid * 1024, va, c * 64 * 1152 + KB(kb) * 128, 0);
    SB0();
  };
  const int oa = (int)(uintptr_t)smem + (wr * 16 + l15) * 128 + ((lq ^ (l15 & 7)) << 4);
  const int ob = (int)(uintptr_t)smem + Y + (lq * 8 + q4) * 128 + (((wc * 2) ^ ((q4 >> 1) | ((lq & 1) << 1))) << 5) + p4 * 8;
  bf8 bw[18][2];
  f4 acc[2];
  auto part = [&](const int g) {
    bf8 af[6];
    const int a0 = oa + g * 24576, a1 = a0 ^ 64, b0 = ob + g * 24576, b1 = b0 ^ 32;
#pragma unroll
    for (int u = 0; u < 6; u++) {
      RDA(af[u], (u & 1) ? a1 : a0, (u >> 1) * 8192);
#pragma unroll
      for (int j = 0; j < 2; j++) { bf4 lo, hi; TRA(lo, j ? b1 : b0, u * 4096); TRA(hi, j ? b1 : b0, u * 4096 + 512); bw[g * 6 + u][j] = cat8(lo, hi); }
    }
#pragma unroll
    for (int u = 0; u < 6; u++) {
      if (u >= 2) asm volatile("s_waitcnt lgkmcnt(%0)" :: "n"(25 - 5 * u) : "memory"); else if (u == 0) asm volatile("s_waitcnt lgkmcnt(15)" ::: "memory");
      asm volatile("" : "+v"(af[u]), "+v"(bw[g * 6 + u][0]), "+v"(bw[g * 6 + u][1]));
#pragma unroll
      for (int j = 0; j < 2; j++) acc[j] = MFMA(bw[g * 6 + u][j], af[u], (g || u) ? acc[j] : ((f4){0, 0, 0, 0}));
      asm volatile("" : "+v"(acc[0]), "+v"(acc[1]));
    }
  };
  auto chunk = [&](const int base) {
    bf8 af[18];
    const int a0 = oa + base, a1 = a0 ^ 64, a2 = a0 + 65536, a3 = a1 + 65536;
#pragma unroll
    for (int u = 0; u < 18; u++) RDA(af[u], u < 16 ? ((u & 1) ? a1 : a0) : ((u & 1) ? a3 : a2), ((u >> 1) & 7) * 8192);
#pragma unroll
    for (int u = 0; u < 18; u++) {
      asm volatile("s_waitcnt lgkmcnt(%0)" :: "n"(u < 2 ? 15 : 17 - u) : "memory");
      asm volatile("" : "+v"(af[u]));
#pragma unroll
      for (int j = 0; j < 2; j++) acc[j] = MFMA(bw[u][j], af[u], u ? acc[j] : ((f4){0, 0, 0, 0}));
      asm volatile("" : "+v"(acc[0]), "+v"(acc[1]));
    }
  };
  const int vc = (wr * 16 + l15) * RP + (wc * 32 + (lq & 1) * 16 + (lq >> 1) * 8) * 2;
  const __amdgpu_buffer_rsrc_t rc = RSRC(BIGP(0, co), 384 * RP);
  auto put = [&](const int c) {
    const u32x4 v = pair16(pk2(acc[0][0], acc[0][1]), pk2(acc[0][2], acc[0][3]), pk2(acc[1][0], acc[1][1]), pk2(acc[1][2], acc[1][3]));
    __builtin_amdgcn_raw_buffer_store_b128(v, rc, vc + ROWO(c * 64), c < 2 ? 0 : kp * 0x200000, 16);
    if (c < 2) __builtin_amdgcn_raw_buffer_store_b128(v, rc, vc + ROWO(c * 64), 0x200000, 16);
    SB0();
  };
  FKS(1); BARV(TQP == 1 || TQP == 2 ? 9 : 8); grp(2);
  if (TQP == 3) tq();
  FKS(2); part(0);
  BARV(TQP == 3 ? 7 : 6); TG_NOTE(151552); fill(1, Y, 0, 3); part(1);
  BARV(TQP == 3 ? 4 : 3); FKS(3); TG_CHECK(151552); fill(1, Y, 3, 6); part(2); FKS(4);
  put(0);
  __builtin_amdgcn_s_barrier(); CFENCE();
  FKS(5);
  fill(1, Y, 6, 9); fill(2, 0, 0, 9);
  BARV(9); FKS(6);
  if (tid == 0) TG_ST(ff, 0u);
  SB0();
  chunk(Y);
  put(1);
  u32x2 kv = TG_LD(lane);
  BARV(3); FKS(7);
  fill(3, Y, 0, 9);
  chunk(0);
  BARV(9); FKS(8);
  asm volatile("" : "+v"(kv));
  TG_KPW(kv, 2);
  if (tid < 4) __hip_atomic_fetch_add(fl + tid, 2u, __ATOMIC_RELAXED, __HIP_MEMORY_SCOPE_AGENT);
  SB0();
  put(2);
  BARV(1); FKS(9);
  chunk(Y);
  put(3); FKS(10);
  BARV(0); FKS(11);
  if (tid < 4) __hip_atomic_fetch_add(fl + 4 + tid, 2u, __ATOMIC_RELAXED, __HIP_MEMORY_SCOPE_AGENT);
}

#define C_BUFA 131072
#define C_BUFB (131072 + 16384)
#define AT_GK ((wid * 4 + (lane >> 4)) * RP + (lane & 15) * 16)
#define AT_GV (AT_GK ^ (((wid & 1) * 2 + (lane >> 5)) * 64))
#define AT_RS(off) RSRC(BIGP(kp, (off) + hd * 256), ACB - 8192 + 256)
constexpr int pj_pack(int S, int ph) { return S == 8 ? (ph == 1 ? 0x01 : ph == 2 ? 0x11 : ph == 3 ? 0x6423 : 0) : S == 0 ? (ph == 0 ? 0x7533 : ph == 1 ? 0xA8EC4 : ph == 2 ? 0xB9FD4 : 0) : 0; }
constexpr int pj_store(int S, int ph) { return ph == 0 ? 0 : S == 8 ? (ph > 1 ? (ph - 2) * 16 + 1 : 0) : S < 2 ? (S * 3 + 1 + ph) * 16 + 1
                                               : S < 6 && ph > 1 ? (S * 2 + 2 + ph) * 16 + 1 : 0; }
__device__ __forceinline__ void proj_slot(const Params& p, const Start& st, const int nu, const int ml, const int rt0, const int rs, const int cb, int& kp, const int hd,
                                          bf8* qr, u32& vfl, char* smem, const int wv) {
  const int tid = TIDX, lane = tid & 63, wid = wv;
  const int l15 = lane & 15, lq = lane >> 4, q4 = l15 >> 2, p4 = lane & 3, wr = wid >> 2, wc = wid & 3;
  const __amdgpu_buffer_rsrc_t rc = RSRC(p.ws, 0x10000000), rd = RSRC(BIG0, 0xE200000);

  int vB = ((wid * 4 + lq) * 4096 + (((l15 >> 1) ^ (lq | (((wid >> 1) & 1) << 2))) * 16 + (lane & 1) * 8)) * 2, vB1 = vB + 32 * 8192;
  int ao = (wr * 64 + l15) * 128 + ((lq ^ (l15 & 7)) << 4);
  int bo = (int)(uintptr_t)smem + 65536 + (lq * 8 + q4) * 256 + (((wc * 2) ^ (q4 | ((lq & 1) << 2))) << 5) + p4 * 8;
  int vD = 0, sp = C_BUFA + tid * 16;
  asm volatile("" : "+v"(sp));
  __amdgpu_buffer_rsrc_t rsA, rsB;
  int k2, vA, vA1, sa, sb, da = 128, xb = 256, yb = 64 * 8192, mn;
  u64 pa, pb;
  const int nb = (rt0 < 8 ? 16 - rt0 : rt0 + 2) * 4;
  auto unit = [&](const int u, const int part) __attribute__((always_inline)) {
    if (part == 0) {
      const __attribute__((address_space(4))) u64* a = (const __attribute__((address_space(4))) u64*)__builtin_amdgcn_kernarg_segment_ptr();
      mn = ((ml >> (u * 4)) & 15) * 4;
      pa = a[(0x110666 >> mn) & 15]; pb = a[(0x4328A9 >> mn) & 15];
    } else if (part == 1) {
      k2 = mn < 16 ? 1152 : 1024;
      sa = ((rt0 + u * rs) & 15) * 128 * k2 - 128; sb = mn == 12 ? (hd & 1) * 256 : 0;
      asm volatile("" : "+s"(k2), "+s"(sa), "+s"(sb));
    } else if (part == 2) {
      vA = TIDX & 63; vA1 = ((vA & 7) ^ (vA >> 3)) * 16; vA >>= 3;
      asm volatile("" : "+v"(vA), "+v"(vA1));
    } else if (part == 3) {
      vA = ((wid >> 1) * 32 + (wid & 1) * 8 + vA) * k2 + vA1; vA1 = vA + 16 * k2;
      asm volatile("" : "+v"(vA), "+v"(vA1));
    } else {
      rsA = RSRC((const char*)(__attribute__((address_space(1))) const char*)pa, 2048 * k2);
      rsB = RSRC((const char*)(__attribute__((address_space(1))) const char*)pb + cb * 512, k2 * 4096 - cb * 512);
      asm volatile("" : "+s"(rsA), "+s"(rsB));
    }
  };
#define PJ_ATT() do { const int lane = TIDX & 63; u32 f_, g_; int it_ = 0; \
    asm volatile("ds_read_b32 %0, %2\n\tds_read_b32 %1, %2 offset:256\n\ts_waitcnt lgkmcnt(0)" : "=&v"(f_), "=&v"(g_) : "v"((int)(uintptr_t)smem + C_BUFA + lane * 4) : "memory"); \
    while (!__all((lane >= nb || f_ >= 8u) && (lane >= 6 || g_ >= 8u)) && ++it_ < SPIN_CAP) { \
      f_ = ld4_sc1(WSP(u32, 0) + SY_BLK(4, cb, lane)); g_ = ld4_sc1(WSP(u32, 0) + SY_BLK(5, cb, lane)); VMW0C(); } \
    rsA = AT_RS(S_VA); rsB = AT_RS(S_KA); vA = AT_GV; vA1 = vA + 32 * RP; vB = AT_GK; vB1 = vB + 32 * RP; \
    k2 = RP / 2; da = yb = ROWO(128); xb = 64 * RP; sa = -ROWO(128); sb = 0; } while (0)
#define PJ_STAGE(dst, rs, v0, v1, so, aux) do { const int so_ = (so); \
    __builtin_amdgcn_raw_ptr_buffer_load_lds(rs, (LDSP void*)(smem + (dst) + wid * 1024), 16, v0, so_, 0, aux); \
    __builtin_amdgcn_raw_ptr_buffer_load_lds(rs, (LDSP void*)(smem + (dst) + wid * 1024 + 8192), 16, v1, so_, 0, aux); } while (0)
#define PJ_SA(c, dst, so) PJ_STAGE(dst, rsA, vA, vA1, so, (c) ? 16 : 0)
#define PJ_SB(c, dst, so) PJ_STAGE(dst, rsB, vB, vB1, so, (c) ? 16 : 0)
#define PJ_LDA(h) do { _Pragma("unroll") for (int m = 0; m < 4; m++) _Pragma("unroll") for (int k = 0; k < 2; k++) \
    At[m][k] = *(const bf8*)(smem + (ao ^ (k * 64)) + (h) * 16384 + m * 2048); } while (0)
#define PJ_TR(dst, a, off) asm volatile("ds_read_b64_tr_b16 %0, %1 offset:%2" : "=&v"(dst) : "v"(a), "i"(off) : "memory")
#define PJ_LDB(D, h) do { const int b1_ = bo ^ 32; _Pragma("unroll") for (int n = 0; n < 2; n++) _Pragma("unroll") for (int k = 0; k < 2; k++) { \
    bf4 lo_, hi_; PJ_TR(lo_, n ? b1_ : bo, (h) * 16384 + k * 8192); PJ_TR(hi_, n ? b1_ : bo, (h) * 16384 + k * 8192 + 1024); D[n][k] = cat8(lo_, hi_); } } while (0)
#define PJ_WV(n) asm volatile("s_waitcnt vmcnt(%0)" :: "n"(n) : "memory")
#define PJ_WL(n) asm volatile("s_waitcnt lgkmcnt(" #n ")" ::: "memory")
#define PJ_WL0 do { __builtin_amdgcn_s_waitcnt(0xC07F); asm volatile("" ::: "memory"); } while (0)
#define PJ_BAR __builtin_amdgcn_s_barrier()
#define PJ_SCHED __builtin_amdgcn_sched_barrier(0)
#define PJ_NOP do { PJ_SCHED; asm volatile("s_nop 7"); PJ_SCHED; } while (0)
#define PJ_P2(z, n, r, f) pk2(acc[(z) >> 3][(z) & 1][((z) >> 1) & 3][n][r] f, acc[(z) >> 3][(z) & 1][((z) >> 1) & 3][n][(r) + 1] f)
#define PJ_PK(z, f) pair16(PJ_P2(z, 0, 0, f), PJ_P2(z, 0, 2, f), PJ_P2(z, 1, 0, f), PJ_P2(z, 1, 2, f))
#define QSC * (1.4426950408889634f * 0.08838834764831845f)
#define PJ_QW(a) do { const int l_ = TIDX & 63; char* const w_ = smem + C_BUFA + (wc * 4 + ((l_ >> 4) & 1) * 2 + (l_ >> 5)) * 512 + wr * 256 + (l_ & 15) * 16; \
    _Pragma("unroll") for (int q = 0; q < 4; q++) *(u32x4*)(w_ + q * 8192) = PJ_PK((a) * 8 + q * 2, QSC); } while (0)
#define PJ_QR() do { const char* a_ = smem + C_BUFA + wc * 8192 + (TIDX & 63) * 16; _Pragma("unroll") for (int i = 0; i < 8; i++) qr[i] = *(const bf8*)(a_ + i * 1024); } while (0)
#define PJ_DS(v, z) do { PJ_SCHED; __builtin_amdgcn_raw_buffer_store_b128(v, rd, vD + ((z) & 1) * 256, (dco ^ (((z) >> 3) * 8192)) + ((z) >> 1) * 32 * RP, 16); \
    asm volatile("s_nop 1"); PJ_SCHED; } while (0)
#define PJ_PUB(a, n) do { if (wid == 4) { const int l_ = TIDX & 63; __builtin_amdgcn_raw_buffer_store_b32(8u, rc, l_ < (n) ? (l_ + (a)) * 4 : PJ_OOB, dfl, 16); } } while (0)
#define PJ_ST()
#define PJ_CV(z, j) asm volatile("v_cvt_pk_bf16_f32 %0, %1, %2" : "=v"(pk[z][j]) \
    : "v"(acc[(z) >> 3][(z) & 1][((z) >> 1) & 3][(j) >> 1][((j) & 1) * 2]), "v"(acc[(z) >> 3][(z) & 1][((z) >> 1) & 3][(j) >> 1][((j) & 1) * 2 + 1]))
#define PJ_DP(z) PJ_DS(((u32x4){pk[z][0], pk[z][1], pk[z][2], pk[z][3]}), z)
#define PJ_OUT(S, ph) do { constexpr int ps_ = pj_store(S, ph), pn_ = pj_store(S, (ph) % 3 + 1) >> 4; if (ps_ & 15) PJ_DP(ps_ >> 4); \
    if (pn_ >= 12) { const u32x4 y_ = *(const u32x4*)(smem + sp + (pn_ - 12) * 8192); _Pragma("unroll") for (int i = 0; i < 4; i++) pk[pn_][i] = y_[i]; } } while (0)
#define PJ_MMA(S, ai, bj, D) do { PJ_SCHED; __builtin_amdgcn_s_setprio(1); \
    constexpr int pp_ = pj_pack(S, (ai) * 2 + (bj)); \
    _Pragma("unroll") for (int i = 0; i < 16; i++) { const int k = i >> 3, m = (i >> 1) & 3, n = i & 1; \
      if (k == 0 && (S) == 0) asm volatile("v_mfma_f32_16x16x32_bf16 %0, %1, %2, 0" : "+v"(acc[ai][bj][m][n]) : "v"(D[n][k]), "v"(At[m][k])); \
      else asm volatile("v_mfma_f32_16x16x32_bf16 %0, %1, %2, %0" : "+v"(acc[ai][bj][m][n]) : "v"(D[n][k]), "v"(At[m][k])); \
      if (i < (pp_ & 15) * 3) { PJ_SCHED; const int z = (pp_ >> (4 + i / 3 * 4)) & 15; \
          \
        if (i % 3 == 0) { PJ_CV(z, 0); PJ_CV(z, 2); PJ_CV(z, 1); } \
        else { if (i % 3 == 1) PJ_CV(z, 3); \
          asm volatile("v_permlane16_swap_b32 %0, %1" : "+v"(pk[z][i % 3 - 1]), "+v"(pk[z][i % 3 + 1])); \
          if (i % 3 == 2 && z >= 12) *(u32x4*)(smem + sp + (z - 12) * 8192) = (u32x4){pk[z][0], pk[z][1], pk[z][2], pk[z][3]}; } \
        PJ_SCHED; } \
      if ((S) == 7 && (ai) + (bj) == 0 && i % 3 == 0 && i < 15) { PJ_SCHED; unit(u + 1 < nu ? u + 1 : u, i / 3); PJ_SCHED; } \
      if ((S) == 8 && (ai) + (bj) == 0 && i % 3 == 0 && i < 12) { PJ_SCHED; dest(u, i / 3); PJ_SCHED; } } \
    __builtin_amdgcn_s_setprio(0); PJ_SCHED; } while (0)
#define PJ_STEP(S) do { PJ_ST(); \
    PJ_LDB(B0, 0); PJ_SCHED; \
    if ((S) == 6 && own && wid == 0) { _Pragma("unroll") for (int i = 0; i < 2; i++) \
      __builtin_amdgcn_raw_ptr_buffer_load_lds(rc, (LDSP void*)((LDSP char*)smem + (C_BUFA + i * 256)), 4, (TIDX & 63) * 4, SY_BLK(4 + i, cb, 0) * 4, 0, 16); } \
    PJ_SCHED; PJ_LDA(0); PJ_SA((S) == 8, (bsel ^ 32768) + 16384, sa + 128 * k2); \
    PJ_WL(8); PJ_BAR; PJ_WL0; PJ_MMA(S, 0, 0, B0); PJ_BAR; PJ_SCHED; \
    if ((S) == 7 && own) PJ_ATT(); \
    PJ_SCHED; PJ_LDB(B1, 1); PJ_SB((S) >= 7, 65536 + bsel, sb); PJ_OUT(S, 1); \
    PJ_BAR; PJ_WL0; PJ_MMA(S, 0, 1, B1); PJ_BAR; \
    if ((S) == 8 && own) { PJ_QW(0); PJ_SCHED; } \
    PJ_LDA(1); sa += (S) >= 7 ? da : 128; PJ_SA((S) >= 7, bsel, sa); PJ_OUT(S, 2); \
    PJ_BAR; PJ_WL0; PJ_MMA(S, 1, 0, B0); PJ_BAR; PJ_SCHED; \
    PJ_SB((S) >= 7, 65536 + bsel + 16384, sb ^ ((S) >= 7 ? xb : 256)); sb += (S) >= 7 ? yb : 64 * 8192; PJ_OUT(S, 3); \
      \
    PJ_WV(6 + (pj_store(S, 1) & 15) + (pj_store(S, 2) & 15) + (pj_store(S, 3) & 15)); \
    PJ_BAR; \
    if ((S) != 6 && (S) != 8) PJ_PUB((S) < 3 || (S) == 7 ? (S) : (S) + 1, (S) == 2 ? 2 : 1); \
    PJ_MMA(S, 1, 1, B1); PJ_BAR; \
    bsel ^= 32768; ao ^= 32768; bo ^= 32768; } while (0)
  f4 acc[2][2][4][2];
#pragma unroll
  for (int a = 0; a < 16; a++) { acc[a >> 3][(a >> 2) & 1][a & 3][0] = (f4){0, 0, 0, 0}; acc[a >> 3][(a >> 2) & 1][a & 3][1] = (f4){0, 0, 0, 0}; }
  bf8 At[4][2], B0[2][2], B1[2][2];
  u32 pk[16][4];
#pragma unroll
  for (int i = 0; i < 4; i++) { pk[2][i] = 0u; pk[4][i] = 0u; pk[6][i] = 0u; }
#pragma unroll
  for (int i = 0; i < 5; i++) unit(0, i);
  int bsel = hd >= 0 ? 32768 : 0;
  ao ^= bsel; bo ^= bsel;
  PJ_SB(0, 65536 + bsel, 0); PJ_SA(0, bsel, sa + 128);
  PJ_SB(0, 65536 + bsel + 16384, 256); PJ_SA(0, bsel + 16384, sa + 128 + 128 * k2);
  if (kp < 0) {
    u32x2 kv = TG_LD(lane);
    TG_KPW(kv, 2);
  }
  if (wr == 1) PJ_BAR;
  PJ_WV(4); PJ_BAR;
  PJ_SB(0, 65536 + (bsel ^ 32768), 64 * 8192); PJ_SA(0, bsel ^ 32768, sa + 256);
  PJ_SB(0, 65536 + (bsel ^ 32768) + 16384, 64 * 8192 + 256);
  PJ_WV(6); PJ_BAR;
  sa += 256; sb = 128 * 8192;
  int dco = PJ_OOB, dfl = PJ_OOB, md, vE;
  auto dest = [&](const int u, const int part) __attribute__((always_inline)) {
    if (part == 0) {
      md = (ml >> (u * 4)) & 15;
      dfl = SY_BLK(md, cb, ((rt0 + u * rs) & 15) * 4) * 4;
      asm volatile("" : "+s"(md), "+s"(dfl));
    } else if (part == 1) {
      dco = md == 3 ? PJ_OOB : (kp + (md < 2)) * 0x200000 + ((0x210354 >> (md * 4)) & 15) * ACB + cb * 512 + ((rt0 + u * rs) & 15) * 128 * RP;
      asm volatile("" : "+s"(dco));
    } else if (part == 2) {
      vD = TIDX; vE = ((vD >> 3) & 2) + ((vD >> 5) & 1) + wc * 4; vD &= 15;
      asm volatile("" : "+v"(vD), "+v"(vE));
    } else {
      vD = vD * (md < 3 ? RP : 16) + (vE * (md < 3 ? 16 : 2 * RP) + wr * (md < 3 ? 16 * RP : RP));
      asm volatile("" : "+v"(vD));
    }
  };
  static_assert(KVO == 0x200000, "places");
#pragma unroll 1
  for (int u = 0; u < nu; u++) {
    const bool own = hd >= 0 && u + 1 == nu;
    PJ_STEP(0); PJ_STEP(1); PJ_STEP(2); PJ_STEP(3); PJ_STEP(4); PJ_STEP(5);
    int more = k2 - 1024; asm volatile("" : "+s"(more));
    if (more) PJ_STEP(6);
    PJ_STEP(7); PJ_STEP(8);
  }
  PJ_ST();
  PJ_NOP;
  if (hd >= 0) {
    vfl = (TIDX & 63) * 4;
    if (wid == 0) asm volatile("s_nop 4\n\tbuffer_load_dword %0, %0, %1, %2 offen sc1" : "+v"(vfl) : "s"(rc), "s"(SY_BLK(5, cb, 0) * 4) : "memory");
    if (wr == 0) PJ_QR();
    dco = kp * 0x200000 + S_QA + (hd ^ 1) * 256 + rt0 * 128 * RP;
#pragma unroll
    for (int z = 0; z < 16; z += 2) PJ_DS(PJ_PK(z + 1, QSC), z);
    if (wr == 0) { PJ_WL0; PJ_BAR; }
    PJ_SCHED; PJ_QW(1); PJ_SCHED;
    if (wr == 1) { PJ_WL0; PJ_BAR; asm volatile("" ::: "memory"); PJ_QR(); }
    return;
  }
#pragma unroll
  for (int z = 2; z < 16; z++) PJ_DS(PJ_PK(z, ), z);
  PJ_WV(0);
  if (wr == 0) PJ_BAR;
  PJ_BAR;
  PJ_PUB(0, 8);
  PJ_ST();
}

namespace fa {
#define ABS(i)
#define ABT()
#define EPS 3
#define RTH 65536.f
constexpr int TS = 16384, KR = 4 * TS;
#define SBAR() __builtin_amdgcn_sched_barrier(0)
#define MF32(a, b, c) __builtin_amdgcn_mfma_f32_32x32x16_bf16(a, b, c, 0, 0, 0)
#define FMX3(a, b, c) __builtin_elementwise_maximum(__builtin_elementwise_maximum(a, b), c)
__device__ __forceinline__ void attn_pair(const Params& p, const char* __restrict__ Kh, const char* __restrict__ Vh, const int hd, const int R, const int kp, bf8* qr, u32 vfl, char* lds, const int wv, u32* qh, int& pre) {
  const int tid = TIDX, wid = wv, lane = tid & 63, grp = wid >> 2;
  const int n = R * 4 + 4;
  float l_reg = 0.f;
  f32x16 o[4] = {}, s0, s1, cm;
  int ko = KR + lane * 16;
  int vo = ((((lane >> 3) & 2) | ((lane >> 1) & 1)) * (512 + 64) + ((lane >> 2) & 3) * 16 + (lane & 1) * 8) ^ ((lane >> 5) * 64), vp;
  const __amdgpu_buffer_rsrc_t rk = RSRC(Kh, ACB - 8192 + 256), rv = RSRC(Vh, ACB - 8192 + 256);
  const int gk = AT_GK, gv = AT_GV;
  u32* const qf = WSP(u32, 0) + SY_BLK(3, hd >> 1, 0);
  int t = 0, rl = 1, ks = ROWO(192), vs;
#define DMA(rs, go, dst, so) __builtin_amdgcn_raw_ptr_buffer_load_lds(rs, (LDSP void*)((LDSP char*)lds + ((dst) + wid * 1024)), 16, go, so, 0, 16)
#define NEXT_TILES() do { const int u_ = t + 4 < n ? t + 4 : t + 4 - n; vs = ks; ks = t < 32 ? ROWO(u_ * 64) : PJ_OOB; asm volatile("" : "+s"(ks)); } while (0)
#define ISSUE(i) DMA((i) < 2 ? rk : rv, (i) < 2 ? gk : gv, ((i) < 2 ? KR + (t & 3) * TS : ((t + 3) & 3) * TS) + ((i) & 1) * 8192, ((i) < 2 ? ks : vs) + ((i) & 1) * 32 * RP)
#define PBAR() do { SBAR(); asm volatile("" ::: "memory"); __builtin_amdgcn_s_barrier(); asm volatile("" ::: "memory"); SBAR(); } while (0)
#define SEND() do { SBAR(); int x_, y_; \
    asm volatile("s_waitcnt vmcnt(12)\n\ts_cmp_lg_u32 %0, 0\n\ts_mov_b32 %0, 0\n\ts_cbranch_scc1 1f\n\ts_waitcnt vmcnt(4)\n1:\n\ts_cmp_lg_u32 %3, 1\n\ts_cbranch_scc1 2f\n\t" \
      "v_mov_b32 %1, %5\n\tv_mov_b32 %2, 1\n\ts_mov_b64 exec, 1\n\ts_nop 1\n\tglobal_atomic_add %1, %2, %4\n\ts_mov_b64 exec, -1\n2:\n\ts_barrier" \
      : "+&s"(rl), "=&v"(x_), "=&v"(y_) : "s"(t), "s"(qf), "s"(R * 32) : "scc", "memory"); \
    SBAR(); } while (0)
#define QO_RS(b) RSRC(BIGP(kp, S_QA + ROWO(((b) ? 7 - R : R) * 256 + wid * 32) + hd * 256), 32 * RP)
#define QO_VO() (((TIDX & 63) >> 4) * RP + (TIDX & 15) * 16)
#define LQ1(i) "s_add_i32 %8, %8, 0x10000\n\tbuffer_load_dwordx4 %" #i ", %9, %10, %8 offen sc1\n\t"
#define QRS "+v"(qr[0]), "+v"(qr[1]), "+v"(qr[2]), "+v"(qr[3]), "+v"(qr[4]), "+v"(qr[5]), "+v"(qr[6]), "+v"(qr[7])
#define LDQ() asm volatile("s_mov_b32 %8, 0xffff0000\n\ts_nop 3\n\t" LQ1(0) LQ1(1) LQ1(2) LQ1(3) LQ1(4) LQ1(5) LQ1(6) LQ1(7) \
    : QRS, "=&s"(rl) : "v"(QO_VO()), "s"(QO_RS(1)) : "scc", "memory")
#define LDK(d0) do { const char* a_ = lds + ko + (d0) * 1024; \
    kf[(d0) % 3][0] = *(const bf8*)a_; kf[(d0) % 3][1] = *(const bf8*)(a_ + 8192); } while (0)
#define LDV(i) do { const int a_ = ((i) & 3) * 2048 + (((i) >> 2) & 1) * 256 + ((i) >> 3) * 8192; vf[(i) & 3] = cat8(TRL(vo + a_), TRL(vp + a_)); } while (0)
#define TRL(o) __builtin_amdgcn_ds_read_tr16_b64_v4i16((LDSP bf4*)((LDSP char*)lds + (o)))
#define QK(NEXT, is) do { _Pragma("unroll") for (int d0 = 0; d0 < 8; ++d0) { \
      if (d0 + 2 < 8) LDK(d0 + 2); else { NEXT(2 * (d0 - 6)); NEXT(2 * (d0 - 6) + 1); } \
      SBAR(); \
      s0 = MF32(kf[d0 % 3][0], qr[d0], d0 ? s0 : (is) ? cm : f32x16{}); s1 = MF32(kf[d0 % 3][1], qr[d0], d0 ? s1 : (is) ? cm : f32x16{}); \
      SBAR(); \
      if ((is) && d0 < 4) ISSUE(d0); } } while (0)
#define NOV(i)
#define TR1(r, c) "s_bfm_b32 exec_lo, " #c ", 0\n\ts_bfm_b32 exec_hi, " #c "+4, 0\n\tv_mov_b32 %" #r ", 0xff800000\n\t"
#define TRI(S) do { float y[16]; _Pragma("unroll") for (int r = 0; r < 16; ++r) y[r] = S[r]; \
      asm volatile(TR1(0, 0) TR1(1, 1) TR1(2, 2) TR1(3, 3) TR1(4, 8) TR1(5, 9) TR1(6, 10) TR1(7, 11) TR1(8, 16) TR1(9, 17) TR1(10, 18) TR1(11, 19) \
          TR1(12, 24) TR1(13, 25) TR1(14, 26) TR1(15, 27) "s_mov_b64 exec, -1" \
          : "+v"(y[0]), "+v"(y[1]), "+v"(y[2]), "+v"(y[3]), "+v"(y[4]), "+v"(y[5]), "+v"(y[6]), "+v"(y[7]), \
            "+v"(y[8]), "+v"(y[9]), "+v"(y[10]), "+v"(y[11]), "+v"(y[12]), "+v"(y[13]), "+v"(y[14]), "+v"(y[15])); \
      _Pragma("unroll") for (int r = 0; r < 16; ++r) S[r] = y[r]; } while (0)
#define MASK() do { SBAR(); asm volatile("s_nop 7\n\ts_nop 3" : "+v"(s0), "+v"(s1)); \
      if (wid & 1) TRI(s1); \
      else { TRI(s0); _Pragma("unroll") for (int r = 0; r < 16; ++r) s1[r] = -__builtin_inff(); } \
      asm volatile("" : "+v"(s0), "+v"(s1)); SBAR(); } while (0)
#define RENEW(floor) do { \
      float mx = FMX3(s0[0], s0[1], s1[0]), my = FMX3(s0[2], s0[3], s1[1]); \
      _Pragma("unroll") for (int r = 4; r < 16; r += 4) { mx = FMX3(mx, s0[r], s0[r + 1]); my = FMX3(my, s0[r + 2], s0[r + 3]); } \
      _Pragma("unroll") for (int r = 2; r < 14; r += 4) { mx = FMX3(mx, s1[r], s1[r + 1]); my = FMX3(my, s1[r + 2], s1[r + 3]); } \
      mx = FMX3(FMX3(mx, s1[14], s1[15]), my, floor); \
      auto rr = __builtin_amdgcn_permlane32_swap(__float_as_uint(mx), __float_as_uint(mx), false, false); \
      up = __builtin_elementwise_maximum(__uint_as_float(rr[0]), __uint_as_float(rr[1])); \
      _Pragma("unroll") for (int r = 0; r < 16; ++r) { s0[r] -= up; s1[r] -= up; } } while (0)
#define SETCM(v) do { const float v_ = (v); _Pragma("unroll") for (int r = 0; r < 16; ++r) { float w_ = v_; asm volatile("" : "+v"(w_)); cm[r] = w_; } } while (0)
  bf8 kf[3][2], vf[4], pa[4];
  float up;
  int b = 0, e;
  auto tile = [&](auto LASTC) __attribute__((always_inline)) {
    constexpr bool LAST = decltype(LASTC)::value;
    ABT();
    if (LAST && grp > b) LDQ();
    if (LAST) MASK();
    float sa;
    u32 pw[16];
#define EXPS() do { float x[32], sb; \
      _Pragma("unroll") for (int r = 0; r < 18; ++r) { \
        SBAR(); \
        if (r < 16) { x[2 * r] = __builtin_amdgcn_exp2f(r < 8 ? s0[2 * r] : s1[2 * r - 16]); x[2 * r + 1] = __builtin_amdgcn_exp2f(r < 8 ? s0[2 * r + 1] : s1[2 * r - 15]); } \
        if (r >= 2) { \
          sa = r == 2 ? x[0] : sa + x[2 * r - 4]; \
          sb = r == 2 ? x[1] : sb + x[2 * r - 3]; \
          pw[r - 2] = pk2(x[2 * r - 4], x[2 * r - 3]); asm volatile("" : "+v"(sa), "+v"(pw[r - 2])); } } \
      SBAR(); \
      sa += sb; } while (0)
    EXPS();
    if (__builtin_expect(__ballot(!(sa < RTH)) != 0, 0)) {
      RENEW(0.f);
      SETCM(cm[0] - up);
      const float alpha = __builtin_amdgcn_exp2f(-up);
      l_reg *= alpha;
#pragma unroll
      for (int d = 0; d < 4; ++d) o[d] *= alpha;
      EXPS();
    }
    l_reg += sa;
#pragma unroll
    for (int i = 0; i < 4; ++i) pa[i] = __builtin_bit_cast(bf8, (u32x4){pw[4 * i], pw[4 * i + 1], pw[4 * i + 2], pw[4 * i + 3]});
    asm volatile("" : "+v"(pa[0]), "+v"(pa[1]), "+v"(pa[2]), "+v"(pa[3]), "+v"(l_reg));
    if constexpr (!LAST) ko ^= (t & 1) ? 3 * TS : TS;
    vp = vo ^ 128; asm volatile("" : "+v"(vp));
    NEXT_TILES();
    SBAR();
    if constexpr (!LAST) { LDK(0); LDK(1); } else { LDV(0); LDV(1); LDV(2); LDV(3); }
    SEND();
    __builtin_amdgcn_s_setprio(1);
    if constexpr (!LAST) QK(LDV, true);
#pragma unroll
    for (int i = 0; i < 16; ++i) {
      SBAR();
      o[i & 3] = MF32(vf[i & 3], pa[i >> 2], o[i & 3]);
      SBAR();
      if (i + 4 < 16) LDV(i + 4);
      if (LAST && i < 4) ISSUE(i);
    }
    __builtin_amdgcn_s_setprio(0);
    if constexpr (!LAST) { vo ^= (t & 1) ? 3 * TS : TS; PBAR(); }
  };
  ABS(40);
#pragma unroll 1
  while (true) {
    e = (b ? 32 : n - 4) + (wid >> 1);
    __builtin_amdgcn_s_setprio(1);
    LDK(0); LDK(1);
    QK(NOV, false);
    __builtin_amdgcn_s_setprio(0);
    if (wid + b == 0) {
      const int nb = (R < 4 ? 8 - R : R + 1) * 8;
      int it = 0;
      asm volatile("s_waitcnt vmcnt(8)" : "+v"(vfl) :: "memory");
      while (!__all(lane >= nb || vfl >= 8u) && ++it < SPIN_CAP) { vfl = ld4_sc1(qf + 2 * 16 * 64 + lane); VMW0C(); }
    }
    PBAR();
    if (t == e) MASK();
    RENEW(-__builtin_inff());
    SETCM(-up);
#pragma unroll 1
    for (; t < e; t++) tile(std::false_type{});
    tile(std::true_type{});
    {
      const float inv = __builtin_amdgcn_rcpf(swadd(l_reg, l_reg));
      const __amdgpu_buffer_rsrc_t ro = QO_RS(b);
      const int v = QO_VO();
#pragma unroll
      for (int i = 0; i < 8; ++i) {
        if (i == EPS && wid < 6) PBAR();
        const int g = (i & 1) * 8;
        u32 w[4];
#pragma unroll
        for (int k = 0; k < 4; k++) w[k] = pk2(o[i >> 1][g + 2 * k] * inv, o[i >> 1][g + 2 * k + 1] * inv);
        auto y0 = __builtin_amdgcn_permlane32_swap(w[0], w[2], false, false);
        auto y1 = __builtin_amdgcn_permlane32_swap(w[1], w[3], false, false);
        SBAR();
        __builtin_amdgcn_raw_buffer_store_b128((u32x4){y0[0], y1[0], y0[1], y1[1]}, ro, v, rl, 16);
        asm volatile("s_add_i32 %0, %0, 0x10000\n\ts_nop 0" : "+s"(rl) :: "scc");
        SBAR();
      }
      SBAR();
    }
#pragma unroll 1
    while ((++t & 3) != 0) {
      if (t != e + 1) PBAR();
      ABT();
      if (b == 0) {
        if (wid == 0 && (t & 3) == 2) {
          int it = 0;
          while (ld4_sc1(qf + (7 - R) * 8) < 8u && ++it < SPIN_CAP) __builtin_amdgcn_s_sleep(2);
          VMW0C();
        }
        if (!grp && (t & 3) == 3) LDQ();
      }
      NEXT_TILES();
      SEND();
#pragma unroll
      for (int i = 0; i < 4; i++) ISSUE(i);
    }
    if (b) break;
    b = 1;
    l_reg = 0.f;
#pragma unroll
    for (int i = 0; i < 4; i++) o[i] = f32x16{};
    ko &= ~(3 * TS); vo &= ~(3 * TS);
    asm volatile("s_waitcnt vmcnt(12)\n\ts_cmp_lg_u32 %8, 0\n\ts_cbranch_scc1 1f\n\ts_waitcnt vmcnt(4)\n1:"
        : QRS : "s"(grp) : "scc", "memory");
  }
  ABS(41);
  CLAIM();
  if (!grp) __builtin_amdgcn_s_barrier();
  VMW0();
  __syncthreads();
}
#undef DMA
#undef ISSUE
#undef LDK
#undef LDV
#undef QK
#undef NOV
#undef MASK
}

__device__ __forceinline__ void xattn_task(const Params& p, int s, int hg, const int kp, char* smem, const int wv) {
  const int tid = TIDX, lane = tid & 63, wid = wv;
  const int l15 = lane & 15, lq = lane >> 4, q4 = l15 >> 2, p4 = lane & 3;
  const int h = hg * 8 + wid;
  const int so = ROWO(s * 32);
  const __amdgpu_buffer_rsrc_t rq = RSRC(BIGP(kp, S_QB + so), 32 * RP), rk = RSRC(BIGP(kp, S_KB + so), 32 * RP), rv = RSRC(BIGP(kp, S_VB + so), 32 * RP);
  const __amdgpu_buffer_rsrc_t rkm = RSRC(WSP(u16, O_KM), 64 * 8 * 4096 * 2), rvm = RSRC(WSP(u16, O_VM), 64 * 8 * 4096 * 2);
  const __amdgpu_buffer_rsrc_t ro = RSRC(BIGP(kp, (s >= SM0 ? S_OL : S_OB) + OROW(s * 32)), 32 * RP);
  char* sV = smem + wid * (40 * 288);
  if (tid < 64) {
    const u32* w = WSP(u32, 0) + SY_BLK(tid < 4 ? 2 : (tid < 8 ? 0 : 1), hg * 4 + (tid & 3), s);
    int it = 0;
    while (!__all(ld4_sc1(w) >= 8u) && ++it < SPIN_CAP) __builtin_amdgcn_s_sleep(64);
  }
  __syncthreads();
  bf8 qf[4][2];
  f4 sc[3][2];
#pragma unroll
  for (int j = 0; j < 3; j++) { sc[j][0] = (f4){0, 0, 0, 0}; sc[j][1] = (f4){0, 0, 0, 0}; }
  {
    bf8 vst[8], kf[4][2];
#pragma unroll
    for (int i = 0; i < 8; i++) { int c = lane + 64 * i, r = c >> 4, ch = c & 15; vst[i] = ldsc1(rv, r * RP + (h * 128 + ch * 8) * 2); }
#pragma unroll
    for (int ks = 0; ks < 4; ks++) {
#pragma unroll
      for (int i = 0; i < 2; i++) qf[ks][i] = ldsc1(rq, (i * 16 + l15) * RP + (h * 128 + ks * 32 + lq * 8) * 2);
#pragma unroll
      for (int j = 0; j < 2; j++) kf[ks][j] = ldsc1(rk, (j * 16 + l15) * RP + (h * 128 + ks * 32 + lq * 8) * 2);
    }
#pragma unroll
    for (int i = 0; i < 8; i++) { int c = lane + 64 * i, r = c >> 4, ch = c & 15; *(bf8*)(sV + r * 288 + ch * 16) = vst[i]; }
#pragma unroll
    for (int ks = 0; ks < 4; ks++)
#pragma unroll
      for (int j = 0; j < 2; j++)
#pragma unroll
        for (int i = 0; i < 2; i++) sc[j][i] = MFMA(kf[ks][j], qf[ks][i], sc[j][i]);
  }
  u32x4 vmr[4];
  if (tid == 0) {
    int it = 0;
    while (ld4_sc1(WSP(u32, 0) + SY_KMF(s)) == 0u && ++it < SPIN_CAP) __builtin_amdgcn_s_sleep(XPS);
  }
  __syncthreads();
  {
    bf8 km[4];
#pragma unroll
    for (int i = 0; i < 4; i++) vmr[i] = __builtin_bit_cast(u32x4, ldsc1(rvm, s * 131072 + h * 4096 + i * 1024 + lane * 16));
#pragma unroll
    for (int ks = 0; ks < 4; ks++) km[ks] = ldsc1(rkm, ((s * 8 + q4 * 2 + (l15 & 1)) * 4096 + h * 128 + ks * 32 + lq * 8) * 2);
#pragma unroll
    for (int ks = 0; ks < 4; ks++)
#pragma unroll
      for (int i = 0; i < 2; i++) sc[2][i] = MFMA(km[ks], qf[ks][i], sc[2][i]);
  }
  const float scale = 0.08838834764831845f;
  bf8 pf[2]; bf4 pm[2];
#pragma unroll
  for (int i = 0; i < 2; i++) {
    const int qi = i * 16 + l15;
    float mx = -1e30f;
#pragma unroll
    for (int j = 0; j < 3; j++)
#pragma unroll
      for (int r = 0; r < 4; r++) {
        float v = rbf(rbf(sc[j][i][r]) * scale);
        bool ok = (j < 2) ? (j * 16 + lq * 4 + r <= qi) : (r < 2);
        v = ok ? v : -1e30f;
        sc[j][i][r] = v; mx = fmaxf(mx, v);
      }
    mx = fmaxf(mx, __shfl_xor(mx, 16)); mx = fmaxf(mx, __shfl_xor(mx, 32));
    float sum = 0.f;
#pragma unroll
    for (int j = 0; j < 3; j++)
#pragma unroll
      for (int r = 0; r < 4; r++) {
        float e = (sc[j][i][r] <= -1e29f) ? 0.f : __expf(sc[j][i][r] - mx);
        sc[j][i][r] = e; sum += e;
      }
    sum += __shfl_xor(sum, 16); sum += __shfl_xor(sum, 32);
    const float inv = 1.f / sum;
    u32x4 w = {pk2(sc[0][i][0] * inv, sc[0][i][1] * inv), pk2(sc[0][i][2] * inv, sc[0][i][3] * inv),
               pk2(sc[1][i][0] * inv, sc[1][i][1] * inv), pk2(sc[1][i][2] * inv, sc[1][i][3] * inv)};
    pf[i] = __builtin_bit_cast(bf8, w);
    u32x2 w2 = {pk2(sc[2][i][0] * inv, sc[2][i][1] * inv), pk2(sc[2][i][1] * inv, sc[2][i][0] * inv)};
    pm[i] = __builtin_bit_cast(bf4, w2);
  }
  __builtin_amdgcn_fence(__ATOMIC_RELEASE, "wavefront");
  __builtin_amdgcn_wave_barrier();
#pragma unroll
  for (int d2 = 0; d2 < 4; d2++) {
    u32 ow[2][2][2];
#pragma unroll
    for (int e = 0; e < 2; e++) {
      const int dt = d2 * 2 + e;
      bf4 v0 = trrd(sV + (lq * 4 + q4) * 288 + dt * 32 + p4 * 8);
      bf4 v1 = trrd(sV + (16 + lq * 4 + q4) * 288 + dt * 32 + p4 * 8);
      bf4 vm = __builtin_bit_cast(bf4, e ? (u32x2){0u, vmr[d2][2]} : (u32x2){vmr[d2][0], 0u});
      bf8 vf = cat8(v0, v1);
#pragma unroll
      for (int i = 0; i < 2; i++) {
        f4 o = (f4){0, 0, 0, 0};
        o = MFMA(vf, pf[i], o);
        f4 o2 = MFMA16(vm, pm[i], ((f4){0, 0, 0, 0}));
        o += o2;
        ow[i][e][0] = pk2(o[0], o[1]); ow[i][e][1] = pk2(o[2], o[3]);
      }
    }
#pragma unroll
    for (int i = 0; i < 2; i++)
      __builtin_amdgcn_raw_buffer_store_b128(pair16(ow[i][0][0], ow[i][0][1], ow[i][1][0], ow[i][1][1]), ro,
          s >= SM0 ? (i * 16 + (h >> 1)) * RP + ((h & 1) * 4 + d2) * 1024 + ((lq & 1) * 32 + (lq >> 1) * 16 + l15) * 16
                   : (d2 * 8 + (lq & 1) * 4 + (lq >> 1) * 2 + i) * RP + h * 256 + l15 * 16, 0, 16);
  }
  VMW0();
  __syncthreads();
}

#define WOP 0
__device__ __forceinline__ void wof_task(const Params& p, const u32x4* st, const int m, const int kc, const int kp, char* smem, const int wv) {
  const int tid = TIDX, lane = tid & 63, wid = wv;
  const int l15 = lane & 15, lq = lane >> 4, q4 = l15 >> 2, p4 = lane & 3;
  const __amdgpu_buffer_rsrc_t rw = RSRC(BIGP(kp, S_WF(m)), 576 * RP);
#pragma unroll
  for (int i = 0; i < 9; i++) {
    const int id = tid + 512 * i, row = id / 72, ch = id % 72;
    *(u32x4*)(smem + row * 1280 + (((ch >> 1) ^ fsw(row)) << 5) + ((ch & 1) << 4)) = st[i];
  }
  asm volatile("s_waitcnt lgkmcnt(0)" ::: "memory"); __builtin_amdgcn_s_barrier(); asm volatile("" ::: "memory");
#pragma unroll
  for (int i = 0; i < 9; i++) {
    const int f = wid * 9 + i, nt = f >> 1, k2 = f & 1;
    const int r0 = k2 * 32 + lq * 8 + q4, r1 = r0 + 4;
    bf4 lo = trrd(smem + r0 * 1280 + ((nt ^ fsw(r0)) << 5) + p4 * 8);
    bf4 hi = trrd(smem + r1 * 1280 + ((nt ^ fsw(r1)) << 5) + p4 * 8);
    __builtin_amdgcn_raw_buffer_store_b128(__builtin_bit_cast(u32x4, cat8(lo, hi)), rw, lane * 16 + WFO(kc, f), 0, 16);
    if (WOP) { SB0(); __builtin_amdgcn_s_sleep(WOP); SB0(); }
  }
  asm volatile("s_waitcnt lgkmcnt(0)" ::: "memory"); __builtin_amdgcn_s_barrier(); asm volatile("" ::: "memory");
}

template <int MT, int NT, int WM, int S>
__device__ __forceinline__ void out_tile(const char* A, const char* W, const int nt0, u16* __restrict__ C, char* smem, const int wv, u32* qh, int& pre) {
  constexpr int WN = 4 / WM, BI = NT * WN / 2, STG = 16384 + NT * WN * 2048, NV = 4 + BI;
  const int tid = TIDX, lane = tid & 63, wid = wv & 3, l15 = lane & 15, lq = lane >> 4, wr = wid / WN, wc = wid % WN;
  if (wv >= 4) {
    const __amdgpu_buffer_rsrc_t ra = RSRC(A, 128 * RP), rw = RSRC(W, 576 * RP);
    const int va = (wid * 32 + lq) * RP + l15 * 16, vw = lane * 16;
    auto fill = [&](const int k, const int bo) {
      const int in = k < 64 ? 0 : PJ_OOB;
#pragma unroll
      for (int i = 0; i < 4; i++) __builtin_amdgcn_raw_ptr_buffer_load_lds(ra, (LDSP void*)(smem + bo + (wid * 4 + i) * 1024), 16, va, ((k >> 1) * 256 + ((k & 1) * 16 + i * 4) * RP) | in, 0, 16);
#pragma unroll
      for (int i = 0; i < BI; i++) { const int f = nt0 * 2 + wid * BI + i;
        __builtin_amdgcn_raw_ptr_buffer_load_lds(rw, (LDSP void*)(smem + bo + 16384 + (wid * BI + i) * 1024), 16, vw, WFO(k, f) | in, 0, 16); }
    };
#define OT_SYNC(n) do { asm volatile("s_waitcnt vmcnt(%0)" :: "n"(n) : "memory"); __builtin_amdgcn_s_barrier(); asm volatile("" ::: "memory"); } while (0)
#pragma unroll
    for (int u = 0; u < S; u++) fill(u, u * STG);
    OT_SYNC((S - 1) * NV);
    int bo = 0;
#pragma unroll 1
    for (int k = 0; k < 64; k++) {
      OT_SYNC((S - 2) * NV);
      fill(k + S, bo);
      bo = bo + STG == S * STG ? 0 : bo + STG;
    }
  } else {
    const int oa = wr * MT * 2048 + lq * 512 + l15 * 16, ob = 16384 + wc * NT * 2048 + lane * 16;
    f4 acc[MT][NT];
#pragma unroll
    for (int i = 0; i < MT; i++)
#pragma unroll
      for (int j = 0; j < NT; j++) acc[i][j] = (f4){0, 0, 0, 0};
    auto rd = [&](const int bo, const int ks, bf8* f, const int i) {
      if (i < NT) f[i] = *(const bf8*)(smem + (ob + bo) + i * 2048 + ks * 1024);
      else f[i] = *(const bf8*)(smem + (oa + bo) + ks * 2048 + ((i - NT) >> 1) * 4096 + ((i - NT) & 1) * 256);
    };
    auto mm = [&](bf8* f, const int bo, const int ks, bf8* g) {
#pragma unroll
      for (int u = 0; u < MT * NT; u++) {
        if (u < MT + NT) rd(bo, ks, g, u);
        SB0();
        acc[u / NT][u % NT] = MFMA(f[u % NT], f[NT + u / NT], acc[u / NT][u % NT]);
        SB0();
      }
    };
    bf8 f0[MT + NT], f1[MT + NT];
    __builtin_amdgcn_s_barrier(); asm volatile("" ::: "memory");
#pragma unroll
    for (int i = 0; i < MT + NT; i++) rd(0, 0, f0, i);
    int bo = 0;
    __builtin_amdgcn_s_setprio(1);
#pragma unroll 1
    for (int k = 0; k < 64; k++) {
      mm(f0, bo, 1, f1);
      asm volatile("" ::: "memory"); __builtin_amdgcn_s_waitcnt(0xC07F); __builtin_amdgcn_s_barrier(); asm volatile("" ::: "memory");
      bo = bo + STG == S * STG ? 0 : bo + STG;
      mm(f1, bo, 0, f0);
    }
    __builtin_amdgcn_s_setprio(0);
    CLAIM();
#pragma unroll
    for (int i = 0; i < MT; i++)
#pragma unroll
      for (int j = 0; j < NT; j += 2) {
        u16* const q = C + (size_t)(wr * (MT * 16) + i * 16 + l15) * 1152 + wc * (NT * 16) + j * 16;
        if (j + 1 == NT) *(u32x2*)(q + lq * 4) = (u32x2){pk2(acc[i][j][0], acc[i][j][1]), pk2(acc[i][j][2], acc[i][j][3])};
        else *(u32x4*)(q + (lq & 1) * 16 + (lq >> 1) * 8) =
            pair16(pk2(acc[i][j][0], acc[i][j][1]), pk2(acc[i][j][2], acc[i][j][3]), pk2(acc[i][(j + 1) % NT][0], acc[i][(j + 1) % NT][1]), pk2(acc[i][(j + 1) % NT][2], acc[i][(j + 1) % NT][3]));
      }
  }
  VMW0();
  __syncthreads();
}

__device__ __forceinline__ void small_out_task(const Params& p, const int set, int hf, int np, const int kp, char* smem, const int wv, u32* qh, int& pre) {
  const int tid = TIDX, lane = tid & 63, wid = wv;
  const int l15 = lane & 15, lq = lane >> 4;
  const __amdgpu_buffer_rsrc_t rw = RSRC(BIGP(kp, S_WF(1)), 576 * RP), ro = RSRC(BIGP(kp, S_OL), ACB);
  if (wid == 0) {
    int it = 0;
    while (ld4_sc1(WSP(u32, 0) + SY_CW) < 128u && ++it < SPIN_CAP) __builtin_amdgcn_s_sleep(8);
  }
  __syncthreads();
  bf8 bfr[2][16];
#pragma unroll
  for (int k = 0; k < 32; k++) bfr[(k >> 1) & 1][(k >> 2) * 2 + (k & 1)] = ldsc1(rw, WFO(wid * 8 + (k >> 2), np * 4 + (k & 3)) + lane * 16);
  int va = (hf * 16 + wid * 2) * RP + lane * 16;
  static_assert(SM0 >= 48 && SM0 < 60, "late segments");
  int u = 0;
#pragma unroll 1
  for (int s = SM0 + (set + 63 - SM0) % 3;; s += 3) {
    if (s > 63) s = 63;
    if (wid == 0) {
      int it = 0;
      const u32* f = WSP(u32, 0) + SY_CS(s < 62 ? s : 61) + (np & 7) * 32;
      while (ld4_sc1(f) < 4u && ++it < SPIN_CAP) __builtin_amdgcn_s_sleep(SPS);
      if (s >= 62) { u32x2 y; while (y = TG_LD(SY_SEG(s, 0) / 2 + lane), !__all(y[0] != 0u && y[1] != 0u) && ++it < SPIN_CAP) __builtin_amdgcn_s_sleep(SP2); }
    }
    CFENCE(); __builtin_amdgcn_s_barrier(); CFENCE();
    const bool mine = s < 63 || (l15 >> 3) == set;
    if (!mine) va = PJ_OOB;
    bf8 a[16];
#pragma unroll
    for (int k = 0; k < 16; k++) a[k] = __builtin_bit_cast(bf8, __builtin_amdgcn_raw_buffer_load_b128(ro, va + (k >> 3) * RP + (k & 7) * 1024, OROW(s * 32), 16));
    SB0();
    f4 acc[2] = {(f4){0, 0, 0, 0}, (f4){0, 0, 0, 0}};
#pragma unroll
    for (int k = 0; k < 16; k++) { acc[0] = MFMA(bfr[0][k], a[k], acc[0]); acc[1] = MFMA(bfr[1][k], a[k], acc[1]); }
    *(f4*)(smem + u + tid * 16) = acc[0];
    *(f4*)(smem + u + 8192 + tid * 16) = acc[1];
    if (s >= 62) CLAIM();
    asm volatile("s_waitcnt lgkmcnt(0)" ::: "memory"); __builtin_amdgcn_s_barrier(); CFENCE();
    if (wid >= 6) {
      f4 t = (f4){0, 0, 0, 0};
#pragma unroll
      for (int w = 0; w < 8; w++) t += *(const f4*)(smem + u + (wid & 1) * 8192 + (w * 64 + lane) * 16);
      if (mine) *(u64*)(p.out + (size_t)(s * 32 + hf * 16 + l15) * 1152 + 576 + np * 32 + (wid & 1) * 16 + lq * 4) = (u64)pk2(t[0], t[1]) | ((u64)pk2(t[2], t[3]) << 32);
    }
    if (s >= 62) break;
    u ^= 16384;
  }
  __syncthreads();
}

#define XS_(x) #x
#define XS(x) XS_(x)
#define GMAX(i)
#define PSTEP_
#define CNTB
#define CNTO
#define PTA
#define PRO
#define PRE
#define PROUT
#define PRDECL(c)
#define PRFIN(c)
#define PT_PROF 99
#define PTS
#define ST0
#define STX "s_memtime s[74:75]\n"
#define ST1
#define ST2
#define ST3
#define ST4
#define ST5
#define ST6
#define ST7
#define ST8
#define ST9
#define ST10
#define ST11
#define ST12
#define ST13
#define ST14
#define ST15
#define ST16
#define ST17
#define ST18
#define ST19
#define ST20
#define ST21
#define ST22
#define ST23
#define ST24
#define ST25
#define ST26
#define ST27
#define ST28
#define ST29
#define ST30
#define ST31
#define ST32
#define ST33
#define ST34
#define ST35
#define ST36
#define ST37
#define ST38
#define ST39
#define ST40
#define ST41
#define ST42
#define ST43
#define ST44
#define ST45
#define ST46
#define ST47
#define ST48
#define ST49
#define STS(i)
#define CPT 22
#define K1 5
#define K2 72
#define KE0 18
#define KE1 4
#define GL0 20
#define DUR0 110
#define DSH 2
#define LIM1 30
#define LIM2 400
#define OD 12
#define OG 12
#define RQD 9
#define GL1 4
#define K3 12
#define DUO0 62
#define DSO 2
#define DUP 64
#define XSD 0
#define TCH2 0
#define TSL 14
#define P2P 2
#define OHX 4
#define SEP 0
#define OGEO(g) (SEP ? XGEO(1) ^ ((g) * 4096) : XGEO(g))
#define TCH (1 - SEP)
#define OHR 2
#define QD 1
#define KD 9
#define BD 4
#define BW
#define BSL 2
#define OSL 12
#define RSL 8
#define SY_KMF0 (SY0 + 704)
#define SY_CS0 (SY0 + 2496)
#define CH_SA \
  "v_add_u32 v244, 0, v249\n" "v_xor_b32 v245, 32, v244\n" "s_waitcnt vmcnt(14)\n" "s_barrier\n" "ds_read_b64_tr_b16 v[0:1], v245 offset:0\n" "ds_read_b64_tr_b16 v[2:3], v245 offset:512\n" \
  "ds_read_b64_tr_b16 v[4:5], v245 offset:8192\n" "ds_read_b64_tr_b16 v[6:7], v245 offset:8704\n" "ds_read_b64_tr_b16 v[72:73], v244 offset:0\n" "ds_read_b64_tr_b16 v[74:75], v244 offset:512\n" \
  "ds_read_b64_tr_b16 v[76:77], v244 offset:8192\n" "ds_read_b64_tr_b16 v[78:79], v244 offset:8704\n" "ds_read_b64_tr_b16 v[144:145], v244 offset:32768\n" \
  "ds_read_b64_tr_b16 v[146:147], v244 offset:33280\n" "ds_read_b64_tr_b16 v[148:149], v244 offset:40960\n" "ds_read_b64_tr_b16 v[150:151], v244 offset:41472\n" "s_waitcnt lgkmcnt(0)\n" \
  "s_barrier\n" "s_add_i32 m0, %[lw], 0\n" "s_mov_b32 %[t], 1572864\n" "buffer_load_dwordx4 v248, %[r0], %[t] offen lds\n" "s_add_i32 m0, %[lw], 8192\n" "s_mov_b32 %[t], 1835008\n" \
  "buffer_load_dwordx4 v248, %[r0], %[t] offen lds\n" "s_add_i32 m0, %[lw], 16384\n" "s_mov_b32 %[t], 1572864\n" "buffer_load_dwordx4 v248, %[r1], %[t] offen lds\n" \
  "s_add_i32 m0, %[lw], 24576\n" "s_mov_b32 %[t], 1835008\n" "buffer_load_dwordx4 v248, %[r1], %[t] offen lds\n" "s_add_i32 m0, %[lw], 32768\n" "s_mov_b32 %[t], 1572864\n" \
  "buffer_load_dwordx4 v248, %[r2], %[t] offen lds\n" "s_add_i32 m0, %[lw], 40960\n" "s_mov_b32 %[t], 1835008\n" "buffer_load_dwordx4 v248, %[r2], %[t] offen lds\n" \
  "v_add_u32 v244, 49152, v249\n" "v_xor_b32 v245, 32, v244\n" "s_waitcnt vmcnt(14)\n" "s_barrier\n" "ds_read_b64_tr_b16 v[8:9], v245 offset:0\n" \
  "ds_read_b64_tr_b16 v[10:11], v245 offset:512\n" "ds_read_b64_tr_b16 v[12:13], v245 offset:8192\n" "ds_read_b64_tr_b16 v[14:15], v245 offset:8704\n" \
  "ds_read_b64_tr_b16 v[80:81], v244 offset:0\n" "ds_read_b64_tr_b16 v[82:83], v244 offset:512\n" "ds_read_b64_tr_b16 v[84:85], v244 offset:8192\n" \
  "ds_read_b64_tr_b16 v[86:87], v244 offset:8704\n" "ds_read_b64_tr_b16 v[152:153], v244 offset:32768\n" "ds_read_b64_tr_b16 v[154:155], v244 offset:33280\n" \
  "ds_read_b64_tr_b16 v[156:157], v244 offset:40960\n" "ds_read_b64_tr_b16 v[158:159], v244 offset:41472\n" "s_waitcnt lgkmcnt(0)\n" "s_barrier\n" "s_add_i32 m0, %[lw], 49152\n" \
  "s_mov_b32 %[t], 2097152\n" "buffer_load_dwordx4 v248, %[r0], %[t] offen lds\n" "s_add_i32 m0, %[lw], 57344\n" "s_mov_b32 %[t], 2359296\n" "buffer_load_dwordx4 v248, %[r0], %[t] offen lds\n" \
  "s_add_i32 m0, %[lw], 65536\n" "s_mov_b32 %[t], 2097152\n" "buffer_load_dwordx4 v248, %[r1], %[t] offen lds\n" "s_add_i32 m0, %[lw], 73728\n" "s_mov_b32 %[t], 2359296\n" \
  "buffer_load_dwordx4 v248, %[r1], %[t] offen lds\n" "s_add_i32 m0, %[lw], 81920\n" "s_mov_b32 %[t], 2097152\n" "buffer_load_dwordx4 v248, %[r2], %[t] offen lds\n" \
  "s_add_i32 m0, %[lw], 90112\n" "s_mov_b32 %[t], 2359296\n" "buffer_load_dwordx4 v248, %[r2], %[t] offen lds\n" "v_add_u32 v244, 98304, v249\n" "v_xor_b32 v245, 32, v244\n" \
  "s_waitcnt vmcnt(12)\n" "s_barrier\n" "ds_read_b64_tr_b16 v[16:17], v245 offset:0\n" "ds_read_b64_tr_b16 v[18:19], v245 offset:512\n" "ds_read_b64_tr_b16 v[20:21], v245 offset:8192\n" \
  "ds_read_b64_tr_b16 v[22:23], v245 offset:8704\n" "ds_read_b64_tr_b16 v[88:89], v244 offset:0\n" "ds_read_b64_tr_b16 v[90:91], v244 offset:512\n" \
  "ds_read_b64_tr_b16 v[92:93], v244 offset:8192\n" "ds_read_b64_tr_b16 v[94:95], v244 offset:8704\n" "ds_read_b64_tr_b16 v[160:161], v244 offset:32768\n" \
  "ds_read_b64_tr_b16 v[162:163], v244 offset:33280\n" "ds_read_b64_tr_b16 v[164:165], v244 offset:40960\n" "ds_read_b64_tr_b16 v[166:167], v244 offset:41472\n" "s_waitcnt lgkmcnt(0)\n" \
  "s_barrier\n" "s_add_i32 m0, %[lw], 98304\n" "s_mov_b32 %[t], 2621440\n" "buffer_load_dwordx4 v248, %[r0], %[t] offen lds\n" "s_add_i32 m0, %[lw], 106496\n" "s_mov_b32 %[t], 2883584\n" \
  "buffer_load_dwordx4 v248, %[r0], %[t] offen lds\n" "s_add_i32 m0, %[lw], 114688\n" "s_mov_b32 %[t], 2621440\n" "buffer_load_dwordx4 v248, %[r1], %[t] offen lds\n" \
  "s_add_i32 m0, %[lw], 122880\n" "s_mov_b32 %[t], 2883584\n" "buffer_load_dwordx4 v248, %[r1], %[t] offen lds\n" "s_add_i32 m0, %[lw], 131072\n" "s_mov_b32 %[t], 2621440\n" \
  "buffer_load_dwordx4 v248, %[r2], %[t] offen lds\n" "s_add_i32 m0, %[lw], 139264\n" "s_mov_b32 %[t], 2883584\n" "buffer_load_dwordx4 v248, %[r2], %[t] offen lds\n" "v_add_u32 v244, 0, v249\n" \
  "v_xor_b32 v245, 32, v244\n" "s_waitcnt vmcnt(12)\n" "s_barrier\n" "ds_read_b64_tr_b16 v[24:25], v245 offset:0\n" "ds_read_b64_tr_b16 v[26:27], v245 offset:512\n" \
  "ds_read_b64_tr_b16 v[28:29], v245 offset:8192\n" "ds_read_b64_tr_b16 v[30:31], v245 offset:8704\n" "ds_read_b64_tr_b16 v[96:97], v244 offset:0\n" \
  "ds_read_b64_tr_b16 v[98:99], v244 offset:512\n" "ds_read_b64_tr_b16 v[100:101], v244 offset:8192\n" "ds_read_b64_tr_b16 v[102:103], v244 offset:8704\n" \
  "ds_read_b64_tr_b16 v[168:169], v244 offset:32768\n" "ds_read_b64_tr_b16 v[170:171], v244 offset:33280\n" "ds_read_b64_tr_b16 v[172:173], v244 offset:40960\n" \
  "ds_read_b64_tr_b16 v[174:175], v244 offset:41472\n" "s_waitcnt lgkmcnt(0)\n" "s_barrier\n" "s_add_i32 m0, %[lw], 0\n" "s_mov_b32 %[t], 3145728\n" \
  "buffer_load_dwordx4 v248, %[r0], %[t] offen lds\n" "s_add_i32 m0, %[lw], 8192\n" "s_mov_b32 %[t], 3407872\n" "buffer_load_dwordx4 v248, %[r0], %[t] offen lds\n" \
  "s_add_i32 m0, %[lw], 16384\n" "s_mov_b32 %[t], 3145728\n" "buffer_load_dwordx4 v248, %[r1], %[t] offen lds\n" "s_add_i32 m0, %[lw], 24576\n" "s_mov_b32 %[t], 3407872\n" \
  "buffer_load_dwordx4 v248, %[r1], %[t] offen lds\n" "s_add_i32 m0, %[lw], 32768\n" "s_mov_b32 %[t], 3145728\n" "buffer_load_dwordx4 v248, %[r2], %[t] offen lds\n" \
  "s_add_i32 m0, %[lw], 40960\n" "s_mov_b32 %[t], 3407872\n" "buffer_load_dwordx4 v248, %[r2], %[t] offen lds\n" "v_add_u32 v244, 49152, v249\n" "v_xor_b32 v245, 32, v244\n" \
  "s_waitcnt vmcnt(12)\n" "s_barrier\n" "ds_read_b64_tr_b16 v[32:33], v245 offset:0\n" "ds_read_b64_tr_b16 v[34:35], v245 offset:512\n" "ds_read_b64_tr_b16 v[36:37], v245 offset:8192\n" \
  "ds_read_b64_tr_b16 v[38:39], v245 offset:8704\n" "ds_read_b64_tr_b16 v[104:105], v244 offset:0\n" "ds_read_b64_tr_b16 v[106:107], v244 offset:512\n" \
  "ds_read_b64_tr_b16 v[108:109], v244 offset:8192\n" "ds_read_b64_tr_b16 v[110:111], v244 offset:8704\n" "ds_read_b64_tr_b16 v[176:177], v244 offset:32768\n" \
  "ds_read_b64_tr_b16 v[178:179], v244 offset:33280\n" "ds_read_b64_tr_b16 v[180:181], v244 offset:40960\n" "ds_read_b64_tr_b16 v[182:183], v244 offset:41472\n" "s_waitcnt lgkmcnt(0)\n" \
  "s_barrier\n" "s_add_i32 m0, %[lw], 49152\n" "s_mov_b32 %[t], 3670016\n" "buffer_load_dwordx4 v248, %[r0], %[t] offen lds\n" "s_add_i32 m0, %[lw], 57344\n" "s_mov_b32 %[t], 3932160\n" \
  "buffer_load_dwordx4 v248, %[r0], %[t] offen lds\n" "s_add_i32 m0, %[lw], 65536\n" "s_mov_b32 %[t], 3670016\n" "buffer_load_dwordx4 v248, %[r1], %[t] offen lds\n" \
  "s_add_i32 m0, %[lw], 73728\n" "s_mov_b32 %[t], 3932160\n" "buffer_load_dwordx4 v248, %[r1], %[t] offen lds\n" "s_add_i32 m0, %[lw], 81920\n" "s_mov_b32 %[t], 3670016\n" \
  "buffer_load_dwordx4 v248, %[r2], %[t] offen lds\n" "s_add_i32 m0, %[lw], 90112\n" "s_mov_b32 %[t], 3932160\n" "buffer_load_dwordx4 v248, %[r2], %[t] offen lds\n" \
  "v_add_u32 v244, 98304, v249\n" "v_xor_b32 v245, 32, v244\n" "s_waitcnt vmcnt(12)\n" "s_barrier\n" "ds_read_b64_tr_b16 v[40:41], v245 offset:0\n" \
  "ds_read_b64_tr_b16 v[42:43], v245 offset:512\n" "ds_read_b64_tr_b16 v[44:45], v245 offset:8192\n" "ds_read_b64_tr_b16 v[46:47], v245 offset:8704\n" \
  "ds_read_b64_tr_b16 v[112:113], v244 offset:0\n" "ds_read_b64_tr_b16 v[114:115], v244 offset:512\n" "ds_read_b64_tr_b16 v[116:117], v244 offset:8192\n" \
  "ds_read_b64_tr_b16 v[118:119], v244 offset:8704\n" "s_waitcnt lgkmcnt(0)\n" "s_barrier\n" "s_add_i32 m0, %[lw], 98304\n" "s_mov_b32 %[t], 4194304\n" \
  "buffer_load_dwordx4 v248, %[r0], %[t] offen lds\n" "s_add_i32 m0, %[lw], 106496\n" "s_mov_b32 %[t], 4456448\n" "buffer_load_dwordx4 v248, %[r0], %[t] offen lds\n" \
  "s_add_i32 m0, %[lw], 114688\n" "s_mov_b32 %[t], 4194304\n" "buffer_load_dwordx4 v248, %[r1], %[t] offen lds\n" "s_add_i32 m0, %[lw], 122880\n" "s_mov_b32 %[t], 4456448\n" \
  "buffer_load_dwordx4 v248, %[r1], %[t] offen lds\n" "s_add_i32 m0, %[lw], 131072\n" "s_mov_b32 %[t], 4194304\n" "buffer_load_dwordx4 v248, %[r2], %[t] offen lds\n" \
  "s_add_i32 m0, %[lw], 139264\n" "s_mov_b32 %[t], 4456448\n" "buffer_load_dwordx4 v248, %[r2], %[t] offen lds\n" "v_add_u32 v244, 0, v249\n" "v_xor_b32 v245, 32, v244\n" \
  "s_waitcnt vmcnt(12)\n" "s_barrier\n" "ds_read_b64_tr_b16 v[48:49], v245 offset:0\n" "ds_read_b64_tr_b16 v[50:51], v245 offset:512\n" "ds_read_b64_tr_b16 v[52:53], v245 offset:8192\n" \
  "ds_read_b64_tr_b16 v[54:55], v245 offset:8704\n" "ds_read_b64_tr_b16 v[120:121], v244 offset:0\n" "ds_read_b64_tr_b16 v[122:123], v244 offset:512\n" \
  "ds_read_b64_tr_b16 v[124:125], v244 offset:8192\n" "ds_read_b64_tr_b16 v[126:127], v244 offset:8704\n" "s_waitcnt lgkmcnt(0)\n" "s_barrier\n" "v_add_u32 v244, 49152, v249\n" \
  "v_xor_b32 v245, 32, v244\n" "s_waitcnt vmcnt(6)\n" "s_barrier\n" "ds_read_b64_tr_b16 v[56:57], v245 offset:0\n" "ds_read_b64_tr_b16 v[58:59], v245 offset:512\n" \
  "ds_read_b64_tr_b16 v[60:61], v245 offset:8192\n" "ds_read_b64_tr_b16 v[62:63], v245 offset:8704\n" "ds_read_b64_tr_b16 v[128:129], v244 offset:0\n" \
  "ds_read_b64_tr_b16 v[130:131], v244 offset:512\n" "ds_read_b64_tr_b16 v[132:133], v244 offset:8192\n" "ds_read_b64_tr_b16 v[134:135], v244 offset:8704\n" "s_waitcnt lgkmcnt(0)\n" \
  "s_barrier\n" "v_add_u32 v244, 98304, v249\n" "v_xor_b32 v245, 32, v244\n" "s_waitcnt vmcnt(0)\n" "s_barrier\n" "ds_read_b64_tr_b16 v[64:65], v245 offset:0\n" \
  "ds_read_b64_tr_b16 v[66:67], v245 offset:512\n" "ds_read_b64_tr_b16 v[68:69], v245 offset:8192\n" "ds_read_b64_tr_b16 v[70:71], v245 offset:8704\n" \
  "ds_read_b64_tr_b16 v[136:137], v244 offset:0\n" "ds_read_b64_tr_b16 v[138:139], v244 offset:512\n" "ds_read_b64_tr_b16 v[140:141], v244 offset:8192\n" \
  "ds_read_b64_tr_b16 v[142:143], v244 offset:8704\n" "s_waitcnt lgkmcnt(0)\n" "s_barrier\n"
#define CH_SB \
  "v_add_u32 v244, 0, v249\n" "v_xor_b32 v245, 32, v244\n" "s_waitcnt vmcnt(14)\n" "s_barrier\n" "ds_read_b64_tr_b16 v[0:1], v245 offset:16384\n" \
  "ds_read_b64_tr_b16 v[2:3], v245 offset:16896\n" "ds_read_b64_tr_b16 v[4:5], v245 offset:24576\n" "ds_read_b64_tr_b16 v[6:7], v245 offset:25088\n" \
  "ds_read_b64_tr_b16 v[48:49], v244 offset:16384\n" "ds_read_b64_tr_b16 v[50:51], v244 offset:16896\n" "ds_read_b64_tr_b16 v[52:53], v244 offset:24576\n" \
  "ds_read_b64_tr_b16 v[54:55], v244 offset:25088\n" "ds_read_b64_tr_b16 v[96:97], v245 offset:32768\n" "ds_read_b64_tr_b16 v[98:99], v245 offset:33280\n" \
  "ds_read_b64_tr_b16 v[100:101], v245 offset:40960\n" "ds_read_b64_tr_b16 v[102:103], v245 offset:41472\n" "s_waitcnt lgkmcnt(0)\n" "s_barrier\n" "s_add_i32 m0, %[lw], 0\n" \
  "s_mov_b32 %[t], 1572864\n" "buffer_load_dwordx4 v248, %[r0], %[t] offen lds\n" "s_add_i32 m0, %[lw], 8192\n" "s_mov_b32 %[t], 1835008\n" "buffer_load_dwordx4 v248, %[r0], %[t] offen lds\n" \
  "s_add_i32 m0, %[lw], 16384\n" "s_mov_b32 %[t], 1572864\n" "buffer_load_dwordx4 v248, %[r1], %[t] offen lds\n" "s_add_i32 m0, %[lw], 24576\n" "s_mov_b32 %[t], 1835008\n" \
  "buffer_load_dwordx4 v248, %[r1], %[t] offen lds\n" "s_add_i32 m0, %[lw], 32768\n" "s_mov_b32 %[t], 1572864\n" "buffer_load_dwordx4 v248, %[r2], %[t] offen lds\n" \
  "s_add_i32 m0, %[lw], 40960\n" "s_mov_b32 %[t], 1835008\n" "buffer_load_dwordx4 v248, %[r2], %[t] offen lds\n" "v_add_u32 v244, 49152, v249\n" "v_xor_b32 v245, 32, v244\n" \
  "s_waitcnt vmcnt(14)\n" "s_barrier\n" "ds_read_b64_tr_b16 v[8:9], v245 offset:16384\n" "ds_read_b64_tr_b16 v[10:11], v245 offset:16896\n" "ds_read_b64_tr_b16 v[12:13], v245 offset:24576\n" \
  "ds_read_b64_tr_b16 v[14:15], v245 offset:25088\n" "ds_read_b64_tr_b16 v[56:57], v244 offset:16384\n" "ds_read_b64_tr_b16 v[58:59], v244 offset:16896\n" \
  "ds_read_b64_tr_b16 v[60:61], v244 offset:24576\n" "ds_read_b64_tr_b16 v[62:63], v244 offset:25088\n" "ds_read_b64_tr_b16 v[104:105], v245 offset:32768\n" \
  "ds_read_b64_tr_b16 v[106:107], v245 offset:33280\n" "ds_read_b64_tr_b16 v[108:109], v245 offset:40960\n" "ds_read_b64_tr_b16 v[110:111], v245 offset:41472\n" "s_waitcnt lgkmcnt(0)\n" \
  "s_barrier\n" "s_add_i32 m0, %[lw], 49152\n" "s_mov_b32 %[t], 2097152\n" "buffer_load_dwordx4 v248, %[r0], %[t] offen lds\n" "s_add_i32 m0, %[lw], 57344\n" "s_mov_b32 %[t], 2359296\n" \
  "buffer_load_dwordx4 v248, %[r0], %[t] offen lds\n" "s_add_i32 m0, %[lw], 65536\n" "s_mov_b32 %[t], 2097152\n" "buffer_load_dwordx4 v248, %[r1], %[t] offen lds\n" \
  "s_add_i32 m0, %[lw], 73728\n" "s_mov_b32 %[t], 2359296\n" "buffer_load_dwordx4 v248, %[r1], %[t] offen lds\n" "s_add_i32 m0, %[lw], 81920\n" "s_mov_b32 %[t], 2097152\n" \
  "buffer_load_dwordx4 v248, %[r2], %[t] offen lds\n" "s_add_i32 m0, %[lw], 90112\n" "s_mov_b32 %[t], 2359296\n" "buffer_load_dwordx4 v248, %[r2], %[t] offen lds\n" \
  "v_add_u32 v244, 98304, v249\n" "v_xor_b32 v245, 32, v244\n" "s_waitcnt vmcnt(12)\n" "s_barrier\n" "ds_read_b64_tr_b16 v[16:17], v245 offset:16384\n" \
  "ds_read_b64_tr_b16 v[18:19], v245 offset:16896\n" "ds_read_b64_tr_b16 v[20:21], v245 offset:24576\n" "ds_read_b64_tr_b16 v[22:23], v245 offset:25088\n" \
  "ds_read_b64_tr_b16 v[64:65], v244 offset:16384\n" "ds_read_b64_tr_b16 v[66:67], v244 offset:16896\n" "ds_read_b64_tr_b16 v[68:69], v244 offset:24576\n" \
  "ds_read_b64_tr_b16 v[70:71], v244 offset:25088\n" "ds_read_b64_tr_b16 v[112:113], v245 offset:32768\n" "ds_read_b64_tr_b16 v[114:115], v245 offset:33280\n" \
  "ds_read_b64_tr_b16 v[116:117], v245 offset:40960\n" "ds_read_b64_tr_b16 v[118:119], v245 offset:41472\n" "s_waitcnt lgkmcnt(0)\n" "s_barrier\n" "s_add_i32 m0, %[lw], 98304\n" \
  "s_mov_b32 %[t], 2621440\n" "buffer_load_dwordx4 v248, %[r0], %[t] offen lds\n" "s_add_i32 m0, %[lw], 106496\n" "s_mov_b32 %[t], 2883584\n" "buffer_load_dwordx4 v248, %[r0], %[t] offen lds\n" \
  "s_add_i32 m0, %[lw], 114688\n" "s_mov_b32 %[t], 2621440\n" "buffer_load_dwordx4 v248, %[r1], %[t] offen lds\n" "s_add_i32 m0, %[lw], 122880\n" "s_mov_b32 %[t], 2883584\n" \
  "buffer_load_dwordx4 v248, %[r1], %[t] offen lds\n" "s_add_i32 m0, %[lw], 131072\n" "s_mov_b32 %[t], 2621440\n" "buffer_load_dwordx4 v248, %[r2], %[t] offen lds\n" \
  "s_add_i32 m0, %[lw], 139264\n" "s_mov_b32 %[t], 2883584\n" "buffer_load_dwordx4 v248, %[r2], %[t] offen lds\n" "v_add_u32 v244, 0, v249\n" "v_xor_b32 v245, 32, v244\n" \
  "s_waitcnt vmcnt(12)\n" "s_barrier\n" "ds_read_b64_tr_b16 v[24:25], v245 offset:16384\n" "ds_read_b64_tr_b16 v[26:27], v245 offset:16896\n" "ds_read_b64_tr_b16 v[28:29], v245 offset:24576\n" \
  "ds_read_b64_tr_b16 v[30:31], v245 offset:25088\n" "ds_read_b64_tr_b16 v[72:73], v244 offset:16384\n" "ds_read_b64_tr_b16 v[74:75], v244 offset:16896\n" \
  "ds_read_b64_tr_b16 v[76:77], v244 offset:24576\n" "ds_read_b64_tr_b16 v[78:79], v244 offset:25088\n" "ds_read_b64_tr_b16 v[120:121], v245 offset:32768\n" \
  "ds_read_b64_tr_b16 v[122:123], v245 offset:33280\n" "ds_read_b64_tr_b16 v[124:125], v245 offset:40960\n" "ds_read_b64_tr_b16 v[126:127], v245 offset:41472\n" "s_waitcnt lgkmcnt(0)\n" \
  "s_barrier\n" "s_add_i32 m0, %[lw], 0\n" "s_mov_b32 %[t], 3145728\n" "buffer_load_dwordx4 v248, %[r0], %[t] offen lds\n" "s_add_i32 m0, %[lw], 8192\n" "s_mov_b32 %[t], 3407872\n" \
  "buffer_load_dwordx4 v248, %[r0], %[t] offen lds\n" "s_add_i32 m0, %[lw], 16384\n" "s_mov_b32 %[t], 3145728\n" "buffer_load_dwordx4 v248, %[r1], %[t] offen lds\n" \
  "s_add_i32 m0, %[lw], 24576\n" "s_mov_b32 %[t], 3407872\n" "buffer_load_dwordx4 v248, %[r1], %[t] offen lds\n" "s_add_i32 m0, %[lw], 32768\n" "s_mov_b32 %[t], 3145728\n" \
  "buffer_load_dwordx4 v248, %[r2], %[t] offen lds\n" "s_add_i32 m0, %[lw], 40960\n" "s_mov_b32 %[t], 3407872\n" "buffer_load_dwordx4 v248, %[r2], %[t] offen lds\n" \
  "v_add_u32 v244, 49152, v249\n" "v_xor_b32 v245, 32, v244\n" "s_waitcnt vmcnt(12)\n" "s_barrier\n" "ds_read_b64_tr_b16 v[32:33], v245 offset:16384\n" \
  "ds_read_b64_tr_b16 v[34:35], v245 offset:16896\n" "ds_read_b64_tr_b16 v[36:37], v245 offset:24576\n" "ds_read_b64_tr_b16 v[38:39], v245 offset:25088\n" \
  "ds_read_b64_tr_b16 v[80:81], v244 offset:16384\n" "ds_read_b64_tr_b16 v[82:83], v244 offset:16896\n" "ds_read_b64_tr_b16 v[84:85], v244 offset:24576\n" \
  "ds_read_b64_tr_b16 v[86:87], v244 offset:25088\n" "ds_read_b64_tr_b16 v[128:129], v245 offset:32768\n" "ds_read_b64_tr_b16 v[130:131], v245 offset:33280\n" \
  "ds_read_b64_tr_b16 v[132:133], v245 offset:40960\n" "ds_read_b64_tr_b16 v[134:135], v245 offset:41472\n" "s_waitcnt lgkmcnt(0)\n" "s_barrier\n" "s_add_i32 m0, %[lw], 49152\n" \
  "s_mov_b32 %[t], 3670016\n" "buffer_load_dwordx4 v248, %[r0], %[t] offen lds\n" "s_add_i32 m0, %[lw], 57344\n" "s_mov_b32 %[t], 3932160\n" "buffer_load_dwordx4 v248, %[r0], %[t] offen lds\n" \
  "s_add_i32 m0, %[lw], 65536\n" "s_mov_b32 %[t], 3670016\n" "buffer_load_dwordx4 v248, %[r1], %[t] offen lds\n" "s_add_i32 m0, %[lw], 73728\n" "s_mov_b32 %[t], 3932160\n" \
  "buffer_load_dwordx4 v248, %[r1], %[t] offen lds\n" "s_add_i32 m0, %[lw], 81920\n" "s_mov_b32 %[t], 3670016\n" "buffer_load_dwordx4 v248, %[r2], %[t] offen lds\n" \
  "s_add_i32 m0, %[lw], 90112\n" "s_mov_b32 %[t], 3932160\n" "buffer_load_dwordx4 v248, %[r2], %[t] offen lds\n" "v_add_u32 v244, 98304, v249\n" "v_xor_b32 v245, 32, v244\n" \
  "s_waitcnt vmcnt(12)\n" "s_barrier\n" "ds_read_b64_tr_b16 v[40:41], v245 offset:16384\n" "ds_read_b64_tr_b16 v[42:43], v245 offset:16896\n" "ds_read_b64_tr_b16 v[44:45], v245 offset:24576\n" \
  "ds_read_b64_tr_b16 v[46:47], v245 offset:25088\n" "ds_read_b64_tr_b16 v[88:89], v244 offset:16384\n" "ds_read_b64_tr_b16 v[90:91], v244 offset:16896\n" \
  "ds_read_b64_tr_b16 v[92:93], v244 offset:24576\n" "ds_read_b64_tr_b16 v[94:95], v244 offset:25088\n" "ds_read_b64_tr_b16 v[136:137], v245 offset:32768\n" \
  "ds_read_b64_tr_b16 v[138:139], v245 offset:33280\n" "ds_read_b64_tr_b16 v[140:141], v245 offset:40960\n" "ds_read_b64_tr_b16 v[142:143], v245 offset:41472\n" \
  "ds_read_b64_tr_b16 v[144:145], v244 offset:32768\n" "ds_read_b64_tr_b16 v[146:147], v244 offset:33280\n" "ds_read_b64_tr_b16 v[148:149], v244 offset:40960\n" \
  "ds_read_b64_tr_b16 v[150:151], v244 offset:41472\n" "s_waitcnt lgkmcnt(0)\n" "s_barrier\n" "s_add_i32 m0, %[lw], 98304\n" "s_mov_b32 %[t], 4194304\n" \
  "buffer_load_dwordx4 v248, %[r0], %[t] offen lds\n" "s_add_i32 m0, %[lw], 106496\n" "s_mov_b32 %[t], 4456448\n" "buffer_load_dwordx4 v248, %[r0], %[t] offen lds\n" \
  "s_add_i32 m0, %[lw], 114688\n" "s_mov_b32 %[t], 4194304\n" "buffer_load_dwordx4 v248, %[r1], %[t] offen lds\n" "s_add_i32 m0, %[lw], 122880\n" "s_mov_b32 %[t], 4456448\n" \
  "buffer_load_dwordx4 v248, %[r1], %[t] offen lds\n" "s_add_i32 m0, %[lw], 131072\n" "s_mov_b32 %[t], 4194304\n" "buffer_load_dwordx4 v248, %[r2], %[t] offen lds\n" \
  "s_add_i32 m0, %[lw], 139264\n" "s_mov_b32 %[t], 4456448\n" "buffer_load_dwordx4 v248, %[r2], %[t] offen lds\n" "v_add_u32 v244, 0, v249\n" "v_xor_b32 v245, 32, v244\n" \
  "s_waitcnt vmcnt(12)\n" "s_barrier\n" "ds_read_b64_tr_b16 v[176:177], v245 offset:16384\n" "ds_read_b64_tr_b16 v[178:179], v245 offset:16896\n" \
  "ds_read_b64_tr_b16 v[180:181], v245 offset:24576\n" "ds_read_b64_tr_b16 v[182:183], v245 offset:25088\n" "ds_read_b64_tr_b16 v[184:185], v244 offset:16384\n" \
  "ds_read_b64_tr_b16 v[186:187], v244 offset:16896\n" "ds_read_b64_tr_b16 v[188:189], v244 offset:24576\n" "ds_read_b64_tr_b16 v[190:191], v244 offset:25088\n" \
  "ds_read_b64_tr_b16 v[192:193], v245 offset:32768\n" "ds_read_b64_tr_b16 v[194:195], v245 offset:33280\n" "ds_read_b64_tr_b16 v[196:197], v245 offset:40960\n" \
  "ds_read_b64_tr_b16 v[198:199], v245 offset:41472\n" "ds_read_b64_tr_b16 v[152:153], v244 offset:32768\n" "ds_read_b64_tr_b16 v[154:155], v244 offset:33280\n" \
  "ds_read_b64_tr_b16 v[156:157], v244 offset:40960\n" "ds_read_b64_tr_b16 v[158:159], v244 offset:41472\n" "s_waitcnt lgkmcnt(0)\n" "s_barrier\n" "ds_write_b128 v250, v[176:179] offset:0\n" \
  "ds_write_b128 v250, v[180:183] offset:1024\n" "ds_write_b128 v250, v[184:187] offset:8192\n" "ds_write_b128 v250, v[188:191] offset:9216\n" "ds_write_b128 v250, v[192:195] offset:16384\n" \
  "ds_write_b128 v250, v[196:199] offset:17408\n" "s_waitcnt lgkmcnt(0)\n" "v_add_u32 v244, 49152, v249\n" "v_xor_b32 v245, 32, v244\n" "s_waitcnt vmcnt(6)\n" "s_barrier\n" \
  "ds_read_b64_tr_b16 v[176:177], v245 offset:16384\n" "ds_read_b64_tr_b16 v[178:179], v245 offset:16896\n" "ds_read_b64_tr_b16 v[180:181], v245 offset:24576\n" \
  "ds_read_b64_tr_b16 v[182:183], v245 offset:25088\n" "ds_read_b64_tr_b16 v[184:185], v244 offset:16384\n" "ds_read_b64_tr_b16 v[186:187], v244 offset:16896\n" \
  "ds_read_b64_tr_b16 v[188:189], v244 offset:24576\n" "ds_read_b64_tr_b16 v[190:191], v244 offset:25088\n" "ds_read_b64_tr_b16 v[192:193], v245 offset:32768\n" \
  "ds_read_b64_tr_b16 v[194:195], v245 offset:33280\n" "ds_read_b64_tr_b16 v[196:197], v245 offset:40960\n" "ds_read_b64_tr_b16 v[198:199], v245 offset:41472\n" \
  "ds_read_b64_tr_b16 v[160:161], v244 offset:32768\n" "ds_read_b64_tr_b16 v[162:163], v244 offset:33280\n" "ds_read_b64_tr_b16 v[164:165], v244 offset:40960\n" \
  "ds_read_b64_tr_b16 v[166:167], v244 offset:41472\n" "s_waitcnt lgkmcnt(0)\n" "s_barrier\n" "v_add_u32 v250, 24576, v250\n" "ds_write_b128 v250, v[176:179] offset:0\n" \
  "ds_write_b128 v250, v[180:183] offset:1024\n" "ds_write_b128 v250, v[184:187] offset:8192\n" "ds_write_b128 v250, v[188:191] offset:9216\n" "ds_write_b128 v250, v[192:195] offset:16384\n" \
  "ds_write_b128 v250, v[196:199] offset:17408\n" "s_waitcnt lgkmcnt(0)\n" "v_add_u32 v244, 98304, v249\n" "v_xor_b32 v245, 32, v244\n" "s_waitcnt vmcnt(0)\n" "s_barrier\n" \
  "ds_read_b64_tr_b16 v[176:177], v245 offset:16384\n" "ds_read_b64_tr_b16 v[178:179], v245 offset:16896\n" "ds_read_b64_tr_b16 v[180:181], v245 offset:24576\n" \
  "ds_read_b64_tr_b16 v[182:183], v245 offset:25088\n" "ds_read_b64_tr_b16 v[184:185], v244 offset:16384\n" "ds_read_b64_tr_b16 v[186:187], v244 offset:16896\n" \
  "ds_read_b64_tr_b16 v[188:189], v244 offset:24576\n" "ds_read_b64_tr_b16 v[190:191], v244 offset:25088\n" "ds_read_b64_tr_b16 v[192:193], v245 offset:32768\n" \
  "ds_read_b64_tr_b16 v[194:195], v245 offset:33280\n" "ds_read_b64_tr_b16 v[196:197], v245 offset:40960\n" "ds_read_b64_tr_b16 v[198:199], v245 offset:41472\n" \
  "ds_read_b64_tr_b16 v[168:169], v244 offset:32768\n" "ds_read_b64_tr_b16 v[170:171], v244 offset:33280\n" "ds_read_b64_tr_b16 v[172:173], v244 offset:40960\n" \
  "ds_read_b64_tr_b16 v[174:175], v244 offset:41472\n" "s_waitcnt lgkmcnt(0)\n" "s_barrier\n" "v_add_u32 v250, 24576, v250\n" "ds_write_b128 v250, v[176:179] offset:0\n" \
  "ds_write_b128 v250, v[180:183] offset:1024\n" "ds_write_b128 v250, v[184:187] offset:8192\n" "ds_write_b128 v250, v[188:191] offset:9216\n" "ds_write_b128 v251, v[192:195] offset:23552\n" \
  "ds_write_b128 v251, v[196:199] offset:27648\n" "s_waitcnt lgkmcnt(0)\n"
#define CH_SO \
  "s_waitcnt vmcnt(10)\n" "ds_read_b64_tr_b16 v[0:1], v218 offset:0\n" "ds_read_b64_tr_b16 v[2:3], v218 offset:128\n" "ds_read_b64_tr_b16 v[4:5], v218 offset:4096\n" \
  "ds_read_b64_tr_b16 v[6:7], v218 offset:4224\n" "v_xor_b32 v200, 16, v218\n" "ds_read_b64_tr_b16 v[8:9], v200 offset:0\n" "ds_read_b64_tr_b16 v[10:11], v200 offset:128\n" \
  "ds_read_b64_tr_b16 v[12:13], v200 offset:4096\n" "ds_read_b64_tr_b16 v[14:15], v200 offset:4224\n" "v_xor_b32 v200, 32, v218\n" "ds_read_b64_tr_b16 v[16:17], v200 offset:0\n" \
  "ds_read_b64_tr_b16 v[18:19], v200 offset:128\n" "ds_read_b64_tr_b16 v[20:21], v200 offset:4096\n" "ds_read_b64_tr_b16 v[22:23], v200 offset:4224\n" "v_xor_b32 v200, 48, v218\n" \
  "ds_read_b64_tr_b16 v[24:25], v200 offset:0\n" "ds_read_b64_tr_b16 v[26:27], v200 offset:128\n" "ds_read_b64_tr_b16 v[28:29], v200 offset:4096\n" \
  "ds_read_b64_tr_b16 v[30:31], v200 offset:4224\n" "s_waitcnt lgkmcnt(0)\n" "s_add_i32 m0, %[lw], 0\n" "s_mov_b32 %[t], 294912\n" "buffer_load_dwordx4 v216, %[r], %[t] offen lds\n" \
  "s_add_i32 m0, %[lw], 1024\n" "s_mov_b32 %[t], 313344\n" "buffer_load_dwordx4 v216, %[r], %[t] offen lds\n" "s_add_i32 m0, %[lw], 2048\n" "s_mov_b32 %[t], 331776\n" \
  "buffer_load_dwordx4 v216, %[r], %[t] offen lds\n" "s_add_i32 m0, %[lw], 3072\n" "s_mov_b32 %[t], 350208\n" "buffer_load_dwordx4 v216, %[r], %[t] offen lds\n" "s_add_i32 m0, %[lw], 4096\n" \
  "s_mov_b32 %[t], 368640\n" "buffer_load_dwordx4 v216, %[r], %[t] offen lds\n" "s_add_i32 m0, %[lw], 5120\n" "s_mov_b32 %[t], 387072\n" "buffer_load_dwordx4 v216, %[r], %[t] offen lds\n" \
  "s_add_i32 m0, %[lw], 6144\n" "s_mov_b32 %[t], 405504\n" "buffer_load_dwordx4 v216, %[r], %[t] offen lds\n" "s_add_i32 m0, %[lw], 7168\n" "s_mov_b32 %[t], 423936\n" \
  "buffer_load_dwordx4 v216, %[r], %[t] offen lds\n" "s_waitcnt vmcnt(8)\n" "buffer_load_dword v221, v220, %[r], 0 offen\n" "ds_read_b64_tr_b16 v[40:41], v218 offset:8192\n" \
  "ds_read_b64_tr_b16 v[42:43], v218 offset:8320\n" "ds_read_b64_tr_b16 v[44:45], v218 offset:12288\n" "ds_read_b64_tr_b16 v[46:47], v218 offset:12416\n" "v_xor_b32 v200, 16, v218\n" \
  "ds_read_b64_tr_b16 v[48:49], v200 offset:8192\n" "ds_read_b64_tr_b16 v[50:51], v200 offset:8320\n" "ds_read_b64_tr_b16 v[52:53], v200 offset:12288\n" \
  "ds_read_b64_tr_b16 v[54:55], v200 offset:12416\n" "v_xor_b32 v200, 32, v218\n" "ds_read_b64_tr_b16 v[56:57], v200 offset:8192\n" "ds_read_b64_tr_b16 v[58:59], v200 offset:8320\n" \
  "ds_read_b64_tr_b16 v[60:61], v200 offset:12288\n" "ds_read_b64_tr_b16 v[62:63], v200 offset:12416\n" "v_xor_b32 v200, 48, v218\n" "ds_read_b64_tr_b16 v[64:65], v200 offset:8192\n" \
  "ds_read_b64_tr_b16 v[66:67], v200 offset:8320\n" "ds_read_b64_tr_b16 v[68:69], v200 offset:12288\n" "ds_read_b64_tr_b16 v[70:71], v200 offset:12416\n" "s_waitcnt lgkmcnt(0)\n" \
  "s_add_i32 m0, %[lw], 8192\n" "s_mov_b32 %[t], 442368\n" "buffer_load_dwordx4 v216, %[r], %[t] offen lds\n" "s_add_i32 m0, %[lw], 9216\n" "s_mov_b32 %[t], 460800\n" \
  "buffer_load_dwordx4 v216, %[r], %[t] offen lds\n" "s_add_i32 m0, %[lw], 10240\n" "s_mov_b32 %[t], 479232\n" "buffer_load_dwordx4 v216, %[r], %[t] offen lds\n" "s_add_i32 m0, %[lw], 11264\n" \
  "s_mov_b32 %[t], 497664\n" "buffer_load_dwordx4 v216, %[r], %[t] offen lds\n" "s_add_i32 m0, %[lw], 12288\n" "s_mov_b32 %[t], 516096\n" "buffer_load_dwordx4 v216, %[r], %[t] offen lds\n" \
  "s_add_i32 m0, %[lw], 13312\n" "s_mov_b32 %[t], 534528\n" "buffer_load_dwordx4 v216, %[r], %[t] offen lds\n" "s_add_i32 m0, %[lw], 14336\n" "s_mov_b32 %[t], 552960\n" \
  "buffer_load_dwordx4 v216, %[r], %[t] offen lds\n" "s_add_i32 m0, %[lw], 15360\n" "s_mov_b32 %[t], 571392\n" "buffer_load_dwordx4 v216, %[r], %[t] offen lds\n" "s_waitcnt vmcnt(8)\n" \
  "ds_read_b64_tr_b16 v[80:81], v218 offset:0\n" "ds_read_b64_tr_b16 v[82:83], v218 offset:128\n" "ds_read_b64_tr_b16 v[84:85], v218 offset:4096\n" \
  "ds_read_b64_tr_b16 v[86:87], v218 offset:4224\n" "v_xor_b32 v200, 16, v218\n" "ds_read_b64_tr_b16 v[88:89], v200 offset:0\n" "ds_read_b64_tr_b16 v[90:91], v200 offset:128\n" \
  "ds_read_b64_tr_b16 v[92:93], v200 offset:4096\n" "ds_read_b64_tr_b16 v[94:95], v200 offset:4224\n" "v_xor_b32 v200, 32, v218\n" "ds_read_b64_tr_b16 v[96:97], v200 offset:0\n" \
  "ds_read_b64_tr_b16 v[98:99], v200 offset:128\n" "ds_read_b64_tr_b16 v[100:101], v200 offset:4096\n" "ds_read_b64_tr_b16 v[102:103], v200 offset:4224\n" "v_xor_b32 v200, 48, v218\n" \
  "ds_read_b64_tr_b16 v[104:105], v200 offset:0\n" "ds_read_b64_tr_b16 v[106:107], v200 offset:128\n" "ds_read_b64_tr_b16 v[108:109], v200 offset:4096\n" \
  "ds_read_b64_tr_b16 v[110:111], v200 offset:4224\n" "s_waitcnt lgkmcnt(0)\n" "s_add_i32 m0, %[lw], 0\n" "s_mov_b32 %[t], 0\n" "buffer_load_dwordx4 v217, %[r], %[t] offen lds\n" \
  "s_add_i32 m0, %[lw], 1024\n" "s_mov_b32 %[t], 73728\n" "buffer_load_dwordx4 v217, %[r], %[t] offen lds\n" "s_add_i32 m0, %[lw], 2048\n" "s_mov_b32 %[t], 147456\n" \
  "buffer_load_dwordx4 v217, %[r], %[t] offen lds\n" "s_add_i32 m0, %[lw], 3072\n" "s_mov_b32 %[t], 221184\n" "buffer_load_dwordx4 v217, %[r], %[t] offen lds\n" "s_add_i32 m0, %[lw], 4096\n" \
  "s_mov_b32 %[t], 294912\n" "buffer_load_dwordx4 v217, %[r], %[t] offen lds\n" "s_add_i32 m0, %[lw], 5120\n" "s_mov_b32 %[t], 368640\n" "buffer_load_dwordx4 v217, %[r], %[t] offen lds\n" \
  "s_add_i32 m0, %[lw], 6144\n" "s_mov_b32 %[t], 442368\n" "buffer_load_dwordx4 v217, %[r], %[t] offen lds\n" "s_add_i32 m0, %[lw], 7168\n" "s_mov_b32 %[t], 516096\n" \
  "buffer_load_dwordx4 v217, %[r], %[t] offen lds\n" "s_waitcnt vmcnt(8)\n" "ds_read_b64_tr_b16 v[120:121], v218 offset:8192\n" "ds_read_b64_tr_b16 v[122:123], v218 offset:8320\n" \
  "ds_read_b64_tr_b16 v[124:125], v218 offset:12288\n" "ds_read_b64_tr_b16 v[126:127], v218 offset:12416\n" "v_xor_b32 v200, 16, v218\n" "ds_read_b64_tr_b16 v[128:129], v200 offset:8192\n" \
  "ds_read_b64_tr_b16 v[130:131], v200 offset:8320\n" "ds_read_b64_tr_b16 v[132:133], v200 offset:12288\n" "ds_read_b64_tr_b16 v[134:135], v200 offset:12416\n" "v_xor_b32 v200, 32, v218\n" \
  "ds_read_b64_tr_b16 v[136:137], v200 offset:8192\n" "ds_read_b64_tr_b16 v[138:139], v200 offset:8320\n" "ds_read_b64_tr_b16 v[140:141], v200 offset:12288\n" \
  "ds_read_b64_tr_b16 v[142:143], v200 offset:12416\n" "v_xor_b32 v200, 48, v218\n" "ds_read_b64_tr_b16 v[144:145], v200 offset:8192\n" "ds_read_b64_tr_b16 v[146:147], v200 offset:8320\n" \
  "ds_read_b64_tr_b16 v[148:149], v200 offset:12288\n" "ds_read_b64_tr_b16 v[150:151], v200 offset:12416\n" "s_waitcnt lgkmcnt(0)\n" "s_waitcnt vmcnt(0)\n" \
  "ds_read_b64_tr_b16 v[32:33], v219 offset:0\n" "ds_read_b64_tr_b16 v[34:35], v219 offset:32\n" "ds_read_b64_tr_b16 v[36:37], v219 offset:1024\n" \
  "ds_read_b64_tr_b16 v[38:39], v219 offset:1056\n" "ds_read_b64_tr_b16 v[72:73], v219 offset:2048\n" "ds_read_b64_tr_b16 v[74:75], v219 offset:2080\n" \
  "ds_read_b64_tr_b16 v[76:77], v219 offset:3072\n" "ds_read_b64_tr_b16 v[78:79], v219 offset:3104\n" "ds_read_b64_tr_b16 v[112:113], v219 offset:4096\n" \
  "ds_read_b64_tr_b16 v[114:115], v219 offset:4128\n" "ds_read_b64_tr_b16 v[116:117], v219 offset:5120\n" "ds_read_b64_tr_b16 v[118:119], v219 offset:5152\n" \
  "ds_read_b64_tr_b16 v[152:153], v219 offset:6144\n" "ds_read_b64_tr_b16 v[154:155], v219 offset:6176\n" "ds_read_b64_tr_b16 v[156:157], v219 offset:7168\n" \
  "ds_read_b64_tr_b16 v[158:159], v219 offset:7200\n" "s_waitcnt lgkmcnt(0)\n"
#define CH_HA \
  "s_setprio 2\n" "s_mov_b32 s56, 0\n" "s_mov_b32 s57, 0x3e0293ee\n" "s_mov_b32 s58, 0x40000\n" "s_mov_b32 s77, 0x84000\n" "s_mov_b32 s59, " XS(SPIN_CAP) "\n" "s_mov_b32 s68, 0x3f803f80\n" \
  "s_mov_b32 s69, s68\n" "s_mov_b32 s71, -18688\n" "s_barrier\n" "v_mbcnt_lo_u32_b32 v200, -1, 0\n" "v_mbcnt_hi_u32_b32 v200, -1, v200\n" "s_and_b32 s60, s56, 1\n" "s_lshl_b32 s60, s60, 13\n" \
  "s_lshl_b32 s61, s52, 11\n" "s_add_i32 s61, s61, s60\n" "s_add_i32 s61, s61, 65536\n" "s_lshl_b32 s62, s52, 2\n" "v_bfe_u32 v201, v200, 2, 2\n" "v_and_b32 v202, 1, v200\n" \
  "v_lshl_add_u32 v202, v201, 1, v202\n" "v_lshl_add_u32 v202, v202, 8, s61\n" "v_add_u32 v201, s62, v201\n" "v_lshrrev_b32 v203, 3, v200\n" "v_and_b32 v203, 6, v203\n" \
  "v_xor_b32 v201, v201, v203\n" "v_bfe_u32 v203, v200, 1, 1\n" "v_xor_b32 v201, v201, v203\n" "v_lshl_add_u32 v202, v201, 4, v202\n" "v_xor_b32 v201, 0x80, v202\n" \
  "ds_read_b128 v[240:243], v202\n" "ds_read_b128 v[244:247], v201\n" "s_add_i32 s61, s60, 98304\n" "v_lshrrev_b32 v201, 4, v200\n" "v_lshlrev_b32 v201, 9, v201\n" \
  "v_bfe_u32 v202, v200, 2, 2\n" "v_mul_u32_u24 v202, 0x840, v202\n" "v_add3_u32 v201, v201, v202, s61\n" "v_and_b32 v202, 16, v200\n" "v_lshl_add_u32 v201, v202, 1, v201\n" \
  "v_and_b32 v202, 3, v200\n" "v_lshl_add_u32 v201, v202, 3, v201\n" "s_lshl_b32 s62, s52, 6\n" "v_xor_b32 v201, s62, v201\n" "v_xor_b32 v202, 32, v201\n" \
  "ds_read_b64_tr_b16 v[248:249], v201\n" "ds_read_b64_tr_b16 v[250:251], v201 offset:256\n" "ds_read_b64_tr_b16 v[252:253], v202 offset:256\n" "ds_read_b64_tr_b16 v[254:255], v202\n" \
  "v_mov_b64 v[228:229], 0\n" "v_mov_b64 v[230:231], 0\n" "v_mov_b64 v[232:233], 0\n" "v_mov_b64 v[234:235], 0\n" "v_mov_b64 v[236:237], 0\n" "v_mov_b64 v[238:239], 0\n" \
  "s_waitcnt lgkmcnt(0)\n" ".p2align 6\n" "ch_ha_loop:\n" "s_nop 0\n" "s_barrier\n" ST0 "ds_read_b128 v[192:195], v185 offset:37120\n" "ds_read_b128 v[196:199], v185 offset:38144\n" \
  "ds_read_b128 v[200:203], v185 offset:39168\n" "ds_read_b128 v[204:207], v185 offset:40192\n" "ds_read_b128 v[208:211], v185 offset:41216\n" "ds_read_b128 v[212:215], v185 offset:42240\n" \
  "ds_read_b128 v[216:219], v185 offset:43264\n" "ds_read_b128 v[220:223], v185 offset:44288\n" "ds_read_b128 v[224:227], v185 offset:45312\n" "s_nop 0\n" "s_waitcnt lgkmcnt(8)\n" \
  "v_smfmac_f32_16x16x64_bf16 v[228:231], v[192:195], v[0:7], v184\n" "s_nop 0\n" "s_waitcnt lgkmcnt(7)\n" "v_smfmac_f32_16x16x64_bf16 v[228:231], v[196:199], v[8:15], v184\n" "s_nop 0\n" \
  "s_waitcnt lgkmcnt(6)\n" "v_smfmac_f32_16x16x64_bf16 v[228:231], v[200:203], v[16:23], v184\n" "s_nop 0\n" "s_waitcnt lgkmcnt(5)\n" \
  "v_smfmac_f32_16x16x64_bf16 v[228:231], v[204:207], v[24:31], v184\n" "s_nop 0\n" "s_waitcnt lgkmcnt(4)\n" "v_smfmac_f32_16x16x64_bf16 v[228:231], v[208:211], v[32:39], v184\n" "s_nop 0\n" \
  "s_waitcnt lgkmcnt(3)\n" "v_smfmac_f32_16x16x64_bf16 v[228:231], v[212:215], v[40:47], v184\n" "s_nop 0\n" "s_waitcnt lgkmcnt(2)\n" \
  "v_smfmac_f32_16x16x64_bf16 v[228:231], v[216:219], v[48:55], v184\n" "s_nop 0\n" "s_waitcnt lgkmcnt(1)\n" "v_smfmac_f32_16x16x64_bf16 v[228:231], v[220:223], v[56:63], v184\n" "s_nop 0\n" \
  "s_waitcnt lgkmcnt(0)\n" "v_smfmac_f32_16x16x64_bf16 v[228:231], v[224:227], v[64:71], v184\n" ST1 "v_smfmac_f32_16x16x64_bf16 v[232:235], v[220:223], v[128:135], v184\n" ST20 \
  "v_smfmac_f32_16x16x64_bf16 v[232:235], v[224:227], v[136:143], v184\n" ST21 "v_smfmac_f32_16x16x64_bf16 v[232:235], v[212:215], v[112:119], v184\n" "v_add_f32_e64 v228, v228, v230\n" ST22 \
  "v_smfmac_f32_16x16x64_bf16 v[232:235], v[216:219], v[120:127], v184\n" "v_add_f32_e64 v229, v229, v231\n" ST23 "v_smfmac_f32_16x16x64_bf16 v[232:235], v[192:195], v[72:79], v184\n" \
  "v_cmp_u_f32_e64 vcc, v228, v229\n" ST24 "v_smfmac_f32_16x16x64_bf16 v[232:235], v[196:199], v[80:87], v184\n" "v_cvt_pk_bf16_f32 v228, v228, v229\n" ST25 \
  "v_smfmac_f32_16x16x64_bf16 v[232:235], v[200:203], v[88:95], v184\n" "ds_write_b32 v187, v228 offset:128\n" "v_mov_b32_e64 v229, s56\n" ST26 \
  "v_smfmac_f32_16x16x64_bf16 v[232:235], v[204:207], v[96:103], v184\n" "s_cbranch_vccnz ch_ha_slow\n" "ch_ha_back:\n" "v_lshrrev_b32 v231, 1, v186\n" ST27 \
  "v_smfmac_f32_16x16x64_bf16 v[232:235], v[208:211], v[104:111], v184\n" "v_mov_b64_e64 v[220:221], s[68:69]\n" ST28 ST2 "v_smfmac_f32_16x16x64_bf16 v[236:239], v[192:195], v[144:151], v184\n" \
  "v_mov_b64_e64 v[222:223], s[68:69]\n" "v_smfmac_f32_16x16x64_bf16 v[236:239], v[196:199], v[152:159], v184\n" "v_add_f32_e64 v232, v232, v234\n" \
  "v_smfmac_f32_16x16x64_bf16 v[236:239], v[200:203], v[160:167], v184\n" "v_add_f32_e64 v233, v233, v235\n" "v_smfmac_f32_16x16x64_bf16 v[236:239], v[204:207], v[168:175], v184\n" \
  "v_cvt_pk_bf16_f32 v232, v232, v233\n" "v_smfmac_f32_16x16x64_bf16 v[236:239], v[208:211], v[176:183], v184\n" "ds_write2st64_b32 v187, v232, v229 offset1:24\n" ST3 "v_mov_b32_e64 v235, 0\n" \
  ".rept " XS(QD) "\n" "s_nop 0\n" "s_nop 0\n" ".endr\n" "ch_ha_p1:\n" "ds_read_b32 v228, v190\n" "ds_read_b64_tr_b16 v[212:213], v186 offset:0\n" \
  "ds_read_b64_tr_b16 v[214:215], v186 offset:512\n" "ds_read_b64_tr_b16 v[216:217], v186 offset:1024\n" "ds_read_b64_tr_b16 v[218:219], v186 offset:1536\n" "s_nop 0\n" "s_waitcnt lgkmcnt(4)\n" \
  "v_cmp_ne_u32 vcc, s56, v228\n" "s_waitcnt lgkmcnt(2)\n" "v_mfma_f32_16x16x32_bf16 v[224:227], v[240:243], v[212:215], 0\n" "s_nop 0\n" "s_waitcnt lgkmcnt(0)\n" \
  "v_mfma_f32_16x16x32_bf16 v[224:227], v[244:247], v[216:219], v[224:227]\n" "s_cmp_lg_u32 vcc_lo, 0\n" "s_cbranch_scc1 ch_ha_p1\n" ST4 "s_setprio 0\n" "s_nop 4\n" \
  "v_add_f32_dpp v244, v226, v224 row_ror:8 row_mask:0xf bank_mask:0x3\n" "v_add_f32_dpp v244, v225, v227 row_ror:8 row_mask:0xf bank_mask:0xc\n" \
  "ds_write2st64_b32 v188, v244, v229 offset0:16 offset1:20\n" ST5 "v_add_f32_e64 v232, v236, v238\n" "v_add_f32 v233, v237, v239\n" ".rept " XS(KD) "\n" "s_nop 0\n" "s_nop 0\n" ".endr\n" \
  "s_setprio " XS(P2P) "\n" "ch_ha_p2:\n" ST10 "ds_read_b32 v228, v190 offset:2048\n" "ds_read_b64_tr_b16 v[192:193], v186 offset:2048\n" "ds_read_b64_tr_b16 v[194:195], v186 offset:2560\n" \
  "ds_read_b64_tr_b16 v[196:197], v186 offset:3072\n" "ds_read_b64_tr_b16 v[198:199], v186 offset:3584\n" "ds_read2st64_b32 v[200:201], v231 offset0:240 offset1:241\n" \
  "ds_read2st64_b32 v[202:203], v231 offset0:242 offset1:243\n" "s_setprio 2\n" "s_waitcnt lgkmcnt(6)\n" "v_cmp_ne_u32 vcc, s56, v228\n" "s_waitcnt lgkmcnt(4)\n" \
  "v_mfma_f32_16x16x32_bf16 v[204:207], v[192:195], v[212:215], 0\n" "s_nop 0\n" "s_waitcnt lgkmcnt(2)\n" "v_mfma_f32_16x16x32_bf16 v[204:207], v[196:199], v[216:219], v[204:207]\n" \
  "s_cbranch_vccnz ch_ha_p2\n" ST6 "s_waitcnt lgkmcnt(0)\n" "v_max3_f32 v245, v200, v201, v202\n" "s_nop 0\n" "s_nop 3\n" "v_add_f32_dpp v244, v206, v204 row_ror:8 row_mask:0xf bank_mask:0x3\n" \
  "v_add_f32_dpp v244, v205, v207 row_ror:8 row_mask:0xf bank_mask:0xc\n" "v_max3_f32 v245, v245, v203, v244\n" "s_nop 0\n" "s_nop 0\n" \
  "v_max_f32_dpp v246, v245, v245 row_ror:8 row_mask:0xf bank_mask:0xf\n" "v_max_f32_dpp v247, v245, v245 row_ror:8 row_mask:0xf bank_mask:0xf\n" "s_nop 1\n" \
  "v_permlane16_swap_b32 v246, v247\n" "v_max_f32 v245, v246, v247\n" "v_max_f32 v247, v246, v247\n" "s_nop 1\n" "v_permlane32_swap_b32 v245, v247\n" "v_max_f32 v245, v245, v247\n" \
  "v_mul_f32 v245, s57, v245\n" "v_fma_f32 v244, v244, s57, -v245\n" "v_fma_f32 v200, v200, s57, -v245\n" "v_exp_f32_e64 v244, v244\n" "v_fma_f32 v201, v201, s57, -v245\n" \
  "ds_read_b128 v[208:211], v189 offset:14336\n" "v_exp_f32_e64 v200, v200\n" "v_mov_b32_dpp v230, v244 row_ror:8 row_mask:0xf bank_mask:0xf\n" "v_exp_f32_e64 v201, v201\n" \
  "v_cvt_pk_bf16_f32 v234, v244, v230\n" "v_fma_f32 v202, v202, s57, -v245\n" "v_fma_f32 v203, v203, s57, -v245\n" "v_mfma_f32_16x16x16_bf16 v[240:243], v[220:221], v[234:235], 0\n" \
  "v_exp_f32_e64 v202, v202\n" "v_exp_f32 v203, v203\n" "ch_ha_p3:\n" "s_waitcnt lgkmcnt(0)\n" "v_mfma_f32_16x16x16_bf16 v[204:207], v[210:211], v[234:235], 0\n" "v_add_f32 v208, v208, v232\n" \
  "v_add_f32 v209, v209, v233\n" "v_cvt_pk_bf16_f32 v228, v208, v209\n" "v_cvt_pk_bf16_f32 v236, v200, v201\n" "v_cvt_pk_bf16_f32 v237, v202, v203\n" \
  "v_mfma_f32_16x16x16_bf16 v[224:227], v[228:229], v[234:235], 0\n" "v_mov_b32_dpp v238, v236 row_ror:8 row_mask:0xf bank_mask:0xf\n" \
  "v_mov_b32_dpp v239, v237 row_ror:8 row_mask:0xf bank_mask:0xf\n" "v_cmp_ne_u32 vcc, s56, v211\n" "s_nop 0\n" ST7 "v_mfma_f32_16x16x32_bf16 v[244:247], v[220:223], v[236:239], v[240:243]\n" \
  "v_mfma_f32_16x16x32_bf16 v[204:207], v[252:255], v[236:239], v[204:207]\n" "v_mfma_f32_16x16x32_bf16 v[224:227], v[248:251], v[236:239], v[224:227]\n" ST8 "s_cbranch_vccnz ch_ha_vlate\n" \
  "v_rcp_f32 v230, v244\n" "s_cmp_eq_u32 s56, 63\n" "s_waitcnt vmcnt(0)\n" "s_nop 0\n" "v_mul_f32 v204, v204, v230\n" "v_mul_f32_e64 v205, v205, v230\n" "v_mul_f32 v206, v206, v230\n" \
  "v_mul_f32 v207, v207, v230\n" "v_mul_f32_dpp v204, v224, v230 quad_perm:[0,1,2,3] row_mask:0xf bank_mask:0x3\n" \
  "v_mul_f32_dpp v205, v225, v230 quad_perm:[0,1,2,3] row_mask:0xf bank_mask:0x3\n" "v_mul_f32_dpp v206, v226, v230 quad_perm:[0,1,2,3] row_mask:0xf bank_mask:0x3\n" \
  "v_mul_f32_dpp v207, v227, v230 quad_perm:[0,1,2,3] row_mask:0xf bank_mask:0x3\n" "v_cvt_pk_bf16_f32 v224, v204, v205\n" "v_cvt_pk_bf16_f32 v225, v206, v207\n" \
  "buffer_store_dwordx2 v[224:225], v191, s[40:43], 0 offen sc1\n" ST9 "s_cbranch_scc1 ch_ha_last\n" "ch_ha_sent:\n" "ds_write_b64 v189, v[228:229] offset:14336\n" "v_mov_b32 v208, v228\n" \
  "s_memrealtime s[60:61]\n" "s_setprio 0\n" "s_add_i32 s70, s56, 64\n" "s_waitcnt lgkmcnt(0)\n" "s_lshl_b32 s60, s60, 6\n" "s_or_b32 s60, s60, s56\n" "v_mov_b32 v230, s60\n" \
  "ds_write_b32 v188, v230 offset:46336\n" PTA "s_cmp_lt_u32 s56, " XS(XS0) "\n" "s_cbranch_scc1 ch_ha_pub\n" "v_mbcnt_lo_u32_b32 v254, -1, 0\n" "v_mbcnt_hi_u32_b32 v254, -1, v254\n" \
  "s_and_b32 s60, s56, 1\n" "s_lshl_b32 s64, s60, 13\n" "s_and_b32 s65, s52, 1\n" "s_lshl_b32 s61, s65, 12\n" "s_add_i32 s61, s61, s64\n" "s_add_i32 s61, s61, 81920\n" \
  "s_add_i32 s62, s64, 65536\n" "v_lshrrev_b32 v255, 3, v254\n" "v_and_b32 v255, 6, v255\n" "v_and_b32 v253, 15, v254\n" "v_xor_b32 v252, v255, v253\n" "v_lshl_add_u32 v252, v253, 4, v252\n" \
  "v_lshl_add_u32 v252, v252, 4, s61\n" "v_bfe_u32 v253, v254, 2, 2\n" "v_and_b32 v254, 3, v254\n" "v_lshl_add_u32 v253, v254, 2, v253\n" "v_xor_b32 v255, v255, v253\n" \
  "v_lshl_add_u32 v253, v253, 5, v255\n" "v_lshl_add_u32 v253, v253, 4, s62\n" "ds_read_b128 v[216:219], v252\n" "ds_read_b128 v[220:223], v253\n" "ds_read_b128 v[224:227], v253 offset:256\n" \
  "v_xor_b32 v254, 16, v252\n" "v_xor_b32 v255, 16, v253\n" "ds_read_b128 v[228:231], v254\n" "ds_read_b128 v[232:235], v255\n" "ds_read_b128 v[236:239], v255 offset:256\n" \
  "v_xor_b32 v254, 128, v252\n" "v_xor_b32 v255, 128, v253\n" "ds_read_b128 v[240:243], v254\n" "ds_read_b128 v[244:247], v255\n" "ds_read_b128 v[248:251], v255 offset:256\n" \
  "s_waitcnt lgkmcnt(6)\n" "v_mfma_f32_16x16x32_bf16 v[200:203], v[220:223], v[216:219], 0\n" "v_mfma_f32_16x16x32_bf16 v[204:207], v[224:227], v[216:219], 0\n" \
  "v_mfma_f32_16x16x32_bf16 v[208:211], v[192:195], v[216:219], 0\n" "v_xor_b32 v254, 144, v252\n" "v_xor_b32 v255, 144, v253\n" "ds_read_b128 v[216:219], v254\n" \
  "ds_read_b128 v[220:223], v255\n" "ds_read_b128 v[224:227], v255 offset:256\n" "s_waitcnt lgkmcnt(6)\n" "v_mfma_f32_16x16x32_bf16 v[200:203], v[232:235], v[228:231], v[200:203]\n" \
  "v_mfma_f32_16x16x32_bf16 v[204:207], v[236:239], v[228:231], v[204:207]\n" "v_mfma_f32_16x16x32_bf16 v[212:215], v[192:195], v[228:231], 0\n" "s_waitcnt lgkmcnt(3)\n" \
  "v_mfma_f32_16x16x32_bf16 v[200:203], v[244:247], v[240:243], v[200:203]\n" "v_mfma_f32_16x16x32_bf16 v[204:207], v[248:251], v[240:243], v[204:207]\n" \
  "v_mfma_f32_16x16x32_bf16 v[208:211], v[196:199], v[240:243], v[208:211]\n" "s_waitcnt lgkmcnt(0)\n" "v_mfma_f32_16x16x32_bf16 v[200:203], v[220:223], v[216:219], v[200:203]\n" \
  "v_mfma_f32_16x16x32_bf16 v[204:207], v[224:227], v[216:219], v[204:207]\n" "v_mfma_f32_16x16x32_bf16 v[212:215], v[196:199], v[216:219], v[212:215]\n" "v_mbcnt_lo_u32_b32 v254, -1, 0\n" \
  "v_mbcnt_hi_u32_b32 v254, -1, v254\n" "s_add_i32 s61, s64, 98304\n" "v_lshrrev_b32 v252, 4, v254\n" "v_lshlrev_b32 v252, 9, v252\n" "v_bfe_u32 v255, v254, 2, 2\n" \
  "v_mul_u32_u24 v255, 0x840, v255\n" "v_add3_u32 v252, v252, v255, s61\n" "v_and_b32 v255, 16, v254\n" "v_lshl_add_u32 v252, v255, 1, v252\n" "v_and_b32 v255, 3, v254\n" \
  "v_lshl_add_u32 v252, v255, 3, v252\n" "s_lshr_b32 s61, s52, 1\n" "s_lshl_b32 s62, s61, 7\n" "s_lshl_b32 s61, s61, 11\n" "s_add_i32 s61, s61, 114688\n" "v_xor_b32 v252, s62, v252\n" \
  "v_lshl_add_u32 v253, v254, 4, s61\n" "ds_read_b64_tr_b16 v[216:217], v252\n" "ds_read_b64_tr_b16 v[218:219], v252 offset:256\n" "v_xor_b32 v255, 32, v252\n" \
  "ds_read_b64_tr_b16 v[220:221], v255\n" "ds_read_b64_tr_b16 v[222:223], v255 offset:256\n" "v_xor_b32 v255, 64, v252\n" "ds_read_b64_tr_b16 v[224:225], v255\n" \
  "ds_read_b64_tr_b16 v[226:227], v255 offset:256\n" "v_xor_b32 v255, 96, v252\n" "ds_read_b64_tr_b16 v[228:229], v255\n" "ds_read_b64_tr_b16 v[230:231], v255 offset:256\n" "ch_ha_xv:\n" \
  "ds_read_b128 v[232:235], v253 offset:14336\n" "ds_read_b128 v[236:239], v253 offset:15360\n" "s_waitcnt lgkmcnt(0)\n" "v_cmp_ne_u32 vcc, s56, v233\n" "v_cmp_ne_u32 s[60:61], s56, v237\n" \
  "s_or_b64 vcc, vcc, s[60:61]\n" "s_cbranch_scc1 ch_ha_xv\n" "v_lshrrev_b32 v255, 3, v254\n" "v_and_b32 v255, 6, v255\n" "v_and_b32 v252, 15, v254\n" "v_sub_u32 v252, v252, v255\n" \
  "v_lshl_add_u32 v252, s65, 4, v252\n" "v_mov_b32 v253, 0xff800000\n" "v_add_f32 v208, v208, v214\n" "v_add_f32 v209, v209, v215\n" "v_cmp_le_i32 vcc, 0, v252\n" \
  "v_cmp_le_i32 s[60:61], 8, v252\n" "v_cmp_le_i32 s[62:63], 16, v252\n" "v_cmp_le_i32 s[66:67], 24, v252\n" "v_cndmask_b32 v200, v253, v200, vcc\n" "v_cndmask_b32 v201, v253, v201, s[60:61]\n" \
  "v_cndmask_b32 v202, v253, v202, s[62:63]\n" "v_cndmask_b32 v203, v253, v203, s[66:67]\n" "v_cmp_le_i32 vcc, 1, v252\n" "v_cmp_le_i32 s[60:61], 9, v252\n" "v_cmp_le_i32 s[62:63], 17, v252\n" \
  "v_cmp_le_i32 s[66:67], 25, v252\n" "v_cndmask_b32 v204, v253, v204, vcc\n" "v_cndmask_b32 v205, v253, v205, s[60:61]\n" "v_cndmask_b32 v206, v253, v206, s[62:63]\n" \
  "v_cndmask_b32 v207, v253, v207, s[66:67]\n" "v_max3_f32 v210, v200, v201, v202\n" "v_max3_f32 v210, v210, v203, v204\n" "v_max3_f32 v210, v210, v205, v206\n" \
  "v_max3_f32 v210, v210, v207, v208\n" "v_max_f32 v211, v210, v209\n" "v_max_f32 v210, v210, v209\n" "s_nop 1\n" "v_permlane16_swap_b32 v210, v211\n" "v_max_f32 v255, v210, v211\n" \
  "v_max_f32 v211, v210, v211\n" "s_nop 1\n" "v_permlane32_swap_b32 v255, v211\n" "v_max_f32 v210, v255, v211\n" "v_mul_f32 v210, s57, v210\n" "v_fma_f32 v200, v200, s57, -v210\n" \
  "v_fma_f32 v201, v201, s57, -v210\n" "v_fma_f32 v202, v202, s57, -v210\n" "v_fma_f32 v203, v203, s57, -v210\n" "v_fma_f32 v204, v204, s57, -v210\n" "v_fma_f32 v205, v205, s57, -v210\n" \
  "v_fma_f32 v206, v206, s57, -v210\n" "v_fma_f32 v207, v207, s57, -v210\n" "v_fma_f32 v208, v208, s57, -v210\n" "v_fma_f32 v209, v209, s57, -v210\n" "v_exp_f32 v200, v200\n" \
  "v_exp_f32 v201, v201\n" "v_exp_f32 v202, v202\n" "v_exp_f32 v203, v203\n" "v_exp_f32 v204, v204\n" "v_exp_f32 v205, v205\n" "v_exp_f32 v206, v206\n" "v_exp_f32 v207, v207\n" \
  "v_exp_f32 v208, v208\n" "v_exp_f32 v209, v209\n" "v_add_f32 v210, v200, v201\n" "v_add_f32 v210, v210, v202\n" "v_add_f32 v210, v210, v203\n" "v_add_f32 v210, v210, v204\n" \
  "v_add_f32 v210, v210, v205\n" "v_add_f32 v210, v210, v206\n" "v_add_f32 v210, v210, v207\n" "v_add_f32 v210, v210, v208\n" "v_add_f32 v211, v210, v209\n" "v_add_f32 v210, v210, v209\n" \
  "s_nop 1\n" "v_permlane16_swap_b32 v210, v211\n" "v_add_f32 v255, v210, v211\n" "v_add_f32 v211, v210, v211\n" "s_nop 1\n" "v_permlane32_swap_b32 v255, v211\n" "v_add_f32 v210, v255, v211\n" \
  "v_rcp_f32 v210, v210\n" "v_mov_b32 v241, 0\n" "v_mov_b64 v[242:243], 0\n" "v_mov_b32 v245, 0\n" "v_mov_b64 v[246:247], 0\n" "v_mul_f32 v200, v200, v210\n" "v_mul_f32 v201, v201, v210\n" \
  "v_mul_f32 v202, v202, v210\n" "v_mul_f32 v203, v203, v210\n" "v_mul_f32 v204, v204, v210\n" "v_mul_f32 v205, v205, v210\n" "v_mul_f32 v206, v206, v210\n" "v_mul_f32 v207, v207, v210\n" \
  "v_mul_f32 v208, v208, v210\n" "v_mul_f32 v209, v209, v210\n" "v_cvt_pk_bf16_f32 v248, v200, v201\n" "v_cvt_pk_bf16_f32 v249, v202, v203\n" "v_cvt_pk_bf16_f32 v250, v204, v205\n" \
  "v_cvt_pk_bf16_f32 v251, v206, v207\n" "v_cvt_pk_bf16_f32 v240, v208, v209\n" "v_cvt_pk_bf16_f32 v244, v209, v208\n" "s_waitcnt lgkmcnt(0)\n" \
  "v_mfma_f32_16x16x32_bf16 v[200:203], v[216:219], v[248:251], 0\n" "v_mfma_f32_16x16x32_bf16 v[204:207], v[220:223], v[248:251], 0\n" \
  "v_mfma_f32_16x16x32_bf16 v[208:211], v[224:227], v[248:251], 0\n" "v_mfma_f32_16x16x32_bf16 v[212:215], v[228:231], v[248:251], 0\n" \
  "v_mfma_f32_16x16x32_bf16 v[200:203], v[232:235], v[240:243], v[200:203]\n" "v_mfma_f32_16x16x32_bf16 v[204:207], v[234:237], v[244:247], v[204:207]\n" \
  "v_mfma_f32_16x16x32_bf16 v[208:211], v[236:239], v[240:243], v[208:211]\n" "v_mfma_f32_16x16x32_bf16 v[212:215], v[238:241], v[244:247], v[212:215]\n" "s_cmp_lt_u32 s56, " XS(SM0) "\n" \
  "s_cselect_b32 s60, 15, 8\n" "s_cselect_b32 s61, s53, s54\n" "s_cselect_b32 s62, 17, 10\n" "s_lshl_b32 s62, 1, s62\n" "s_lshl_b32 s63, s56, 19\n" "s_add_i32 s61, s61, s63\n" \
  "s_lshl_b32 s63, s56, 11\n" "s_andn2_b32 s63, 0x2000, s63\n" "s_add_i32 s64, s61, s63\n" "s_add_i32 s65, s64, s62\n" "v_bfe_u32 v255, v254, 4, 1\n" "v_lshrrev_b32 v252, 5, v254\n" \
  "v_lshl_add_u32 v252, v255, 1, v252\n" "v_lshlrev_b32 v252, s60, v252\n" "v_and_b32 v255, 15, v254\n" "v_lshl_add_u32 v224, v255, 4, v252\n" "v_cvt_pk_bf16_f32 v216, v200, v201\n" \
  "v_cvt_pk_bf16_f32 v217, v202, v203\n" "v_cvt_pk_bf16_f32 v218, v204, v205\n" "v_cvt_pk_bf16_f32 v219, v206, v207\n" "v_cvt_pk_bf16_f32 v220, v208, v209\n" \
  "v_cvt_pk_bf16_f32 v221, v210, v211\n" "v_cvt_pk_bf16_f32 v222, v212, v213\n" "v_cvt_pk_bf16_f32 v223, v214, v215\n" "s_nop 1\n" "v_permlane16_swap_b32 v216, v218\n" \
  "v_permlane16_swap_b32 v217, v219\n" "v_permlane16_swap_b32 v220, v222\n" "v_permlane16_swap_b32 v221, v223\n" "s_cmp_eq_u32 s56, 63\n" "s_cbranch_scc1 ch_ha_fin\n" \
  "v_mbcnt_lo_u32_b32 v200, -1, 0\n" "v_mbcnt_hi_u32_b32 v200, -1, v200\n" "v_lshrrev_b32 v204, 4, v200\n" "v_lshlrev_b32 v204, 10, v204\n" "v_add_u32 v204, 129036, v204\n" "ch_ha_w1a:\n" \
  "s_sleep 1\n" "ds_read_b32 v201, v204\n" "s_waitcnt lgkmcnt(0)\n" "v_cmp_ne_u32 vcc, s70, v201\n" "s_lshl_b64 vcc, vcc, 16\n" "s_cbranch_scc1 ch_ha_w1a\n" "s_and_b32 s60, s56, 1\n" \
  "s_xor_b32 s60, s60, 1\n" "s_lshl_b32 s60, s60, 13\n" "s_lshl_b32 s61, s52, 11\n" "s_add_i32 s61, s61, s60\n" "s_add_i32 s61, s61, 65536\n" "s_lshl_b32 s62, s52, 2\n" \
  "v_bfe_u32 v201, v200, 2, 2\n" "v_and_b32 v202, 1, v200\n" "v_lshl_add_u32 v202, v201, 1, v202\n" "v_lshl_add_u32 v202, v202, 8, s61\n" "v_add_u32 v201, s62, v201\n" \
  "v_lshrrev_b32 v203, 3, v200\n" "v_and_b32 v203, 6, v203\n" "v_xor_b32 v201, v201, v203\n" "v_bfe_u32 v203, v200, 1, 1\n" "v_xor_b32 v201, v201, v203\n" "v_lshl_add_u32 v202, v201, 4, v202\n" \
  "v_xor_b32 v201, 0x80, v202\n" "ds_read_b128 v[240:243], v202\n" "ds_read_b128 v[244:247], v201\n" "s_add_i32 s61, s60, 98304\n" "v_lshrrev_b32 v201, 4, v200\n" \
  "v_lshlrev_b32 v201, 9, v201\n" "v_bfe_u32 v202, v200, 2, 2\n" "v_mul_u32_u24 v202, 0x840, v202\n" "v_add3_u32 v201, v201, v202, s61\n" "v_and_b32 v202, 16, v200\n" \
  "v_lshl_add_u32 v201, v202, 1, v201\n" "v_and_b32 v202, 3, v200\n" "v_lshl_add_u32 v201, v202, 3, v201\n" "s_lshl_b32 s62, s52, 6\n" "v_xor_b32 v201, s62, v201\n" "v_xor_b32 v202, 32, v201\n" \
  "ds_read_b64_tr_b16 v[248:249], v201\n" "ds_read_b64_tr_b16 v[250:251], v201 offset:256\n" "ds_read_b64_tr_b16 v[252:253], v202 offset:256\n" "ds_read_b64_tr_b16 v[254:255], v202\n" \
  "v_mov_b64 v[228:229], 0\n" "v_mov_b64 v[230:231], 0\n" "v_mov_b64 v[232:233], 0\n" "v_mov_b64 v[234:235], 0\n" "v_mov_b64 v[236:237], 0\n" "v_mov_b64 v[238:239], 0\n" "ch_ha_w2:\n" \
  "s_sleep 1\n" "ds_read_b32 v200, v204\n" "s_waitcnt lgkmcnt(0)\n" "v_cmp_ne_u32 vcc, s70, v200\n" "s_cbranch_vccnz ch_ha_w2\n" "s_sleep " XS(XSD) "\n" \
  "buffer_store_dwordx4 v[216:219], v224, s[44:47], s64 offen sc1\n" "buffer_store_dwordx4 v[220:223], v224, s[44:47], s65 offen sc1\n" "ch_ha_next:\n" "v_xor_b32 v200, s77, v191\n" \
  "v_mov_b64 v[202:203], -1\n" "v_xor_b32 v191, s58, v191\n" "buffer_store_dwordx2 v[202:203], v200, s[40:43], 0 offen sc1\n" "s_mov_b32 s60, s58\n" "s_xor_b32 s58, s58, s77\n" \
  "s_mov_b32 s77, s60\n" "s_add_i32 s56, s56, 1\n" "s_setprio 2\n" "s_branch ch_ha_loop\n" "ch_ha_pub:\n" "v_mbcnt_lo_u32_b32 v216, -1, 0\n" "v_mbcnt_hi_u32_b32 v216, -1, v216\n" \
  "s_and_b32 s60, s53, 0x1f00\n" "s_lshl_b32 s61, s56, 16\n" "s_add_i32 s64, s61, s60\n" "s_add_i32 s64, s64, " XS(O_KM) "\n" "s_lshl_b32 s60, s60, 4\n" "s_lshl_b32 s61, s61, 1\n" \
  "s_add_i32 s65, s61, s60\n" "s_add_i32 s65, s65, " XS(O_VM) "-114688\n" "v_bfe_u32 v217, v216, 2, 2\n" "v_and_b32 v218, 1, v216\n" "v_lshl_add_u32 v217, v217, 1, v218\n" \
  "v_lshlrev_b32 v217, 13, v217\n" "v_and_b32 v218, 0x30, v216\n" "v_lshl_add_u32 v217, v218, 1, v217\n" "v_and_b32 v218, 2, v216\n" "v_lshl_add_u32 v217, v218, 3, v217\n" \
  "v_mbcnt_lo_u32_b32 v200, -1, 0\n" "v_mbcnt_hi_u32_b32 v200, -1, v200\n" "v_lshrrev_b32 v204, 4, v200\n" "v_lshlrev_b32 v204, 10, v204\n" "v_add_u32 v204, 129036, v204\n" "ch_ha_w1b:\n" \
  "s_sleep 1\n" "ds_read_b32 v201, v204\n" "s_waitcnt lgkmcnt(0)\n" "v_cmp_ne_u32 vcc, s70, v201\n" "s_lshl_b64 vcc, vcc, 16\n" "s_cbranch_scc1 ch_ha_w1b\n" "s_and_b32 s60, s56, 1\n" \
  "s_xor_b32 s60, s60, 1\n" "s_lshl_b32 s60, s60, 13\n" "s_lshl_b32 s61, s52, 11\n" "s_add_i32 s61, s61, s60\n" "s_add_i32 s61, s61, 65536\n" "s_lshl_b32 s62, s52, 2\n" \
  "v_bfe_u32 v201, v200, 2, 2\n" "v_and_b32 v202, 1, v200\n" "v_lshl_add_u32 v202, v201, 1, v202\n" "v_lshl_add_u32 v202, v202, 8, s61\n" "v_add_u32 v201, s62, v201\n" \
  "v_lshrrev_b32 v203, 3, v200\n" "v_and_b32 v203, 6, v203\n" "v_xor_b32 v201, v201, v203\n" "v_bfe_u32 v203, v200, 1, 1\n" "v_xor_b32 v201, v201, v203\n" "v_lshl_add_u32 v202, v201, 4, v202\n" \
  "v_xor_b32 v201, 0x80, v202\n" "ds_read_b128 v[240:243], v202\n" "ds_read_b128 v[244:247], v201\n" "s_add_i32 s61, s60, 98304\n" "v_lshrrev_b32 v201, 4, v200\n" \
  "v_lshlrev_b32 v201, 9, v201\n" "v_bfe_u32 v202, v200, 2, 2\n" "v_mul_u32_u24 v202, 0x840, v202\n" "v_add3_u32 v201, v201, v202, s61\n" "v_and_b32 v202, 16, v200\n" \
  "v_lshl_add_u32 v201, v202, 1, v201\n" "v_and_b32 v202, 3, v200\n" "v_lshl_add_u32 v201, v202, 3, v201\n" "s_lshl_b32 s62, s52, 6\n" "v_xor_b32 v201, s62, v201\n" "v_xor_b32 v202, 32, v201\n" \
  "ds_read_b64_tr_b16 v[248:249], v201\n" "ds_read_b64_tr_b16 v[250:251], v201 offset:256\n" "ds_read_b64_tr_b16 v[252:253], v202 offset:256\n" "ds_read_b64_tr_b16 v[254:255], v202\n" \
  "v_mov_b64 v[228:229], 0\n" "v_mov_b64 v[230:231], 0\n" "v_mov_b64 v[232:233], 0\n" "v_mov_b64 v[234:235], 0\n" "v_mov_b64 v[236:237], 0\n" "v_mov_b64 v[238:239], 0\n" "ch_ha_w3:\n" \
  "s_sleep 1\n" "ds_read_b32 v200, v204\n" "s_waitcnt lgkmcnt(0)\n" "v_cmp_ne_u32 vcc, s70, v200\n" "s_cbranch_vccnz ch_ha_w3\n" \
  "buffer_store_dwordx4 v[208:211], v189, s[48:51], s65 offen sc1\n" "s_cmp_lg_u32 s52, 0\n" "s_cbranch_scc1 ch_ha_next\n" "buffer_store_dwordx4 v[192:195], v217, s[48:51], s64 offen sc1\n" \
  "buffer_store_dwordx4 v[196:199], v217, s[48:51], s64 offen offset:128 sc1\n" "s_branch ch_ha_next\n" "ch_ha_vlate:\n" "ds_read_b128 v[208:211], v189 offset:14336\n" "s_branch ch_ha_p3\n" \
  "ch_ha_slow:\n" "s_cmp_eq_u32 s56, 0\n" "s_cbranch_scc1 ch_ha_back\n" "v_max3_u32 v230, v192, v193, v194\n" "v_max3_u32 v230, v230, v195, v196\n" "v_max3_u32 v230, v230, v197, v198\n" \
  "v_max3_u32 v230, v230, v199, v200\n" "v_max3_u32 v230, v230, v201, v202\n" "v_max3_u32 v230, v230, v203, v204\n" "v_max3_u32 v230, v230, v205, v206\n" "v_max3_u32 v230, v230, v207, v208\n" \
  "v_max3_u32 v230, v230, v209, v210\n" "v_max3_u32 v230, v230, v211, v212\n" "v_max3_u32 v230, v230, v213, v214\n" "v_max3_u32 v230, v230, v215, v216\n" "v_max3_u32 v230, v230, v217, v218\n" \
  "v_max3_u32 v230, v230, v219, v220\n" "v_max3_u32 v230, v230, v221, v222\n" "v_max3_u32 v230, v230, v223, v224\n" "v_max3_u32 v230, v230, v225, v226\n" "v_max_u32 v230, v230, v227\n" \
  "v_cmp_eq_u32 vcc, -1, v230\n" "s_cbranch_vccz ch_ha_back\n" "s_sub_u32 s59, s59, 1\n" "s_cselect_b32 s59, 0, s59\n" "s_cbranch_scc1 ch_ha_back\n" "s_nop 7\n" "v_mov_b64 v[228:229], 0\n" \
  "v_mov_b64 v[230:231], 0\n" "v_mov_b64 v[232:233], 0\n" "v_mov_b64 v[234:235], 0\n" "v_mov_b64 v[236:237], 0\n" "v_mov_b64 v[238:239], 0\n" "v_add_u32 v185, s71, v185\n" \
  "s_sub_i32 s71, 0, s71\n" "s_branch ch_ha_loop\n" "ch_ha_last:\n" "v_mov_b32 v230, 0\n" "s_mov_b64 exec, 1\n" "buffer_store_dword v184, v230, s[48:51], s55 offen sc1\n" "s_mov_b64 exec, -1\n" \
  "s_branch ch_ha_sent\n" "ch_ha_fin:\n" "buffer_store_dwordx4 v[216:219], v224, s[44:47], s64 offen sc1\n" "buffer_store_dwordx4 v[220:223], v224, s[44:47], s65 offen sc1\n" \
  "v_mov_b32 v228, 0\n" "s_mov_b64 exec, 1\n" "s_waitcnt vmcnt(0)\n" "buffer_store_dword v184, v228, s[48:51], s55 offen offset:512 sc1\n" "s_mov_b64 exec, -1\n"
#define CH_HB \
  "s_setprio 1\n" "s_mov_b32 s56, 0\n" "s_mov_b32 s59, " XS(SPIN_CAP) "\n" "s_mov_b32 s68, 0\n" "s_mov_b32 s58, " XS(DUR0) "*64\n" "s_add_i32 s70, s53, 9*0x8000\n" \
  "s_add_i32 s69, s53, 6*0x8000\n" "s_mov_b32 s71, -18688\n" "s_mov_b32 s77, 151808\n" "s_lshl_b32 s60, s52, 8\n" "s_add_i32 s60, s60, 118784\n" "v_mov_b32 v254, s60\n" \
  "ds_read_b128 v[176:179], v188 offset:0\n" "ds_read_b128 v[180:183], v188 offset:1024\n" "ds_read_b128 v[192:195], v188 offset:24576\n" "ds_read_b128 v[196:199], v188 offset:25600\n" \
  "ds_read_b128 v[200:203], v188 offset:49152\n" "ds_read_b128 v[204:207], v188 offset:50176\n" "v_mov_b64 v[244:245], 0\n" "v_mov_b64 v[246:247], 0\n" "v_mov_b64 v[248:249], 0\n" \
  "v_mov_b64 v[250:251], 0\n" "s_cmp_gt_u32 s52, 5\n" "s_cbranch_scc1 ch_hb_i1\n" "s_mov_b32 s60, 0\n" "s_lshl_b32 s61, s60, 19\n" "s_add_i32 s61, s61, s53\n" "s_lshl_b32 s62, s60, 11\n" \
  "s_and_b32 s62, s62, 0x2000\n" "s_add_i32 s61, s61, s62\n" "s_lshl_b32 s62, s60, 13\n" "s_and_b32 s62, s62, 0x2000\n" "s_add_i32 s62, s62, s54\n" "s_and_b32 s63, s55, 0xff\n" \
  "s_bfe_u32 s64, s55, 0x80010\n" "s_lshr_b32 s65, s55, 24\n" "v_mov_b32 v252, v189\n" "s_add_i32 m0, s62, 0\n" "s_add_i32 s66, s61, 0\n" \
  "buffer_load_dwordx4 v252, s[44:47], s66 offen sc1 lds\n" "v_xor_b32 v252, s63, v252\n" "s_add_i32 m0, s62, 1024\n" "s_add_i32 s66, s61, 65536\n" \
  "buffer_load_dwordx4 v252, s[44:47], s66 offen sc1 lds\n" "v_xor_b32 v252, s64, v252\n" "s_add_i32 m0, s62, 3072\n" "s_add_i32 s66, s61, 196608\n" \
  "buffer_load_dwordx4 v252, s[44:47], s66 offen sc1 lds\n" "v_xor_b32 v252, s63, v252\n" "s_add_i32 m0, s62, 2048\n" "s_add_i32 s66, s61, 131072\n" \
  "buffer_load_dwordx4 v252, s[44:47], s66 offen sc1 lds\n" "v_xor_b32 v252, s65, v252\n" "s_add_i32 m0, s62, 6144\n" "s_add_i32 s66, s61, 393216\n" \
  "buffer_load_dwordx4 v252, s[44:47], s66 offen sc1 lds\n" "v_xor_b32 v252, s63, v252\n" "s_add_i32 m0, s62, 7168\n" "s_add_i32 s66, s61, 458752\n" \
  "buffer_load_dwordx4 v252, s[44:47], s66 offen sc1 lds\n" "v_xor_b32 v252, s64, v252\n" "s_add_i32 m0, s62, 5120\n" "s_add_i32 s66, s61, 327680\n" \
  "buffer_load_dwordx4 v252, s[44:47], s66 offen sc1 lds\n" "v_xor_b32 v252, s63, v252\n" "s_add_i32 m0, s62, 4096\n" "s_add_i32 s66, s61, 262144\n" \
  "buffer_load_dwordx4 v252, s[44:47], s66 offen sc1 lds\n" "s_waitcnt vmcnt(0)\n" "ch_hb_i1:\n" "s_barrier\n" "s_cmp_lg_u32 s52, 7\n" "s_cbranch_scc1 ch_hb_bar\n" "s_memrealtime s[60:61]\n" \
  "s_waitcnt lgkmcnt(0)\n" "s_add_i32 s57, s60, " XS(GL0) "\n" "s_lshl_b32 s57, s57, 6\n" "s_andn2_b32 s60, s58, 63\n" "s_add_i32 s60, s60, s57\n" "s_add_i32 s60, s60, s56\n" \
  "v_mov_b32 v252, s60\n" "buffer_store_dword v252, v191, s[40:43], 0 offen sc1\n" ".p2align 6\n" "ch_hb_bar:\n" "ch_hb_loop:\n" "v_and_b32 v253, 0x3f0, v187\n" "v_bfe_u32 v252, v253, 2, 7\n" \
  "v_lshl_add_u32 v252, v253, 3, v252\n" "v_add_u32 v252, s77, v252\n" "v_mov_b32 v255, s56\n" "s_waitcnt vmcnt(1)\n" "s_barrier\n" ST0 ".rept " XS(BD) "\n" "s_nop 3\n" ".endr\n" ".p2align 3\n" \
  "ds_read_b32 v252, v252\n" "ds_read_b32 v253, v185 offset:45312\n" "ds_read_b128 v[232:235], v185 offset:43264\n" "ds_read_b128 v[236:239], v185 offset:44288\n" \
  "ds_read_b128 v[240:243], v185 offset:45312\n" "ds_read_b128 v[208:211], v185 offset:37120\n" "ds_read_b128 v[212:215], v185 offset:38144\n" "ds_read_b128 v[216:219], v185 offset:39168\n" \
  "ds_read_b128 v[220:223], v185 offset:40192\n" "ds_read_b128 v[224:227], v185 offset:41216\n" "ds_read_b128 v[228:231], v185 offset:42240\n" "s_setprio 3\n" "s_waitcnt lgkmcnt(6)\n" \
  "v_smfmac_f32_16x16x64_bf16 v[244:247], v[232:235], v[176:183], v184\n" "v_max_u32_e64 v252, v252, v253\n" "v_smfmac_f32_16x16x64_bf16 v[244:247], v[236:239], v[192:199], v184\n" \
  "v_cmp_eq_u32_e64 vcc, -1, v252\n" "v_smfmac_f32_16x16x64_bf16 v[244:247], v[240:243], v[200:207], v184\n" "s_nop 0\n" "s_setprio 1\n" ST30 "ds_read_b128 v[176:179], v188 offset:8192\n" \
  "ds_read_b128 v[180:183], v188 offset:9216\n" "ds_read_b128 v[192:195], v188 offset:32768\n" "ds_read_b128 v[196:199], v188 offset:33792\n" "ds_read_b128 v[200:203], v188 offset:57344\n" \
  "ds_read_b128 v[204:207], v188 offset:58368\n" "s_cbranch_vccnz ch_hb_miss\n" "ch_hb_bk2:\n" "s_waitcnt lgkmcnt(6)\n" "v_smfmac_f32_16x16x64_bf16 v[244:247], v[208:211], v[0:7], v184\n" \
  "v_smfmac_f32_16x16x64_bf16 v[244:247], v[212:215], v[8:15], v184\n" "v_smfmac_f32_16x16x64_bf16 v[244:247], v[216:219], v[16:23], v184\n" \
  "v_smfmac_f32_16x16x64_bf16 v[244:247], v[220:223], v[24:31], v184\n" "v_smfmac_f32_16x16x64_bf16 v[244:247], v[224:227], v[32:39], v184\n" "s_nop 0\n" "s_waitcnt lgkmcnt(0)\n" \
  "v_smfmac_f32_16x16x64_bf16 v[244:247], v[228:231], v[40:47], v184\n" ST1 "v_smfmac_f32_16x16x64_bf16 v[248:251], v[232:235], v[176:183], v184\n" \
  "ds_read_b128 v[176:179], v188 offset:16384\n" ST20 "v_smfmac_f32_16x16x64_bf16 v[248:251], v[236:239], v[192:199], v184\n" "ds_read_b128 v[180:183], v188 offset:17408\n" ST21 \
  "v_smfmac_f32_16x16x64_bf16 v[248:251], v[240:243], v[200:207], v184\n" "v_add_f32_e64 v252, v244, v246\n" ST22 "v_smfmac_f32_16x16x64_bf16 v[248:251], v[208:211], v[48:55], v184\n" \
  "ds_read_b128 v[192:195], v188 offset:40960\n" "v_add_f32_e64 v253, v245, v247\n" ST23 "v_smfmac_f32_16x16x64_bf16 v[248:251], v[212:215], v[56:63], v184\n" \
  "ds_read_b128 v[196:199], v188 offset:41984\n" "v_cmp_u_f32_e64 vcc, v252, v253\n" ST24 "v_smfmac_f32_16x16x64_bf16 v[248:251], v[216:219], v[64:71], v184\n" \
  "ds_read_b128 v[200:203], v187 offset:23552\n" "v_cvt_pk_bf16_f32 v252, v252, v253\n" ST25 "v_smfmac_f32_16x16x64_bf16 v[248:251], v[220:223], v[72:79], v184\n" \
  "ds_write_b32 v186, v252 offset:128\n" "v_mov_b64_e64 v[244:245], 0\n" ST26 "v_smfmac_f32_16x16x64_bf16 v[248:251], v[224:227], v[80:87], v184\n" "s_cbranch_vccnz ch_hb_slow\n" \
  "ch_hb_back:\n" "v_mov_b64 v[246:247], 0\n" ST27 "v_smfmac_f32_16x16x64_bf16 v[248:251], v[228:231], v[88:95], v184\n" "ds_read_b128 v[204:207], v187 offset:27648\n" ST28 ST2 \
  "v_smfmac_f32_16x16x64_bf16 v[244:247], v[208:211], v[96:103], v184\n" "v_smfmac_f32_16x16x64_bf16 v[244:247], v[212:215], v[104:111], v184\n" "v_add_f32 v252, v248, v250\n" \
  "v_add_f32 v253, v249, v251\n" "v_cvt_pk_bf16_f32 v252, v252, v253\n" "v_smfmac_f32_16x16x64_bf16 v[244:247], v[216:219], v[112:119], v184\n" "ds_write2st64_b32 v186, v252, v255 offset1:24\n" \
  ST3 "v_smfmac_f32_16x16x64_bf16 v[244:247], v[220:223], v[120:127], v184\n" "v_smfmac_f32_16x16x64_bf16 v[244:247], v[224:227], v[128:135], v184\n" \
  "v_smfmac_f32_16x16x64_bf16 v[244:247], v[228:231], v[136:143], v184\n" "s_waitcnt lgkmcnt(0)\n" "v_mov_b64 v[248:249], 0\n" \
  "v_smfmac_f32_16x16x64_bf16 v[244:247], v[232:235], v[176:183], v184\n" "v_mov_b64_e64 v[250:251], 0\n" "v_smfmac_f32_16x16x64_bf16 v[244:247], v[236:239], v[192:199], v184\n" \
  "v_smfmac_f32_16x16x64_bf16 v[244:247], v[240:243], v[200:207], v184\n" ST4 "v_smfmac_f32_16x16x64_bf16 v[248:251], v[228:231], v[144:151], v184\n" \
  "v_smfmac_f32_16x16x64_bf16 v[248:251], v[232:235], v[152:159], v184\n" "v_add_f32_e64 v252, v244, v246\n" "v_smfmac_f32_16x16x64_bf16 v[248:251], v[236:239], v[160:167], v184\n" \
  "v_add_f32_e64 v253, v245, v247\n" "v_smfmac_f32_16x16x64_bf16 v[248:251], v[240:243], v[168:175], v184\n" ST5 "v_cvt_pk_bf16_f32 v246, v253, v252\n" "v_mov_b32_e64 v247, s56\n" \
  "v_add_u32 v255, 64, v255\n" "s_nop 3\n" "v_add_f32 v244, v248, v250\n" "v_add_f32 v245, v249, v251\n" "ds_write_b128 v187, v[244:247] offset:10240\n" ST6 "s_setprio 0\n" \
  "s_cmp_eq_u32 s56, 63\n" "s_cbranch_scc1 ch_hb_fin\n" "s_sleep " XS(BSL) "\n" PTA "ch_hb_w0:\n" "s_sleep 1\n" "ds_read_b32 v252, v254 offset:41216\n" "s_waitcnt lgkmcnt(0)\n" \
  "v_readfirstlane_b32 s67, v252\n" "s_xor_b32 s61, s67, s56\n" "s_and_b32 s61, s61, 63\n" "s_cbranch_scc1 ch_hb_w0\n" "s_cmp_eq_u32 s52, 7\n" "s_cbranch_scc1 ch_hb_w7\n" \
  "s_add_i32 s56, s56, 1\n" "s_mov_b32 s60, s56\n" "s_cmp_eq_u32 s52, 6\n" "s_cselect_b32 s61, " XS(XS0) ", 0\n" "s_cmp_lt_u32 s60, s61\n" "s_cbranch_scc1 ch_hb_nr\n" "s_sleep " XS(RQD) "\n" \
  "s_lshl_b32 s61, s60, 19\n" "s_add_i32 s61, s61, s53\n" "s_lshl_b32 s62, s60, 11\n" "s_and_b32 s62, s62, 0x2000\n" "s_add_i32 s61, s61, s62\n" "s_lshl_b32 s62, s60, 13\n" \
  "s_and_b32 s62, s62, 0x2000\n" "s_add_i32 s62, s62, s54\n" "s_and_b32 s63, s55, 0xff\n" "s_bfe_u32 s64, s55, 0x80010\n" "s_lshr_b32 s65, s55, 24\n" "v_mov_b32 v252, v189\n" \
  "s_add_i32 m0, s62, 0\n" "s_add_i32 s66, s61, 0\n" "buffer_load_dwordx4 v252, s[44:47], s66 offen sc1 lds\n" "v_xor_b32 v252, s63, v252\n" "s_add_i32 m0, s62, 1024\n" \
  "s_add_i32 s66, s61, 65536\n" "buffer_load_dwordx4 v252, s[44:47], s66 offen sc1 lds\n" "v_xor_b32 v252, s64, v252\n" "s_add_i32 m0, s62, 3072\n" "s_add_i32 s66, s61, 196608\n" \
  "buffer_load_dwordx4 v252, s[44:47], s66 offen sc1 lds\n" "v_xor_b32 v252, s63, v252\n" "s_add_i32 m0, s62, 2048\n" "s_add_i32 s66, s61, 131072\n" \
  "buffer_load_dwordx4 v252, s[44:47], s66 offen sc1 lds\n" "v_xor_b32 v252, s65, v252\n" "s_add_i32 m0, s62, 6144\n" "s_add_i32 s66, s61, 393216\n" \
  "buffer_load_dwordx4 v252, s[44:47], s66 offen sc1 lds\n" "v_xor_b32 v252, s63, v252\n" "s_add_i32 m0, s62, 7168\n" "s_add_i32 s66, s61, 458752\n" \
  "buffer_load_dwordx4 v252, s[44:47], s66 offen sc1 lds\n" "v_xor_b32 v252, s64, v252\n" "s_add_i32 m0, s62, 5120\n" "s_add_i32 s66, s61, 327680\n" \
  "buffer_load_dwordx4 v252, s[44:47], s66 offen sc1 lds\n" "v_xor_b32 v252, s63, v252\n" "s_add_i32 m0, s62, 4096\n" "s_add_i32 s66, s61, 262144\n" \
  "buffer_load_dwordx4 v252, s[44:47], s66 offen sc1 lds\n" "ch_hb_nr:\n" "ds_read_b128 v[176:179], v188 offset:0\n" "ds_read_b128 v[180:183], v188 offset:1024\n" \
  "ds_read_b128 v[192:195], v188 offset:24576\n" "ds_read_b128 v[196:199], v188 offset:25600\n" "ds_read_b128 v[200:203], v188 offset:49152\n" "ds_read_b128 v[204:207], v188 offset:50176\n" \
  "v_mov_b64 v[244:245], 0\n" "v_mov_b64 v[246:247], 0\n" "v_mov_b64 v[248:249], 0\n" "v_mov_b64 v[250:251], 0\n" "s_waitcnt vmcnt(0)\n" "ds_write_b32 v187, v255 offset:10252\n" "s_setprio 1\n" \
  "s_branch ch_hb_bar\n" "ch_hb_w7:\n" "buffer_load_dword v208, v189, s[40:43], s70 offen sc1\n" "ds_read_b128 v[176:179], v188 offset:0\n" "ds_read_b128 v[180:183], v188 offset:1024\n" \
  "ds_read_b128 v[192:195], v188 offset:24576\n" "ds_read_b128 v[196:199], v188 offset:25600\n" "ds_read_b128 v[200:203], v188 offset:49152\n" "ds_read_b128 v[204:207], v188 offset:50176\n" \
  "v_mov_b64 v[244:245], 0\n" "v_mov_b64 v[246:247], 0\n" "v_mov_b64 v[248:249], 0\n" "v_mov_b64 v[250:251], 0\n" "s_add_i32 s64, s56, 4\n" "s_min_u32 s64, s64, 64\n" "s_cmp_ge_u32 s68, s64\n" \
  "s_cbranch_scc1 ch_hb_kn\n" "v_lshrrev_b32 v252, 2, v189\n" "s_add_i32 s60, s54, 0x1000\n" "s_add_i32 s61, s54, 0x2000\n" "s_mov_b32 s62, " XS(SPIN_CAP) "\n" "ch_hb_fl:\n" \
  "buffer_load_dword v209, v252, s[48:51], s54 offen sc1\n" "buffer_load_dword v210, v252, s[48:51], s60 offen sc1\n" "buffer_load_dword v211, v252, s[48:51], s61 offen sc1\n" \
  "v_cmp_gt_u32 vcc, " XS(XS0) "*4, v252\n" "s_waitcnt vmcnt(0)\n" "v_cndmask_b32 v211, v211, v209, vcc\n" "v_min3_u32 v209, v209, v210, v211\n" "v_cmp_gt_u32 vcc, 8, v209\n" \
  "s_ff1_i32_b64 s68, vcc\n" "s_min_u32 s68, s68, 64\n" "s_cmp_ge_u32 s68, s64\n" "s_cbranch_scc1 ch_hb_kn\n" "s_sleep 1\n" "s_sub_u32 s62, s62, 1\n" "s_cbranch_scc0 ch_hb_fl\n" "ch_hb_kn:\n" \
  "s_or_b32 s60, s67, 63\n" "s_sub_i32 s60, s60, s57\n" "s_min_u32 s60, s60, 300*64\n" "s_sub_i32 s60, s60, s58\n" "s_ashr_i32 s60, s60, " XS(DSH) "\n" "s_cmp_eq_u32 s56, 0\n" \
  "s_cselect_b32 s60, 0, s60\n" "s_add_i32 s58, s58, s60\n" "s_add_i32 s69, s69, 3*0x8000\n" "s_sub_i32 s60, s69, s53\n" "s_cmp_eq_u32 s60, 9*0x8000\n" "s_cselect_b32 s69, s53, s69\n" \
  "s_cmp_lt_u32 s56, 2\n" "s_cselect_b32 s66, " XS(KE0) ", 0\n" "s_add_i32 s66, s66, " XS(K1) "+" XS(K3) "\n" PSTEP_ "s_setprio 3\n" "s_movk_i32 s60, 0x1000\n" "ch_hb_anc:\n" \
  "s_waitcnt vmcnt(0)\n" "v_subrev_u32 v209, s56, v208\n" "v_and_b32 v209, 63, v209\n" "v_cmp_lt_u32 vcc, 1, v209\n" "s_cmp_eq_u32 vcc_lo, 0\n" "s_cbranch_scc1 ch_hb_ano\n" "ch_hb_anr:\n" \
  "s_sub_u32 s60, s60, 1\n" "s_cbranch_scc1 ch_hb_ane\n" "s_sleep 1\n" "buffer_load_dword v208, v189, s[40:43], s70 offen sc1\n" "s_branch ch_hb_anc\n" "ch_hb_ano:\n" "s_memrealtime s[62:63]\n" \
  "v_cmp_eq_u32 vcc, 0, v209\n" "v_bfrev_b32 v210, 1\n" "s_waitcnt lgkmcnt(0)\n" "s_lshl_b32 s61, s62, 6\n" "v_subrev_u32 v209, s61, v208\n" "v_cndmask_b32 v209, v210, v209, vcc\n" \
  "s_mov_b32 exec_lo, 0\n" "v_mov_b32 v209, v208\n" "s_mov_b32 exec_lo, -1\n" "s_nop 1\n" "v_max_i32_dpp v209, v209, v209 row_ror:8 row_mask:0xf bank_mask:0xf\n" "s_nop 1\n" \
  "v_max_i32_dpp v209, v209, v209 row_ror:4 row_mask:0xf bank_mask:0xf\n" "s_nop 1\n" "v_max_i32_dpp v209, v209, v209 quad_perm:[2,3,0,1] row_mask:0xf bank_mask:0xf\n" "s_nop 1\n" \
  "v_max_i32_dpp v209, v209, v209 quad_perm:[1,0,3,2] row_mask:0xf bank_mask:0xf\n" "s_nop 1\n" "v_readlane_b32 s61, v209, 0\n" "v_readlane_b32 s63, v209, 16\n" "v_readlane_b32 s64, v209, 32\n" \
  "v_readlane_b32 s65, v209, 48\n" "s_max_i32 s61, s61, s63\n" "s_ashr_i32 s61, s61, 6\n" "s_add_i32 s61, s61, s66\n" "s_max_i32 s64, s64, s65\n" "s_ashr_i32 s64, s64, 6\n" \
  "s_max_i32 s64, s64, 40\n" "s_min_i32 s64, s64, 100\n" "s_add_i32 s61, s61, s64\n" "s_add_i32 s61, s61, -3\n" "s_add_i32 s63, s61, s62\n" "s_add_i32 s57, s63, 3\n" "s_cmp_lt_i32 s61, -3\n" \
  "s_cselect_b32 s57, s62, s57\n" "s_cmp_gt_i32 s61, " XS(LIM2) "\n" "s_cbranch_scc1 ch_hb_anr\n" "s_cmp_le_i32 s61, 0\n" "s_cbranch_scc1 ch_hb_ane\n" "s_movk_i32 s60, 100\n" \
  "s_mov_b32 vcc_lo, s62\n" "ch_hb_an1:\n" ".rept 8\n" "s_memrealtime vcc\n" "s_sub_u32 s61, vcc_lo, s63\n" "s_cbranch_scc0 ch_hb_an2\n" ".endr\n" "s_sub_u32 s60, s60, 1\n" \
  "s_cbranch_scc0 ch_hb_an1\n" "ch_hb_an2:\n" "s_min_u32 s61, s61, 3\n" "s_mul_i32 s61, s61, " XS(CPT) "\n" "s_and_b32 s61, s61, -4\n" "s_waitcnt lgkmcnt(0)\n" "s_getpc_b64 vcc\n" \
  "s_add_i32 s61, s61, 16\n" "s_add_u32 vcc_lo, vcc_lo, s61\n" "s_addc_u32 vcc_hi, vcc_hi, 0\n" "s_setpc_b64 vcc\n" ".rept 3*" XS(CPT) "/4\n" "s_nop 0\n" ".endr\n" "ch_hb_ane:\n" ST12 \
  "s_add_i32 m0, s77, 4096\n" "s_add_i32 s60, s69, 0x8000\n" "buffer_load_dwordx4 v189, s[40:43], s60 offen offset:0 sc1 lds\n" \
  "buffer_load_dwordx4 v189, s[40:43], s60 offen offset:1024 sc1 lds\n" "buffer_load_dwordx4 v189, s[40:43], s60 offen offset:2048 sc1 lds\n" \
  "buffer_load_dwordx4 v189, s[40:43], s60 offen offset:3072 sc1 lds\n" "s_mov_b32 m0, s77\n" "s_add_i32 s61, s69, 0x10000\n" "buffer_load_dwordx4 v189, s[40:43], s69 offen offset:0 sc1 lds\n" \
  "buffer_load_dwordx4 v189, s[40:43], s69 offen offset:1024 sc1 lds\n" "buffer_load_dwordx4 v189, s[40:43], s69 offen offset:2048 sc1 lds\n" \
  "buffer_load_dwordx4 v189, s[40:43], s69 offen offset:3072 sc1 lds\n" "s_add_i32 m0, s77, 8192\n" "s_nop 0\n" "buffer_load_dwordx4 v190, s[40:43], s61 offen sc1 lds\n" ST13 \
  "ds_write_b32 v187, v255 offset:10252\n" "s_lshl_b32 s57, s57, 6\n" "s_andn2_b32 s60, s58, 63\n" "s_add_i32 s60, s60, s57\n" "s_add_i32 s60, s60, s56\n" "s_add_i32 s60, s60, 1\n" \
  "v_mov_b32 v252, s60\n" "buffer_store_dword v252, v191, s[40:43], 0 offen sc1\n" "s_xor_b32 s70, s70, 0x400\n" "s_add_i32 s56, s56, 1\n" "s_setprio 1\n" "s_branch ch_hb_loop\n" \
  "ch_hb_slow:\n" "s_cmp_eq_u32 s56, 0\n" "s_cbranch_scc1 ch_hb_back\n" "v_max3_u32 v253, v208, v209, v210\n" "v_max3_u32 v253, v253, v211, v212\n" "v_max3_u32 v253, v253, v213, v214\n" \
  "v_max3_u32 v253, v253, v215, v216\n" "v_max3_u32 v253, v253, v217, v218\n" "v_max3_u32 v253, v253, v219, v220\n" "v_max3_u32 v253, v253, v221, v222\n" "v_max3_u32 v253, v253, v223, v224\n" \
  "v_max3_u32 v253, v253, v225, v226\n" "v_max3_u32 v253, v253, v227, v228\n" "v_max3_u32 v253, v253, v229, v230\n" "v_max3_u32 v253, v253, v231, v232\n" "v_max3_u32 v253, v253, v233, v234\n" \
  "v_max3_u32 v253, v253, v235, v236\n" "v_max3_u32 v253, v253, v237, v238\n" "v_max3_u32 v253, v253, v239, v240\n" "v_max3_u32 v253, v253, v241, v242\n" "v_max_u32 v253, v253, v243\n" \
  "v_cmp_eq_u32 vcc, -1, v253\n" "s_cbranch_vccz ch_hb_back\n" "s_sub_u32 s59, s59, 1\n" "s_cselect_b32 s59, 0, s59\n" "s_cbranch_scc1 ch_hb_back\n" "s_waitcnt lgkmcnt(0)\n" "s_nop 7\n" \
  "ch_hb_redo:\n" CNTB "v_add_u32 v185, s71, v185\n" "s_add_i32 s77, s77, s71\n" "s_sub_i32 s71, 0, s71\n" "s_cmp_lg_u32 s52, 7\n" "s_cbranch_scc1 ch_hb_rd2\n" "s_memrealtime s[62:63]\n" \
  "s_add_i32 m0, s77, 4096\n" "s_add_i32 s60, s69, 0x8000\n" "buffer_load_dwordx4 v189, s[40:43], s60 offen offset:0 sc1 lds\n" \
  "buffer_load_dwordx4 v189, s[40:43], s60 offen offset:1024 sc1 lds\n" "buffer_load_dwordx4 v189, s[40:43], s60 offen offset:2048 sc1 lds\n" \
  "buffer_load_dwordx4 v189, s[40:43], s60 offen offset:3072 sc1 lds\n" "s_mov_b32 m0, s77\n" "s_add_i32 s61, s69, 0x10000\n" "buffer_load_dwordx4 v189, s[40:43], s69 offen offset:0 sc1 lds\n" \
  "buffer_load_dwordx4 v189, s[40:43], s69 offen offset:1024 sc1 lds\n" "buffer_load_dwordx4 v189, s[40:43], s69 offen offset:2048 sc1 lds\n" \
  "buffer_load_dwordx4 v189, s[40:43], s69 offen offset:3072 sc1 lds\n" "s_add_i32 m0, s77, 8192\n" "s_nop 0\n" "buffer_load_dwordx4 v190, s[40:43], s61 offen sc1 lds\n" \
  "s_waitcnt lgkmcnt(0)\n" "s_add_i32 s57, s62, " XS(GL1) "\n" "s_lshl_b32 s57, s57, 6\n" "s_andn2_b32 s60, s58, 63\n" "s_add_i32 s60, s60, s57\n" "s_add_i32 s60, s60, s56\n" \
  "v_mov_b32 v252, s60\n" "buffer_store_dword v252, v191, s[40:43], 0 offen sc1\n" "ch_hb_rd2:\n" "s_waitcnt lgkmcnt(0)\n" "s_nop 7\n" "ds_read_b128 v[176:179], v188 offset:0\n" \
  "ds_read_b128 v[180:183], v188 offset:1024\n" "ds_read_b128 v[192:195], v188 offset:24576\n" "ds_read_b128 v[196:199], v188 offset:25600\n" "ds_read_b128 v[200:203], v188 offset:49152\n" \
  "ds_read_b128 v[204:207], v188 offset:50176\n" "v_mov_b64 v[244:245], 0\n" "v_mov_b64 v[246:247], 0\n" "v_mov_b64 v[248:249], 0\n" "v_mov_b64 v[250:251], 0\n" "s_branch ch_hb_loop\n" \
  "ch_hb_miss:\n" "s_cmp_eq_u32 s56, 0\n" "s_cbranch_scc1 ch_hb_bk2\n" "s_sub_u32 s59, s59, 1\n" "s_cselect_b32 s59, 0, s59\n" "s_cbranch_scc0 ch_hb_redo\n" "s_branch ch_hb_bk2\n" \
  "ch_hb_fin:\n" "s_waitcnt vmcnt(0)\n"
#define CH_O5 \
  "s_mov_b32 s56, 0\n" "s_mov_b32 s59, " XS(SPIN_CAP) "\n" "s_mov_b32 s57, 0\n" "s_mov_b32 s58, " XS(DUO0) "*64\n" "s_mov_b32 s69, 0x40000\n" "s_mov_b32 s70, 0x84000\n" "ch_o5_loop:\n" \
  "v_mov_b64 v[176:177], 0\n" "v_mov_b64 v[178:179], 0\n" "v_mov_b64 v[180:181], 0\n" "v_mov_b64 v[182:183], 0\n" "v_mov_b64 v[184:185], 0\n" "v_mov_b64 v[186:187], 0\n" \
  "v_mov_b64 v[188:189], 0\n" "v_mov_b64 v[190:191], 0\n" "v_mov_b64 v[192:193], 0\n" "v_mov_b64 v[194:195], 0\n" "s_cmp_lg_u32 s52, 7\n" "s_cbranch_scc1 ch_o5_go\n" "s_sleep " XS(RSL) "\n" \
  "v_mov_b32 v212, s58\n" "buffer_store_dword v212, v220, s[40:43], 0 offen sc1\n" "s_sleep " XS(OSL) "\n" "v_and_b32 v215, 0x1f0, v217\n" \
  "buffer_load_dword v214, v215, s[40:43], s54 offen sc1\n" "s_cmp_lt_u32 s56, 2\n" "s_cselect_b32 s66, " XS(KE1) ", 0\n" "s_add_i32 s66, s66, " XS(K1) "\n" "s_movk_i32 s60, 0x1000\n" \
  "ch_o5_anc:\n" "s_waitcnt vmcnt(0)\n" "v_subrev_u32 v212, s56, v214\n" "v_and_b32 v212, 63, v212\n" "v_cmp_lt_u32 vcc, 1, v212\n" "s_cmp_eq_u32 vcc_lo, 0\n" "s_cbranch_scc1 ch_o5_ano\n" \
  "ch_o5_anr:\n" "s_sub_u32 s60, s60, 1\n" "s_cbranch_scc1 ch_o5_ane\n" "s_sleep 1\n" "buffer_load_dword v214, v215, s[40:43], s54 offen sc1\n" "s_branch ch_o5_anc\n" "ch_o5_ano:\n" \
  "s_memrealtime s[62:63]\n" "v_cmp_eq_u32 vcc, 0, v212\n" "v_bfrev_b32 v213, 1\n" "s_waitcnt lgkmcnt(0)\n" "s_lshl_b32 s61, s62, 6\n" "v_subrev_u32 v212, s61, v214\n" \
  "v_cndmask_b32 v212, v213, v212, vcc\n" "s_mov_b32 exec_lo, 0\n" "v_mov_b32 v212, v214\n" "s_mov_b32 exec_lo, -1\n" "s_nop 1\n" \
  "v_max_i32_dpp v212, v212, v212 row_ror:8 row_mask:0xf bank_mask:0xf\n" "s_nop 1\n" "v_max_i32_dpp v212, v212, v212 row_ror:4 row_mask:0xf bank_mask:0xf\n" "s_nop 1\n" \
  "v_max_i32_dpp v212, v212, v212 quad_perm:[2,3,0,1] row_mask:0xf bank_mask:0xf\n" "s_nop 1\n" "v_max_i32_dpp v212, v212, v212 quad_perm:[1,0,3,2] row_mask:0xf bank_mask:0xf\n" "s_nop 1\n" \
  "v_readlane_b32 s61, v212, 0\n" "v_readlane_b32 s63, v212, 16\n" "s_max_i32 s61, s61, s63\n" "s_ashr_i32 s61, s61, 6\n" "s_add_i32 s61, s61, s66\n" "s_add_i32 s61, s61, -3\n" \
  "s_add_i32 s63, s61, s62\n" "s_add_i32 s68, s63, 3\n" "s_cmp_lt_i32 s61, -3\n" "s_cselect_b32 s68, s62, s68\n" "s_cmp_gt_i32 s61, " XS(LIM1) "\n" "s_cbranch_scc1 ch_o5_anr\n" \
  "s_cmp_le_i32 s61, 0\n" "s_cbranch_scc1 ch_o5_ane\n" "s_movk_i32 s60, 100\n" "s_mov_b32 vcc_lo, s62\n" "ch_o5_an1:\n" ".rept 8\n" "s_memrealtime vcc\n" "s_sub_u32 s61, vcc_lo, s63\n" \
  "s_cbranch_scc0 ch_o5_an2\n" ".endr\n" "s_sub_u32 s60, s60, 1\n" "s_cbranch_scc0 ch_o5_an1\n" "ch_o5_an2:\n" "s_min_u32 s61, s61, 3\n" "s_mul_i32 s61, s61, " XS(CPT) "\n" \
  "s_and_b32 s61, s61, -4\n" "s_waitcnt lgkmcnt(0)\n" "s_getpc_b64 vcc\n" "s_add_i32 s61, s61, 16\n" "s_add_u32 vcc_lo, vcc_lo, s61\n" "s_addc_u32 vcc_hi, vcc_hi, 0\n" "s_setpc_b64 vcc\n" \
  ".rept 3*" XS(CPT) "/4\n" "s_nop 0\n" ".endr\n" "ch_o5_ane:\n" "s_xor_b32 s54, s54, 0x400\n" ".p2align 3\n" "ch_o5_go:\n" "s_nop 0\n" "s_cmp_lt_u32 s52, 4\n" "s_barrier\n" ST0 \
  "s_cbranch_scc0 ch_o5_l2\n" "ch_o5_l1:\n" "buffer_load_dwordx4 v[160:163], v217, s[40:43], s53 offen offset:0 sc1\n" "s_nop " XS(OG) "\n" "s_nop 0\n" \
  "buffer_load_dwordx4 v[164:167], v217, s[40:43], s53 offen offset:1024 sc1\n" "s_nop " XS(OG) "\n" "s_nop 0\n" "buffer_load_dwordx4 v[168:171], v217, s[40:43], s53 offen offset:2048 sc1\n" \
  "s_nop " XS(OG) "\n" "s_nop 0\n" "buffer_load_dwordx4 v[172:175], v217, s[40:43], s53 offen offset:3072 sc1\n" "ch_o5_mul:\n" "s_nop 0\n" "s_waitcnt vmcnt(3)\n" ST1 \
  "v_smfmac_f32_16x16x64_bf16 v[176:179], v[160:163], v[0:7], v216\n" "v_cmp_eq_u32_e64 vcc, -1, v160\n" "v_smfmac_f32_16x16x64_bf16 v[180:183], v[160:163], v[8:15], v216\n" \
  "v_smfmac_f32_16x16x64_bf16 v[184:187], v[160:163], v[16:23], v216\n" "s_nop 0\n" "s_cbranch_vccnz ch_o5_slow2\n" "v_smfmac_f32_16x16x64_bf16 v[188:191], v[160:163], v[24:31], v216\n" \
  "v_smfmac_f32_16x16x64_bf16 v[192:195], v[160:163], v[32:39], v216\n" "s_nop 0\n" "s_waitcnt vmcnt(2)\n" "v_smfmac_f32_16x16x64_bf16 v[176:179], v[164:167], v[40:47], v216\n" \
  "v_cmp_eq_u32_e64 vcc, -1, v164\n" "v_smfmac_f32_16x16x64_bf16 v[180:183], v[164:167], v[48:55], v216\n" "v_smfmac_f32_16x16x64_bf16 v[184:187], v[164:167], v[56:63], v216\n" "s_nop 0\n" \
  "s_cbranch_vccnz ch_o5_slow2\n" "v_smfmac_f32_16x16x64_bf16 v[188:191], v[164:167], v[64:71], v216\n" "v_smfmac_f32_16x16x64_bf16 v[192:195], v[164:167], v[72:79], v216\n" "s_nop 0\n" \
  "s_waitcnt vmcnt(1)\n" "v_smfmac_f32_16x16x64_bf16 v[176:179], v[168:171], v[80:87], v216\n" "v_smfmac_f32_16x16x64_bf16 v[180:183], v[168:171], v[88:95], v216\n" \
  "v_smfmac_f32_16x16x64_bf16 v[184:187], v[168:171], v[96:103], v216\n" "v_smfmac_f32_16x16x64_bf16 v[188:191], v[168:171], v[104:111], v216\n" \
  "v_smfmac_f32_16x16x64_bf16 v[192:195], v[168:171], v[112:119], v216\n" "s_nop 0\n" "s_waitcnt vmcnt(0)\n" ST2 "v_smfmac_f32_16x16x64_bf16 v[176:179], v[172:175], v[120:127], v216\n" \
  "v_smfmac_f32_16x16x64_bf16 v[180:183], v[172:175], v[128:135], v216\n" "v_smfmac_f32_16x16x64_bf16 v[184:187], v[172:175], v[136:143], v216\n" \
  "v_smfmac_f32_16x16x64_bf16 v[188:191], v[172:175], v[144:151], v216\n" "v_smfmac_f32_16x16x64_bf16 v[192:195], v[172:175], v[152:159], v216\n" ST3 "v_add_f32 v196, v176, v177\n" \
  "v_add_f32 v200, v178, v179\n" "v_add_f32_dpp v200, v180, v181 quad_perm:[0,1,2,3] row_mask:0xf bank_mask:0x3\n" \
  "v_add_f32_dpp v196, v182, v183 quad_perm:[0,1,2,3] row_mask:0xf bank_mask:0xc\n" "v_add_f32 v197, v184, v185\n" "v_add_f32 v201, v186, v187\n" \
  "v_add_f32_dpp v201, v188, v189 quad_perm:[0,1,2,3] row_mask:0xf bank_mask:0x3\n" "v_add_f32_dpp v197, v190, v191 quad_perm:[0,1,2,3] row_mask:0xf bank_mask:0xc\n" \
  "v_add_f32 v202, v194, v195\n" "v_add_f32 v198, v192, v193\n" "v_add_f32_dpp v196, v200, v196 row_ror:8 row_mask:0xf bank_mask:0xf\n" \
  "v_add_f32_dpp v197, v201, v197 row_ror:8 row_mask:0xf bank_mask:0xf\n" "v_add_f32_dpp v198, v202, v198 row_ror:8 row_mask:0xf bank_mask:0xf\n" \
  "ds_write2st64_b32 v218, v196, v197 offset1:8\n" "ds_write_b32 v218, v198 offset:4096\n" "v_cmp_u_f32 vcc, v196, v196\n" "s_cbranch_vccnz ch_o5_slow\n" "ch_o5_back:\n" "s_cmp_gt_u32 s52, 2\n" \
  "s_waitcnt lgkmcnt(0)\n" "s_barrier\n" "s_cbranch_scc1 ch_o5_rest\n" ST4 "ds_read_b128 v[204:207], v219\n" "ds_read_b128 v[208:211], v219 offset:1024\n" "s_add_i32 s60, s57, 6*0x8000\n" \
  "s_cmp_ge_u32 s60, 9*0x8000\n" "s_cselect_b32 s61, 9*0x8000, 0\n" "s_sub_i32 s60, s60, s61\n" "v_mov_b32 v213, -1\n" ST7 "s_waitcnt lgkmcnt(1)\n" "v_add_f32 v204, v204, v205\n" \
  "v_add_f32 v206, v206, v207\n" "s_waitcnt lgkmcnt(0)\n" "v_add_f32 v208, v208, v209\n" "v_add_f32 v210, v210, v211\n" "v_add_f32 v208, v208, v204\n" "v_add_f32 v210, v210, v206\n" \
  "v_add_f32 v204, v208, v210\n" "v_add_f32 v205, v208, v210\n" "s_nop 1\n" "v_permlane32_swap_b32 v204, v205\n" "v_cvt_pk_bf16_f32 v204, v204, v205\n" ST6 \
  "buffer_store_dword v204, v220, s[40:43], s57 offen sc1\n" ST5 "s_sleep " XS(RSL) "\n" "buffer_store_dword v213, v220, s[40:43], s60 offen sc1\n" "ch_o5_nxt:\n" PTS PTA \
  "s_add_i32 s57, s57, 3*0x8000\n" "s_cmp_eq_u32 s57, 9*0x8000\n" "s_cselect_b32 s57, 0, s57\n" "s_xor_b32 s53, s53, s69\n" "s_mov_b32 s60, s69\n" "s_xor_b32 s69, s69, s70\n" \
  "s_mov_b32 s70, s60\n" "s_add_i32 s56, s56, 1\n" "s_cmp_lt_u32 s56, 63\n" "s_cbranch_scc1 ch_o5_loop\n" "s_branch ch_o5_fin\n" "ch_o5_tim:\n" "s_memrealtime s[60:61]\n" \
  "s_waitcnt lgkmcnt(0)\n" "s_sub_i32 s60, s60, s68\n" "s_lshl_b32 s60, s60, 6\n" "s_sub_i32 s60, s60, s58\n" "s_ashr_i32 s60, s60, " XS(DSO) "\n" "s_min_i32 s60, s60, " XS(DUP) "\n" \
  "s_add_i32 s58, s58, s60\n" "s_branch ch_o5_nxt\n" "ch_o5_tch:\n" "s_add_i32 s60, s56, 2\n" "s_cmp_eq_u32 s52, 5\n" "s_cselect_b32 s62, " XS(XS0) ", 3\n" "s_cmp_lt_u32 s60, s62\n" \
  "s_cbranch_scc1 ch_o5_nxt\n" "s_cmp_eq_u32 s60, 64\n" "s_cbranch_scc1 ch_o5_nxt\n" "s_lshl_b32 s61, s60, 19\n" "s_lshl_b32 s62, s60, 11\n" "s_and_b32 s62, s62, 0x2000\n" \
  "s_add_i32 s61, s61, s62\n" "s_sleep " XS(TSL) "\n" ".if " XS(TCH) "\n" "buffer_load_dword v214, v220, s[44:47], s61 offen sc1\n" ".endif\n" ".if " XS(TCH2) "\n" \
  "buffer_load_dword v214, v220, s[44:47], s61 offen offset:64 sc1\n" ".endif\n" "s_branch ch_o5_nxt\n" "ch_o5_rest:\n" "s_cmp_eq_u32 s52, 7\n" "s_cbranch_scc1 ch_o5_tim\n" \
  "s_cmp_lg_u32 s52, 6\n" "s_cbranch_scc1 ch_o5_tch\n" "s_and_b32 s60, s56, 31\n" "s_cmp_lg_u32 s60, s55\n" "s_cbranch_scc1 ch_o5_nxt\n" "s_sub_u32 s60, s56, 1\n" "s_cbranch_scc1 ch_o5_nxt\n" \
  "s_lshl_b32 s61, s60, 7\n" "s_add_i32 s62, s61, " XS(SY_KMF0) "*4\n" "s_add_i32 s63, s61, " XS(SY_CS0) "*4\n" "s_cmp_lt_u32 s60, " XS(XS0) "\n" "s_cselect_b32 s62, s62, s63\n" \
  "s_lshl_b32 s61, s60, 10\n" "s_add_i32 s63, s61, " XS(SY_CS0) "*4-48*7*128\n" "s_cmp_lt_u32 s60, 48\n" "s_cselect_b32 s62, s62, s63\n" "s_cselect_b32 exec_lo, 1, 255\n" \
  "s_mov_b32 exec_hi, 0\n" "s_sleep " XS(RSL) "\n" "v_lshlrev_b32 v212, 3, v217\n" "buffer_store_dword v216, v212, s[48:51], s62 offen sc1\n" "s_mov_b64 exec, -1\n" "s_branch ch_o5_nxt\n" \
  "ch_o5_l2:\n" ".rept " XS(OD) "\n" "s_nop 3\n" ".endr\n" "s_branch ch_o5_l1\n" "ch_o5_slow:\n" "v_max3_u32 v212, v160, v161, v162\n" "v_max3_u32 v212, v212, v163, v164\n" \
  "v_max3_u32 v212, v212, v165, v166\n" "v_max3_u32 v212, v212, v167, v168\n" "v_max3_u32 v212, v212, v169, v170\n" "v_max3_u32 v212, v212, v171, v172\n" "v_max3_u32 v212, v212, v173, v174\n" \
  "v_max_u32 v212, v212, v175\n" "v_cmp_eq_u32 vcc, -1, v212\n" "s_cbranch_vccz ch_o5_back\n" "s_sub_u32 s59, s59, 1\n" "s_cselect_b32 s59, 0, s59\n" "s_cbranch_scc1 ch_o5_back\n" \
  "ch_o5_slow2:\n" CNTO "s_waitcnt vmcnt(0)\n" "ch_o5_wait:\n" "v_max3_u32 v212, v160, v161, v162\n" "v_max_u32 v212, v212, v163\n" "v_cmp_eq_u32 vcc, -1, v212\n" "s_cbranch_vccz ch_o5_h0\n" \
  "buffer_load_dwordx4 v[160:163], v217, s[40:43], s53 offen offset:0 sc1\n" "ch_o5_h0:\n" "v_max3_u32 v212, v164, v165, v166\n" "v_max_u32 v212, v212, v167\n" "v_cmp_eq_u32 vcc, -1, v212\n" \
  "s_cbranch_vccz ch_o5_h1\n" "buffer_load_dwordx4 v[164:167], v217, s[40:43], s53 offen offset:1024 sc1\n" "ch_o5_h1:\n" "v_max3_u32 v212, v168, v169, v170\n" "v_max_u32 v212, v212, v171\n" \
  "v_cmp_eq_u32 vcc, -1, v212\n" "s_cbranch_vccz ch_o5_h2\n" "buffer_load_dwordx4 v[168:171], v217, s[40:43], s53 offen offset:2048 sc1\n" "ch_o5_h2:\n" "v_max3_u32 v212, v172, v173, v174\n" \
  "v_max_u32 v212, v212, v175\n" "v_cmp_eq_u32 vcc, -1, v212\n" "s_cbranch_vccz ch_o5_h3\n" "buffer_load_dwordx4 v[172:175], v217, s[40:43], s53 offen offset:3072 sc1\n" "ch_o5_h3:\n" \
  "v_mov_b64 v[176:177], 0\n" "v_mov_b64 v[178:179], 0\n" "v_mov_b64 v[180:181], 0\n" "v_mov_b64 v[182:183], 0\n" "v_mov_b64 v[184:185], 0\n" "v_mov_b64 v[186:187], 0\n" \
  "v_mov_b64 v[188:189], 0\n" "v_mov_b64 v[190:191], 0\n" "v_mov_b64 v[192:193], 0\n" "v_mov_b64 v[194:195], 0\n" "s_waitcnt vmcnt(0)\n" "v_max3_u32 v212, v160, v161, v162\n" \
  "v_max3_u32 v212, v212, v163, v164\n" "v_max3_u32 v212, v212, v165, v166\n" "v_max3_u32 v212, v212, v167, v168\n" "v_max3_u32 v212, v212, v169, v170\n" "v_max3_u32 v212, v212, v171, v172\n" \
  "v_max3_u32 v212, v212, v173, v174\n" "v_max_u32 v212, v212, v175\n" "v_cmp_eq_u32 vcc, -1, v212\n" "s_cbranch_vccz ch_o5_mul\n" "s_sub_u32 s59, s59, 1\n" "s_cselect_b32 s59, 0, s59\n" \
  "s_cbranch_scc0 ch_o5_wait\n" "v_mov_b32 v160, 0\n" "v_mov_b32 v164, 0\n" "v_mov_b32 v168, 0\n" "v_mov_b32 v172, 0\n" "s_mov_b32 s59, 0\n" "s_nop 0\n" "s_branch ch_o5_mul\n" "ch_o5_fin:\n" \
  "s_waitcnt vmcnt(0)\n"
#define CH_O4 \
  "s_mov_b32 s56, 0\n" "s_mov_b32 s59, " XS(SPIN_CAP) "\n" "s_mov_b32 s57, 0\n" "s_mov_b32 s58, " XS(DUO0) "*64\n" "s_mov_b32 s69, 0x40000\n" "s_mov_b32 s70, 0x84000\n" "ch_o4_loop:\n" \
  "v_mov_b64 v[176:177], 0\n" "v_mov_b64 v[178:179], 0\n" "v_mov_b64 v[180:181], 0\n" "v_mov_b64 v[182:183], 0\n" "v_mov_b64 v[184:185], 0\n" "v_mov_b64 v[186:187], 0\n" \
  "v_mov_b64 v[188:189], 0\n" "v_mov_b64 v[190:191], 0\n" "s_cmp_lg_u32 s52, 7\n" "s_cbranch_scc1 ch_o4_go\n" "s_sleep " XS(RSL) "\n" "v_mov_b32 v212, s58\n" \
  "buffer_store_dword v212, v220, s[40:43], 0 offen sc1\n" "s_sleep " XS(OSL) "\n" "v_and_b32 v215, 0x1f0, v217\n" "buffer_load_dword v214, v215, s[40:43], s54 offen sc1\n" \
  "s_cmp_lt_u32 s56, 2\n" "s_cselect_b32 s66, " XS(KE1) ", 0\n" "s_add_i32 s66, s66, " XS(K1) "\n" "s_movk_i32 s60, 0x1000\n" "ch_o4_anc:\n" "s_waitcnt vmcnt(0)\n" \
  "v_subrev_u32 v212, s56, v214\n" "v_and_b32 v212, 63, v212\n" "v_cmp_lt_u32 vcc, 1, v212\n" "s_cmp_eq_u32 vcc_lo, 0\n" "s_cbranch_scc1 ch_o4_ano\n" "ch_o4_anr:\n" "s_sub_u32 s60, s60, 1\n" \
  "s_cbranch_scc1 ch_o4_ane\n" "s_sleep 1\n" "buffer_load_dword v214, v215, s[40:43], s54 offen sc1\n" "s_branch ch_o4_anc\n" "ch_o4_ano:\n" "s_memrealtime s[62:63]\n" \
  "v_cmp_eq_u32 vcc, 0, v212\n" "v_bfrev_b32 v213, 1\n" "s_waitcnt lgkmcnt(0)\n" "s_lshl_b32 s61, s62, 6\n" "v_subrev_u32 v212, s61, v214\n" "v_cndmask_b32 v212, v213, v212, vcc\n" \
  "s_mov_b32 exec_lo, 0\n" "v_mov_b32 v212, v214\n" "s_mov_b32 exec_lo, -1\n" "s_nop 1\n" "v_max_i32_dpp v212, v212, v212 row_ror:8 row_mask:0xf bank_mask:0xf\n" "s_nop 1\n" \
  "v_max_i32_dpp v212, v212, v212 row_ror:4 row_mask:0xf bank_mask:0xf\n" "s_nop 1\n" "v_max_i32_dpp v212, v212, v212 quad_perm:[2,3,0,1] row_mask:0xf bank_mask:0xf\n" "s_nop 1\n" \
  "v_max_i32_dpp v212, v212, v212 quad_perm:[1,0,3,2] row_mask:0xf bank_mask:0xf\n" "s_nop 1\n" "v_readlane_b32 s61, v212, 0\n" "v_readlane_b32 s63, v212, 16\n" "s_max_i32 s61, s61, s63\n" \
  "s_ashr_i32 s61, s61, 6\n" "s_add_i32 s61, s61, s66\n" "s_add_i32 s61, s61, -3\n" "s_add_i32 s63, s61, s62\n" "s_add_i32 s68, s63, 3\n" "s_cmp_lt_i32 s61, -3\n" \
  "s_cselect_b32 s68, s62, s68\n" "s_cmp_gt_i32 s61, " XS(LIM1) "\n" "s_cbranch_scc1 ch_o4_anr\n" "s_cmp_le_i32 s61, 0\n" "s_cbranch_scc1 ch_o4_ane\n" "s_movk_i32 s60, 100\n" \
  "s_mov_b32 vcc_lo, s62\n" "ch_o4_an1:\n" ".rept 8\n" "s_memrealtime vcc\n" "s_sub_u32 s61, vcc_lo, s63\n" "s_cbranch_scc0 ch_o4_an2\n" ".endr\n" "s_sub_u32 s60, s60, 1\n" \
  "s_cbranch_scc0 ch_o4_an1\n" "ch_o4_an2:\n" "s_min_u32 s61, s61, 3\n" "s_mul_i32 s61, s61, " XS(CPT) "\n" "s_and_b32 s61, s61, -4\n" "s_waitcnt lgkmcnt(0)\n" "s_getpc_b64 vcc\n" \
  "s_add_i32 s61, s61, 16\n" "s_add_u32 vcc_lo, vcc_lo, s61\n" "s_addc_u32 vcc_hi, vcc_hi, 0\n" "s_setpc_b64 vcc\n" ".rept 3*" XS(CPT) "/4\n" "s_nop 0\n" ".endr\n" "ch_o4_ane:\n" \
  "s_xor_b32 s54, s54, 0x400\n" ".p2align 3\n" "ch_o4_go:\n" "s_nop 0\n" "s_cmp_lt_u32 s52, 4\n" "s_barrier\n" ST0 "s_cbranch_scc0 ch_o4_l2\n" "ch_o4_l1:\n" \
  "buffer_load_dwordx4 v[160:163], v217, s[40:43], s53 offen offset:0 sc1\n" "s_nop " XS(OG) "\n" "s_nop 0\n" "buffer_load_dwordx4 v[164:167], v217, s[40:43], s53 offen offset:1024 sc1\n" \
  "s_nop " XS(OG) "\n" "s_nop 0\n" "buffer_load_dwordx4 v[168:171], v217, s[40:43], s53 offen offset:2048 sc1\n" "s_nop " XS(OG) "\n" "s_nop 0\n" \
  "buffer_load_dwordx4 v[172:175], v217, s[40:43], s53 offen offset:3072 sc1\n" "ch_o4_mul:\n" "s_nop 0\n" "s_waitcnt vmcnt(3)\n" ST1 \
  "v_smfmac_f32_16x16x64_bf16 v[176:179], v[160:163], v[0:7], v216\n" "v_cmp_eq_u32_e64 vcc, -1, v160\n" "v_smfmac_f32_16x16x64_bf16 v[180:183], v[160:163], v[8:15], v216\n" \
  "v_smfmac_f32_16x16x64_bf16 v[184:187], v[160:163], v[16:23], v216\n" "s_nop 0\n" "s_cbranch_vccnz ch_o4_slow2\n" "v_smfmac_f32_16x16x64_bf16 v[188:191], v[160:163], v[24:31], v216\n" \
  "s_nop 0\n" "s_waitcnt vmcnt(2)\n" "v_smfmac_f32_16x16x64_bf16 v[176:179], v[164:167], v[40:47], v216\n" "v_cmp_eq_u32_e64 vcc, -1, v164\n" \
  "v_smfmac_f32_16x16x64_bf16 v[180:183], v[164:167], v[48:55], v216\n" "v_smfmac_f32_16x16x64_bf16 v[184:187], v[164:167], v[56:63], v216\n" "s_nop 0\n" "s_cbranch_vccnz ch_o4_slow2\n" \
  "v_smfmac_f32_16x16x64_bf16 v[188:191], v[164:167], v[64:71], v216\n" "s_nop 0\n" "s_waitcnt vmcnt(1)\n" "v_smfmac_f32_16x16x64_bf16 v[176:179], v[168:171], v[80:87], v216\n" \
  "v_smfmac_f32_16x16x64_bf16 v[180:183], v[168:171], v[88:95], v216\n" "v_smfmac_f32_16x16x64_bf16 v[184:187], v[168:171], v[96:103], v216\n" \
  "v_smfmac_f32_16x16x64_bf16 v[188:191], v[168:171], v[104:111], v216\n" "s_nop 0\n" "s_waitcnt vmcnt(0)\n" ST2 "v_smfmac_f32_16x16x64_bf16 v[176:179], v[172:175], v[120:127], v216\n" \
  "v_smfmac_f32_16x16x64_bf16 v[180:183], v[172:175], v[128:135], v216\n" "v_smfmac_f32_16x16x64_bf16 v[184:187], v[172:175], v[136:143], v216\n" \
  "v_smfmac_f32_16x16x64_bf16 v[188:191], v[172:175], v[144:151], v216\n" ST3 "v_add_f32 v196, v176, v177\n" "v_add_f32 v200, v178, v179\n" \
  "v_add_f32_dpp v200, v180, v181 quad_perm:[0,1,2,3] row_mask:0xf bank_mask:0x3\n" "v_add_f32_dpp v196, v182, v183 quad_perm:[0,1,2,3] row_mask:0xf bank_mask:0xc\n" \
  "v_add_f32_e64 v197, v184, v185\n" "v_add_f32 v201, v186, v187\n" "s_nop 1\n" "v_add_f32_dpp v201, v188, v189 quad_perm:[0,1,2,3] row_mask:0xf bank_mask:0x3\n" \
  "v_add_f32_dpp v197, v190, v191 quad_perm:[0,1,2,3] row_mask:0xf bank_mask:0xc\n" "v_add_f32_dpp v196, v200, v196 row_ror:8 row_mask:0xf bank_mask:0xf\n" \
  "v_add_f32_dpp v197, v201, v197 row_ror:8 row_mask:0xf bank_mask:0xf\n" "ds_write2st64_b32 v218, v196, v197 offset1:8\n" "v_cmp_u_f32 vcc, v196, v196\n" "s_cbranch_vccnz ch_o4_slow\n" \
  "ch_o4_back:\n" "s_cmp_gt_u32 s52, 2\n" "s_waitcnt lgkmcnt(0)\n" "s_barrier\n" "s_cbranch_scc1 ch_o4_rest\n" ST4 "ds_read_b128 v[204:207], v219\n" \
  "ds_read_b128 v[208:211], v219 offset:1024\n" "s_add_i32 s60, s57, 6*0x8000\n" "s_cmp_ge_u32 s60, 9*0x8000\n" "s_cselect_b32 s61, 9*0x8000, 0\n" "s_sub_i32 s60, s60, s61\n" \
  "v_mov_b32 v213, -1\n" ST7 "s_waitcnt lgkmcnt(1)\n" "v_add_f32 v204, v204, v205\n" "v_add_f32 v206, v206, v207\n" "s_waitcnt lgkmcnt(0)\n" "v_add_f32 v208, v208, v209\n" \
  "v_add_f32 v210, v210, v211\n" "v_add_f32 v208, v208, v204\n" "v_add_f32 v210, v210, v206\n" "v_add_f32 v204, v208, v210\n" "v_add_f32 v205, v208, v210\n" "s_nop 1\n" \
  "v_permlane32_swap_b32 v204, v205\n" "v_cvt_pk_bf16_f32 v204, v204, v205\n" ST6 "buffer_store_dword v204, v220, s[40:43], s57 offen sc1\n" ST5 "s_sleep " XS(RSL) "\n" \
  "buffer_store_dword v213, v220, s[40:43], s60 offen sc1\n" "ch_o4_nxt:\n" PTS PTA "s_add_i32 s57, s57, 3*0x8000\n" "s_cmp_eq_u32 s57, 9*0x8000\n" "s_cselect_b32 s57, 0, s57\n" \
  "s_xor_b32 s53, s53, s69\n" "s_mov_b32 s60, s69\n" "s_xor_b32 s69, s69, s70\n" "s_mov_b32 s70, s60\n" "s_add_i32 s56, s56, 1\n" "s_cmp_lt_u32 s56, 63\n" "s_cbranch_scc1 ch_o4_loop\n" \
  "s_branch ch_o4_fin\n" "ch_o4_tim:\n" "s_memrealtime s[60:61]\n" "s_waitcnt lgkmcnt(0)\n" "s_sub_i32 s60, s60, s68\n" "s_lshl_b32 s60, s60, 6\n" "s_sub_i32 s60, s60, s58\n" \
  "s_ashr_i32 s60, s60, " XS(DSO) "\n" "s_min_i32 s60, s60, " XS(DUP) "\n" "s_add_i32 s58, s58, s60\n" "s_branch ch_o4_nxt\n" "ch_o4_tch:\n" "s_add_i32 s60, s56, 2\n" "s_cmp_eq_u32 s52, 5\n" \
  "s_cselect_b32 s62, " XS(XS0) ", 3\n" "s_cmp_lt_u32 s60, s62\n" "s_cbranch_scc1 ch_o4_nxt\n" "s_cmp_eq_u32 s60, 64\n" "s_cbranch_scc1 ch_o4_nxt\n" "s_lshl_b32 s61, s60, 19\n" \
  "s_lshl_b32 s62, s60, 11\n" "s_and_b32 s62, s62, 0x2000\n" "s_add_i32 s61, s61, s62\n" "s_sleep " XS(TSL) "\n" ".if " XS(TCH) "\n" "buffer_load_dword v214, v220, s[44:47], s61 offen sc1\n" \
  ".endif\n" ".if " XS(TCH2) "\n" "buffer_load_dword v214, v220, s[44:47], s61 offen offset:64 sc1\n" ".endif\n" "s_branch ch_o4_nxt\n" "ch_o4_rest:\n" "s_cmp_eq_u32 s52, 7\n" \
  "s_cbranch_scc1 ch_o4_tim\n" "s_cmp_lg_u32 s52, 6\n" "s_cbranch_scc1 ch_o4_tch\n" "s_and_b32 s60, s56, 31\n" "s_cmp_lg_u32 s60, s55\n" "s_cbranch_scc1 ch_o4_nxt\n" "s_sub_u32 s60, s56, 1\n" \
  "s_cbranch_scc1 ch_o4_nxt\n" "s_lshl_b32 s61, s60, 7\n" "s_add_i32 s62, s61, " XS(SY_KMF0) "*4\n" "s_add_i32 s63, s61, " XS(SY_CS0) "*4\n" "s_cmp_lt_u32 s60, " XS(XS0) "\n" \
  "s_cselect_b32 s62, s62, s63\n" "s_lshl_b32 s61, s60, 10\n" "s_add_i32 s63, s61, " XS(SY_CS0) "*4-48*7*128\n" "s_cmp_lt_u32 s60, 48\n" "s_cselect_b32 s62, s62, s63\n" \
  "s_cselect_b32 exec_lo, 1, 255\n" "s_mov_b32 exec_hi, 0\n" "s_sleep " XS(RSL) "\n" "v_lshlrev_b32 v212, 3, v217\n" "buffer_store_dword v216, v212, s[48:51], s62 offen sc1\n" \
  "s_mov_b64 exec, -1\n" "s_branch ch_o4_nxt\n" "ch_o4_l2:\n" ".rept " XS(OD) "\n" "s_nop 3\n" ".endr\n" "s_branch ch_o4_l1\n" "ch_o4_slow:\n" "v_max3_u32 v212, v160, v161, v162\n" \
  "v_max3_u32 v212, v212, v163, v164\n" "v_max3_u32 v212, v212, v165, v166\n" "v_max3_u32 v212, v212, v167, v168\n" "v_max3_u32 v212, v212, v169, v170\n" "v_max3_u32 v212, v212, v171, v172\n" \
  "v_max3_u32 v212, v212, v173, v174\n" "v_max_u32 v212, v212, v175\n" "v_cmp_eq_u32 vcc, -1, v212\n" "s_cbranch_vccz ch_o4_back\n" "s_sub_u32 s59, s59, 1\n" "s_cselect_b32 s59, 0, s59\n" \
  "s_cbranch_scc1 ch_o4_back\n" "ch_o4_slow2:\n" CNTO "s_waitcnt vmcnt(0)\n" "ch_o4_wait:\n" "v_max3_u32 v212, v160, v161, v162\n" "v_max_u32 v212, v212, v163\n" "v_cmp_eq_u32 vcc, -1, v212\n" \
  "s_cbranch_vccz ch_o4_h0\n" "buffer_load_dwordx4 v[160:163], v217, s[40:43], s53 offen offset:0 sc1\n" "ch_o4_h0:\n" "v_max3_u32 v212, v164, v165, v166\n" "v_max_u32 v212, v212, v167\n" \
  "v_cmp_eq_u32 vcc, -1, v212\n" "s_cbranch_vccz ch_o4_h1\n" "buffer_load_dwordx4 v[164:167], v217, s[40:43], s53 offen offset:1024 sc1\n" "ch_o4_h1:\n" "v_max3_u32 v212, v168, v169, v170\n" \
  "v_max_u32 v212, v212, v171\n" "v_cmp_eq_u32 vcc, -1, v212\n" "s_cbranch_vccz ch_o4_h2\n" "buffer_load_dwordx4 v[168:171], v217, s[40:43], s53 offen offset:2048 sc1\n" "ch_o4_h2:\n" \
  "v_max3_u32 v212, v172, v173, v174\n" "v_max_u32 v212, v212, v175\n" "v_cmp_eq_u32 vcc, -1, v212\n" "s_cbranch_vccz ch_o4_h3\n" \
  "buffer_load_dwordx4 v[172:175], v217, s[40:43], s53 offen offset:3072 sc1\n" "ch_o4_h3:\n" "v_mov_b64 v[176:177], 0\n" "v_mov_b64 v[178:179], 0\n" "v_mov_b64 v[180:181], 0\n" \
  "v_mov_b64 v[182:183], 0\n" "v_mov_b64 v[184:185], 0\n" "v_mov_b64 v[186:187], 0\n" "v_mov_b64 v[188:189], 0\n" "v_mov_b64 v[190:191], 0\n" "s_waitcnt vmcnt(0)\n" \
  "v_max3_u32 v212, v160, v161, v162\n" "v_max3_u32 v212, v212, v163, v164\n" "v_max3_u32 v212, v212, v165, v166\n" "v_max3_u32 v212, v212, v167, v168\n" "v_max3_u32 v212, v212, v169, v170\n" \
  "v_max3_u32 v212, v212, v171, v172\n" "v_max3_u32 v212, v212, v173, v174\n" "v_max_u32 v212, v212, v175\n" "v_cmp_eq_u32 vcc, -1, v212\n" "s_cbranch_vccz ch_o4_mul\n" \
  "s_sub_u32 s59, s59, 1\n" "s_cselect_b32 s59, 0, s59\n" "s_cbranch_scc0 ch_o4_wait\n" "v_mov_b32 v160, 0\n" "v_mov_b32 v164, 0\n" "v_mov_b32 v168, 0\n" "v_mov_b32 v172, 0\n" \
  "s_mov_b32 s59, 0\n" "s_nop 0\n" "s_branch ch_o4_mul\n" "ch_o4_fin:\n" "s_waitcnt vmcnt(0)\n"
typedef __attribute__((ext_vector_type(16))) unsigned u32x16;
typedef __attribute__((ext_vector_type(32))) unsigned u32x32;
#define C_R0 114688
#define C_IMG (C_R0 + 37120)
__device__ __forceinline__ void count_votes(const u32 fv, int& xk, int& xb) {
  xk = 0; xb = X_W2;
#pragma unroll
  for (int i = 0; i < 3; i++) {
    const u32 m = (u32)__ballot((fv >> i) & 1u);
    const int a = i == 1 ? __popc(m & 0xF0000u) > 2 : __popc(m & 255u) > 4, b = i == 1 ? __popc(m & 0xF00000u) > 2 : __popc(m & 0xFF00u) > 4;
    xk |= a << (12 + i); xb |= (a ^ b) << (12 + i);
  }
}
#define TG_CLAIM2(n) do { SB0(); VMW(n); asm volatile("" : "+v"(st.ep)); const int t_ = TIDX; \
    st.l = ((u32)__builtin_amdgcn_readfirstlane((int)st.ep[0]) + 16u) & ~15u; st.h = (u32)__builtin_amdgcn_readfirstlane((int)st.ep[1]); \
    st.old = (u32x2){st.l, st.h}; \
    __builtin_amdgcn_raw_buffer_store_b64(st.old, RSS, (t_ & 63) ? PJ_OOB : 0, st.duty >= 0 ? TG_CF(st.duty) * 8 : PJ_OOB, 16); \
    asm volatile("s_nop 4\n\tbuffer_atomic_swap_x2 %0, %1, %2, 0 offen sc0" : "+{v[252:253]}"(st.old) : "v"(t_ == 448 ? TG_CL(st.slot) * 8 : PJ_OOB), "s"(RSS) : "memory"); SB0(); } while (0)
__device__ __forceinline__ u32x4 rs4(const void* q, u32 bytes) {
  return (u32x4){(u32)(uintptr_t)q, (u32)((uintptr_t)q >> 32) & 0xFFFFu, bytes, 0x00020000u};
}
__device__ __forceinline__ u32x16 sargs(u32x4 a, u32x4 b, u32x4 c, int d0, int d1, int d2, int d3) {
  u32x16 r;
#pragma unroll
  for (int i = 0; i < 4; i++) { r[i] = (u32)__builtin_amdgcn_readfirstlane((int)a[i]); r[4 + i] = (u32)__builtin_amdgcn_readfirstlane((int)b[i]); r[8 + i] = (u32)__builtin_amdgcn_readfirstlane((int)c[i]); }
  r[12] = (u32)__builtin_amdgcn_readfirstlane(d0); r[13] = (u32)__builtin_amdgcn_readfirstlane(d1); r[14] = (u32)__builtin_amdgcn_readfirstlane(d2); r[15] = (u32)__builtin_amdgcn_readfirstlane(d3);
  return r;
}
#define CV8(a) "v" #a "0", "v" #a "1", "v" #a "2", "v" #a "3", "v" #a "4", "v" #a "5", "v" #a "6", "v" #a "7"
#define CV10(a) CV8(a), "v" #a "8", "v" #a "9"
#define CS10(a) "s" #a "0", "s" #a "1", "s" #a "2", "s" #a "3", "s" #a "4", "s" #a "5", "s" #a "6", "s" #a "7", "s" #a "8", "s" #a "9"
#define CLS "s56", "s57", "s58", "s59", CS10(6), CS10(7), "vcc", "scc", "memory"
#define WIN() ((char*)(((uintptr_t)p.ws + O_XW + 0x1FFFFFu) & ~(uintptr_t)0x1FFFFFu))
#define AWAIT_ALL(fk) do { const int io_ = lane < 24 ? TG_VOTE(lane) : lane < 32 ? (fk) : TG_INIT(lane - 32); int it_ = 0; fv = TG_LD(io_); fp = TG_LD(lane); \
    __syncthreads(); TG_CHECK(151552); \
    while (!__all(TG_OK(fv)) && ++it_ < SPIN_CAP) { __builtin_amdgcn_s_sleep(1); fv = TG_LD(io_); } \
    count_votes(fv[0], xk, xb); TG_KPW(fp, 2); } while (0)
__device__ __forceinline__ void head_body(const Params& p, Start& st, char* smem, const int h, const int wv) {
  TG_BEGIN();
  const int wid = wv, w = wv & 3;
  u32x4 va;
  const __amdgpu_buffer_rsrc_t rq = RSRC(p.BWq, 576 * 8192), rk = RSRC(p.BWk, 576 * 8192), rv = RSRC(p.BWv, 576 * 8192);
  {
    const int tid = TIDX, lane = tid & 63, l15 = lane & 15, lq = lane >> 4;
    const int mo = ((l15 >> 2) * 2 + (l15 & 1)) * 1152 + lq * 32 + (l15 & 2) * 8;
    DMA16(RSRC(p.Bm, 8 * 1152), C_IMG + wid * 1024, mo, wid * 128, 0);
    DMA16(RSRC(p.Bm, 8 * 1152), C_IMG + 8192, mo, 1024, 0);
    va[0] = (wid * 4 + lq) * 8192 + h * 256 + (((l15 >> 1) ^ ((lq & 1) | (w << 1))) << 5) + (lane & 1) * 16;
    auto req = [&](const int r) {
#pragma unroll
      for (int i = 0; i < 6; i++) DMA16(i < 2 ? rq : i < 4 ? rk : rv, r * 49152 + i * 8192 + wid * 1024, va[0], (r * 64 + (i & 1) * 32) * 8192, 0);
      SB0();
    };
    req(0); req(1);
    const int r2 = l15 >> 2;
    va[1] = ((lq >> 1) * 16 + (lq & 1) * 4 + (r2 >> 1) * 8 + (r2 & 1)) * 256 + (((2 * w) ^ ((r2 & 1) | ((lq & 1) << 1) | ((r2 >> 1) << 2))) << 5) + (lane & 3) * 8;
    va[2] = w * 2048 + lane * 16; va[3] = C_R0 + tid * 16;
    TG_CLAIM2(14);
    req(2);
  }
  u32x32 w0, w1, w2, w3, w4; u32x16 w5; u32x8 w6;
  int t_;
#define W_OUT "={v[0:31]}"(w0), "={v[32:63]}"(w1), "={v[64:95]}"(w2), "={v[96:127]}"(w3), "={v[128:159]}"(w4), "={v[160:175]}"(w5)
#define W_IN "{v[0:31]}"(w0), "{v[32:63]}"(w1), "{v[64:95]}"(w2), "{v[96:127]}"(w3), "{v[128:159]}"(w4), "{v[160:175]}"(w5)
#define S_OPS "+{v[248:251]}"(va), "+{v[252:253]}"(st.old), [t] "=&s"(t_) : [lw] "s"(wid * 1024), [r0] "s"(rq), [r1] "s"(rk), [r2] "s"(rv)
  int xk, xb, kp;
  u32x2 fv, fp;
  PRDECL(h);
  asm volatile("s_cmp_lt_u32 %[lw], 0x1000\n s_cbranch_scc0 ch_s1\n" CH_SA "s_branch ch_s2\n ch_s1:\n" CH_SB "ch_s2:\n"
               : W_OUT, "={v[176:183]}"(w6), S_OPS : "v184", "v185", "v186", "v187", "v188", "v189", CV10(19), "v244", "v245", "scc", "memory");
  {
    const int tid = TIDX, lane = tid & 63, l15 = lane & 15, lq = lane >> 4;
    TG_NOTE(151552);
#pragma unroll
    for (int i = 0; i < 2; i++) *(u32x4*)(smem + C_R0 + 2048 + i * 8192 + tid * 16) = (u32x4){~0u, ~0u, ~0u, ~0u};
    *(u32*)(smem + C_R0 + 46336 + (tid & 255) * 4) = ~0u;
    GMAX(85); AWAIT_ALL(TG_FK(h * 4 + (lane & 3))); GMAX(69);
    const int wa = C_R0 + (w >> 1) * 1024 + ((l15 >> 2) & 1) * 512 + (w & 1) * 256 + (l15 & 3) * 32;
    const bool lo = wid < 4;
    u32x8 vb = {(l15 & 2) ? 0xEEEEu : 0x4444u, (u32)(C_R0 + lane * 16),
                (u32)(lo ? C_R0 + lane * 8 : wa + 2048 + lq * 8 + (l15 >> 3) * 4),
                (u32)(lo ? wa + (l15 >> 3) * 16 + lq * 4 : C_R0 + tid * 16),
                (u32)(lo ? C_R0 + tid * 4 : lane * 16 + w * 2048),
                (u32)(lo ? C_R0 + tid * 16 : w == 3 ? lane * 16 : lq * RP + h * 256 + ((l15 ^ (w == 0 ? lq >> 1 : w == 1 ? lq & 2 : lq)) << 4)),
                (u32)(lo ? C_R0 + (lane < 32 ? 6144 + (lane & 16) * 64 + (lane & 8) * 36 : 3072 + 16 + (lane & 24) * 36) : (lq * 4 + (l15 & 2) + (l15 >> 3)) * 128 + (((l15 >> 2) & 1) * 2 + (l15 & 1)) * 16),
                (u32)(lo ? (h >> 2) * 32768 + (OGEO((l15 >> 2) & 1) ^ xk) + (h & 3) * 1024 + w * 256 + (l15 & 3) * 64 + (l15 >> 3) * 32 + lq * 8
                         : w < 3 ? (lane >> 1) * RP + h * 256 + (lane & 1) * 128 : lane < 4 ? ((41 * 32768 + 4096) ^ XGEO(lane >> 1) ^ xk) + h * 16 + (lane & 1) * 1024 : PJ_OOB)};
    static_assert(S_OB == S_QB, "one descriptor");
    u32x16 sa = sargs(rs4(WIN(), X_W2), rs4(BIGP(kp, S_QB), 3 * ACB + KVO), rs4(p.ws, O_XW), wid,
                      lo ? ((w >> 1) * 16 + (w & 1)) * RP + h * 256 : w == 3 ? (32 * 32768 + 4096) ^ XGEO(SEP ? 0 : h >> 4) ^ xk : w == 2 ? 0 : (w + 1) * ACB + KVO,
                      lo ? ((w & 1) * 16 + (h >> 1)) * RP + ((h & 1) * 4 + (w >> 1) * 2) * 1024 : w == 3 ? SY_BLK(0, h >> 1, 0) * 4 : w == 0 ? 65536 : w == 1 ? 98304 : 81920,
                      lo ? SY_SEG(62, h * 4 + w) * 4 : w == 0 ? (int)0x80402020u : w == 1 ? (int)0x80402000u : 0x00800040);
    asm volatile(PRO "s_cmp_lt_u32 s52, 4\n s_cbranch_scc0 ch_h1\n" CH_HA "s_branch ch_hb_fin\n ch_h1:\n" CH_HB PRE
                 : "+{s[40:55]}"(sa), "+{v[184:191]}"(vb), "+{v[176:183]}"(w6) PROUT : W_IN
                 : "v192", "v193", "v194", "v195", "v196", "v197", "v198", "v199", CV10(20), CV10(21), CV10(22), CV10(23), CV10(24), "v250", "v251", "v252", "v253", CLS);
  }
  PRFIN(h);
  if (TIDX == 64 && h == 0) st4_sc1(WSP(u32, 0) + TG_EP * 2, ld4_sc1(WSP(u32, 0) + TG_EP * 2) + 16u);
}
__device__ __forceinline__ void outp_body(const Params& p, Start& st, char* smem, const int tg, const int j, const int wv) {
  TG_BEGIN();
  const int wid = wv, hw = wv ^ (tg * OHX) ^ ((j & 1) * OHR);
  u32x4 va; int vt;
  const __amdgpu_buffer_rsrc_t ro = RSRC(p.BWo, 4096 * 1152);
  {
    const int tid = TIDX, lane = tid & 63, l15 = lane & 15, lq = lane >> 4, r2 = l15 >> 2;
    va[0] = (hw * 512 + (lane >> 2)) * 1152 + j * 64 + (((lane & 3) ^ lq) << 4);
#pragma unroll
    for (int i = 0; i < 16; i++) DMA16(ro, wid * 16384 + i * 1024, va[0], i * 16 * 1152, 0);
    SB0();
    va[1] = j < 8 ? (hw * 512 + lane) * 1152 + 1024 + j * 16 : PJ_OOB;
    const int dim = (lq >> 1) * 32 + (lane & 2) * 8 + (r2 >> 1) * 8 + (lq & 1) * 4 + (r2 & 1);
    va[2] = wid * 16384 + dim * 64 + (((lq & 1) | (r2 & 2)) << 4) + (lane & 1) * 8;
    va[3] = wid * 16384 + dim * 16 + (lane & 1) * 8;
    vt = j < 8 ? PJ_OOB : (wid * 512 + (j & 7) * 64 + lane) * 1152 + 1024;
    TG_CLAIM2(16);
  }
  u32x32 w0, w1, w2, w3, w4;
  int t_, xk, xb, kp;
  u32x2 fv, fp;
  PRDECL(32 + tg * 16 + j);
  asm volatile(CH_SO : "={v[0:31]}"(w0), "={v[32:63]}"(w1), "={v[64:95]}"(w2), "={v[96:127]}"(w3), "={v[128:159]}"(w4), "+{v[216:219]}"(va), "+{v[252:253]}"(st.old), [t] "=&s"(t_)
               : [lw] "s"(wid * 16384), [r] "s"(ro), "{v220}"(vt) : "v200", "v221", "scc", "memory");
  const int tid = TIDX, lane = tid & 63, l = lane & 31;
  TG_NOTE(151552);
  GMAX(84);
  AWAIT_ALL(TG_VOTE(0));
  const int cp = SEP && lane >= 32 ? PJ_OOB : (32 * 32768 + 4096) ^ XGEO(lane >> 5) ^ xk;
  u32x8 vb = {(lane & 1) ? 0xEEEEu : 0x4444u, (u32)(lane * 16), (u32)((wid >> 2) * 1024 + ((lane & 1) * 32 + (lane >> 5) * 16 + (lane & 8) + ((lane >> 2) & 4) + ((lane >> 1) & 3)) * 16 + ((wid + ((lane >> 2) & 2) + (lane & 1)) & 3) * 4),
              (u32)(wid * 2048 + (wid < 2 ? lane : (lane & 0x27) + (lane & 8) * 2) * 16),
              (u32)(wid == 7 ? (lane < 4 ? ((41 * 32768 + 4096) ^ XGEO(lane >> 1) ^ xk) + (32 + tg * 16 + j) * 16 + (lane & 1) * 1024 : PJ_OOB) : wid > 2 ? (wid == 5 ? 0 : (wid - 2) * ACB + KVO) + (lane >> 1) * RP + (tg * 16 + j) * 256 + (lane & 1) * 128 : wid < 2 ? cp + (j >> 3) * 32768 + (j & 7) * 512 + wid * 256 + tg * 128 + l * 4 : j < 8 ? cp + 65536 + (j * 2 + tg) * 128 + l * 4 : PJ_OOB), 0u, 0u, 0u};
  u32x16 sa = sargs(rs4(WIN(), X_W2), rs4(BIGP(kp, S_QB), 3 * ACB + KVO), rs4(p.ws, O_XW), wid, hw * 32768 + (OGEO(tg) ^ xk), (41 * 32768 + 4096) ^ XGEO(SEP ? 1 : tg) ^ xk, tg * 16 + j);
  asm volatile(PRO "s_bitcmp1_b32 s55, 3\n s_cbranch_scc1 ch_o1\n" CH_O5 "s_branch ch_o4_fin\n ch_o1:\n" CH_O4 PRE : "+{s[40:55]}"(sa), "+{v[216:223]}"(vb) PROUT : "{v[0:31]}"(w0), "{v[32:63]}"(w1), "{v[64:95]}"(w2), "{v[96:127]}"(w3), "{v[128:159]}"(w4)
               : CV10(16), CV10(17), CV10(18), CV10(19), CV10(20), "v210", "v211", "v212", "v213", "v214", "v215", "v224", "v225", CLS);
  PRFIN(32 + tg * 16 + j);
}

__device__ __forceinline__ void wait_ge(const u32* w, u32 val, const int wv) {
  if (TIDX == 0) {
    int it = 0;
    while (ld4_sc1(w) < val && ++it < SPIN_CAP) __builtin_amdgcn_s_sleep(WPS);
  }
}
__device__ __forceinline__ void acquire_all(const int wv) {
  if (TIDX == 0) {
    __builtin_amdgcn_fence(__ATOMIC_ACQUIRE, "agent");
    VMW0();
  }
  __syncthreads();
}

#define KROT 0
#define OPD 4
#define PPD 2
#define WWM 2
#define WWN 2
#define NWM 4
#define NWN 1
#define WPD 2
#define WAD false
#define NAD false
#define KPX 0
#define NAW 0
#define NBW 10
#define NAM 4
#define NBM 2
#define NKV 8
#define CFG 1
#define TMASK 0x0D
#define SBM 256
#define SPD 4
#define SMASK 0x70
#define TU 10
#define PORD 0x354
#define SDL 0
#define PWV 1
#define NPF 2
#define NPS 4
#define BST 0
#define NPR 3
#define QUIET 0
#define TGAP 6
#define TOUCH 1
__host__ __device__ __forceinline__ int decode_task(int q, int u, int& a, int& b) {
  if (q < 5) {
    const int nw = q < 4 ? NAW : NBW, nm = q < 4 ? NAM : NBM, n = q < 4 ? 4 : SM0 / 4;
    if (u < nw * 3) { a = u / 3; b = u % 3; }
    else if ((u -= nw * 3) < nm * 6) { a = nw + u / 6; b = 3 + u % 6; }
    else { u -= nm * 6; a = nw + nm + u / 9; b = 9 + u % 9; }
    if (a >= n) return -1;
    if (q < 4) a += q * 4;
    return q < 4 ? 2 : 3;
  }
  if (u >= 108) return -1;
  a = (u / 36 + SM0) % 3; b = u % 36; return 4;
}

#define TP(code)
#define TPC(ty, a, b) TP(((ty) << 16) | ((a) << 8) | (b))
#if defined(__HIP_DEVICE_COMPILE__) && !defined(KEEP_PK)
#define NOPK __attribute__((target("no-packed-fp32-ops")))
#else
#define NOPK
#endif
#define P0V 2
__device__ __forceinline__ Params prm() {
  const __attribute__((address_space(4))) u64* a = (const __attribute__((address_space(4))) u64*)__builtin_amdgcn_kernarg_segment_ptr();
  asm volatile("" : "+s"(a));
#define GP(T, x) ((T*)(__attribute__((address_space(1))) T*)(x))
  Params p;
  p.Ax = GP(const u16, a[0]); p.Ac = GP(const u16, a[1]); p.Awq = GP(const u16, a[2]); p.Awk = GP(const u16, a[3]); p.Awv = GP(const u16, a[4]); p.Awo = GP(const u16, a[5]);
  p.Bx = GP(const u16, a[6]); p.Bm = GP(const u16, a[7]); p.BWq = GP(const u16, a[8]); p.BWk = GP(const u16, a[9]); p.BWv = GP(const u16, a[10]); p.BWo = GP(const u16, a[11]);
  p.out = GP(u16, a[12]); p.ws = GP(char, a[13]); p.big = GP(char, a[14]);
  return p;
}
#define p prm()
__device__ __forceinline__ void mega_body(char* smem) {
  int* const s_task = (int*)(smem + 151552);
  const int wv = __builtin_amdgcn_readfirstlane(threadIdx.x >> 6);
  if (blockIdx.x == 0 && TIDX == 0) asm volatile("s_nop 0" ::: "memory");
  STS(0);
#define tid TIDX
  const int xcc = (int)(__builtin_amdgcn_s_getreg(20 | (3 << 11)) & 7u);
  Start st;
  const int bid = blockIdx.x;
  const int slot = bid >= 256 ? bid & 255 : xcc * 32 + (bid >> 3);
  const int x = (slot >> 5) & 3, j = slot & 31;
  st.slot = slot; st.duty = bid >= 200 && bid < 256 && (bid & 7) == 3 ? bid - 203 + wv : -1;
  const int wk = slot >> 7;
  const int role = x == 3 ? 64 + j : x == 2 ? 96 + j : SEP ? x * 32 + j : (j & 16) * 2 + x * 16 + (j & 15);
  int kp = 0, pre = 0, q = wk ? x : 4;
  u32* qh = WSP(u32, 0) + SY_QH(q);
  if (!wk && role < 64) {
    if (role < 32) head_body(p, st, smem, role, wv); else outp_body(p, st, smem, (role >> 4) & 1, role & 15, wv);
    GMAX(65); GMAX(68);
    return;
  } else {
    TPC(10, 0, 0);
    auto touch = [&](const int mask, const int b, const int rot) {
#pragma unroll
      for (int m = 0; m < 7; m++) {
        if (!((mask >> m) & 1)) continue;
        const int bytes = m == 2 ? 2048 * 1024 : (m == 3 || m == 4 ? 512 * 8192 : (m == 0 || m == 5 ? 2048 * 1152 : 576 * 8192));
        __builtin_amdgcn_raw_ptr_buffer_load_lds(RSRC(m == 0 ? p.Bx : m == 1 ? p.BWq : m == 2 ? p.Ac : m == 3 ? p.Awk : m == 4 ? p.Awv : m == 5 ? p.Ax : p.Awq, bytes),
                                                 (LDSP void*)(smem + C_BUFB + wv * 256), 4, (tid & 63) * 128, (rot * ((wv + m * 5) & 7) * 128 + b) * 8192, 0, 0);
      }
    };
    if (!wk && role >= 96) {
      const Params p0 = p;
#undef p
#define p p0
      TG_BEGIN();
      const int v = role & 31;
      const __amdgpu_buffer_rsrc_t rsX = RSRC((((uintptr_t)p.ws + O_XW + 0x1FFFFFu) & ~(uintptr_t)0x1FFFFFu), X_END);
      const int ty = (v >> 1) & 3, vi = (v >> 3) * 2 + (v & 1);
      {
        const __amdgpu_buffer_rsrc_t r = ty ? rsX : RSRC(BIGP(0, vi * 0x1800000), 32768);
        const int o = (ty ? (v & 1) * X_W2 + 63 * 32768 : 0) + (v >> 1) * 256 + (wv & 1) * 128;
        u32* const bs = (u32*)smem;
        if (tid < 8) bs[tid] = ~0u;
        auto probe = [&](const int k) {
          __syncthreads();
          if (wv < PWV) {
            const u32 t0 = (u32)__builtin_readcyclecounter();
            __builtin_amdgcn_raw_buffer_store_b32(0u, r, k * 4096, o, 16);
            VMW0();
            const u32 t = (u32)__builtin_readcyclecounter() - t0;
            if ((tid & 63) == 0) __hip_atomic_fetch_min(bs + k, t, __ATOMIC_RELAXED, __HIP_MEMORY_SCOPE_WORKGROUP);
          }
        };
        if (ty) probe(0);
        TG_CLAIM(0);
        if (ty) probe(2);
        VMW0(); TG_NOTE(151552);
        __syncthreads();
        TG_CHECK(151552);
#pragma unroll 1
        for (int n = ty ? 2 : 0; n < (ty < 2 ? 2 * NPR : NPS); n++) probe((n & 1) * 2);
        __syncthreads();
        const int sd = __builtin_amdgcn_readfirstlane(bs[2] < bs[0] ? 2 : 0);
        int kb = sd;
        if (ty >= 2) {
#pragma unroll 1
          for (int n = 1; n < 4 * NPF; n++) probe(sd | (n & 1) | ((n & 2) << 1));
          __syncthreads();
          const u32 l0 = bs[sd], l1 = bs[sd | 1], l4 = bs[sd | 4], l5 = bs[sd | 5];
          kb = sd | (l1 + l5 < l0 + l4 ? 1 : 0) | (l4 + l5 < l0 + l1 ? 4 : 0);
        }
        kb ^= 5 ^ (XGEO(xcc) >> 12) ^ ((xcc >> 2) << 1);
        if (tid == 0) TG_ST(ty == 0 ? TG_KP(vi) : ty == 1 ? TG_VOTE(16 + (v & 1) * 4 + (v >> 3)) : TG_VOTE((v & 1) * 8 + (v >> 3) * 2 + (ty & 1)),
                            ty ? (u32)kb : (u32)(((kb >> 1) ^ KPX ^ 1) & 1));
      }
      TPC(14, 0, 0);
      {
        u32x2 fv; int it = 0;
        while (fv = TG_LD(TG_VOTE(16 + (tid & 7))), !__all(TG_OK(fv)) && ++it < SPIN_CAP) __builtin_amdgcn_s_sleep(4);
        TPC(15, 0, 0);
        int xk, xb;
        count_votes(fv[0], xk, xb);
        const int cv = (v & 1) * X_W2 + ((xk ^ ((v & 1) ? xb : 0)) & 8192) + tid * 16;
#pragma unroll
        for (int i = 0; i < 8; i++) {
          const int n = (v >> 1) * 4 + (i >> 1);
          u32 z0 = ~0u; asm volatile("" : "+v"(z0));
          __builtin_amdgcn_raw_buffer_store_b128((u32x4){z0, z0, z0, z0}, rsX, n < 42 ? cv + n * 32768 + (i & 1) * 16384 : PJ_OOB, 0, 16);
        }
        VMW0();
        __syncthreads();
        if (tid == 0) TG_ST(TG_INIT(v), 0u);
        u32x2 kv = TG_LD(tid & 63);
        TG_KPW(kv, 4);
      }
#undef p
#define p prm()
      TPC(11, 0, 0);
#pragma unroll
      for (int i = 0; i < 3; i++) touch(SMASK, (i * 8 + wv) * 32 + v, 0);
      __syncthreads();
    }
    if (wk) {
      first_kv(p, st, j >> 4, x * 1024 + (j & 15) * 64, slot & 127, kp, smem, wv);
      touch(TMASK, slot & 127, 1);
    }
    kp = __builtin_amdgcn_readfirstlane(kp);
    {
      TPC(12, wk, 0);
      if (!wk && x == 3) {
        kp = -1;
        TG_BEGIN();
        if (TFL & 6) {
#pragma unroll
          for (int i = 0; i < 3; i++) {
            const int u = i * 8 + wv, row = (u < 16 ? (u >> 1) * 32 + (u & 1) * 256 : (u + 0) * 32) + (role & 31);
            __builtin_amdgcn_raw_ptr_buffer_load_lds(RSRC((TFL & 2) ? p.BWq : p.BWo, 576 * 8192), (LDSP void*)(smem + C_BUFB + wv * 256), 4, (tid & 63) * 128, row * 8192, 0, 0);
            __builtin_amdgcn_s_barrier(); SB0();
          }
        }
        TG_CLAIM(3);
        VMW0(); TG_NOTE(151552);
        __syncthreads();
        TG_CHECK(151552);
        for (int i = 0; i < SDL; i++) __builtin_amdgcn_s_sleep(32);
      }
      const int hd = wk ? x * 8 + (j >> 3) * 2 + ((j & 4) == 0) : -1;
      bf8 qr[8]; u32 vfl;
      proj_slot(p, st, wk ? 3 : x == 2 ? 4 : 7, wk ? PORD : x == 2 ? 0x2222 : ((role >> 4) & 1) * 0x11111111, wk ? (j & 7) * 2 : x == 2 ? 2 + (j >> 4) * 2 : 2, wk ? 0 : x == 2 ? 4 : 2,
                wk ? x * 4 + (j >> 3) : role & 15, kp,
                hd, qr, vfl, smem, wv);
      if (wk) {
        TPC(0, j & 7, hd);
        fa::attn_pair(p, BIGP(kp, S_KA + hd * 256), BIGP(kp, S_VA + hd * 256), hd, j & 7, kp, qr, vfl, smem, wv, qh, pre);
        if (tid < 2) __hip_atomic_fetch_add(WSP(u32, 0) + SY_CC((j & 7) ^ (tid * 7)), 1u, __ATOMIC_RELAXED, __HIP_MEMORY_SCOPE_AGENT);
        GMAX(80);
      } else {
        __syncthreads();
        GMAX(82);
        if (x == 2) {
          static_assert(XS0 == 8, "one segment per workgroup");
#pragma unroll 1
          for (int u = j; u < 128; u += 32) {
            TPC(13, 0, 0);
            u32x4 sv[9];
            const __amdgpu_buffer_rsrc_t r = RSRC((u & 64) ? p.Awo : p.BWo, 4096 * 1152);
#pragma unroll
            for (int i = 0; i < 9; i++) { sv[i] = __builtin_amdgcn_raw_buffer_load_b128(r, tid * 16, (u & 63) * 64 * 1152 + i * 8192, 2); if (WOP) { SB0(); __builtin_amdgcn_s_sleep(WOP); SB0(); } }
            wof_task(p, sv, (u >> 6) ^ 1, u & 63, kp, smem, wv);
          }
          VMW0();
          __syncthreads();
          if (tid == 0) __hip_atomic_fetch_add(WSP(u32, 0) + SY_CW, 4u, __ATOMIC_RELAXED, __HIP_MEMORY_SCOPE_AGENT);
          TPC(1, j >> 2, j & 3);
          xattn_task(p, j >> 2, j & 3, kp, smem, wv);
          if (tid == 0) __hip_atomic_fetch_add(WSP(u32, 0) + SY_CS(j >> 2), 1u, __ATOMIC_RELAXED, __HIP_MEMORY_SCOPE_AGENT);
          GMAX(83);
        }
      }
    }
    {
      if (!wk) {
        for (int i = 0; i < BST * (role & 31); i++) __builtin_amdgcn_s_sleep(4);
        CLAIM();
      }
    }
  }
  while (true) {
    if (tid == 0) *s_task = pre;
    __syncthreads();
    const int t = __builtin_amdgcn_readfirstlane(*s_task);
    __syncthreads();
    int a, b;
    const int ty = decode_task(q, t, a, b);
    if (ty < 0) {
      if (q == 5) break;
      if (!wk && QUIET) break;
      q = q < 4 ? 4 : 5; qh = WSP(u32, 0) + SY_QH(q);
      CLAIM();
      continue;
    }
    TPC(ty, a, b);
    if (ty < 4) {
      const int nt0 = b < 3 ? b * 12 : b < 9 ? (b - 3) * 6 : (b - 9) * 4;
      wait_ge(WSP(u32, 0) + SY_CW, 128u, wv);
      if (ty == 2) wait_ge(WSP(u32, 0) + SY_CC(a >> 1), 32u, wv);
      else for (int i = 0; i < 4; i++) wait_ge(WSP(u32, 0) + SY_CS(a * 4 + i), 4u, wv);
      __syncthreads();
      TPC(ty + 20, a, b);
      const char* A = BIGP(kp, ty == 2 ? S_CA + ROWO(a * 128) : S_OB + OROW(a * 128));
      u16* C = p.out + (size_t)a * 128 * 1152 + (ty - 2) * 576 + nt0 * 16;
      if (b < 3) out_tile<4, 6, 2, 4>(A, BIGP(kp, S_WF(ty - 2)), nt0, C, smem, wv, qh, pre);
      else if (b < 9) out_tile<4, 3, 2, 5>(A, BIGP(kp, S_WF(ty - 2)), nt0, C, smem, wv, qh, pre);
      else out_tile<4, 2, 2, 6>(A, BIGP(kp, S_WF(ty - 2)), nt0, C, smem, wv, qh, pre);
      GMAX(64 + ty);
    } else {
      GMAX(81);
      small_out_task(p, a, b & 1, b >> 1, kp, smem, wv, qh, pre);
    }
  }
  TPC(99, 0, 0);
}

#undef tid
#undef p
__global__ NOPK void __launch_bounds__(512, 2) mega(Params p_) {
  __shared__ __attribute__((aligned(16))) char smem[LDS_ALL];
#define CODE_KB 80
#define CPM 8
  int t;
  asm volatile("s_load_dword s8, %1, 0\n\ts_load_dword s9, %1, 0x40\n\t"
               "s_getpc_b64 s[4:5]\n\ts_and_b32 s4, s4, 0xffffff00\n\ts_mov_b32 s6, %4\n\ts_mov_b32 s7, 0x20000\n\ts_mov_b32 m0, %5\n\ts_and_b32 s3, %2, %6\n\ts_lshl_b32 s3, s3, 13\n\t"
               "v_lshlrev_b32 %0, 7, %3\n\tbuffer_load_dword %0, s[4:7], s3 offen lds\n\ts_waitcnt lgkmcnt(0)"
               : "=&v"(t) : "s"(__builtin_amdgcn_kernarg_segment_ptr()), "s"(blockIdx.x), "v"(threadIdx.x), "n"(CODE_KB * 1024), "n"(150528), "n"(CPM)
               : "s3", "s4", "s5", "s6", "s7", "s8", "s9", "scc", "memory");
  mega_body(smem);
}
extern "C" void kernel_launch(void* const* d_in, const int* in_sizes, int n_in, void* d_out, int out_size, void* d_ws,
                              size_t ws_size, hipStream_t stream) {
  Params p;
  p.Ax = (const u16*)d_in[0]; p.Ac = (const u16*)d_in[1]; p.Awq = (const u16*)d_in[2]; p.Awk = (const u16*)d_in[3];
  p.Awv = (const u16*)d_in[4]; p.Awo = (const u16*)d_in[5]; p.Bx = (const u16*)d_in[6]; p.Bm = (const u16*)d_in[7];
  p.BWq = (const u16*)d_in[8]; p.BWk = (const u16*)d_in[9]; p.BWv = (const u16*)d_in[10]; p.BWo = (const u16*)d_in[11];
  p.out = (u16*)d_out;
  p.ws = (char*)d_ws;
  p.big = (char*)d_ws + O_BIG + 0x200000;
  mega<<<256, 512, 0, stream>>>(p);
}
```

```cpp
#include <hip/hip_runtime.h>
#include <hip/hip_bf16.h>
#include <stdint.h>
#include <type_traits>

typedef unsigned short u16;
typedef unsigned int u32;
typedef unsigned long long u64;
typedef __attribute__((ext_vector_type(8))) short bf8;
typedef __attribute__((ext_vector_type(4))) short bf4;
typedef __attribute__((ext_vector_type(4))) float f4;
typedef __attribute__((ext_vector_type(2))) float f2v;
typedef __attribute__((ext_vector_type(2))) __bf16 bf2v;
typedef __attribute__((ext_vector_type(4))) unsigned u32x4;
typedef __attribute__((ext_vector_type(2))) unsigned u32x2;
typedef float f32x16 __attribute__((ext_vector_type(16)));
typedef __attribute__((ext_vector_type(8))) unsigned u32x8;
typedef __attribute__((ext_vector_type(8))) __bf16 bh8;
typedef __attribute__((ext_vector_type(16))) __bf16 bh16;
#define LDSP __attribute__((address_space(3)))

__device__ __forceinline__ u32 pk2(float a, float b) {
  f2v x = {a, b};
  bf2v y = __builtin_convertvector(x, bf2v);
  return __builtin_bit_cast(u32, y);
}
__device__ __forceinline__ u32 umin(u32 a, u32 b) { return a < b ? a : b; }
__device__ __forceinline__ u32 umax(u32 a, u32 b) { return a > b ? a : b; }
__device__ __forceinline__ float rbf(float f) { return __uint_as_float(pk2(f, f) << 16); }
#define MFMA(a, b, c) __builtin_amdgcn_mfma_f32_16x16x32_bf16(a, b, c, 0, 0, 0)
#define MFMA16(a, b, c) __builtin_amdgcn_mfma_f32_16x16x16bf16_1k(a, b, c, 0, 0, 0)
#define SMF(a, b, c, idx) __builtin_amdgcn_smfmac_f32_16x16x64_bf16(__builtin_bit_cast(bh8, a), __builtin_bit_cast(bh16, b), c, idx, 0, 0)
__device__ __forceinline__ u32x8 ilv(bf4 a, bf4 b, bf4 c, bf4 d) {
  const u32x2 x = __builtin_bit_cast(u32x2, a), y = __builtin_bit_cast(u32x2, b), z = __builtin_bit_cast(u32x2, c), w = __builtin_bit_cast(u32x2, d);
  return (u32x8){x[0], x[1], y[0], y[1], z[0], z[1], w[0], w[1]};
}
__device__ __forceinline__ u32x8 cat16(bf8 a, bf8 b) {
  const u32x4 x = __builtin_bit_cast(u32x4, a), y = __builtin_bit_cast(u32x4, b);
  return (u32x8){x[0], x[1], x[2], x[3], y[0], y[1], y[2], y[3]};
}
__device__ __forceinline__ float swadd(float a, float b) {
  auto x = __builtin_amdgcn_permlane32_swap(__float_as_uint(a), __float_as_uint(b), false, false);
  return __uint_as_float(x[0]) + __uint_as_float(x[1]);
}
typedef __attribute__((ext_vector_type(2))) _Float16 h2v;
__device__ __forceinline__ u32x4 enc8(const float* v, u32 tag) {
  float m = fmaxf(fmaxf(fabsf(v[0]), fabsf(v[1])), fabsf(v[2]));
  m = fmaxf(fmaxf(m, fabsf(v[3])), fabsf(v[4]));
  m = fmaxf(fmaxf(m, fabsf(v[5])), fabsf(v[6]));
  m = fmaxf(m, fabsf(v[7]));
  m = m >= 0x1p-23f ? fmaxf(m, 0x1p-15f) : m;
  const u32 code = __float_as_uint(m) >> 27;
  const float inv = __uint_as_float((253u << 23) - (code << 27));
  u32x4 w;
#pragma unroll
  for (int i = 0; i < 4; i++) {
    f2v x = {v[2 * i], v[2 * i + 1]};
    x *= inv;
    w[i] = __builtin_bit_cast(u32, __builtin_convertvector(x, h2v));
  }
  const u32 t = code | (code << 15);
  w[0] = (w[0] & ~1u) | (tag & 1u);
  w[2] = (w[2] & ~1u) | (tag & 1u);
  w[1] = (w[1] & ~0x10001u) | (t & 0x10001u);
  w[3] = (w[3] & ~0x10001u) | ((t >> 2) & 0x10001u);
  return w;
}
#define FMIX_LO(acc, w, S) asm("v_fma_mix_f32 %0, %1, %2, %0 op_sel_hi:[1,0,0]" : "+v"(acc) : "v"(w), "v"(S))
#define FMIX_HI(acc, w, S) asm("v_fma_mix_f32 %0, %1, %2, %0 op_sel:[1,0,0] op_sel_hi:[1,0,0]" : "+v"(acc) : "v"(w), "v"(S))
__device__ __forceinline__ bf4 trrd(const char* p) {
  return __builtin_amdgcn_ds_read_tr16_b64_v4i16((LDSP bf4*)(p));
}
__device__ __forceinline__ bf8 cat8(bf4 a, bf4 b) { return __builtin_shufflevector(a, b, 0, 1, 2, 3, 4, 5, 6, 7); }
__device__ __forceinline__ void st8_sc1(void* p, u64 v) { __hip_atomic_store((u64*)p, v, __ATOMIC_RELAXED, __HIP_MEMORY_SCOPE_AGENT); }
__device__ __forceinline__ u32 ld4_sc1(const u32* p) { return __hip_atomic_load(p, __ATOMIC_RELAXED, __HIP_MEMORY_SCOPE_AGENT); }
__device__ __forceinline__ void st4_sc1(u32* p, u32 v) { __hip_atomic_store(p, v, __ATOMIC_RELAXED, __HIP_MEMORY_SCOPE_AGENT); }
#define VMW0() asm volatile("s_waitcnt vmcnt(0)" ::: "memory")
#define CFENCE() asm volatile("" ::: "memory")
#define VMW0C() do { __builtin_amdgcn_s_waitcnt(0x0F70); asm volatile("" ::: "memory"); } while (0)
#define SPIN_CAP (1 << 22)
#define XPS 16
#define SPS 4
#define SP2 4
#define WPS 8
__device__ __forceinline__ int tid_now(int wv) {
  int l; asm volatile("v_mbcnt_lo_u32_b32 %0, -1, 0\n\tv_mbcnt_hi_u32_b32 %0, -1, %0" : "=v"(l));
  return wv * 64 + l;
}
#define TIDX tid_now(wv)
#define CLAIM() do { if (TIDX == 0) pre = (int)__hip_atomic_fetch_add(qh, 1u, __ATOMIC_RELAXED, __HIP_MEMORY_SCOPE_AGENT); } while (0)

#define TSD 6
#define TFL 5
struct Params {
  const u16 *Ax, *Ac, *Awq, *Awk, *Awv, *Awo, *Bx, *Bm, *BWq, *BWk, *BWv, *BWo;
  u16* out;
  char* ws;
  char* big;
};
#define TG_KP(i) (i)
#define TG_CF(i) (8 + (i))
#define TG_VOTE(i) (64 + (i))
#define TG_INIT(i) (96 + (i))
#define TG_FK(i) (128 + (i))
#define TG_CL(i) (256 + (i))
#define TG_EP 512
#define SY0 2048
#define SY_QH(q) (SY0 + (q) * 32)
#define SY_CC(rb) (SY0 + 224 + (rb) * 32)
#define SY_CW (SY0 + 480)
#define SY_SEG(s, c) (SY_CS(62) + ((s) - 62) * 128 + (c))
#define SY_KMF(s) (SY0 + 704 + (s) * 32)
#define SY_CS(s) (SY0 + 2496 + ((s) < 48 ? (s) : 48 + ((s) - 48) * 8) * 32)
#define BLKS 1
#define SY_BLK(mi, cb, g) (SY0 + 8128 + ((mi) * 16 + (cb)) * 64 + (g))
#define SY_WORDS (SY0 + 56 * 256)

#define AL256(x) (((x) + 255) & ~255)
#define O_SYNC_END (SY_WORDS * 4)
#define RP 16384
#define ACB (2048 * RP)
#define ROWO(r) ((r) * RP + (((r) >> 7) & 1) * 8192)
#define S_QA 0
#define S_KA (1 * ACB)
#define S_VA (2 * ACB)
#define S_CA S_QA
#define S_QB (3 * ACB)
#define KVO 0x200000
#define S_KB (4 * ACB + KVO)
#define S_VB (5 * ACB + KVO)
#define S_OB S_QB
#define OBN 1
#define S_OL S_OB
#define OROW(r) (ROWO(r) ^ 8192)
#define S_WF(m) (6 * ACB + (8 << 20) + (m) * (12 << 20))
#define WFO(kc, f) (ROWO((kc) * 9 + ((f) >> 3)) + ((f) & 7) * 1024)
#define O_BIG (20 << 20)
#define BIG0 ((char*)((uintptr_t)p.big & ~(uintptr_t)0x1FFFFFu))
#define BIGP(kp, off) (BIG0 + (u32)((kp) * 0x200000 + (off)))
#define O_KM AL256(O_SYNC_END)
#define O_VM (O_KM + 64 * 8 * 4096 * 2)
#define O_XW (O_VM + 64 * 8 * 4096 * 2)
#define XGEO(x) ((((x) & 1) << 14) | (((x) & 2) << 11))
#define XBLK(n) ((n) * 32768 + (((n) & 16) << 10) + (((n) & 32) << 7))
#define X_M2 (56 * 32768)
#define X_ANN (62 * 32768)
#define X_W2 (1 << 21)
#define X_END (2 * X_W2)
#define WSP(T, off) ((T*)(p.ws + (off)))

#define XS0 8
#define SM0 52
#define LDS_TOTAL (131072 + 8192 + 16384)
#define LDS_ALL 163840

#define SB0() __builtin_amdgcn_sched_barrier(0)
#define RSRC(ptr, bytes) __builtin_amdgcn_make_buffer_rsrc((void*)(ptr), 0, (bytes), 0x00020000)
__device__ __forceinline__ bf8 ldsc1(__amdgpu_buffer_rsrc_t r, int off) {
  return __builtin_bit_cast(bf8, __builtin_amdgcn_raw_buffer_load_b128(r, off, 0, 16));
}
__device__ __forceinline__ u32x4 pair16(u32 a0, u32 a1, u32 b0, u32 b1) {
  auto r0 = __builtin_amdgcn_permlane16_swap(a0, b0, false, false);
  auto r1 = __builtin_amdgcn_permlane16_swap(a1, b1, false, false);
  return (u32x4){r0[0], r1[0], r0[1], r1[1]};
}
__device__ __forceinline__ int fsw(int k) { return (k & 3) | (((k >> 3) & 1) << 2); }

#define PJ_OOB 0x7F000000
#define VMW(n) asm volatile("s_waitcnt vmcnt(%0)" :: "n"(n) : "memory")
#define BARV(n) do { VMW(n); __builtin_amdgcn_s_barrier(); CFENCE(); } while (0)
#define BARL(n) do { asm volatile("s_waitcnt vmcnt(%0) lgkmcnt(0)" :: "n"(n) : "memory"); __builtin_amdgcn_s_barrier(); CFENCE(); } while (0)
#define DMA16(rs, dst, vo, so, aux) __builtin_amdgcn_raw_ptr_buffer_load_lds(rs, (LDSP void*)(smem + (dst)), 16, vo, so, 0, aux)
#define TRA(dst, a, off) asm volatile("ds_read_b64_tr_b16 %0, %1 offset:%2" : "=&v"(dst) : "v"(a), "i"(off) : "memory")
#define RDA(dst, a, off) asm volatile("ds_read_b128 %0, %1 offset:%2" : "=&v"(dst) : "v"(a), "i"(off) : "memory")
struct Start { u32x2 ep, old; u32 l, h; int slot, duty; };
#define RSS RSRC(p.ws, SY_WORDS * 4)
#define TG_OK(x) (((((x)[0] ^ st.l) >> 4) | ((x)[1] ^ st.h)) == 0u)
#define TG_LD(i) __builtin_bit_cast(u32x2, __hip_atomic_load(WSP(u64, 0) + (i), __ATOMIC_RELAXED, __HIP_MEMORY_SCOPE_AGENT))
#define TG_ST(i, v) st8_sc1(WSP(u64, 0) + (i), (u64)(st.l | (v)) | ((u64)st.h << 32))
#define TG_BEGIN() do { \
    __builtin_amdgcn_raw_buffer_store_b128((u32x4){0u, 0u, 0u, 0u}, RSS, (TIDX & 63) * 16, st.duty >= 0 ? SY0 * 4 + st.duty * 1024 : PJ_OOB, 16); \
    SB0(); asm volatile("s_nop 1" ::: "memory"); SB0(); st.ep = TG_LD(TG_EP); SB0(); } while (0)
#define TG_CLAIM(n) do { SB0(); VMW(n); asm volatile("" : "+v"(st.ep)); const int t_ = TIDX; \
    st.l = ((u32)__builtin_amdgcn_readfirstlane((int)st.ep[0]) + 16u) & ~15u; st.h = (u32)__builtin_amdgcn_readfirstlane((int)st.ep[1]); \
    st.old = (u32x2){st.l, st.h}; \
    __builtin_amdgcn_raw_buffer_store_b64(st.old, RSS, (t_ & 63) ? PJ_OOB : 0, st.duty >= 0 ? TG_CF(st.duty) * 8 : PJ_OOB, 16); \
    asm volatile("s_nop 4\n\tbuffer_atomic_swap_x2 %0, %1, %2, 0 offen sc0" : "+v"(st.old) : "v"(t_ == 448 ? TG_CL(st.slot) * 8 : PJ_OOB), "s"(RSS) : "memory"); SB0(); } while (0)
#define TG_NOTE(off) do { if (wv == 7) { asm volatile("" : "+v"(st.old)); \
    asm volatile("ds_write_b32 %0, %1\n\ts_waitcnt lgkmcnt(0)" :: "v"((int)(uintptr_t)smem + (off)), "v"(__builtin_amdgcn_readfirstlane(TG_OK(st.old) ? 1 : 0)) : "memory"); } } while (0)
#define TG_CHECK(off) do { int x_, y_; asm volatile("ds_read_b32 %0, %2\n\ts_waitcnt lgkmcnt(0)\n\tv_readfirstlane_b32 %1, %0\n\ts_cmp_eq_u32 %1, 0\n\ts_cbranch_scc1 1f\n\t" \
      "s_waitcnt vmcnt(0)\n\ts_barrier\n\ts_mov_b64 exec, -1\n\ts_mov_b64 vcc, %3\n\ts_mov_b32 m0, %4\n\ts_mov_b64 s[0:1], vcc\n\ts_and_b32 s2, m0, 0x1ff\n\ts_lshr_b32 s3, m0, 10\n\t" \
      "v_mbcnt_lo_u32_b32 v0, -1, 0\n\tv_mbcnt_hi_u32_b32 v0, -1, v0\n\tv_add_u32 v0, s3, v0\n\t" \
      "s_getpc_b64 s[4:5]\n\ts_add_u32 s4, s4, _Z4mega6Params@rel32@lo+4\n\ts_addc_u32 s5, s5, _Z4mega6Params@rel32@hi+12\n\ts_setpc_b64 s[4:5]\n1:" \
      : "=&v"(x_), "=&s"(y_) : "v"((int)(uintptr_t)smem + (off)), "s"(__builtin_amdgcn_kernarg_segment_ptr()), "s"((wv << 16) | 256 | ((st.slot + 1) & 255)) : "scc", "memory"); } while (0)
#define TG_KPW(kv, sl) do { if (!__all(TG_OK(kv))) { int it_ = 0; \
      do { __builtin_amdgcn_s_sleep(sl); kv = TG_LD(TIDX & 63); } while (!__all(TG_OK(kv)) && ++it_ < SPIN_CAP); } \
    kp = __builtin_amdgcn_readfirstlane(__popc((u32)__ballot((kv)[0] & 1u) & 255u) > 4); } while (0)
#define FKS(i)
#define C_BUFA 131072
#define C_BUFB (131072 + 16384)
#define AT_GK ((wid * 4 + (lane >> 4)) * RP + (lane & 15) * 16)
#define AT_GV (AT_GK ^ (((wid & 1) * 2 + (lane >> 5)) * 64))
#define AT_RS(off) RSRC(BIGP(kp, (off) + hd * 256), ACB - 8192 + 256)
constexpr int pj_pack(int S, int ph) { return S == 8 ? (ph == 1 ? 0x01 : ph == 2 ? 0x11 : ph == 3 ? 0x6423 : 0) : S == 0 ? (ph == 0 ? 0x7533 : ph == 1 ? 0xA8EC4 : ph == 2 ? 0xB9FD4 : 0) : 0; }
constexpr int pj_store(int S, int ph) { return ph == 0 ? 0 : S == 8 ? (ph > 1 ? (ph - 2) * 16 + 1 : 0) : S < 2 ? (S * 3 + 1 + ph) * 16 + 1
                                               : S < 6 && ph > 1 ? (S * 2 + 2 + ph) * 16 + 1 : 0; }
template <class F>
__device__ __forceinline__ void proj_slot(F early, const Params& p, Start& st, const int nu, const int ml, const int rt0, const int rs, const int cb, int& kp, const int hd,
                                          bf8* qr, u32& vfl, char* smem, const int wv) {
  const int tid = TIDX, lane = tid & 63, wid = wv;
  const int l15 = lane & 15, lq = lane >> 4, q4 = l15 >> 2, p4 = lane & 3, wr = wid >> 2, wc = wid & 3;
  const __amdgpu_buffer_rsrc_t rc = RSRC(p.ws, 0x10000000), rd = RSRC(BIG0, 0xE200000);

  int vB = ((wid * 4 + lq) * 4096 + (((l15 >> 1) ^ (lq | (((wid >> 1) & 1) << 2))) * 16 + (lane & 1) * 8)) * 2, vB1 = vB + 32 * 8192;
  int ao = (wr * 64 + l15) * 128 + ((lq ^ (l15 & 7)) << 4);
  int bo = (int)(uintptr_t)smem + 65536 + (lq * 8 + q4) * 256 + (((wc * 2) ^ (q4 | ((lq & 1) << 2))) << 5) + p4 * 8;
  int vD = 0, sp = C_BUFA + tid * 16;
  asm volatile("" : "+v"(sp));
  __amdgpu_buffer_rsrc_t rsA, rsB;
  int k2, vA, vA1, sa, sb, da = 128, xb = 256, yb = 64 * 8192, mn;
  u64 pa, pb;
  const int nb = (rt0 < 8 ? 16 - rt0 : rt0 + 2) * 4;
  auto unit = [&](const int u, const int part) __attribute__((always_inline)) {
    if (part == 0) {
      const __attribute__((address_space(4))) u64* a = (const __attribute__((address_space(4))) u64*)__builtin_amdgcn_kernarg_segment_ptr();
      mn = ((ml >> (u * 4)) & 15) * 4;
      pa = a[(0x110666 >> mn) & 15]; pb = a[(0x4328A9 >> mn) & 15];
    } else if (part == 1) {
      k2 = mn < 16 ? 1152 : 1024;
      sa = ((rt0 + u * rs) & 15) * 128 * k2 - 128; sb = mn == 12 ? (hd & 1) * 256 : 0;
      asm volatile("" : "+s"(k2), "+s"(sa), "+s"(sb));
    } else if (part == 2) {
      vA = TIDX & 63; vA1 = ((vA & 7) ^ (vA >> 3)) * 16; vA >>= 3;
      asm volatile("" : "+v"(vA), "+v"(vA1));
    } else if (part == 3) {
      vA = ((wid >> 1) * 32 + (wid & 1) * 8 + vA) * k2 + vA1; vA1 = vA + 16 * k2;
      asm volatile("" : "+v"(vA), "+v"(vA1));
    } else {
      rsA = RSRC((const char*)(__attribute__((address_space(1))) const char*)pa, 2048 * k2);
      rsB = RSRC((const char*)(__attribute__((address_space(1))) const char*)pb + cb * 512, k2 * 4096 - cb * 512);
      asm volatile("" : "+s"(rsA), "+s"(rsB));
    }
  };
#define PJ_ATT() do { const int lane = TIDX & 63; u32 f_, g_; int it_ = 0; \
    asm volatile("ds_read_b32 %0, %2\n\tds_read_b32 %1, %2 offset:256\n\ts_waitcnt lgkmcnt(0)" : "=&v"(f_), "=&v"(g_) : "v"((int)(uintptr_t)smem + C_BUFA + lane * 4) : "memory"); \
    while (!__all((lane >= nb || f_ >= 8u) && (lane >= 6 || g_ >= 8u)) && ++it_ < SPIN_CAP) { \
      f_ = ld4_sc1(WSP(u32, 0) + SY_BLK(4, cb, lane)); g_ = ld4_sc1(WSP(u32, 0) + SY_BLK(5, cb, lane)); VMW0C(); } \
    rsA = AT_RS(S_VA); rsB = AT_RS(S_KA); vA = AT_GV; vA1 = vA + 32 * RP; vB = AT_GK; vB1 = vB + 32 * RP; \
    k2 = RP / 2; da = yb = ROWO(128); xb = 64 * RP; sa = -ROWO(128); sb = 0; } while (0)
#define PJ_STAGE(dst, rs, v0, v1, so, aux) do { const int so_ = (so); \
    __builtin_amdgcn_raw_ptr_buffer_load_lds(rs, (LDSP void*)(smem + (dst) + wid * 1024), 16, v0, so_, 0, aux); \
    __builtin_amdgcn_raw_ptr_buffer_load_lds(rs, (LDSP void*)(smem + (dst) + wid * 1024 + 8192), 16, v1, so_, 0, aux); } while (0)
#define PJ_SA(c, dst, so) PJ_STAGE(dst, rsA, vA, vA1, so, (c) ? 16 : 0)
#define PJ_SB(c, dst, so) PJ_STAGE(dst, rsB, vB, vB1, so, (c) ? 16 : 0)
#define PJ_LDA(h) do { _Pragma("unroll") for (int m = 0; m < 4; m++) _Pragma("unroll") for (int k = 0; k < 2; k++) \
    At[m][k] = *(const bf8*)(smem + (ao ^ (k * 64)) + (h) * 16384 + m * 2048); } while (0)
#define PJ_TR(dst, a, off) asm volatile("ds_read_b64_tr_b16 %0, %1 offset:%2" : "=&v"(dst) : "v"(a), "i"(off) : "memory")
#define PJ_LDB(D, h) do { const int b1_ = bo ^ 32; _Pragma("unroll") for (int n = 0; n < 2; n++) _Pragma("unroll") for (int k = 0; k < 2; k++) { \
    bf4 lo_, hi_; PJ_TR(lo_, n ? b1_ : bo, (h) * 16384 + k * 8192); PJ_TR(hi_, n ? b1_ : bo, (h) * 16384 + k * 8192 + 1024); D[n][k] = cat8(lo_, hi_); } } while (0)
#define PJ_WV(n) asm volatile("s_waitcnt vmcnt(%0)" :: "n"(n) : "memory")
#define PJ_WL(n) asm volatile("s_waitcnt lgkmcnt(" #n ")" ::: "memory")
#define PJ_WL0 do { __builtin_amdgcn_s_waitcnt(0xC07F); asm volatile("" ::: "memory"); } while (0)
#define PJ_BAR __builtin_amdgcn_s_barrier()
#define PJ_SCHED __builtin_amdgcn_sched_barrier(0)
#define PJ_NOP do { PJ_SCHED; asm volatile("s_nop 7"); PJ_SCHED; } while (0)
#define PJ_P2(z, n, r, f) pk2(acc[(z) >> 3][(z) & 1][((z) >> 1) & 3][n][r] f, acc[(z) >> 3][(z) & 1][((z) >> 1) & 3][n][(r) + 1] f)
#define PJ_PK(z, f) pair16(PJ_P2(z, 0, 0, f), PJ_P2(z, 0, 2, f), PJ_P2(z, 1, 0, f), PJ_P2(z, 1, 2, f))
#define QSC * (1.4426950408889634f * 0.08838834764831845f)
#define PJ_QW(a) do { const int l_ = TIDX & 63; char* const w_ = smem + C_BUFA + (wc * 4 + ((l_ >> 4) & 1) * 2 + (l_ >> 5)) * 512 + wr * 256 + (l_ & 15) * 16; \
    _Pragma("unroll") for (int q = 0; q < 4; q++) *(u32x4*)(w_ + q * 8192) = PJ_PK((a) * 8 + q * 2, QSC); } while (0)
#define PJ_QR() do { const char* a_ = smem + C_BUFA + wc * 8192 + (TIDX & 63) * 16; _Pragma("unroll") for (int i = 0; i < 8; i++) qr[i] = *(const bf8*)(a_ + i * 1024); } while (0)
#define PJ_DS(v, z) do { PJ_SCHED; __builtin_amdgcn_raw_buffer_store_b128(v, rd, vD + ((z) & 1) * 256, (dco ^ (((z) >> 3) * 8192)) + ((z) >> 1) * 32 * RP, 16); \
    asm volatile("s_nop 1"); PJ_SCHED; } while (0)
#define PJ_PUB(a, n) do { if (wid == 4) { const int l_ = TIDX & 63; __builtin_amdgcn_raw_buffer_store_b32(8u, rc, l_ < (n) ? (l_ + (a)) * 4 : PJ_OOB, dfl, 16); } } while (0)
#define PJ_ST()
#define PJ_CV(z, j) asm volatile("v_cvt_pk_bf16_f32 %0, %1, %2" : "=v"(pk[z][j]) \
    : "v"(acc[(z) >> 3][(z) & 1][((z) >> 1) & 3][(j) >> 1][((j) & 1) * 2]), "v"(acc[(z) >> 3][(z) & 1][((z) >> 1) & 3][(j) >> 1][((j) & 1) * 2 + 1]))
#define PJ_DP(z) PJ_DS(((u32x4){pk[z][0], pk[z][1], pk[z][2], pk[z][3]}), z)
#define PJ_OUT(S, ph) do { constexpr int ps_ = pj_store(S, ph), pn_ = pj_store(S, (ph) % 3 + 1) >> 4; if (ps_ & 15) PJ_DP(ps_ >> 4); \
    if (pn_ >= 12) { const u32x4 y_ = *(const u32x4*)(smem + sp + (pn_ - 12) * 8192); _Pragma("unroll") for (int i = 0; i < 4; i++) pk[pn_][i] = y_[i]; } } while (0)
#define PJ_MMA(S, ai, bj, D) do { PJ_SCHED; __builtin_amdgcn_s_setprio(1); \
    constexpr int pp_ = pj_pack(S, (ai) * 2 + (bj)); \
    _Pragma("unroll") for (int i = 0; i < 16; i++) { const int k = i >> 3, m = (i >> 1) & 3, n = i & 1; \
      if (k == 0 && (S) == 0) asm volatile("v_mfma_f32_16x16x32_bf16 %0, %1, %2, 0" : "+v"(acc[ai][bj][m][n]) : "v"(D[n][k]), "v"(At[m][k])); \
      else asm volatile("v_mfma_f32_16x16x32_bf16 %0, %1, %2, %0" : "+v"(acc[ai][bj][m][n]) : "v"(D[n][k]), "v"(At[m][k])); \
      if (i < (pp_ & 15) * 3) { PJ_SCHED; const int z = (pp_ >> (4 + i / 3 * 4)) & 15; \
          \
        if (i % 3 == 0) { PJ_CV(z, 0); PJ_CV(z, 2); PJ_CV(z, 1); } \
        else { if (i % 3 == 1) PJ_CV(z, 3); \
          asm volatile("v_permlane16_swap_b32 %0, %1" : "+v"(pk[z][i % 3 - 1]), "+v"(pk[z][i % 3 + 1])); \
          if (i % 3 == 2 && z >= 12) *(u32x4*)(smem + sp + (z - 12) * 8192) = (u32x4){pk[z][0], pk[z][1], pk[z][2], pk[z][3]}; } \
        PJ_SCHED; } \
      if ((S) == 7 && (ai) + (bj) == 0 && i % 3 == 0 && i < 15) { PJ_SCHED; unit(u + 1 < nu ? u + 1 : u, i / 3); PJ_SCHED; } \
      if ((S) == 8 && (ai) + (bj) == 0 && i % 3 == 0 && i < 12) { PJ_SCHED; dest(u, i / 3); PJ_SCHED; } } \
    __builtin_amdgcn_s_setprio(0); PJ_SCHED; } while (0)
#define PJ_STEP(S) do { PJ_ST(); \
    PJ_LDB(B0, 0); PJ_SCHED; \
    if ((S) == 6 && own && wid == 0) { _Pragma("unroll") for (int i = 0; i < 2; i++) \
      __builtin_amdgcn_raw_ptr_buffer_load_lds(rc, (LDSP void*)((LDSP char*)smem + (C_BUFA + i * 256)), 4, (TIDX & 63) * 4, SY_BLK(4 + i, cb, 0) * 4, 0, 16); } \
    PJ_SCHED; PJ_LDA(0); PJ_SA((S) == 8, (bsel ^ 32768) + 16384, sa + 128 * k2); \
    PJ_WL(8); PJ_BAR; PJ_WL0; PJ_MMA(S, 0, 0, B0); PJ_BAR; PJ_SCHED; \
    if ((S) == 7 && own) PJ_ATT(); \
    PJ_SCHED; PJ_LDB(B1, 1); PJ_SB((S) >= 7, 65536 + bsel, sb); PJ_OUT(S, 1); \
    PJ_BAR; PJ_WL0; PJ_MMA(S, 0, 1, B1); PJ_BAR; \
    if ((S) == 8 && own) { PJ_QW(0); PJ_SCHED; } \
    PJ_LDA(1); sa += (S) >= 7 ? da : 128; PJ_SA((S) >= 7, bsel, sa); PJ_OUT(S, 2); \
    PJ_BAR; PJ_WL0; PJ_MMA(S, 1, 0, B0); PJ_BAR; PJ_SCHED; \
    PJ_SB((S) >= 7, 65536 + bsel + 16384, sb ^ ((S) >= 7 ? xb : 256)); sb += (S) >= 7 ? yb : 64 * 8192; PJ_OUT(S, 3); \
      \
    PJ_WV(6 + (pj_store(S, 1) & 15) + (pj_store(S, 2) & 15) + (pj_store(S, 3) & 15)); \
    PJ_BAR; \
    if ((S) != 6 && (S) != 8) PJ_PUB((S) < 3 || (S) == 7 ? (S) : (S) + 1, (S) == 2 ? 2 : 1); \
    PJ_MMA(S, 1, 1, B1); PJ_BAR; \
    bsel ^= 32768; ao ^= 32768; bo ^= 32768; } while (0)
  f4 acc[2][2][4][2];
#pragma unroll
  for (int a = 0; a < 16; a++) { acc[a >> 3][(a >> 2) & 1][a & 3][0] = (f4){0, 0, 0, 0}; acc[a >> 3][(a >> 2) & 1][a & 3][1] = (f4){0, 0, 0, 0}; }
  bf8 At[4][2], B0[2][2], B1[2][2];
  u32 pk[16][4];
#pragma unroll
  for (int i = 0; i < 4; i++) { pk[2][i] = 0u; pk[4][i] = 0u; pk[6][i] = 0u; }
#pragma unroll
  for (int i = 0; i < 5; i++) unit(0, i);
  int bsel = hd >= 0 ? 32768 : 0;
  ao ^= bsel; bo ^= bsel;
  PJ_SB(0, 65536 + bsel, 0); PJ_SA(0, bsel, sa + 128);
  PJ_SB(0, 65536 + bsel + 16384, 256); PJ_SA(0, bsel + 16384, sa + 128 + 128 * k2);
  if (kp < 0) {
    TG_CLAIM(9);
    u32x2 kv = TG_LD(lane);
    VMW0(); TG_NOTE(151552);
    __syncthreads();
    TG_CHECK(151552);
    TG_KPW(kv, 2);
  }
  if (wr == 1) PJ_BAR;
  PJ_WV(4); PJ_BAR;
  PJ_SB(0, 65536 + (bsel ^ 32768), 64 * 8192); PJ_SA(0, bsel ^ 32768, sa + 256);
  PJ_SB(0, 65536 + (bsel ^ 32768) + 16384, 64 * 8192 + 256);
  PJ_WV(6); PJ_BAR;
  early();
  sa += 256; sb = 128 * 8192;
  int dco = PJ_OOB, dfl = PJ_OOB, md, vE;
  auto dest = [&](const int u, const int part) __attribute__((always_inline)) {
    if (part == 0) {
      md = (ml >> (u * 4)) & 15;
      dfl = SY_BLK(md, cb, ((rt0 + u * rs) & 15) * 4) * 4;
      asm volatile("" : "+s"(md), "+s"(dfl));
    } else if (part == 1) {
      dco = md == 3 ? PJ_OOB : (kp + (md < 2)) * 0x200000 + ((0x210354 >> (md * 4)) & 15) * ACB + cb * 512 + ((rt0 + u * rs) & 15) * 128 * RP;
      asm volatile("" : "+s"(dco));
    } else if (part == 2) {
      vD = TIDX; vE = ((vD >> 3) & 2) + ((vD >> 5) & 1) + wc * 4; vD &= 15;
      asm volatile("" : "+v"(vD), "+v"(vE));
    } else {
      vD = vD * (md < 3 ? RP : 16) + (vE * (md < 3 ? 16 : 2 * RP) + wr * (md < 3 ? 16 * RP : RP));
      asm volatile("" : "+v"(vD));
    }
  };
  static_assert(KVO == 0x200000, "places");
#pragma unroll 1
  for (int u = 0; u < nu; u++) {
    const bool own = hd >= 0 && u + 1 == nu;
    PJ_STEP(0); PJ_STEP(1); PJ_STEP(2); PJ_STEP(3); PJ_STEP(4); PJ_STEP(5);
    int more = k2 - 1024; asm volatile("" : "+s"(more));
    if (more) PJ_STEP(6);
    PJ_STEP(7); PJ_STEP(8);
  }
  PJ_ST();
  PJ_NOP;
  if (hd >= 0) {
    vfl = (TIDX & 63) * 4;
    if (wid == 0) asm volatile("s_nop 4\n\tbuffer_load_dword %0, %0, %1, %2 offen sc1" : "+v"(vfl) : "s"(rc), "s"(SY_BLK(5, cb, 0) * 4) : "memory");
    if (wr == 0) PJ_QR();
    dco = kp * 0x200000 + S_QA + (hd ^ 1) * 256 + rt0 * 128 * RP;
#pragma unroll
    for (int z = 0; z < 16; z += 2) PJ_DS(PJ_PK(z + 1, QSC), z);
    if (wr == 0) { PJ_WL0; PJ_BAR; }
    PJ_SCHED; PJ_QW(1); PJ_SCHED;
    if (wr == 1) { PJ_WL0; PJ_BAR; asm volatile("" ::: "memory"); PJ_QR(); }
    return;
  }
#pragma unroll
  for (int z = 2; z < 16; z++) PJ_DS(PJ_PK(z, ), z);
  PJ_WV(0);
  if (wr == 0) PJ_BAR;
  PJ_BAR;
  PJ_PUB(0, 8);
  PJ_ST();
}

namespace fa {
#define ABS(i)
#define ABT()
#define EPS 3
#define RTH 65536.f
constexpr int TS = 16384, KR = 4 * TS;
#define SBAR() __builtin_amdgcn_sched_barrier(0)
#define MF32(a, b, c) __builtin_amdgcn_mfma_f32_32x32x16_bf16(a, b, c, 0, 0, 0)
#define FMX3(a, b, c) __builtin_elementwise_maximum(__builtin_elementwise_maximum(a, b), c)
__device__ __forceinline__ void attn_pair(const Params& p, const char* __restrict__ Kh, const char* __restrict__ Vh, const int hd, const int R, const int kp, bf8* qr, u32 vfl, char* lds, const int wv, u32* qh, int& pre) {
  const int tid = TIDX, wid = wv, lane = tid & 63, grp = wid >> 2;
  const int n = R * 4 + 4;
  float l_reg = 0.f;
  f32x16 o[4] = {}, s0, s1, cm;
  int ko = KR + lane * 16;
  int vo = ((((lane >> 3) & 2) | ((lane >> 1) & 1)) * (512 + 64) + ((lane >> 2) & 3) * 16 + (lane & 1) * 8) ^ ((lane >> 5) * 64), vp;
  const __amdgpu_buffer_rsrc_t rk = RSRC(Kh, ACB - 8192 + 256), rv = RSRC(Vh, ACB - 8192 + 256);
  const int gk = AT_GK, gv = AT_GV;
  u32* const qf = WSP(u32, 0) + SY_BLK(3, hd >> 1, 0);
  int t = 0, rl = 1, ks = ROWO(192), vs;
#define DMA(rs, go, dst, so) __builtin_amdgcn_raw_ptr_buffer_load_lds(rs, (LDSP void*)((LDSP char*)lds + ((dst) + wid * 1024)), 16, go, so, 0, 16)
#define NEXT_TILES() do { const int u_ = t + 4 < n ? t + 4 : t + 4 - n; vs = ks; ks = t < 32 ? ROWO(u_ * 64) : PJ_OOB; asm volatile("" : "+s"(ks)); } while (0)
#define ISSUE(i) DMA((i) < 2 ? rk : rv, (i) < 2 ? gk : gv, ((i) < 2 ? KR + (t & 3) * TS : ((t + 3) & 3) * TS) + ((i) & 1) * 8192, ((i) < 2 ? ks : vs) + ((i) & 1) * 32 * RP)
#define PBAR() do { SBAR(); asm volatile("" ::: "memory"); __builtin_amdgcn_s_barrier(); asm volatile("" ::: "memory"); SBAR(); } while (0)
#define SEND() do { SBAR(); int x_, y_; \
    asm volatile("s_waitcnt vmcnt(12)\n\ts_cmp_lg_u32 %0, 0\n\ts_mov_b32 %0, 0\n\ts_cbranch_scc1 1f\n\ts_waitcnt vmcnt(4)\n1:\n\ts_cmp_lg_u32 %3, 1\n\ts_cbranch_scc1 2f\n\t" \
      "v_mov_b32 %1, %5\n\tv_mov_b32 %2, 1\n\ts_mov_b64 exec, 1\n\ts_nop 1\n\tglobal_atomic_add %1, %2, %4\n\ts_mov_b64 exec, -1\n2:\n\ts_barrier" \
      : "+&s"(rl), "=&v"(x_), "=&v"(y_) : "s"(t), "s"(qf), "s"(R * 32) : "scc", "memory"); \
    SBAR(); } while (0)
#define QO_RS(b) RSRC(BIGP(kp, S_QA + ROWO(((b) ? 7 - R : R) * 256 + wid * 32) + hd * 256), 32 * RP)
#define QO_VO() (((TIDX & 63) >> 4) * RP + (TIDX & 15) * 16)
#define LQ1(i) "s_add_i32 %8, %8, 0x10000\n\tbuffer_load_dwordx4 %" #i ", %9, %10, %8 offen sc1\n\t"
#define QRS "+v"(qr[0]), "+v"(qr[1]), "+v"(qr[2]), "+v"(qr[3]), "+v"(qr[4]), "+v"(qr[5]), "+v"(qr[6]), "+v"(qr[7])
#define LDQ() asm volatile("s_mov_b32 %8, 0xffff0000\n\ts_nop 3\n\t" LQ1(0) LQ1(1) LQ1(2) LQ1(3) LQ1(4) LQ1(5) LQ1(6) LQ1(7) \
    : QRS, "=&s"(rl) : "v"(QO_VO()), "s"(QO_RS(1)) : "scc", "memory")
#define LDK(d0) do { const char* a_ = lds + ko + (d0) * 1024; \
    kf[(d0) % 3][0] = *(const bf8*)a_; kf[(d0) % 3][1] = *(const bf8*)(a_ + 8192); } while (0)
#define LDV(i) do { const int a_ = ((i) & 3) * 2048 + (((i) >> 2) & 1) * 256 + ((i) >> 3) * 8192; vf[(i) & 3] = cat8(TRL(vo + a_), TRL(vp + a_)); } while (0)
#define TRL(o) __builtin_amdgcn_ds_read_tr16_b64_v4i16((LDSP bf4*)((LDSP char*)lds + (o)))
#define QK(NEXT, is) do { _Pragma("unroll") for (int d0 = 0; d0 < 8; ++d0) { \
      if (d0 + 2 < 8) LDK(d0 + 2); else { NEXT(2 * (d0 - 6)); NEXT(2 * (d0 - 6) + 1); } \
      SBAR(); \
      s0 = MF32(kf[d0 % 3][0], qr[d0], d0 ? s0 : (is) ? cm : f32x16{}); s1 = MF32(kf[d0 % 3][1], qr[d0], d0 ? s1 : (is) ? cm : f32x16{}); \
      SBAR(); \
      if ((is) && d0 < 4) ISSUE(d0); } } while (0)
#define NOV(i)
#define TR1(r, c) "s_bfm_b32 exec_lo, " #c ", 0\n\ts_bfm_b32 exec_hi, " #c "+4, 0\n\tv_mov_b32 %" #r ", 0xff800000\n\t"
#define TRI(S) do { float y[16]; _Pragma("unroll") for (int r = 0; r < 16; ++r) y[r] = S[r]; \
      asm volatile(TR1(0, 0) TR1(1, 1) TR1(2, 2) TR1(3, 3) TR1(4, 8) TR1(5, 9) TR1(6, 10) TR1(7, 11) TR1(8, 16) TR1(9, 17) TR1(10, 18) TR1(11, 19) \
          TR1(12, 24) TR1(13, 25) TR1(14, 26) TR1(15, 27) "s_mov_b64 exec, -1" \
          : "+v"(y[0]), "+v"(y[1]), "+v"(y[2]), "+v"(y[3]), "+v"(y[4]), "+v"(y[5]), "+v"(y[6]), "+v"(y[7]), \
            "+v"(y[8]), "+v"(y[9]), "+v"(y[10]), "+v"(y[11]), "+v"(y[12]), "+v"(y[13]), "+v"(y[14]), "+v"(y[15])); \
      _Pragma("unroll") for (int r = 0; r < 16; ++r) S[r] = y[r]; } while (0)
#define MASK() do { SBAR(); asm volatile("s_nop 7\n\ts_nop 3" : "+v"(s0), "+v"(s1)); \
      if (wid & 1) TRI(s1); \
      else { TRI(s0); _Pragma("unroll") for (int r = 0; r < 16; ++r) s1[r] = -__builtin_inff(); } \
      asm volatile("" : "+v"(s0), "+v"(s1)); SBAR(); } while (0)
#define RENEW(floor) do { \
      float mx = FMX3(s0[0], s0[1], s1[0]), my = FMX3(s0[2], s0[3], s1[1]); \
      _Pragma("unroll") for (int r = 4; r < 16; r += 4) { mx = FMX3(mx, s0[r], s0[r + 1]); my = FMX3(my, s0[r + 2], s0[r + 3]); } \
      _Pragma("unroll") for (int r = 2; r < 14; r += 4) { mx = FMX3(mx, s1[r], s1[r + 1]); my = FMX3(my, s1[r + 2], s1[r + 3]); } \
      mx = FMX3(FMX3(mx, s1[14], s1[15]), my, floor); \
      auto rr = __builtin_amdgcn_permlane32_swap(__float_as_uint(mx), __float_as_uint(mx), false, false); \
      up = __builtin_elementwise_maximum(__uint_as_float(rr[0]), __uint_as_float(rr[1])); \
      _Pragma("unroll") for (int r = 0; r < 16; ++r) { s0[r] -= up; s1[r] -= up; } } while (0)
#define SETCM(v) do { const float v_ = (v); _Pragma("unroll") for (int r = 0; r < 16; ++r) { float w_ = v_; asm volatile("" : "+v"(w_)); cm[r] = w_; } } while (0)
  bf8 kf[3][2], vf[4], pa[4];
  float up;
  int b = 0, e;
  auto tile = [&](auto LASTC) __attribute__((always_inline)) {
    constexpr bool LAST = decltype(LASTC)::value;
    ABT();
    if (LAST && grp > b) LDQ();
    if (LAST) MASK();
    float sa;
    u32 pw[16];
#define EXPS() do { float x[32], sb; \
      _Pragma("unroll") for (int r = 0; r < 18; ++r) { \
        SBAR(); \
        if (r < 16) { x[2 * r] = __builtin_amdgcn_exp2f(r < 8 ? s0[2 * r] : s1[2 * r - 16]); x[2 * r + 1] = __builtin_amdgcn_exp2f(r < 8 ? s0[2 * r + 1] : s1[2 * r - 15]); } \
        if (r >= 2) { \
          sa = r == 2 ? x[0] : sa + x[2 * r - 4]; \
          sb = r == 2 ? x[1] : sb + x[2 * r - 3]; \
          pw[r - 2] = pk2(x[2 * r - 4], x[2 * r - 3]); asm volatile("" : "+v"(sa), "+v"(pw[r - 2])); } } \
      SBAR(); \
      sa += sb; } while (0)
    EXPS();
    if (__builtin_expect(__ballot(!(sa < RTH)) != 0, 0)) {
      RENEW(0.f);
      SETCM(cm[0] - up);
      const float alpha = __builtin_amdgcn_exp2f(-up);
      l_reg *= alpha;
#pragma unroll
      for (int d = 0; d < 4; ++d) o[d] *= alpha;
      EXPS();
    }
    l_reg += sa;
#pragma unroll
    for (int i = 0; i < 4; ++i) pa[i] = __builtin_bit_cast(bf8, (u32x4){pw[4 * i], pw[4 * i + 1], pw[4 * i + 2], pw[4 * i + 3]});
    asm volatile("" : "+v"(pa[0]), "+v"(pa[1]), "+v"(pa[2]), "+v"(pa[3]), "+v"(l_reg));
    if constexpr (!LAST) ko ^= (t & 1) ? 3 * TS : TS;
    vp = vo ^ 128; asm volatile("" : "+v"(vp));
    NEXT_TILES();
    SBAR();
    if constexpr (!LAST) { LDK(0); LDK(1); } else { LDV(0); LDV(1); LDV(2); LDV(3); }
    SEND();
    __builtin_amdgcn_s_setprio(1);
    if constexpr (!LAST) QK(LDV, true);
#pragma unroll
    for (int i = 0; i < 16; ++i) {
      SBAR();
      o[i & 3] = MF32(vf[i & 3], pa[i >> 2], o[i & 3]);
      SBAR();
      if (i + 4 < 16) LDV(i + 4);
      if (LAST && i < 4) ISSUE(i);
    }
    __builtin_amdgcn_s_setprio(0);
    if constexpr (!LAST) { vo ^= (t & 1) ? 3 * TS : TS; PBAR(); }
  };
  ABS(40);
#pragma unroll 1
  while (true) {
    e = (b ? 32 : n - 4) + (wid >> 1);
    __builtin_amdgcn_s_setprio(1);
    LDK(0); LDK(1);
    QK(NOV, false);
    __builtin_amdgcn_s_setprio(0);
    if (wid + b == 0) {
      const int nb = (R < 4 ? 8 - R : R + 1) * 8;
      int it = 0;
      asm volatile("s_waitcnt vmcnt(8)" : "+v"(vfl) :: "memory");
      while (!__all(lane >= nb || vfl >= 8u) && ++it < SPIN_CAP) { vfl = ld4_sc1(qf + 2 * 16 * 64 + lane); VMW0C(); }
    }
    PBAR();
    if (t == e) MASK();
    RENEW(-__builtin_inff());
    SETCM(-up);
#pragma unroll 1
    for (; t < e; t++) tile(std::false_type{});
    tile(std::true_type{});
    {
      const float inv = __builtin_amdgcn_rcpf(swadd(l_reg, l_reg));
      const __amdgpu_buffer_rsrc_t ro = QO_RS(b);
      const int v = QO_VO();
#pragma unroll
      for (int i = 0; i < 8; ++i) {
        if (i == EPS && wid < 6) PBAR();
        const int g = (i & 1) * 8;
        u32 w[4];
#pragma unroll
        for (int k = 0; k < 4; k++) w[k] = pk2(o[i >> 1][g + 2 * k] * inv, o[i >> 1][g + 2 * k + 1] * inv);
        auto y0 = __builtin_amdgcn_permlane32_swap(w[0], w[2], false, false);
        auto y1 = __builtin_amdgcn_permlane32_swap(w[1], w[3], false, false);
        SBAR();
        __builtin_amdgcn_raw_buffer_store_b128((u32x4){y0[0], y1[0], y0[1], y1[1]}, ro, v, rl, 16);
        asm volatile("s_add_i32 %0, %0, 0x10000\n\ts_nop 0" : "+s"(rl) :: "scc");
        SBAR();
      }
      SBAR();
    }
#pragma unroll 1
    while ((++t & 3) != 0) {
      if (t != e + 1) PBAR();
      ABT();
      if (b == 0) {
        if (wid == 0 && (t & 3) == 2) {
          int it = 0;
          while (ld4_sc1(qf + (7 - R) * 8) < 8u && ++it < SPIN_CAP) __builtin_amdgcn_s_sleep(2);
          VMW0C();
        }
        if (!grp && (t & 3) == 3) LDQ();
      }
      NEXT_TILES();
      SEND();
#pragma unroll
      for (int i = 0; i < 4; i++) ISSUE(i);
    }
    if (b) break;
    b = 1;
    l_reg = 0.f;
#pragma unroll
    for (int i = 0; i < 4; i++) o[i] = f32x16{};
    ko &= ~(3 * TS); vo &= ~(3 * TS);
    asm volatile("s_waitcnt vmcnt(12)\n\ts_cmp_lg_u32 %8, 0\n\ts_cbranch_scc1 1f\n\ts_waitcnt vmcnt(4)\n1:"
        : QRS : "s"(grp) : "scc", "memory");
  }
  ABS(41);
  CLAIM();
  if (!grp) __builtin_amdgcn_s_barrier();
  VMW0();
  __syncthreads();
}
#undef DMA
#undef ISSUE
#undef LDK
#undef LDV
#undef QK
#undef NOV
#undef MASK
}

#define WOP 0
__device__ __forceinline__ void wof_task(const Params& p, const u32x4* st, const int m, const int kc, const int kp, char* smem, const int wv) {
  const int tid = TIDX, lane = tid & 63, wid = wv;
  const int l15 = lane & 15, lq = lane >> 4, q4 = l15 >> 2, p4 = lane & 3;
  const __amdgpu_buffer_rsrc_t rw = RSRC(BIGP(kp, S_WF(m)), 576 * RP);
#pragma unroll
  for (int i = 0; i < 9; i++) {
    const int id = tid + 512 * i, row = id / 72, ch = id % 72;
    *(u32x4*)(smem + row * 1280 + (((ch >> 1) ^ fsw(row)) << 5) + ((ch & 1) << 4)) = st[i];
  }
  asm volatile("s_waitcnt lgkmcnt(0)" ::: "memory"); __builtin_amdgcn_s_barrier(); asm volatile("" ::: "memory");
#pragma unroll
  for (int i = 0; i < 9; i++) {
    const int f = wid * 9 + i, nt = f >> 1, k2 = f & 1;
    const int r0 = k2 * 32 + lq * 8 + q4, r1 = r0 + 4;
    bf4 lo = trrd(smem + r0 * 1280 + ((nt ^ fsw(r0)) << 5) + p4 * 8);
    bf4 hi = trrd(smem + r1 * 1280 + ((nt ^ fsw(r1)) << 5) + p4 * 8);
    __builtin_amdgcn_raw_buffer_store_b128(__builtin_bit_cast(u32x4, cat8(lo, hi)), rw, lane * 16 + WFO(kc, f), 0, 16);
    if (WOP) { SB0(); __builtin_amdgcn_s_sleep(WOP); SB0(); }
  }
  asm volatile("s_waitcnt lgkmcnt(0)" ::: "memory"); __builtin_amdgcn_s_barrier(); asm volatile("" ::: "memory");
}

template <int MT, int NT, int WM, int S>
__device__ __forceinline__ void out_tile(const char* A, const char* W, const int nt0, u16* __restrict__ C, char* smem, const int wv, u32* qh, int& pre) {
  constexpr int WN = 4 / WM, BI = NT * WN / 2, STG = 16384 + NT * WN * 2048, NV = 4 + BI;
  const int tid = TIDX, lane = tid & 63, wid = wv & 3, l15 = lane & 15, lq = lane >> 4, wr = wid / WN, wc = wid % WN;
  if (wv >= 4) {
    const __amdgpu_buffer_rsrc_t ra = RSRC(A, 128 * RP), rw = RSRC(W, 576 * RP);
    const int va = (wid * 32 + lq) * RP + l15 * 16, vw = lane * 16;
    auto fill = [&](const int k, const int bo) {
      const int in = k < 64 ? 0 : PJ_OOB;
#pragma unroll
      for (int i = 0; i < 4; i++) __builtin_amdgcn_raw_ptr_buffer_load_lds(ra, (LDSP void*)(smem + bo + (wid * 4 + i) * 1024), 16, va, ((k >> 1) * 256 + ((k & 1) * 16 + i * 4) * RP) | in, 0, 16);
#pragma unroll
      for (int i = 0; i < BI; i++) { const int f = nt0 * 2 + wid * BI + i;
        __builtin_amdgcn_raw_ptr_buffer_load_lds(rw, (LDSP void*)(smem + bo + 16384 + (wid * BI + i) * 1024), 16, vw, WFO(k, f) | in, 0, 16); }
    };
#define OT_SYNC(n) do { asm volatile("s_waitcnt vmcnt(%0)" :: "n"(n) : "memory"); __builtin_amdgcn_s_barrier(); asm volatile("" ::: "memory"); } while (0)
#pragma unroll
    for (int u = 0; u < S; u++) fill(u, u * STG);
    OT_SYNC((S - 1) * NV);
    int bo = 0;
#pragma unroll 1
    for (int k = 0; k < 64; k++) {
      OT_SYNC((S - 2) * NV);
      fill(k + S, bo);
      bo = bo + STG == S * STG ? 0 : bo + STG;
    }
  } else {
    const int oa = wr * MT * 2048 + lq * 512 + l15 * 16, ob = 16384 + wc * NT * 2048 + lane * 16;
    f4 acc[MT][NT];
#pragma unroll
    for (int i = 0; i < MT; i++)
#pragma unroll
      for (int j = 0; j < NT; j++) acc[i][j] = (f4){0, 0, 0, 0};
    auto rd = [&](const int bo, const int ks, bf8* f, const int i) {
      if (i < NT) f[i] = *(const bf8*)(smem + (ob + bo) + i * 2048 + ks * 1024);
      else f[i] = *(const bf8*)(smem + (oa + bo) + ks * 2048 + ((i - NT) >> 1) * 4096 + ((i - NT) & 1) * 256);
    };
    auto mm = [&](bf8* f, const int bo, const int ks, bf8* g) {
#pragma unroll
      for (int u = 0; u < MT * NT; u++) {
        if (u < MT + NT) rd(bo, ks, g, u);
        SB0();
        acc[u / NT][u % NT] = MFMA(f[u % NT], f[NT + u / NT], acc[u / NT][u % NT]);
        SB0();
      }
    };
    bf8 f0[MT + NT], f1[MT + NT];
    __builtin_amdgcn_s_barrier(); asm volatile("" ::: "memory");
#pragma unroll
    for (int i = 0; i < MT + NT; i++) rd(0, 0, f0, i);
    int bo = 0;
    __builtin_amdgcn_s_setprio(1);
#pragma unroll 1
    for (int k = 0; k < 64; k++) {
      mm(f0, bo, 1, f1);
      asm volatile("" ::: "memory"); __builtin_amdgcn_s_waitcnt(0xC07F); __builtin_amdgcn_s_barrier(); asm volatile("" ::: "memory");
      bo = bo + STG == S * STG ? 0 : bo + STG;
      mm(f1, bo, 0, f0);
    }
    __builtin_amdgcn_s_setprio(0);
    CLAIM();
#pragma unroll
    for (int i = 0; i < MT; i++)
#pragma unroll
      for (int j = 0; j < NT; j += 2) {
        u16* const q = C + (size_t)(wr * (MT * 16) + i * 16 + l15) * 1152 + wc * (NT * 16) + j * 16;
        if (j + 1 == NT) *(u32x2*)(q + lq * 4) = (u32x2){pk2(acc[i][j][0], acc[i][j][1]), pk2(acc[i][j][2], acc[i][j][3])};
        else *(u32x4*)(q + (lq & 1) * 16 + (lq >> 1) * 8) =
            pair16(pk2(acc[i][j][0], acc[i][j][1]), pk2(acc[i][j][2], acc[i][j][3]), pk2(acc[i][(j + 1) % NT][0], acc[i][(j + 1) % NT][1]), pk2(acc[i][(j + 1) % NT][2], acc[i][(j + 1) % NT][3]));
      }
  }
  VMW0();
  __syncthreads();
}

__device__ __forceinline__ void small_out_task(const Params& p, const int set, int hf, int np, const int kp, char* smem, const int wv, u32* qh, int& pre) {
  const int tid = TIDX, lane = tid & 63, wid = wv;
  const int l15 = lane & 15, lq = lane >> 4;
  const __amdgpu_buffer_rsrc_t rw = RSRC(BIGP(kp, S_WF(1)), 576 * RP), ro = RSRC(BIGP(kp, S_OL), ACB);
  if (wid == 0) {
    int it = 0;
    while (ld4_sc1(WSP(u32, 0) + SY_CW) < 128u && ++it < SPIN_CAP) __builtin_amdgcn_s_sleep(8);
  }
  __syncthreads();
  bf8 bfr[2][16];
#pragma unroll
  for (int k = 0; k < 32; k++) bfr[(k >> 1) & 1][(k >> 2) * 2 + (k & 1)] = ldsc1(rw, WFO(wid * 8 + (k >> 2), np * 4 + (k & 3)) + lane * 16);
  int va = (hf * 16 + wid * 2) * RP + lane * 16;
  static_assert(SM0 >= 48 && SM0 < 60, "late segments");
  int u = 0;
#pragma unroll 1
  for (int s = SM0 + (set + 63 - SM0) % 3;; s += 3) {
    if (s > 63) s = 63;
    if (wid == 0) {
      int it = 0;
      const u32* f = WSP(u32, 0) + SY_CS(s < 62 ? s : 61) + (np & 7) * 32;
      while (ld4_sc1(f) < 4u && ++it < SPIN_CAP) __builtin_amdgcn_s_sleep(SPS);
      if (s >= 62) { u32x2 y; while (y = TG_LD(SY_SEG(s, 0) / 2 + lane), !__all(y[0] != 0u && y[1] != 0u) && ++it < SPIN_CAP) __builtin_amdgcn_s_sleep(SP2); }
    }
    CFENCE(); __builtin_amdgcn_s_barrier(); CFENCE();
    const bool mine = s < 63 || (l15 >> 3) == set;
    if (!mine) va = PJ_OOB;
    bf8 a[16];
#pragma unroll
    for (int k = 0; k < 16; k++) a[k] = __builtin_bit_cast(bf8, __builtin_amdgcn_raw_buffer_load_b128(ro, va + (k >> 3) * RP + (k & 7) * 1024, OROW(s * 32), 16));
    SB0();
    f4 acc[2] = {(f4){0, 0, 0, 0}, (f4){0, 0, 0, 0}};
#pragma unroll
    for (int k = 0; k < 16; k++) { acc[0] = MFMA(bfr[0][k], a[k], acc[0]); acc[1] = MFMA(bfr[1][k], a[k], acc[1]); }
    *(f4*)(smem + u + tid * 16) = acc[0];
    *(f4*)(smem + u + 8192 + tid * 16) = acc[1];
    if (s >= 62) CLAIM();
    asm volatile("s_waitcnt lgkmcnt(0)" ::: "memory"); __builtin_amdgcn_s_barrier(); CFENCE();
    if (wid >= 6) {
      f4 t = (f4){0, 0, 0, 0};
#pragma unroll
      for (int w = 0; w < 8; w++) t += *(const f4*)(smem + u + (wid & 1) * 8192 + (w * 64 + lane) * 16);
      if (mine) *(u64*)(p.out + (size_t)(s * 32 + hf * 16 + l15) * 1152 + 576 + np * 32 + (wid & 1) * 16 + lq * 4) = (u64)pk2(t[0], t[1]) | ((u64)pk2(t[2], t[3]) << 32);
    }
    if (s >= 62) break;
    u ^= 16384;
  }
  __syncthreads();
}

#define XS_(x) #x
#define XS(x) XS_(x)
#define GMAX(i)
#define PSTEP_
#define CNTB
#define CNTO
#define PTA
#define PRO
#define PRE
#define PROUT
#define PRDECL(c)
#define PRFIN(c)
#define PT_PROF 99
#define PTS
#define ST0
#define STX "s_memtime s[74:75]\n"
#define ST1
#define ST2
#define ST3
#define ST4
#define ST5
#define ST6
#define ST7
#define ST8
#define ST9
#define ST10
#define ST11
#define ST12
#define ST13
#define ST14
#define ST15
#define ST16
#define ST17
#define ST18
#define ST19
#define ST20
#define ST21
#define ST22
#define ST23
#define ST24
#define ST25
#define ST26
#define ST27
#define ST28
#define ST29
#define ST30
#define ST31
#define ST32
#define ST33
#define ST34
#define ST35
#define ST36
#define ST37
#define ST38
#define ST39
#define ST40
#define ST41
#define ST42
#define ST43
#define ST44
#define ST45
#define ST46
#define ST47
#define ST48
#define ST49
#define STS(i)
#define CPT 22
#define K1 5
#define K2 72
#define KE0 18
#define KE1 4
#define GL0 20
#define DUR0 110
#define DSH 2
#define LIM1 30
#define LIM2 400
#define OD 12
#define OG 12
#define RQD 9
#define GL1 4
#define K3 12
#define DUO0 62
#define DSO 2
#define DUP 64
#define XSD 0
#define TCH2 0
#define TSL 14
#define P2P 2
#define OHX 4
#define SEP 0
#define OGEO(g) (SEP ? XGEO(1) ^ ((g) * 4096) : XGEO(g))
#define TCH (1 - SEP)
#define OHR 2
#define QD 1
#define KD 9
#define BD 4
#define BW
#define BSL 2
#define OSL 12
#define RSL 8
#define SY_KMF0 (SY0 + 704)
#define SY_CS0 (SY0 + 2496)
#define CH_SA \
  "v_add_u32 v244, 0, v249\n" "v_xor_b32 v245, 32, v244\n" "s_waitcnt vmcnt(14)\n" "s_barrier\n" "ds_read_b64_tr_b16 v[0:1], v245 offset:0\n" "ds_read_b64_tr_b16 v[2:3], v245 offset:512\n" \
  "ds_read_b64_tr_b16 v[4:5], v245 offset:8192\n" "ds_read_b64_tr_b16 v[6:7], v245 offset:8704\n" "ds_read_b64_tr_b16 v[72:73], v244 offset:0\n" "ds_read_b64_tr_b16 v[74:75], v244 offset:512\n" \
  "ds_read_b64_tr_b16 v[76:77], v244 offset:8192\n" "ds_read_b64_tr_b16 v[78:79], v244 offset:8704\n" "ds_read_b64_tr_b16 v[144:145], v244 offset:32768\n" \
  "ds_read_b64_tr_b16 v[146:147], v244 offset:33280\n" "ds_read_b64_tr_b16 v[148:149], v244 offset:40960\n" "ds_read_b64_tr_b16 v[150:151], v244 offset:41472\n" "s_waitcnt lgkmcnt(0)\n" \
  "s_barrier\n" "s_add_i32 m0, %[lw], 0\n" "s_mov_b32 %[t], 1572864\n" "buffer_load_dwordx4 v248, %[r0], %[t] offen lds\n" "s_add_i32 m0, %[lw], 8192\n" "s_mov_b32 %[t], 1835008\n" \
  "buffer_load_dwordx4 v248, %[r0], %[t] offen lds\n" "s_add_i32 m0, %[lw], 16384\n" "s_mov_b32 %[t], 1572864\n" "buffer_load_dwordx4 v248, %[r1], %[t] offen lds\n" \
  "s_add_i32 m0, %[lw], 24576\n" "s_mov_b32 %[t], 1835008\n" "buffer_load_dwordx4 v248, %[r1], %[t] offen lds\n" "s_add_i32 m0, %[lw], 32768\n" "s_mov_b32 %[t], 1572864\n" \
  "buffer_load_dwordx4 v248, %[r2], %[t] offen lds\n" "s_add_i32 m0, %[lw], 40960\n" "s_mov_b32 %[t], 1835008\n" "buffer_load_dwordx4 v248, %[r2], %[t] offen lds\n" \
  "v_add_u32 v244, 49152, v249\n" "v_xor_b32 v245, 32, v244\n" "s_waitcnt vmcnt(14)\n" "s_barrier\n" "ds_read_b64_tr_b16 v[8:9], v245 offset:0\n" \
  "ds_read_b64_tr_b16 v[10:11], v245 offset:512\n" "ds_read_b64_tr_b16 v[12:13], v245 offset:8192\n" "ds_read_b64_tr_b16 v[14:15], v245 offset:8704\n" \
  "ds_read_b64_tr_b16 v[80:81], v244 offset:0\n" "ds_read_b64_tr_b16 v[82:83], v244 offset:512\n" "ds_read_b64_tr_b16 v[84:85], v244 offset:8192\n" \
  "ds_read_b64_tr_b16 v[86:87], v244 offset:8704\n" "ds_read_b64_tr_b16 v[152:153], v244 offset:32768\n" "ds_read_b64_tr_b16 v[154:155], v244 offset:33280\n" \
  "ds_read_b64_tr_b16 v[156:157], v244 offset:40960\n" "ds_read_b64_tr_b16 v[158:159], v244 offset:41472\n" "s_waitcnt lgkmcnt(0)\n" "s_barrier\n" "s_add_i32 m0, %[lw], 49152\n" \
  "s_mov_b32 %[t], 2097152\n" "buffer_load_dwordx4 v248, %[r0], %[t] offen lds\n" "s_add_i32 m0, %[lw], 57344\n" "s_mov_b32 %[t], 2359296\n" "buffer_load_dwordx4 v248, %[r0], %[t] offen lds\n" \
  "s_add_i32 m0, %[lw], 65536\n" "s_mov_b32 %[t], 2097152\n" "buffer_load_dwordx4 v248, %[r1], %[t] offen lds\n" "s_add_i32 m0, %[lw], 73728\n" "s_mov_b32 %[t], 2359296\n" \
  "buffer_load_dwordx4 v248, %[r1], %[t] offen lds\n" "s_add_i32 m0, %[lw], 81920\n" "s_mov_b32 %[t], 2097152\n" "buffer_load_dwordx4 v248, %[r2], %[t] offen lds\n" \
  "s_add_i32 m0, %[lw], 90112\n" "s_mov_b32 %[t], 2359296\n" "buffer_load_dwordx4 v248, %[r2], %[t] offen lds\n" "v_add_u32 v244, 98304, v249\n" "v_xor_b32 v245, 32, v244\n" \
  "s_waitcnt vmcnt(12)\n" "s_barrier\n" "ds_read_b64_tr_b16 v[16:17], v245 offset:0\n" "ds_read_b64_tr_b16 v[18:19], v245 offset:512\n" "ds_read_b64_tr_b16 v[20:21], v245 offset:8192\n" \
  "ds_read_b64_tr_b16 v[22:23], v245 offset:8704\n" "ds_read_b64_tr_b16 v[88:89], v244 offset:0\n" "ds_read_b64_tr_b16 v[90:91], v244 offset:512\n" \
  "ds_read_b64_tr_b16 v[92:93], v244 offset:8192\n" "ds_read_b64_tr_b16 v[94:95], v244 offset:8704\n" "ds_read_b64_tr_b16 v[160:161], v244 offset:32768\n" \
  "ds_read_b64_tr_b16 v[162:163], v244 offset:33280\n" "ds_read_b64_tr_b16 v[164:165], v244 offset:40960\n" "ds_read_b64_tr_b16 v[166:167], v244 offset:41472\n" "s_waitcnt lgkmcnt(0)\n" \
  "s_barrier\n" "s_add_i32 m0, %[lw], 98304\n" "s_mov_b32 %[t], 2621440\n" "buffer_load_dwordx4 v248, %[r0], %[t] offen lds\n" "s_add_i32 m0, %[lw], 106496\n" "s_mov_b32 %[t], 2883584\n" \
  "buffer_load_dwordx4 v248, %[r0], %[t] offen lds\n" "s_add_i32 m0, %[lw], 114688\n" "s_mov_b32 %[t], 2621440\n" "buffer_load_dwordx4 v248, %[r1], %[t] offen lds\n" \
  "s_add_i32 m0, %[lw], 122880\n" "s_mov_b32 %[t], 2883584\n" "buffer_load_dwordx4 v248, %[r1], %[t] offen lds\n" "s_add_i32 m0, %[lw], 131072\n" "s_mov_b32 %[t], 2621440\n" \
  "buffer_load_dwordx4 v248, %[r2], %[t] offen lds\n" "s_add_i32 m0, %[lw], 139264\n" "s_mov_b32 %[t], 2883584\n" "buffer_load_dwordx4 v248, %[r2], %[t] offen lds\n" "v_add_u32 v244, 0, v249\n" \
  "v_xor_b32 v245, 32, v244\n" "s_waitcnt vmcnt(12)\n" "s_barrier\n" "ds_read_b64_tr_b16 v[24:25], v245 offset:0\n" "ds_read_b64_tr_b16 v[26:27], v245 offset:512\n" \
  "ds_read_b64_tr_b16 v[28:29], v245 offset:8192\n" "ds_read_b64_tr_b16 v[30:31], v245 offset:8704\n" "ds_read_b64_tr_b16 v[96:97], v244 offset:0\n" \
  "ds_read_b64_tr_b16 v[98:99], v244 offset:512\n" "ds_read_b64_tr_b16 v[100:101], v244 offset:8192\n" "ds_read_b64_tr_b16 v[102:103], v244 offset:8704\n" \
  "ds_read_b64_tr_b16 v[168:169], v244 offset:32768\n" "ds_read_b64_tr_b16 v[170:171], v244 offset:33280\n" "ds_read_b64_tr_b16 v[172:173], v244 offset:40960\n" \
  "ds_read_b64_tr_b16 v[174:175], v244 offset:41472\n" "s_waitcnt lgkmcnt(0)\n" "s_barrier\n" "s_add_i32 m0, %[lw], 0\n" "s_mov_b32 %[t], 3145728\n" \
  "buffer_load_dwordx4 v248, %[r0], %[t] offen lds\n" "s_add_i32 m0, %[lw], 8192\n" "s_mov_b32 %[t], 3407872\n" "buffer_load_dwordx4 v248, %[r0], %[t] offen lds\n" \
  "s_add_i32 m0, %[lw], 16384\n" "s_mov_b32 %[t], 3145728\n" "buffer_load_dwordx4 v248, %[r1], %[t] offen lds\n" "s_add_i32 m0, %[lw], 24576\n" "s_mov_b32 %[t], 3407872\n" \
  "buffer_load_dwordx4 v248, %[r1], %[t] offen lds\n" "s_add_i32 m0, %[lw], 32768\n" "s_mov_b32 %[t], 3145728\n" "buffer_load_dwordx4 v248, %[r2], %[t] offen lds\n" \
  "s_add_i32 m0, %[lw], 40960\n" "s_mov_b32 %[t], 3407872\n" "buffer_load_dwordx4 v248, %[r2], %[t] offen lds\n" "v_add_u32 v244, 49152, v249\n" "v_xor_b32 v245, 32, v244\n" \
  "s_waitcnt vmcnt(12)\n" "s_barrier\n" "ds_read_b64_tr_b16 v[32:33], v245 offset:0\n" "ds_read_b64_tr_b16 v[34:35], v245 offset:512\n" "ds_read_b64_tr_b16 v[36:37], v245 offset:8192\n" \
  "ds_read_b64_tr_b16 v[38:39], v245 offset:8704\n" "ds_read_b64_tr_b16 v[104:105], v244 offset:0\n" "ds_read_b64_tr_b16 v[106:107], v244 offset:512\n" \
  "ds_read_b64_tr_b16 v[108:109], v244 offset:8192\n" "ds_read_b64_tr_b16 v[110:111], v244 offset:8704\n" "ds_read_b64_tr_b16 v[176:177], v244 offset:32768\n" \
  "ds_read_b64_tr_b16 v[178:179], v244 offset:33280\n" "ds_read_b64_tr_b16 v[180:181], v244 offset:40960\n" "ds_read_b64_tr_b16 v[182:183], v244 offset:41472\n" "s_waitcnt lgkmcnt(0)\n" \
  "s_barrier\n" "s_add_i32 m0, %[lw], 49152\n" "s_mov_b32 %[t], 3670016\n" "buffer_load_dwordx4 v248, %[r0], %[t] offen lds\n" "s_add_i32 m0, %[lw], 57344\n" "s_mov_b32 %[t], 3932160\n" \
  "buffer_load_dwordx4 v248, %[r0], %[t] offen lds\n" "s_add_i32 m0, %[lw], 65536\n" "s_mov_b32 %[t], 3670016\n" "buffer_load_dwordx4 v248, %[r1], %[t] offen lds\n" \
  "s_add_i32 m0, %[lw], 73728\n" "s_mov_b32 %[t], 3932160\n" "buffer_load_dwordx4 v248, %[r1], %[t] offen lds\n" "s_add_i32 m0, %[lw], 81920\n" "s_mov_b32 %[t], 3670016\n" \
  "buffer_load_dwordx4 v248, %[r2], %[t] offen lds\n" "s_add_i32 m0, %[lw], 90112\n" "s_mov_b32 %[t], 3932160\n" "buffer_load_dwordx4 v248, %[r2], %[t] offen lds\n" \
  "v_add_u32 v244, 98304, v249\n" "v_xor_b32 v245, 32, v244\n" "s_waitcnt vmcnt(12)\n" "s_barrier\n" "ds_read_b64_tr_b16 v[40:41], v245 offset:0\n" \
  "ds_read_b64_tr_b16 v[42:43], v245 offset:512\n" "ds_read_b64_tr_b16 v[44:45], v245 offset:8192\n" "ds_read_b64_tr_b16 v[46:47], v245 offset:8704\n" \
  "ds_read_b64_tr_b16 v[112:113], v244 offset:0\n" "ds_read_b64_tr_b16 v[114:115], v244 offset:512\n" "ds_read_b64_tr_b16 v[116:117], v244 offset:8192\n" \
  "ds_read_b64_tr_b16 v[118:119], v244 offset:8704\n" "s_waitcnt lgkmcnt(0)\n" "s_barrier\n" "s_add_i32 m0, %[lw], 98304\n" "s_mov_b32 %[t], 4194304\n" \
  "buffer_load_dwordx4 v248, %[r0], %[t] offen lds\n" "s_add_i32 m0, %[lw], 106496\n" "s_mov_b32 %[t], 4456448\n" "buffer_load_dwordx4 v248, %[r0], %[t] offen lds\n" \
  "s_add_i32 m0, %[lw], 114688\n" "s_mov_b32 %[t], 4194304\n" "buffer_load_dwordx4 v248, %[r1], %[t] offen lds\n" "s_add_i32 m0, %[lw], 122880\n" "s_mov_b32 %[t], 4456448\n" \
  "buffer_load_dwordx4 v248, %[r1], %[t] offen lds\n" "s_add_i32 m0, %[lw], 131072\n" "s_mov_b32 %[t], 4194304\n" "buffer_load_dwordx4 v248, %[r2], %[t] offen lds\n" \
  "s_add_i32 m0, %[lw], 139264\n" "s_mov_b32 %[t], 4456448\n" "buffer_load_dwordx4 v248, %[r2], %[t] offen lds\n" "v_add_u32 v244, 0, v249\n" "v_xor_b32 v245, 32, v244\n" \
  "s_waitcnt vmcnt(12)\n" "s_barrier\n" "ds_read_b64_tr_b16 v[48:49], v245 offset:0\n" "ds_read_b64_tr_b16 v[50:51], v245 offset:512\n" "ds_read_b64_tr_b16 v[52:53], v245 offset:8192\n" \
  "ds_read_b64_tr_b16 v[54:55], v245 offset:8704\n" "ds_read_b64_tr_b16 v[120:121], v244 offset:0\n" "ds_read_b64_tr_b16 v[122:123], v244 offset:512\n" \
  "ds_read_b64_tr_b16 v[124:125], v244 offset:8192\n" "ds_read_b64_tr_b16 v[126:127], v244 offset:8704\n" "s_waitcnt lgkmcnt(0)\n" "s_barrier\n" "v_add_u32 v244, 49152, v249\n" \
  "v_xor_b32 v245, 32, v244\n" "s_waitcnt vmcnt(6)\n" "s_barrier\n" "ds_read_b64_tr_b16 v[56:57], v245 offset:0\n" "ds_read_b64_tr_b16 v[58:59], v245 offset:512\n" \
  "ds_read_b64_tr_b16 v[60:61], v245 offset:8192\n" "ds_read_b64_tr_b16 v[62:63], v245 offset:8704\n" "ds_read_b64_tr_b16 v[128:129], v244 offset:0\n" \
  "ds_read_b64_tr_b16 v[130:131], v244 offset:512\n" "ds_read_b64_tr_b16 v[132:133], v244 offset:8192\n" "ds_read_b64_tr_b16 v[134:135], v244 offset:8704\n" "s_waitcnt lgkmcnt(0)\n" \
  "s_barrier\n" "v_add_u32 v244, 98304, v249\n" "v_xor_b32 v245, 32, v244\n" "s_waitcnt vmcnt(0)\n" "s_barrier\n" "ds_read_b64_tr_b16 v[64:65], v245 offset:0\n" \
  "ds_read_b64_tr_b16 v[66:67], v245 offset:512\n" "ds_read_b64_tr_b16 v[68:69], v245 offset:8192\n" "ds_read_b64_tr_b16 v[70:71], v245 offset:8704\n" \
  "ds_read_b64_tr_b16 v[136:137], v244 offset:0\n" "ds_read_b64_tr_b16 v[138:139], v244 offset:512\n" "ds_read_b64_tr_b16 v[140:141], v244 offset:8192\n" \
  "ds_read_b64_tr_b16 v[142:143], v244 offset:8704\n" "s_waitcnt lgkmcnt(0)\n" "s_barrier\n"
#define CH_SB \
  "v_add_u32 v244, 0, v249\n" "v_xor_b32 v245, 32, v244\n" "s_waitcnt vmcnt(14)\n" "s_barrier\n" "ds_read_b64_tr_b16 v[0:1], v245 offset:16384\n" \
  "ds_read_b64_tr_b16 v[2:3], v245 offset:16896\n" "ds_read_b64_tr_b16 v[4:5], v245 offset:24576\n" "ds_read_b64_tr_b16 v[6:7], v245 offset:25088\n" \
  "ds_read_b64_tr_b16 v[48:49], v244 offset:16384\n" "ds_read_b64_tr_b16 v[50:51], v244 offset:16896\n" "ds_read_b64_tr_b16 v[52:53], v244 offset:24576\n" \
  "ds_read_b64_tr_b16 v[54:55], v244 offset:25088\n" "ds_read_b64_tr_b16 v[96:97], v245 offset:32768\n" "ds_read_b64_tr_b16 v[98:99], v245 offset:33280\n" \
  "ds_read_b64_tr_b16 v[100:101], v245 offset:40960\n" "ds_read_b64_tr_b16 v[102:103], v245 offset:41472\n" "s_waitcnt lgkmcnt(0)\n" "s_barrier\n" "s_add_i32 m0, %[lw], 0\n" \
  "s_mov_b32 %[t], 1572864\n" "buffer_load_dwordx4 v248, %[r0], %[t] offen lds\n" "s_add_i32 m0, %[lw], 8192\n" "s_mov_b32 %[t], 1835008\n" "buffer_load_dwordx4 v248, %[r0], %[t] offen lds\n" \
  "s_add_i32 m0, %[lw], 16384\n" "s_mov_b32 %[t], 1572864\n" "buffer_load_dwordx4 v248, %[r1], %[t] offen lds\n" "s_add_i32 m0, %[lw], 24576\n" "s_mov_b32 %[t], 1835008\n" \
  "buffer_load_dwordx4 v248, %[r1], %[t] offen lds\n" "s_add_i32 m0, %[lw], 32768\n" "s_mov_b32 %[t], 1572864\n" "buffer_load_dwordx4 v248, %[r2], %[t] offen lds\n" \
  "s_add_i32 m0, %[lw], 40960\n" "s_mov_b32 %[t], 1835008\n" "buffer_load_dwordx4 v248, %[r2], %[t] offen lds\n" "v_add_u32 v244, 49152, v249\n" "v_xor_b32 v245, 32, v244\n" \
  "s_waitcnt vmcnt(14)\n" "s_barrier\n" "ds_read_b64_tr_b16 v[8:9], v245 offset:16384\n" "ds_read_b64_tr_b16 v[10:11], v245 offset:16896\n" "ds_read_b64_tr_b16 v[12:13], v245 offset:24576\n" \
  "ds_read_b64_tr_b16 v[14:15], v245 offset:25088\n" "ds_read_b64_tr_b16 v[56:57], v244 offset:16384\n" "ds_read_b64_tr_b16 v[58:59], v244 offset:16896\n" \
  "ds_read_b64_tr_b16 v[60:61], v244 offset:24576\n" "ds_read_b64_tr_b16 v[62:63], v244 offset:25088\n" "ds_read_b64_tr_b16 v[104:105], v245 offset:32768\n" \
  "ds_read_b64_tr_b16 v[106:107], v245 offset:33280\n" "ds_read_b64_tr_b16 v[108:109], v245 offset:40960\n" "ds_read_b64_tr_b16 v[110:111], v245 offset:41472\n" "s_waitcnt lgkmcnt(0)\n" \
  "s_barrier\n" "s_add_i32 m0, %[lw], 49152\n" "s_mov_b32 %[t], 2097152\n" "buffer_load_dwordx4 v248, %[r0], %[t] offen lds\n" "s_add_i32 m0, %[lw], 57344\n" "s_mov_b32 %[t], 2359296\n" \
  "buffer_load_dwordx4 v248, %[r0], %[t] offen lds\n" "s_add_i32 m0, %[lw], 65536\n" "s_mov_b32 %[t], 2097152\n" "buffer_load_dwordx4 v248, %[r1], %[t] offen lds\n" \
  "s_add_i32 m0, %[lw], 73728\n" "s_mov_b32 %[t], 2359296\n" "buffer_load_dwordx4 v248, %[r1], %[t] offen lds\n" "s_add_i32 m0, %[lw], 81920\n" "s_mov_b32 %[t], 2097152\n" \
  "buffer_load_dwordx4 v248, %[r2], %[t] offen lds\n" "s_add_i32 m0, %[lw], 90112\n" "s_mov_b32 %[t], 2359296\n" "buffer_load_dwordx4 v248, %[r2], %[t] offen lds\n" \
  "v_add_u32 v244, 98304, v249\n" "v_xor_b32 v245, 32, v244\n" "s_waitcnt vmcnt(12)\n" "s_barrier\n" "ds_read_b64_tr_b16 v[16:17], v245 offset:16384\n" \
  "ds_read_b64_tr_b16 v[18:19], v245 offset:16896\n" "ds_read_b64_tr_b16 v[20:21], v245 offset:24576\n" "ds_read_b64_tr_b16 v[22:23], v245 offset:25088\n" \
  "ds_read_b64_tr_b16 v[64:65], v244 offset:16384\n" "ds_read_b64_tr_b16 v[66:67], v244 offset:16896\n" "ds_read_b64_tr_b16 v[68:69], v244 offset:24576\n" \
  "ds_read_b64_tr_b16 v[70:71], v244 offset:25088\n" "ds_read_b64_tr_b16 v[112:113], v245 offset:32768\n" "ds_read_b64_tr_b16 v[114:115], v245 offset:33280\n" \
  "ds_read_b64_tr_b16 v[116:117], v245 offset:40960\n" "ds_read_b64_tr_b16 v[118:119], v245 offset:41472\n" "s_waitcnt lgkmcnt(0)\n" "s_barrier\n" "s_add_i32 m0, %[lw], 98304\n" \
  "s_mov_b32 %[t], 2621440\n" "buffer_load_dwordx4 v248, %[r0], %[t] offen lds\n" "s_add_i32 m0, %[lw], 106496\n" "s_mov_b32 %[t], 2883584\n" "buffer_load_dwordx4 v248, %[r0], %[t] offen lds\n" \
  "s_add_i32 m0, %[lw], 114688\n" "s_mov_b32 %[t], 2621440\n" "buffer_load_dwordx4 v248, %[r1], %[t] offen lds\n" "s_add_i32 m0, %[lw], 122880\n" "s_mov_b32 %[t], 2883584\n" \
  "buffer_load_dwordx4 v248, %[r1], %[t] offen lds\n" "s_add_i32 m0, %[lw], 131072\n" "s_mov_b32 %[t], 2621440\n" "buffer_load_dwordx4 v248, %[r2], %[t] offen lds\n" \
  "s_add_i32 m0, %[lw], 139264\n" "s_mov_b32 %[t], 2883584\n" "buffer_load_dwordx4 v248, %[r2], %[t] offen lds\n" "v_add_u32 v244, 0, v249\n" "v_xor_b32 v245, 32, v244\n" \
  "s_waitcnt vmcnt(12)\n" "s_barrier\n" "ds_read_b64_tr_b16 v[24:25], v245 offset:16384\n" "ds_read_b64_tr_b16 v[26:27], v245 offset:16896\n" "ds_read_b64_tr_b16 v[28:29], v245 offset:24576\n" \
  "ds_read_b64_tr_b16 v[30:31], v245 offset:25088\n" "ds_read_b64_tr_b16 v[72:73], v244 offset:16384\n" "ds_read_b64_tr_b16 v[74:75], v244 offset:16896\n" \
  "ds_read_b64_tr_b16 v[76:77], v244 offset:24576\n" "ds_read_b64_tr_b16 v[78:79], v244 offset:25088\n" "ds_read_b64_tr_b16 v[120:121], v245 offset:32768\n" \
  "ds_read_b64_tr_b16 v[122:123], v245 offset:33280\n" "ds_read_b64_tr_b16 v[124:125], v245 offset:40960\n" "ds_read_b64_tr_b16 v[126:127], v245 offset:41472\n" "s_waitcnt lgkmcnt(0)\n" \
  "s_barrier\n" "s_add_i32 m0, %[lw], 0\n" "s_mov_b32 %[t], 3145728\n" "buffer_load_dwordx4 v248, %[r0], %[t] offen lds\n" "s_add_i32 m0, %[lw], 8192\n" "s_mov_b32 %[t], 3407872\n" \
  "buffer_load_dwordx4 v248, %[r0], %[t] offen lds\n" "s_add_i32 m0, %[lw], 16384\n" "s_mov_b32 %[t], 3145728\n" "buffer_load_dwordx4 v248, %[r1], %[t] offen lds\n" \
  "s_add_i32 m0, %[lw], 24576\n" "s_mov_b32 %[t], 3407872\n" "buffer_load_dwordx4 v248, %[r1], %[t] offen lds\n" "s_add_i32 m0, %[lw], 32768\n" "s_mov_b32 %[t], 3145728\n" \
  "buffer_load_dwordx4 v248, %[r2], %[t] offen lds\n" "s_add_i32 m0, %[lw], 40960\n" "s_mov_b32 %[t], 3407872\n" "buffer_load_dwordx4 v248, %[r2], %[t] offen lds\n" \
  "v_add_u32 v244, 49152, v249\n" "v_xor_b32 v245, 32, v244\n" "s_waitcnt vmcnt(12)\n" "s_barrier\n" "ds_read_b64_tr_b16 v[32:33], v245 offset:16384\n" \
  "ds_read_b64_tr_b16 v[34:35], v245 offset:16896\n" "ds_read_b64_tr_b16 v[36:37], v245 offset:24576\n" "ds_read_b64_tr_b16 v[38:39], v245 offset:25088\n" \
  "ds_read_b64_tr_b16 v[80:81], v244 offset:16384\n" "ds_read_b64_tr_b16 v[82:83], v244 offset:16896\n" "ds_read_b64_tr_b16 v[84:85], v244 offset:24576\n" \
  "ds_read_b64_tr_b16 v[86:87], v244 offset:25088\n" "ds_read_b64_tr_b16 v[128:129], v245 offset:32768\n" "ds_read_b64_tr_b16 v[130:131], v245 offset:33280\n" \
  "ds_read_b64_tr_b16 v[132:133], v245 offset:40960\n" "ds_read_b64_tr_b16 v[134:135], v245 offset:41472\n" "s_waitcnt lgkmcnt(0)\n" "s_barrier\n" "s_add_i32 m0, %[lw], 49152\n" \
  "s_mov_b32 %[t], 3670016\n" "buffer_load_dwordx4 v248, %[r0], %[t] offen lds\n" "s_add_i32 m0, %[lw], 57344\n" "s_mov_b32 %[t], 3932160\n" "buffer_load_dwordx4 v248, %[r0], %[t] offen lds\n" \
  "s_add_i32 m0, %[lw], 65536\n" "s_mov_b32 %[t], 3670016\n" "buffer_load_dwordx4 v248, %[r1], %[t] offen lds\n" "s_add_i32 m0, %[lw], 73728\n" "s_mov_b32 %[t], 3932160\n" \
  "buffer_load_dwordx4 v248, %[r1], %[t] offen lds\n" "s_add_i32 m0, %[lw], 81920\n" "s_mov_b32 %[t], 3670016\n" "buffer_load_dwordx4 v248, %[r2], %[t] offen lds\n" \
  "s_add_i32 m0, %[lw], 90112\n" "s_mov_b32 %[t], 3932160\n" "buffer_load_dwordx4 v248, %[r2], %[t] offen lds\n" "v_add_u32 v244, 98304, v249\n" "v_xor_b32 v245, 32, v244\n" \
  "s_waitcnt vmcnt(12)\n" "s_barrier\n" "ds_read_b64_tr_b16 v[40:41], v245 offset:16384\n" "ds_read_b64_tr_b16 v[42:43], v245 offset:16896\n" "ds_read_b64_tr_b16 v[44:45], v245 offset:24576\n" \
  "ds_read_b64_tr_b16 v[46:47], v245 offset:25088\n" "ds_read_b64_tr_b16 v[88:89], v244 offset:16384\n" "ds_read_b64_tr_b16 v[90:91], v244 offset:16896\n" \
  "ds_read_b64_tr_b16 v[92:93], v244 offset:24576\n" "ds_read_b64_tr_b16 v[94:95], v244 offset:25088\n" "ds_read_b64_tr_b16 v[136:137], v245 offset:32768\n" \
  "ds_read_b64_tr_b16 v[138:139], v245 offset:33280\n" "ds_read_b64_tr_b16 v[140:141], v245 offset:40960\n" "ds_read_b64_tr_b16 v[142:143], v245 offset:41472\n" \
  "ds_read_b64_tr_b16 v[144:145], v244 offset:32768\n" "ds_read_b64_tr_b16 v[146:147], v244 offset:33280\n" "ds_read_b64_tr_b16 v[148:149], v244 offset:40960\n" \
  "ds_read_b64_tr_b16 v[150:151], v244 offset:41472\n" "s_waitcnt lgkmcnt(0)\n" "s_barrier\n" "s_add_i32 m0, %[lw], 98304\n" "s_mov_b32 %[t], 4194304\n" \
  "buffer_load_dwordx4 v248, %[r0], %[t] offen lds\n" "s_add_i32 m0, %[lw], 106496\n" "s_mov_b32 %[t], 4456448\n" "buffer_load_dwordx4 v248, %[r0], %[t] offen lds\n" \
  "s_add_i32 m0, %[lw], 114688\n" "s_mov_b32 %[t], 4194304\n" "buffer_load_dwordx4 v248, %[r1], %[t] offen lds\n" "s_add_i32 m0, %[lw], 122880\n" "s_mov_b32 %[t], 4456448\n" \
  "buffer_load_dwordx4 v248, %[r1], %[t] offen lds\n" "s_add_i32 m0, %[lw], 131072\n" "s_mov_b32 %[t], 4194304\n" "buffer_load_dwordx4 v248, %[r2], %[t] offen lds\n" \
  "s_add_i32 m0, %[lw], 139264\n" "s_mov_b32 %[t], 4456448\n" "buffer_load_dwordx4 v248, %[r2], %[t] offen lds\n" "v_add_u32 v244, 0, v249\n" "v_xor_b32 v245, 32, v244\n" \
  "s_waitcnt vmcnt(12)\n" "s_barrier\n" "ds_read_b64_tr_b16 v[176:177], v245 offset:16384\n" "ds_read_b64_tr_b16 v[178:179], v245 offset:16896\n" \
  "ds_read_b64_tr_b16 v[180:181], v245 offset:24576\n" "ds_read_b64_tr_b16 v[182:183], v245 offset:25088\n" "ds_read_b64_tr_b16 v[184:185], v244 offset:16384\n" \
  "ds_read_b64_tr_b16 v[186:187], v244 offset:16896\n" "ds_read_b64_tr_b16 v[188:189], v244 offset:24576\n" "ds_read_b64_tr_b16 v[190:191], v244 offset:25088\n" \
  "ds_read_b64_tr_b16 v[192:193], v245 offset:32768\n" "ds_read_b64_tr_b16 v[194:195], v245 offset:33280\n" "ds_read_b64_tr_b16 v[196:197], v245 offset:40960\n" \
  "ds_read_b64_tr_b16 v[198:199], v245 offset:41472\n" "ds_read_b64_tr_b16 v[152:153], v244 offset:32768\n" "ds_read_b64_tr_b16 v[154:155], v244 offset:33280\n" \
  "ds_read_b64_tr_b16 v[156:157], v244 offset:40960\n" "ds_read_b64_tr_b16 v[158:159], v244 offset:41472\n" "s_waitcnt lgkmcnt(0)\n" "s_barrier\n" "ds_write_b128 v250, v[176:179] offset:0\n" \
  "ds_write_b128 v250, v[180:183] offset:1024\n" "ds_write_b128 v250, v[184:187] offset:8192\n" "ds_write_b128 v250, v[188:191] offset:9216\n" "ds_write_b128 v250, v[192:195] offset:16384\n" \
  "ds_write_b128 v250, v[196:199] offset:17408\n" "s_waitcnt lgkmcnt(0)\n" "v_add_u32 v244, 49152, v249\n" "v_xor_b32 v245, 32, v244\n" "s_waitcnt vmcnt(6)\n" "s_barrier\n" \
  "ds_read_b64_tr_b16 v[176:177], v245 offset:16384\n" "ds_read_b64_tr_b16 v[178:179], v245 offset:16896\n" "ds_read_b64_tr_b16 v[180:181], v245 offset:24576\n" \
  "ds_read_b64_tr_b16 v[182:183], v245 offset:25088\n" "ds_read_b64_tr_b16 v[184:185], v244 offset:16384\n" "ds_read_b64_tr_b16 v[186:187], v244 offset:16896\n" \
  "ds_read_b64_tr_b16 v[188:189], v244 offset:24576\n" "ds_read_b64_tr_b16 v[190:191], v244 offset:25088\n" "ds_read_b64_tr_b16 v[192:193], v245 offset:32768\n" \
  "ds_read_b64_tr_b16 v[194:195], v245 offset:33280\n" "ds_read_b64_tr_b16 v[196:197], v245 offset:40960\n" "ds_read_b64_tr_b16 v[198:199], v245 offset:41472\n" \
  "ds_read_b64_tr_b16 v[160:161], v244 offset:32768\n" "ds_read_b64_tr_b16 v[162:163], v244 offset:33280\n" "ds_read_b64_tr_b16 v[164:165], v244 offset:40960\n" \
  "ds_read_b64_tr_b16 v[166:167], v244 offset:41472\n" "s_waitcnt lgkmcnt(0)\n" "s_barrier\n" "v_add_u32 v250, 24576, v250\n" "ds_write_b128 v250, v[176:179] offset:0\n" \
  "ds_write_b128 v250, v[180:183] offset:1024\n" "ds_write_b128 v250, v[184:187] offset:8192\n" "ds_write_b128 v250, v[188:191] offset:9216\n" "ds_write_b128 v250, v[192:195] offset:16384\n" \
  "ds_write_b128 v250, v[196:199] offset:17408\n" "s_waitcnt lgkmcnt(0)\n" "v_add_u32 v244, 98304, v249\n" "v_xor_b32 v245, 32, v244\n" "s_waitcnt vmcnt(0)\n" "s_barrier\n" \
  "ds_read_b64_tr_b16 v[176:177], v245 offset:16384\n" "ds_read_b64_tr_b16 v[178:179], v245 offset:16896\n" "ds_read_b64_tr_b16 v[180:181], v245 offset:24576\n" \
  "ds_read_b64_tr_b16 v[182:183], v245 offset:25088\n" "ds_read_b64_tr_b16 v[184:185], v244 offset:16384\n" "ds_read_b64_tr_b16 v[186:187], v244 offset:16896\n" \
  "ds_read_b64_tr_b16 v[188:189], v244 offset:24576\n" "ds_read_b64_tr_b16 v[190:191], v244 offset:25088\n" "ds_read_b64_tr_b16 v[192:193], v245 offset:32768\n" \
  "ds_read_b64_tr_b16 v[194:195], v245 offset:33280\n" "ds_read_b64_tr_b16 v[196:197], v245 offset:40960\n" "ds_read_b64_tr_b16 v[198:199], v245 offset:41472\n" \
  "ds_read_b64_tr_b16 v[168:169], v244 offset:32768\n" "ds_read_b64_tr_b16 v[170:171], v244 offset:33280\n" "ds_read_b64_tr_b16 v[172:173], v244 offset:40960\n" \
  "ds_read_b64_tr_b16 v[174:175], v244 offset:41472\n" "s_waitcnt lgkmcnt(0)\n" "s_barrier\n" "v_add_u32 v250, 24576, v250\n" "ds_write_b128 v250, v[176:179] offset:0\n" \
  "ds_write_b128 v250, v[180:183] offset:1024\n" "ds_write_b128 v250, v[184:187] offset:8192\n" "ds_write_b128 v250, v[188:191] offset:9216\n" "ds_write_b128 v251, v[192:195] offset:23552\n" \
  "ds_write_b128 v251, v[196:199] offset:27648\n" "s_waitcnt lgkmcnt(0)\n"
#define CH_SO \
  "s_waitcnt vmcnt(10)\n" "ds_read_b64_tr_b16 v[0:1], v218 offset:0\n" "ds_read_b64_tr_b16 v[2:3], v218 offset:128\n" "ds_read_b64_tr_b16 v[4:5], v218 offset:4096\n" \
  "ds_read_b64_tr_b16 v[6:7], v218 offset:4224\n" "v_xor_b32 v200, 16, v218\n" "ds_read_b64_tr_b16 v[8:9], v200 offset:0\n" "ds_read_b64_tr_b16 v[10:11], v200 offset:128\n" \
  "ds_read_b64_tr_b16 v[12:13], v200 offset:4096\n" "ds_read_b64_tr_b16 v[14:15], v200 offset:4224\n" "v_xor_b32 v200, 32, v218\n" "ds_read_b64_tr_b16 v[16:17], v200 offset:0\n" \
  "ds_read_b64_tr_b16 v[18:19], v200 offset:128\n" "ds_read_b64_tr_b16 v[20:21], v200 offset:4096\n" "ds_read_b64_tr_b16 v[22:23], v200 offset:4224\n" "v_xor_b32 v200, 48, v218\n" \
  "ds_read_b64_tr_b16 v[24:25], v200 offset:0\n" "ds_read_b64_tr_b16 v[26:27], v200 offset:128\n" "ds_read_b64_tr_b16 v[28:29], v200 offset:4096\n" \
  "ds_read_b64_tr_b16 v[30:31], v200 offset:4224\n" "s_waitcnt lgkmcnt(0)\n" "s_add_i32 m0, %[lw], 0\n" "s_mov_b32 %[t], 294912\n" "buffer_load_dwordx4 v216, %[r], %[t] offen lds\n" \
  "s_add_i32 m0, %[lw], 1024\n" "s_mov_b32 %[t], 313344\n" "buffer_load_dwordx4 v216, %[r], %[t] offen lds\n" "s_add_i32 m0, %[lw], 2048\n" "s_mov_b32 %[t], 331776\n" \
  "buffer_load_dwordx4 v216, %[r], %[t] offen lds\n" "s_add_i32 m0, %[lw], 3072\n" "s_mov_b32 %[t], 350208\n" "buffer_load_dwordx4 v216, %[r], %[t] offen lds\n" "s_add_i32 m0, %[lw], 4096\n" \
  "s_mov_b32 %[t], 368640\n" "buffer_load_dwordx4 v216, %[r], %[t] offen lds\n" "s_add_i32 m0, %[lw], 5120\n" "s_mov_b32 %[t], 387072\n" "buffer_load_dwordx4 v216, %[r], %[t] offen lds\n" \
  "s_add_i32 m0, %[lw], 6144\n" "s_mov_b32 %[t], 405504\n" "buffer_load_dwordx4 v216, %[r], %[t] offen lds\n" "s_add_i32 m0, %[lw], 7168\n" "s_mov_b32 %[t], 423936\n" \
  "buffer_load_dwordx4 v216, %[r], %[t] offen lds\n" "s_waitcnt vmcnt(8)\n" "buffer_load_dword v221, v220, %[r], 0 offen\n" "ds_read_b64_tr_b16 v[40:41], v218 offset:8192\n" \
  "ds_read_b64_tr_b16 v[42:43], v218 offset:8320\n" "ds_read_b64_tr_b16 v[44:45], v218 offset:12288\n" "ds_read_b64_tr_b16 v[46:47], v218 offset:12416\n" "v_xor_b32 v200, 16, v218\n" \
  "ds_read_b64_tr_b16 v[48:49], v200 offset:8192\n" "ds_read_b64_tr_b16 v[50:51], v200 offset:8320\n" "ds_read_b64_tr_b16 v[52:53], v200 offset:12288\n" \
  "ds_read_b64_tr_b16 v[54:55], v200 offset:12416\n" "v_xor_b32 v200, 32, v218\n" "ds_read_b64_tr_b16 v[56:57], v200 offset:8192\n" "ds_read_b64_tr_b16 v[58:59], v200 offset:8320\n" \
  "ds_read_b64_tr_b16 v[60:61], v200 offset:12288\n" "ds_read_b64_tr_b16 v[62:63], v200 offset:12416\n" "v_xor_b32 v200, 48, v218\n" "ds_read_b64_tr_b16 v[64:65], v200 offset:8192\n" \
  "ds_read_b64_tr_b16 v[66:67], v200 offset:8320\n" "ds_read_b64_tr_b16 v[68:69], v200 offset:12288\n" "ds_read_b64_tr_b16 v[70:71], v200 offset:12416\n" "s_waitcnt lgkmcnt(0)\n" \
  "s_add_i32 m0, %[lw], 8192\n" "s_mov_b32 %[t], 442368\n" "buffer_load_dwordx4 v216, %[r], %[t] offen lds\n" "s_add_i32 m0, %[lw], 9216\n" "s_mov_b32 %[t], 460800\n" \
  "buffer_load_dwordx4 v216, %[r], %[t] offen lds\n" "s_add_i32 m0, %[lw], 10240\n" "s_mov_b32 %[t], 479232\n" "buffer_load_dwordx4 v216, %[r], %[t] offen lds\n" "s_add_i32 m0, %[lw], 11264\n" \
  "s_mov_b32 %[t], 497664\n" "buffer_load_dwordx4 v216, %[r], %[t] offen lds\n" "s_add_i32 m0, %[lw], 12288\n" "s_mov_b32 %[t], 516096\n" "buffer_load_dwordx4 v216, %[r], %[t] offen lds\n" \
  "s_add_i32 m0, %[lw], 13312\n" "s_mov_b32 %[t], 534528\n" "buffer_load_dwordx4 v216, %[r], %[t] offen lds\n" "s_add_i32 m0, %[lw], 14336\n" "s_mov_b32 %[t], 552960\n" \
  "buffer_load_dwordx4 v216, %[r], %[t] offen lds\n" "s_add_i32 m0, %[lw], 15360\n" "s_mov_b32 %[t], 571392\n" "buffer_load_dwordx4 v216, %[r], %[t] offen lds\n" "s_waitcnt vmcnt(8)\n" \
  "ds_read_b64_tr_b16 v[80:81], v218 offset:0\n" "ds_read_b64_tr_b16 v[82:83], v218 offset:128\n" "ds_read_b64_tr_b16 v[84:85], v218 offset:4096\n" \
  "ds_read_b64_tr_b16 v[86:87], v218 offset:4224\n" "v_xor_b32 v200, 16, v218\n" "ds_read_b64_tr_b16 v[88:89], v200 offset:0\n" "ds_read_b64_tr_b16 v[90:91], v200 offset:128\n" \
  "ds_read_b64_tr_b16 v[92:93], v200 offset:4096\n" "ds_read_b64_tr_b16 v[94:95], v200 offset:4224\n" "v_xor_b32 v200, 32, v218\n" "ds_read_b64_tr_b16 v[96:97], v200 offset:0\n" \
  "ds_read_b64_tr_b16 v[98:99], v200 offset:128\n" "ds_read_b64_tr_b16 v[100:101], v200 offset:4096\n" "ds_read_b64_tr_b16 v[102:103], v200 offset:4224\n" "v_xor_b32 v200, 48, v218\n" \
  "ds_read_b64_tr_b16 v[104:105], v200 offset:0\n" "ds_read_b64_tr_b16 v[106:107], v200 offset:128\n" "ds_read_b64_tr_b16 v[108:109], v200 offset:4096\n" \
  "ds_read_b64_tr_b16 v[110:111], v200 offset:4224\n" "s_waitcnt lgkmcnt(0)\n" "s_add_i32 m0, %[lw], 0\n" "s_mov_b32 %[t], 0\n" "buffer_load_dwordx4 v217, %[r], %[t] offen lds\n" \
  "s_add_i32 m0, %[lw], 1024\n" "s_mov_b32 %[t], 73728\n" "buffer_load_dwordx4 v217, %[r], %[t] offen lds\n" "s_add_i32 m0, %[lw], 2048\n" "s_mov_b32 %[t], 147456\n" \
  "buffer_load_dwordx4 v217, %[r], %[t] offen lds\n" "s_add_i32 m0, %[lw], 3072\n" "s_mov_b32 %[t], 221184\n" "buffer_load_dwordx4 v217, %[r], %[t] offen lds\n" "s_add_i32 m0, %[lw], 4096\n" \
  "s_mov_b32 %[t], 294912\n" "buffer_load_dwordx4 v217, %[r], %[t] offen lds\n" "s_add_i32 m0, %[lw], 5120\n" "s_mov_b32 %[t], 368640\n" "buffer_load_dwordx4 v217, %[r], %[t] offen lds\n" \
  "s_add_i32 m0, %[lw], 6144\n" "s_mov_b32 %[t], 442368\n" "buffer_load_dwordx4 v217, %[r], %[t] offen lds\n" "s_add_i32 m0, %[lw], 7168\n" "s_mov_b32 %[t], 516096\n" \
  "buffer_load_dwordx4 v217, %[r], %[t] offen lds\n" "s_waitcnt vmcnt(8)\n" "ds_read_b64_tr_b16 v[120:121], v218 offset:8192\n" "ds_read_b64_tr_b16 v[122:123], v218 offset:8320\n" \
  "ds_read_b64_tr_b16 v[124:125], v218 offset:12288\n" "ds_read_b64_tr_b16 v[126:127], v218 offset:12416\n" "v_xor_b32 v200, 16, v218\n" "ds_read_b64_tr_b16 v[128:129], v200 offset:8192\n" \
  "ds_read_b64_tr_b16 v[130:131], v200 offset:8320\n" "ds_read_b64_tr_b16 v[132:133], v200 offset:12288\n" "ds_read_b64_tr_b16 v[134:135], v200 offset:12416\n" "v_xor_b32 v200, 32, v218\n" \
  "ds_read_b64_tr_b16 v[136:137], v200 offset:8192\n" "ds_read_b64_tr_b16 v[138:139], v200 offset:8320\n" "ds_read_b64_tr_b16 v[140:141], v200 offset:12288\n" \
  "ds_read_b64_tr_b16 v[142:143], v200 offset:12416\n" "v_xor_b32 v200, 48, v218\n" "ds_read_b64_tr_b16 v[144:145], v200 offset:8192\n" "ds_read_b64_tr_b16 v[146:147], v200 offset:8320\n" \
  "ds_read_b64_tr_b16 v[148:149], v200 offset:12288\n" "ds_read_b64_tr_b16 v[150:151], v200 offset:12416\n" "s_waitcnt lgkmcnt(0)\n" "s_waitcnt vmcnt(0)\n" \
  "ds_read_b64_tr_b16 v[32:33], v219 offset:0\n" "ds_read_b64_tr_b16 v[34:35], v219 offset:32\n" "ds_read_b64_tr_b16 v[36:37], v219 offset:1024\n" \
  "ds_read_b64_tr_b16 v[38:39], v219 offset:1056\n" "ds_read_b64_tr_b16 v[72:73], v219 offset:2048\n" "ds_read_b64_tr_b16 v[74:75], v219 offset:2080\n" \
  "ds_read_b64_tr_b16 v[76:77], v219 offset:3072\n" "ds_read_b64_tr_b16 v[78:79], v219 offset:3104\n" "ds_read_b64_tr_b16 v[112:113], v219 offset:4096\n" \
  "ds_read_b64_tr_b16 v[114:115], v219 offset:4128\n" "ds_read_b64_tr_b16 v[116:117], v219 offset:5120\n" "ds_read_b64_tr_b16 v[118:119], v219 offset:5152\n" \
  "ds_read_b64_tr_b16 v[152:153], v219 offset:6144\n" "ds_read_b64_tr_b16 v[154:155], v219 offset:6176\n" "ds_read_b64_tr_b16 v[156:157], v219 offset:7168\n" \
  "ds_read_b64_tr_b16 v[158:159], v219 offset:7200\n" "s_waitcnt lgkmcnt(0)\n"
#define CH_ST1 \
  "v_xor_b32 v217, 64, v212\n" "v_add_u32 v200, 36864, v211\n" "v_xor_b32 v201, 32, v200\n" "s_waitcnt vmcnt(10)\n" "s_barrier\n" "ds_read_b64_tr_b16 v[128:129], v200 offset:0\n" \
  "ds_read_b64_tr_b16 v[130:131], v200 offset:2048\n" "ds_read_b64_tr_b16 v[132:133], v201 offset:0\n" "ds_read_b64_tr_b16 v[134:135], v201 offset:2048\n" \
  "ds_read_b64_tr_b16 v[136:137], v200 offset:16384\n" "ds_read_b64_tr_b16 v[138:139], v200 offset:18432\n" "ds_read_b64_tr_b16 v[140:141], v201 offset:16384\n" \
  "ds_read_b64_tr_b16 v[142:143], v201 offset:18432\n" "ds_read_b128 v[176:179], v212 offset:32768\n" "ds_read_b128 v[180:183], v212 offset:34816\n" \
  "ds_read_b128 v[184:187], v217 offset:32768\n" "ds_read_b128 v[188:191], v217 offset:34816\n" "s_waitcnt lgkmcnt(0)\n" "s_barrier\n" "s_add_i32 m0, s52, 36864\n" "s_mov_b32 s63, 2621440\n" \
  "buffer_load_dwordx4 v210, s[40:43], s63 offen lds\n" "s_add_i32 m0, s52, 45056\n" "s_mov_b32 s63, 2752512\n" "buffer_load_dwordx4 v210, s[40:43], s63 offen lds\n" \
  "s_add_i32 m0, s52, 53248\n" "s_mov_b32 s63, 2883584\n" "buffer_load_dwordx4 v210, s[40:43], s63 offen lds\n" "s_add_i32 m0, s52, 61440\n" "s_mov_b32 s63, 3014656\n" \
  "buffer_load_dwordx4 v210, s[40:43], s63 offen lds\n" "v_mfma_f32_16x16x32_bf16 v[144:147], v[128:131], v[176:179], 0\n" "v_mfma_f32_16x16x32_bf16 v[148:151], v[132:135], v[176:179], 0\n" \
  "v_mfma_f32_16x16x32_bf16 v[152:155], v[128:131], v[180:183], 0\n" "v_mfma_f32_16x16x32_bf16 v[156:159], v[132:135], v[180:183], 0\n" \
  "v_mfma_f32_16x16x32_bf16 v[144:147], v[136:139], v[184:187], v[144:147]\n" "v_mfma_f32_16x16x32_bf16 v[148:151], v[140:143], v[184:187], v[148:151]\n" \
  "v_mfma_f32_16x16x32_bf16 v[152:155], v[136:139], v[188:191], v[152:155]\n" "v_mfma_f32_16x16x32_bf16 v[156:159], v[140:143], v[188:191], v[156:159]\n" "v_add_u32 v200, 73728, v211\n" \
  "v_xor_b32 v201, 32, v200\n" "s_waitcnt vmcnt(10)\n" "s_barrier\n" "ds_read_b64_tr_b16 v[112:113], v200 offset:0\n" "ds_read_b64_tr_b16 v[114:115], v200 offset:2048\n" \
  "ds_read_b64_tr_b16 v[116:117], v201 offset:0\n" "ds_read_b64_tr_b16 v[118:119], v201 offset:2048\n" "ds_read_b64_tr_b16 v[120:121], v200 offset:16384\n" \
  "ds_read_b64_tr_b16 v[122:123], v200 offset:18432\n" "ds_read_b64_tr_b16 v[124:125], v201 offset:16384\n" "ds_read_b64_tr_b16 v[126:127], v201 offset:18432\n" \
  "ds_read_b128 v[176:179], v212 offset:28672\n" "ds_read_b128 v[180:183], v212 offset:30720\n" "ds_read_b128 v[184:187], v217 offset:28672\n" "ds_read_b128 v[188:191], v217 offset:30720\n" \
  "s_waitcnt lgkmcnt(0)\n" "s_barrier\n" "s_add_i32 m0, s52, 73728\n" "s_mov_b32 s63, 2097152\n" "buffer_load_dwordx4 v210, s[40:43], s63 offen lds\n" "s_add_i32 m0, s52, 81920\n" \
  "s_mov_b32 s63, 2228224\n" "buffer_load_dwordx4 v210, s[40:43], s63 offen lds\n" "s_add_i32 m0, s52, 90112\n" "s_mov_b32 s63, 2359296\n" "buffer_load_dwordx4 v210, s[40:43], s63 offen lds\n" \
  "s_add_i32 m0, s52, 98304\n" "s_mov_b32 s63, 2490368\n" "buffer_load_dwordx4 v210, s[40:43], s63 offen lds\n" "v_mfma_f32_16x16x32_bf16 v[144:147], v[112:115], v[176:179], v[144:147]\n" \
  "v_mfma_f32_16x16x32_bf16 v[148:151], v[116:119], v[176:179], v[148:151]\n" "v_mfma_f32_16x16x32_bf16 v[152:155], v[112:115], v[180:183], v[152:155]\n" \
  "v_mfma_f32_16x16x32_bf16 v[156:159], v[116:119], v[180:183], v[156:159]\n" "v_mfma_f32_16x16x32_bf16 v[144:147], v[120:123], v[184:187], v[144:147]\n" \
  "v_mfma_f32_16x16x32_bf16 v[148:151], v[124:127], v[184:187], v[148:151]\n" "v_mfma_f32_16x16x32_bf16 v[152:155], v[120:123], v[188:191], v[152:155]\n" \
  "v_mfma_f32_16x16x32_bf16 v[156:159], v[124:127], v[188:191], v[156:159]\n" "v_add_u32 v200, 110592, v211\n" "v_xor_b32 v201, 32, v200\n" "s_waitcnt vmcnt(8)\n" "s_barrier\n" \
  "ds_read_b64_tr_b16 v[96:97], v200 offset:0\n" "ds_read_b64_tr_b16 v[98:99], v200 offset:2048\n" "ds_read_b64_tr_b16 v[100:101], v201 offset:0\n" \
  "ds_read_b64_tr_b16 v[102:103], v201 offset:2048\n" "ds_read_b64_tr_b16 v[104:105], v200 offset:16384\n" "ds_read_b64_tr_b16 v[106:107], v200 offset:18432\n" \
  "ds_read_b64_tr_b16 v[108:109], v201 offset:16384\n" "ds_read_b64_tr_b16 v[110:111], v201 offset:18432\n" "ds_read_b128 v[176:179], v212 offset:24576\n" \
  "ds_read_b128 v[180:183], v212 offset:26624\n" "ds_read_b128 v[184:187], v217 offset:24576\n" "ds_read_b128 v[188:191], v217 offset:26624\n" "s_waitcnt lgkmcnt(0)\n" "s_barrier\n" \
  "s_add_i32 m0, s52, 110592\n" "s_mov_b32 s63, 1572864\n" "buffer_load_dwordx4 v210, s[40:43], s63 offen lds\n" "s_add_i32 m0, s52, 118784\n" "s_mov_b32 s63, 1703936\n" \
  "buffer_load_dwordx4 v210, s[40:43], s63 offen lds\n" "s_add_i32 m0, s52, 126976\n" "s_mov_b32 s63, 1835008\n" "buffer_load_dwordx4 v210, s[40:43], s63 offen lds\n" \
  "s_add_i32 m0, s52, 135168\n" "s_mov_b32 s63, 1966080\n" "buffer_load_dwordx4 v210, s[40:43], s63 offen lds\n" "v_mfma_f32_16x16x32_bf16 v[144:147], v[96:99], v[176:179], v[144:147]\n" \
  "v_mfma_f32_16x16x32_bf16 v[148:151], v[100:103], v[176:179], v[148:151]\n" "v_mfma_f32_16x16x32_bf16 v[152:155], v[96:99], v[180:183], v[152:155]\n" \
  "v_mfma_f32_16x16x32_bf16 v[156:159], v[100:103], v[180:183], v[156:159]\n" "v_mfma_f32_16x16x32_bf16 v[144:147], v[104:107], v[184:187], v[144:147]\n" \
  "v_mfma_f32_16x16x32_bf16 v[148:151], v[108:111], v[184:187], v[148:151]\n" "v_mfma_f32_16x16x32_bf16 v[152:155], v[104:107], v[188:191], v[152:155]\n" \
  "v_mfma_f32_16x16x32_bf16 v[156:159], v[108:111], v[188:191], v[156:159]\n" "v_add_u32 v200, 36864, v211\n" "v_xor_b32 v201, 32, v200\n" "s_waitcnt vmcnt(8)\n" "s_barrier\n" \
  "ds_read_b64_tr_b16 v[80:81], v200 offset:0\n" "ds_read_b64_tr_b16 v[82:83], v200 offset:2048\n" "ds_read_b64_tr_b16 v[84:85], v201 offset:0\n" \
  "ds_read_b64_tr_b16 v[86:87], v201 offset:2048\n" "ds_read_b64_tr_b16 v[88:89], v200 offset:16384\n" "ds_read_b64_tr_b16 v[90:91], v200 offset:18432\n" \
  "ds_read_b64_tr_b16 v[92:93], v201 offset:16384\n" "ds_read_b64_tr_b16 v[94:95], v201 offset:18432\n" "ds_read_b128 v[176:179], v212 offset:20480\n" \
  "ds_read_b128 v[180:183], v212 offset:22528\n" "ds_read_b128 v[184:187], v217 offset:20480\n" "ds_read_b128 v[188:191], v217 offset:22528\n" "s_waitcnt lgkmcnt(0)\n" "s_barrier\n" \
  "s_add_i32 m0, s52, 36864\n" "s_mov_b32 s63, 1048576\n" "buffer_load_dwordx4 v210, s[40:43], s63 offen lds\n" "s_add_i32 m0, s52, 45056\n" "s_mov_b32 s63, 1179648\n" \
  "buffer_load_dwordx4 v210, s[40:43], s63 offen lds\n" "s_add_i32 m0, s52, 53248\n" "s_mov_b32 s63, 1310720\n" "buffer_load_dwordx4 v210, s[40:43], s63 offen lds\n" \
  "s_add_i32 m0, s52, 61440\n" "s_mov_b32 s63, 1441792\n" "buffer_load_dwordx4 v210, s[40:43], s63 offen lds\n" "v_mfma_f32_16x16x32_bf16 v[144:147], v[80:83], v[176:179], v[144:147]\n" \
  "v_mfma_f32_16x16x32_bf16 v[148:151], v[84:87], v[176:179], v[148:151]\n" "v_mfma_f32_16x16x32_bf16 v[152:155], v[80:83], v[180:183], v[152:155]\n" \
  "v_mfma_f32_16x16x32_bf16 v[156:159], v[84:87], v[180:183], v[156:159]\n" "v_mfma_f32_16x16x32_bf16 v[144:147], v[88:91], v[184:187], v[144:147]\n" \
  "v_mfma_f32_16x16x32_bf16 v[148:151], v[92:95], v[184:187], v[148:151]\n" "v_mfma_f32_16x16x32_bf16 v[152:155], v[88:91], v[188:191], v[152:155]\n" \
  "v_mfma_f32_16x16x32_bf16 v[156:159], v[92:95], v[188:191], v[156:159]\n" "v_add_u32 v200, 73728, v211\n" "v_xor_b32 v201, 32, v200\n" "s_waitcnt vmcnt(8)\n" "s_barrier\n" \
  "ds_read_b64_tr_b16 v[64:65], v200 offset:0\n" "ds_read_b64_tr_b16 v[66:67], v200 offset:2048\n" "ds_read_b64_tr_b16 v[68:69], v201 offset:0\n" \
  "ds_read_b64_tr_b16 v[70:71], v201 offset:2048\n" "ds_read_b64_tr_b16 v[72:73], v200 offset:16384\n" "ds_read_b64_tr_b16 v[74:75], v200 offset:18432\n" \
  "ds_read_b64_tr_b16 v[76:77], v201 offset:16384\n" "ds_read_b64_tr_b16 v[78:79], v201 offset:18432\n" "ds_read_b128 v[176:179], v212 offset:16384\n" \
  "ds_read_b128 v[180:183], v212 offset:18432\n" "ds_read_b128 v[184:187], v217 offset:16384\n" "ds_read_b128 v[188:191], v217 offset:18432\n" "s_waitcnt lgkmcnt(0)\n" "s_barrier\n" \
  "s_add_i32 m0, s52, 73728\n" "s_mov_b32 s63, 524288\n" "buffer_load_dwordx4 v210, s[40:43], s63 offen lds\n" "s_add_i32 m0, s52, 81920\n" "s_mov_b32 s63, 655360\n" \
  "buffer_load_dwordx4 v210, s[40:43], s63 offen lds\n" "s_add_i32 m0, s52, 90112\n" "s_mov_b32 s63, 786432\n" "buffer_load_dwordx4 v210, s[40:43], s63 offen lds\n" "s_add_i32 m0, s52, 98304\n" \
  "s_mov_b32 s63, 917504\n" "buffer_load_dwordx4 v210, s[40:43], s63 offen lds\n" "v_mfma_f32_16x16x32_bf16 v[144:147], v[64:67], v[176:179], v[144:147]\n" \
  "v_mfma_f32_16x16x32_bf16 v[148:151], v[68:71], v[176:179], v[148:151]\n" "v_mfma_f32_16x16x32_bf16 v[152:155], v[64:67], v[180:183], v[152:155]\n" \
  "v_mfma_f32_16x16x32_bf16 v[156:159], v[68:71], v[180:183], v[156:159]\n" "v_mfma_f32_16x16x32_bf16 v[144:147], v[72:75], v[184:187], v[144:147]\n" \
  "v_mfma_f32_16x16x32_bf16 v[148:151], v[76:79], v[184:187], v[148:151]\n" "v_mfma_f32_16x16x32_bf16 v[152:155], v[72:75], v[188:191], v[152:155]\n" \
  "v_mfma_f32_16x16x32_bf16 v[156:159], v[76:79], v[188:191], v[156:159]\n" "v_add_u32 v200, 110592, v211\n" "v_xor_b32 v201, 32, v200\n" "s_waitcnt vmcnt(8)\n" "s_barrier\n" \
  "ds_read_b64_tr_b16 v[48:49], v200 offset:0\n" "ds_read_b64_tr_b16 v[50:51], v200 offset:2048\n" "ds_read_b64_tr_b16 v[52:53], v201 offset:0\n" \
  "ds_read_b64_tr_b16 v[54:55], v201 offset:2048\n" "ds_read_b64_tr_b16 v[56:57], v200 offset:16384\n" "ds_read_b64_tr_b16 v[58:59], v200 offset:18432\n" \
  "ds_read_b64_tr_b16 v[60:61], v201 offset:16384\n" "ds_read_b64_tr_b16 v[62:63], v201 offset:18432\n" "ds_read_b128 v[176:179], v212 offset:12288\n" \
  "ds_read_b128 v[180:183], v212 offset:14336\n" "ds_read_b128 v[184:187], v217 offset:12288\n" "ds_read_b128 v[188:191], v217 offset:14336\n" "s_waitcnt lgkmcnt(0)\n" "s_barrier\n" \
  "s_add_i32 m0, s52, 110592\n" "s_mov_b32 s63, 0\n" "buffer_load_dwordx4 v210, s[40:43], s63 offen lds\n" "s_add_i32 m0, s52, 118784\n" "s_mov_b32 s63, 131072\n" \
  "buffer_load_dwordx4 v210, s[40:43], s63 offen lds\n" "s_add_i32 m0, s52, 126976\n" "s_mov_b32 s63, 262144\n" "buffer_load_dwordx4 v210, s[40:43], s63 offen lds\n" \
  "s_add_i32 m0, s52, 135168\n" "s_mov_b32 s63, 393216\n" "buffer_load_dwordx4 v210, s[40:43], s63 offen lds\n" "v_mfma_f32_16x16x32_bf16 v[144:147], v[48:51], v[176:179], v[144:147]\n" \
  "v_mfma_f32_16x16x32_bf16 v[148:151], v[52:55], v[176:179], v[148:151]\n" "v_mfma_f32_16x16x32_bf16 v[152:155], v[48:51], v[180:183], v[152:155]\n" \
  "v_mfma_f32_16x16x32_bf16 v[156:159], v[52:55], v[180:183], v[156:159]\n" "v_mfma_f32_16x16x32_bf16 v[144:147], v[56:59], v[184:187], v[144:147]\n" \
  "v_mfma_f32_16x16x32_bf16 v[148:151], v[60:63], v[184:187], v[148:151]\n" "v_mfma_f32_16x16x32_bf16 v[152:155], v[56:59], v[188:191], v[152:155]\n" \
  "v_mfma_f32_16x16x32_bf16 v[156:159], v[60:63], v[188:191], v[156:159]\n" "v_add_u32 v200, 36864, v211\n" "v_xor_b32 v201, 32, v200\n" "s_waitcnt vmcnt(8)\n" "s_barrier\n" \
  "ds_read_b64_tr_b16 v[32:33], v200 offset:0\n" "ds_read_b64_tr_b16 v[34:35], v200 offset:2048\n" "ds_read_b64_tr_b16 v[36:37], v201 offset:0\n" \
  "ds_read_b64_tr_b16 v[38:39], v201 offset:2048\n" "ds_read_b64_tr_b16 v[40:41], v200 offset:16384\n" "ds_read_b64_tr_b16 v[42:43], v200 offset:18432\n" \
  "ds_read_b64_tr_b16 v[44:45], v201 offset:16384\n" "ds_read_b64_tr_b16 v[46:47], v201 offset:18432\n" "ds_read_b128 v[176:179], v212 offset:8192\n" \
  "ds_read_b128 v[180:183], v212 offset:10240\n" "ds_read_b128 v[184:187], v217 offset:8192\n" "ds_read_b128 v[188:191], v217 offset:10240\n" "s_waitcnt lgkmcnt(0)\n" "s_barrier\n" \
  "buffer_load_dwordx2 v[250:251], v213, s[48:51], 0 offen sc1\n" "s_add_i32 s61, s52, 36864\n" "s_mov_b32 s62, 36864\n" "s_add_i32 m0, s61, 0\n" "s_add_i32 s63, s62, 0\n" \
  "buffer_load_dwordx4 v208, s[44:47], s63 offen lds\n" "s_add_i32 m0, s61, 8192\n" "s_add_i32 s63, s62, 256\n" "buffer_load_dwordx4 v208, s[44:47], s63 offen lds\n" \
  "s_add_i32 m0, s61, 16384\n" "s_add_i32 s63, s62, 512\n" "buffer_load_dwordx4 v208, s[44:47], s63 offen lds\n" "s_add_i32 m0, s61, 24576\n" "s_add_i32 s63, s62, 768\n" \
  "buffer_load_dwordx4 v208, s[44:47], s63 offen lds\n" "s_add_i32 m0, s61, s54\n" "s_add_i32 s63, s62, 1024\n" "buffer_load_dwordx4 v209, s[44:47], s63 offen lds\n" \
  "v_mfma_f32_16x16x32_bf16 v[144:147], v[32:35], v[176:179], v[144:147]\n" "v_mfma_f32_16x16x32_bf16 v[148:151], v[36:39], v[176:179], v[148:151]\n" \
  "v_mfma_f32_16x16x32_bf16 v[152:155], v[32:35], v[180:183], v[152:155]\n" "v_mfma_f32_16x16x32_bf16 v[156:159], v[36:39], v[180:183], v[156:159]\n" \
  "v_mfma_f32_16x16x32_bf16 v[144:147], v[40:43], v[184:187], v[144:147]\n" "v_mfma_f32_16x16x32_bf16 v[148:151], v[44:47], v[184:187], v[148:151]\n" \
  "v_mfma_f32_16x16x32_bf16 v[152:155], v[40:43], v[188:191], v[152:155]\n" "v_mfma_f32_16x16x32_bf16 v[156:159], v[44:47], v[188:191], v[156:159]\n" "v_add_u32 v200, 73728, v211\n" \
  "v_xor_b32 v201, 32, v200\n" "s_waitcnt vmcnt(10)\n" "s_barrier\n" "ds_read_b64_tr_b16 v[16:17], v200 offset:0\n" "ds_read_b64_tr_b16 v[18:19], v200 offset:2048\n" \
  "ds_read_b64_tr_b16 v[20:21], v201 offset:0\n" "ds_read_b64_tr_b16 v[22:23], v201 offset:2048\n" "ds_read_b64_tr_b16 v[24:25], v200 offset:16384\n" \
  "ds_read_b64_tr_b16 v[26:27], v200 offset:18432\n" "ds_read_b64_tr_b16 v[28:29], v201 offset:16384\n" "ds_read_b64_tr_b16 v[30:31], v201 offset:18432\n" \
  "ds_read_b128 v[176:179], v212 offset:4096\n" "ds_read_b128 v[180:183], v212 offset:6144\n" "ds_read_b128 v[184:187], v217 offset:4096\n" "ds_read_b128 v[188:191], v217 offset:6144\n" \
  "s_waitcnt lgkmcnt(0)\n" "s_barrier\n" "s_add_i32 s61, s52, 73728\n" "s_mov_b32 s62, 73728\n" "s_add_i32 m0, s61, 0\n" "s_add_i32 s63, s62, 0\n" \
  "buffer_load_dwordx4 v208, s[44:47], s63 offen lds\n" "s_add_i32 m0, s61, 8192\n" "s_add_i32 s63, s62, 256\n" "buffer_load_dwordx4 v208, s[44:47], s63 offen lds\n" \
  "s_add_i32 m0, s61, 16384\n" "s_add_i32 s63, s62, 512\n" "buffer_load_dwordx4 v208, s[44:47], s63 offen lds\n" "s_add_i32 m0, s61, 24576\n" "s_add_i32 s63, s62, 768\n" \
  "buffer_load_dwordx4 v208, s[44:47], s63 offen lds\n" "s_add_i32 m0, s61, s54\n" "s_add_i32 s63, s62, 1024\n" "buffer_load_dwordx4 v209, s[44:47], s63 offen lds\n" \
  "v_mfma_f32_16x16x32_bf16 v[144:147], v[16:19], v[176:179], v[144:147]\n" "v_mfma_f32_16x16x32_bf16 v[148:151], v[20:23], v[176:179], v[148:151]\n" \
  "v_mfma_f32_16x16x32_bf16 v[152:155], v[16:19], v[180:183], v[152:155]\n" "v_mfma_f32_16x16x32_bf16 v[156:159], v[20:23], v[180:183], v[156:159]\n" \
  "v_mfma_f32_16x16x32_bf16 v[144:147], v[24:27], v[184:187], v[144:147]\n" "v_mfma_f32_16x16x32_bf16 v[148:151], v[28:31], v[184:187], v[148:151]\n" \
  "v_mfma_f32_16x16x32_bf16 v[152:155], v[24:27], v[188:191], v[152:155]\n" "v_mfma_f32_16x16x32_bf16 v[156:159], v[28:31], v[188:191], v[156:159]\n" "v_add_u32 v200, 110592, v211\n" \
  "v_xor_b32 v201, 32, v200\n" "s_waitcnt vmcnt(11)\n" "s_barrier\n" "ds_read_b64_tr_b16 v[0:1], v200 offset:0\n" "ds_read_b64_tr_b16 v[2:3], v200 offset:2048\n" \
  "ds_read_b64_tr_b16 v[4:5], v201 offset:0\n" "ds_read_b64_tr_b16 v[6:7], v201 offset:2048\n" "ds_read_b64_tr_b16 v[8:9], v200 offset:16384\n" \
  "ds_read_b64_tr_b16 v[10:11], v200 offset:18432\n" "ds_read_b64_tr_b16 v[12:13], v201 offset:16384\n" "ds_read_b64_tr_b16 v[14:15], v201 offset:18432\n" \
  "ds_read_b128 v[176:179], v212 offset:0\n" "ds_read_b128 v[180:183], v212 offset:2048\n" "ds_read_b128 v[184:187], v217 offset:0\n" "ds_read_b128 v[188:191], v217 offset:2048\n" \
  "s_waitcnt lgkmcnt(0)\n" "s_barrier\n" "s_add_i32 s61, s52, 110592\n" "s_mov_b32 s62, 110592\n" "s_add_i32 m0, s61, 0\n" "s_add_i32 s63, s62, 0\n" \
  "buffer_load_dwordx4 v208, s[44:47], s63 offen lds\n" "s_add_i32 m0, s61, 8192\n" "s_add_i32 s63, s62, 256\n" "buffer_load_dwordx4 v208, s[44:47], s63 offen lds\n" \
  "s_add_i32 m0, s61, 16384\n" "s_add_i32 s63, s62, 512\n" "buffer_load_dwordx4 v208, s[44:47], s63 offen lds\n" "s_add_i32 m0, s61, 24576\n" "s_add_i32 s63, s62, 768\n" \
  "buffer_load_dwordx4 v208, s[44:47], s63 offen lds\n" "s_add_i32 m0, s61, s54\n" "s_add_i32 s63, s62, 1024\n" "buffer_load_dwordx4 v209, s[44:47], s63 offen lds\n" \
  "v_mfma_f32_16x16x32_bf16 v[144:147], v[0:3], v[176:179], v[144:147]\n" "v_mfma_f32_16x16x32_bf16 v[148:151], v[4:7], v[176:179], v[148:151]\n" \
  "v_mfma_f32_16x16x32_bf16 v[152:155], v[0:3], v[180:183], v[152:155]\n" "v_mfma_f32_16x16x32_bf16 v[156:159], v[4:7], v[180:183], v[156:159]\n" \
  "v_mfma_f32_16x16x32_bf16 v[144:147], v[8:11], v[184:187], v[144:147]\n" "v_mfma_f32_16x16x32_bf16 v[148:151], v[12:15], v[184:187], v[148:151]\n" \
  "v_mfma_f32_16x16x32_bf16 v[152:155], v[8:11], v[188:191], v[152:155]\n" "v_mfma_f32_16x16x32_bf16 v[156:159], v[12:15], v[188:191], v[156:159]\n" "s_waitcnt vmcnt(15)\n" "s_nop 7\n"
#define CH_ST2 \
  "s_mov_b32 s56, 1\n" "s_mov_b32 s57, s52\n" "s_mov_b32 s58, 0\n" "s_mov_b32 s59, 0\n" "s_mov_b32 s64, 36864\n" "s_mov_b32 s65, -110592\n" "v_mov_b32 v216, 1\n" "v_xor_b32 v217, 64, v212\n" \
  "s_add_i32 s59, s59, 1\n" "s_and_b32 s59, s59, 3\n" "s_cselect_b32 s60, s64, s65\n" "v_add_u32 v212, s60, v212\n" "v_add_u32 v217, s60, v217\n" "s_waitcnt vmcnt(10)\n" "s_barrier\n" \
  "ds_read_b128 v[176:179], v212 offset:0\n" "ds_read_b128 v[180:183], v212 offset:2048\n" "ds_read_b128 v[184:187], v217 offset:0\n" "ds_read_b128 v[188:191], v217 offset:2048\n" \
  "ds_read_b128 v[192:195], v212 offset:4096\n" ".p2align 3\n" "ch_st2_loop:\n" "ds_read_b128 v[196:199], v212 offset:6144\n" "s_waitcnt lgkmcnt(5)\n" \
  "v_mfma_f32_16x16x32_bf16 v[160:163], v[0:3], v[176:179], 0\n" "v_mfma_f32_16x16x32_bf16 v[164:167], v[4:7], v[176:179], 0\n" "ds_read_b128 v[176:179], v217 offset:4096\n" \
  "s_waitcnt lgkmcnt(5)\n" "v_mfma_f32_16x16x32_bf16 v[168:171], v[0:3], v[180:183], 0\n" "v_cvt_pk_bf16_f32 v200, v144, v145\n" "v_mfma_f32_16x16x32_bf16 v[172:175], v[4:7], v[180:183], 0\n" \
  "v_cvt_pk_bf16_f32 v202, v148, v149\n" "ds_read_b128 v[180:183], v217 offset:6144\n" "s_waitcnt lgkmcnt(5)\n" "v_mfma_f32_16x16x32_bf16 v[160:163], v[8:11], v[184:187], v[160:163]\n" \
  "v_cvt_pk_bf16_f32 v201, v146, v147\n" "v_mfma_f32_16x16x32_bf16 v[164:167], v[12:15], v[184:187], v[164:167]\n" "v_cvt_pk_bf16_f32 v203, v150, v151\n" \
  "ds_read_b128 v[184:187], v212 offset:8192\n" "s_waitcnt lgkmcnt(5)\n" "v_mfma_f32_16x16x32_bf16 v[168:171], v[8:11], v[188:191], v[168:171]\n" \
  "v_mfma_f32_16x16x32_bf16 v[172:175], v[12:15], v[188:191], v[172:175]\n" "v_permlane16_swap_b32 v200, v202\n" "ds_read_b128 v[188:191], v212 offset:10240\n" "s_waitcnt lgkmcnt(5)\n" \
  "v_mfma_f32_16x16x32_bf16 v[160:163], v[16:19], v[192:195], v[160:163]\n" "v_permlane16_swap_b32 v201, v203\n" "v_mfma_f32_16x16x32_bf16 v[164:167], v[20:23], v[192:195], v[164:167]\n" \
  "buffer_store_dwordx4 v[200:203], v214, s[40:43], s58 offen offset:0 sc1\n" "ds_read_b128 v[192:195], v217 offset:8192\n" "s_waitcnt lgkmcnt(5)\n" \
  "v_mfma_f32_16x16x32_bf16 v[168:171], v[16:19], v[196:199], v[168:171]\n" "v_cvt_pk_bf16_f32 v204, v152, v153\n" "v_mfma_f32_16x16x32_bf16 v[172:175], v[20:23], v[196:199], v[172:175]\n" \
  "v_cvt_pk_bf16_f32 v206, v156, v157\n" "ds_read_b128 v[196:199], v217 offset:10240\n" "s_waitcnt lgkmcnt(5)\n" "v_mfma_f32_16x16x32_bf16 v[160:163], v[24:27], v[176:179], v[160:163]\n" \
  "v_cvt_pk_bf16_f32 v205, v154, v155\n" "v_mfma_f32_16x16x32_bf16 v[164:167], v[28:31], v[176:179], v[164:167]\n" "v_cvt_pk_bf16_f32 v207, v158, v159\n" \
  "ds_read_b128 v[176:179], v212 offset:12288\n" "s_waitcnt lgkmcnt(5)\n" "v_mfma_f32_16x16x32_bf16 v[168:171], v[24:27], v[180:183], v[168:171]\n" \
  "v_mfma_f32_16x16x32_bf16 v[172:175], v[28:31], v[180:183], v[172:175]\n" "v_permlane16_swap_b32 v204, v206\n" "ds_read_b128 v[180:183], v212 offset:14336\n" "s_waitcnt lgkmcnt(5)\n" \
  "v_mfma_f32_16x16x32_bf16 v[160:163], v[32:35], v[184:187], v[160:163]\n" "v_permlane16_swap_b32 v205, v207\n" "v_mfma_f32_16x16x32_bf16 v[164:167], v[36:39], v[184:187], v[164:167]\n" \
  "buffer_store_dwordx4 v[204:207], v214, s[40:43], s58 offen offset:256 sc1\n" "ds_read_b128 v[184:187], v217 offset:12288\n" "s_waitcnt lgkmcnt(5)\n" \
  "v_mfma_f32_16x16x32_bf16 v[168:171], v[32:35], v[188:191], v[168:171]\n" "v_mfma_f32_16x16x32_bf16 v[172:175], v[36:39], v[188:191], v[172:175]\n" \
  "ds_read_b128 v[188:191], v217 offset:14336\n" "s_waitcnt lgkmcnt(5)\n" "v_mfma_f32_16x16x32_bf16 v[160:163], v[40:43], v[192:195], v[160:163]\n" \
  "v_mfma_f32_16x16x32_bf16 v[164:167], v[44:47], v[192:195], v[164:167]\n" "ds_read_b128 v[192:195], v212 offset:16384\n" "s_waitcnt lgkmcnt(5)\n" \
  "v_mfma_f32_16x16x32_bf16 v[168:171], v[40:43], v[196:199], v[168:171]\n" "v_mfma_f32_16x16x32_bf16 v[172:175], v[44:47], v[196:199], v[172:175]\n" \
  "ds_read_b128 v[196:199], v212 offset:18432\n" "s_waitcnt lgkmcnt(5)\n" "v_mfma_f32_16x16x32_bf16 v[160:163], v[48:51], v[176:179], v[160:163]\n" "s_waitcnt vmcnt(7)\n" "s_barrier\n" \
  "s_add_i32 s61, s56, 3\n" "s_mul_i32 s62, s61, 36864\n" "v_mfma_f32_16x16x32_bf16 v[164:167], v[52:55], v[176:179], v[164:167]\n" "ds_read_b128 v[176:179], v217 offset:16384\n" \
  "s_waitcnt lgkmcnt(5)\n" "v_mfma_f32_16x16x32_bf16 v[168:171], v[48:51], v[180:183], v[168:171]\n" "s_add_i32 m0, s57, 0\n" "s_add_i32 s63, s62, 0\n" \
  "buffer_load_dwordx4 v208, s[44:47], s63 offen lds\n" "v_mfma_f32_16x16x32_bf16 v[172:175], v[52:55], v[180:183], v[172:175]\n" "ds_read_b128 v[180:183], v217 offset:18432\n" \
  "s_waitcnt lgkmcnt(5)\n" "v_mfma_f32_16x16x32_bf16 v[160:163], v[56:59], v[184:187], v[160:163]\n" "v_mfma_f32_16x16x32_bf16 v[164:167], v[60:63], v[184:187], v[164:167]\n" \
  "ds_read_b128 v[184:187], v212 offset:20480\n" "s_waitcnt lgkmcnt(5)\n" "v_mfma_f32_16x16x32_bf16 v[168:171], v[56:59], v[188:191], v[168:171]\n" "s_add_i32 m0, s57, 8192\n" \
  "s_add_i32 s63, s62, 256\n" "buffer_load_dwordx4 v208, s[44:47], s63 offen lds\n" "v_mfma_f32_16x16x32_bf16 v[172:175], v[60:63], v[188:191], v[172:175]\n" \
  "ds_read_b128 v[188:191], v212 offset:22528\n" "s_waitcnt lgkmcnt(5)\n" "v_mfma_f32_16x16x32_bf16 v[160:163], v[64:67], v[192:195], v[160:163]\n" \
  "v_mfma_f32_16x16x32_bf16 v[164:167], v[68:71], v[192:195], v[164:167]\n" "ds_read_b128 v[192:195], v217 offset:20480\n" "s_waitcnt lgkmcnt(5)\n" \
  "v_mfma_f32_16x16x32_bf16 v[168:171], v[64:67], v[196:199], v[168:171]\n" "s_add_i32 m0, s57, 16384\n" "s_add_i32 s63, s62, 512\n" "buffer_load_dwordx4 v208, s[44:47], s63 offen lds\n" \
  "v_mfma_f32_16x16x32_bf16 v[172:175], v[68:71], v[196:199], v[172:175]\n" "ds_read_b128 v[196:199], v217 offset:22528\n" "s_waitcnt lgkmcnt(5)\n" \
  "v_mfma_f32_16x16x32_bf16 v[160:163], v[72:75], v[176:179], v[160:163]\n" "v_mfma_f32_16x16x32_bf16 v[164:167], v[76:79], v[176:179], v[164:167]\n" \
  "ds_read_b128 v[176:179], v212 offset:24576\n" "s_waitcnt lgkmcnt(5)\n" "v_mfma_f32_16x16x32_bf16 v[168:171], v[72:75], v[180:183], v[168:171]\n" "s_add_i32 m0, s57, 24576\n" \
  "s_add_i32 s63, s62, 768\n" "buffer_load_dwordx4 v208, s[44:47], s63 offen lds\n" "v_mfma_f32_16x16x32_bf16 v[172:175], v[76:79], v[180:183], v[172:175]\n" \
  "ds_read_b128 v[180:183], v212 offset:26624\n" "s_waitcnt lgkmcnt(5)\n" "v_mfma_f32_16x16x32_bf16 v[160:163], v[80:83], v[184:187], v[160:163]\n" \
  "v_mfma_f32_16x16x32_bf16 v[164:167], v[84:87], v[184:187], v[164:167]\n" "ds_read_b128 v[184:187], v217 offset:24576\n" "s_waitcnt lgkmcnt(5)\n" \
  "v_mfma_f32_16x16x32_bf16 v[168:171], v[80:83], v[188:191], v[168:171]\n" "s_add_i32 m0, s57, s54\n" "s_add_i32 s63, s62, 1024\n" "buffer_load_dwordx4 v209, s[44:47], s63 offen lds\n" \
  "v_mfma_f32_16x16x32_bf16 v[172:175], v[84:87], v[188:191], v[172:175]\n" "ds_read_b128 v[188:191], v217 offset:26624\n" "s_waitcnt lgkmcnt(5)\n" \
  "v_mfma_f32_16x16x32_bf16 v[160:163], v[88:91], v[192:195], v[160:163]\n" "v_mfma_f32_16x16x32_bf16 v[164:167], v[92:95], v[192:195], v[164:167]\n" \
  "ds_read_b128 v[192:195], v212 offset:28672\n" "s_waitcnt lgkmcnt(5)\n" "v_mfma_f32_16x16x32_bf16 v[168:171], v[88:91], v[196:199], v[168:171]\n" \
  "v_mfma_f32_16x16x32_bf16 v[172:175], v[92:95], v[196:199], v[172:175]\n" "ds_read_b128 v[196:199], v212 offset:30720\n" "s_waitcnt lgkmcnt(5)\n" \
  "v_mfma_f32_16x16x32_bf16 v[160:163], v[96:99], v[176:179], v[160:163]\n" "v_mfma_f32_16x16x32_bf16 v[164:167], v[100:103], v[176:179], v[164:167]\n" \
  "ds_read_b128 v[176:179], v217 offset:28672\n" "s_waitcnt lgkmcnt(5)\n" "v_mfma_f32_16x16x32_bf16 v[168:171], v[96:99], v[180:183], v[168:171]\n" \
  "v_mfma_f32_16x16x32_bf16 v[172:175], v[100:103], v[180:183], v[172:175]\n" "ds_read_b128 v[180:183], v217 offset:30720\n" "s_waitcnt lgkmcnt(5)\n" \
  "v_mfma_f32_16x16x32_bf16 v[160:163], v[104:107], v[184:187], v[160:163]\n" "v_mfma_f32_16x16x32_bf16 v[164:167], v[108:111], v[184:187], v[164:167]\n" \
  "ds_read_b128 v[184:187], v212 offset:32768\n" "s_waitcnt lgkmcnt(5)\n" "v_mfma_f32_16x16x32_bf16 v[168:171], v[104:107], v[188:191], v[168:171]\n" "s_lshl_b32 s58, s56, 19\n" \
  "s_lshl_b32 s60, s56, 11\n" "s_and_b32 s60, s60, 0x2000\n" "v_mfma_f32_16x16x32_bf16 v[172:175], v[108:111], v[188:191], v[172:175]\n" "ds_read_b128 v[188:191], v212 offset:34816\n" \
  "s_waitcnt lgkmcnt(5)\n" "v_mfma_f32_16x16x32_bf16 v[160:163], v[112:115], v[192:195], v[160:163]\n" "s_add_i32 s58, s58, s60\n" "s_and_b32 s60, s56, 3\n" "s_cselect_b32 s60, s64, s65\n" \
  "v_mfma_f32_16x16x32_bf16 v[164:167], v[116:119], v[192:195], v[164:167]\n" "ds_read_b128 v[192:195], v217 offset:32768\n" "s_waitcnt lgkmcnt(5)\n" \
  "v_mfma_f32_16x16x32_bf16 v[168:171], v[112:115], v[196:199], v[168:171]\n" "s_add_i32 s57, s57, s60\n" "s_add_i32 s56, s56, 1\n" \
  "v_mfma_f32_16x16x32_bf16 v[172:175], v[116:119], v[196:199], v[172:175]\n" "ds_read_b128 v[196:199], v217 offset:34816\n" "s_waitcnt lgkmcnt(5)\n" \
  "v_mfma_f32_16x16x32_bf16 v[160:163], v[120:123], v[176:179], v[160:163]\n" "v_mfma_f32_16x16x32_bf16 v[164:167], v[124:127], v[176:179], v[164:167]\n" "s_add_i32 s59, s59, 1\n" \
  "s_and_b32 s59, s59, 3\n" "s_cselect_b32 s60, s64, s65\n" "v_add_u32 v212, s60, v212\n" "v_add_u32 v217, s60, v217\n" "ds_read_b128 v[176:179], v212 offset:0\n" "s_waitcnt lgkmcnt(5)\n" \
  "v_mfma_f32_16x16x32_bf16 v[168:171], v[120:123], v[180:183], v[168:171]\n" "v_mfma_f32_16x16x32_bf16 v[172:175], v[124:127], v[180:183], v[172:175]\n" \
  "ds_read_b128 v[180:183], v212 offset:2048\n" "s_waitcnt lgkmcnt(5)\n" "v_mfma_f32_16x16x32_bf16 v[160:163], v[128:131], v[184:187], v[160:163]\n" \
  "v_mfma_f32_16x16x32_bf16 v[164:167], v[132:135], v[184:187], v[164:167]\n" "ds_read_b128 v[184:187], v217 offset:0\n" "s_waitcnt lgkmcnt(5)\n" \
  "v_mfma_f32_16x16x32_bf16 v[168:171], v[128:131], v[188:191], v[168:171]\n" "v_mfma_f32_16x16x32_bf16 v[172:175], v[132:135], v[188:191], v[172:175]\n" \
  "ds_read_b128 v[188:191], v217 offset:2048\n" "s_waitcnt lgkmcnt(5)\n" "v_mfma_f32_16x16x32_bf16 v[160:163], v[136:139], v[192:195], v[160:163]\n" "s_waitcnt vmcnt(5)\n" \
  "buffer_atomic_add v216, v215, s[48:51], s53 offen\n" "s_add_i32 s53, s53, 4\n" "v_mfma_f32_16x16x32_bf16 v[164:167], v[140:143], v[192:195], v[164:167]\n" \
  "ds_read_b128 v[192:195], v212 offset:4096\n" "s_waitcnt lgkmcnt(5)\n" "v_mfma_f32_16x16x32_bf16 v[168:171], v[136:139], v[196:199], v[168:171]\n" \
  "v_mfma_f32_16x16x32_bf16 v[172:175], v[140:143], v[196:199], v[172:175]\n" "s_cmp_eq_u32 s56, 64\n" "s_cbranch_scc1 ch_st2_end\n" "ds_read_b128 v[196:199], v212 offset:6144\n" \
  "s_waitcnt lgkmcnt(5)\n" "v_mfma_f32_16x16x32_bf16 v[144:147], v[0:3], v[176:179], 0\n" "v_mfma_f32_16x16x32_bf16 v[148:151], v[4:7], v[176:179], 0\n" \
  "ds_read_b128 v[176:179], v217 offset:4096\n" "s_waitcnt lgkmcnt(5)\n" "v_mfma_f32_16x16x32_bf16 v[152:155], v[0:3], v[180:183], 0\n" "v_cvt_pk_bf16_f32 v200, v160, v161\n" \
  "v_mfma_f32_16x16x32_bf16 v[156:159], v[4:7], v[180:183], 0\n" "v_cvt_pk_bf16_f32 v202, v164, v165\n" "ds_read_b128 v[180:183], v217 offset:6144\n" "s_waitcnt lgkmcnt(5)\n" \
  "v_mfma_f32_16x16x32_bf16 v[144:147], v[8:11], v[184:187], v[144:147]\n" "v_cvt_pk_bf16_f32 v201, v162, v163\n" "v_mfma_f32_16x16x32_bf16 v[148:151], v[12:15], v[184:187], v[148:151]\n" \
  "v_cvt_pk_bf16_f32 v203, v166, v167\n" "ds_read_b128 v[184:187], v212 offset:8192\n" "s_waitcnt lgkmcnt(5)\n" "v_mfma_f32_16x16x32_bf16 v[152:155], v[8:11], v[188:191], v[152:155]\n" \
  "v_mfma_f32_16x16x32_bf16 v[156:159], v[12:15], v[188:191], v[156:159]\n" "v_permlane16_swap_b32 v200, v202\n" "ds_read_b128 v[188:191], v212 offset:10240\n" "s_waitcnt lgkmcnt(5)\n" \
  "v_mfma_f32_16x16x32_bf16 v[144:147], v[16:19], v[192:195], v[144:147]\n" "v_permlane16_swap_b32 v201, v203\n" "v_mfma_f32_16x16x32_bf16 v[148:151], v[20:23], v[192:195], v[148:151]\n" \
  "buffer_store_dwordx4 v[200:203], v214, s[40:43], s58 offen offset:0 sc1\n" "ds_read_b128 v[192:195], v217 offset:8192\n" "s_waitcnt lgkmcnt(5)\n" \
  "v_mfma_f32_16x16x32_bf16 v[152:155], v[16:19], v[196:199], v[152:155]\n" "v_cvt_pk_bf16_f32 v204, v168, v169\n" "v_mfma_f32_16x16x32_bf16 v[156:159], v[20:23], v[196:199], v[156:159]\n" \
  "v_cvt_pk_bf16_f32 v206, v172, v173\n" "ds_read_b128 v[196:199], v217 offset:10240\n" "s_waitcnt lgkmcnt(5)\n" "v_mfma_f32_16x16x32_bf16 v[144:147], v[24:27], v[176:179], v[144:147]\n" \
  "v_cvt_pk_bf16_f32 v205, v170, v171\n" "v_mfma_f32_16x16x32_bf16 v[148:151], v[28:31], v[176:179], v[148:151]\n" "v_cvt_pk_bf16_f32 v207, v174, v175\n" \
  "ds_read_b128 v[176:179], v212 offset:12288\n" "s_waitcnt lgkmcnt(5)\n" "v_mfma_f32_16x16x32_bf16 v[152:155], v[24:27], v[180:183], v[152:155]\n" \
  "v_mfma_f32_16x16x32_bf16 v[156:159], v[28:31], v[180:183], v[156:159]\n" "v_permlane16_swap_b32 v204, v206\n" "ds_read_b128 v[180:183], v212 offset:14336\n" "s_waitcnt lgkmcnt(5)\n" \
  "v_mfma_f32_16x16x32_bf16 v[144:147], v[32:35], v[184:187], v[144:147]\n" "v_permlane16_swap_b32 v205, v207\n" "v_mfma_f32_16x16x32_bf16 v[148:151], v[36:39], v[184:187], v[148:151]\n" \
  "buffer_store_dwordx4 v[204:207], v214, s[40:43], s58 offen offset:256 sc1\n" "ds_read_b128 v[184:187], v217 offset:12288\n" "s_waitcnt lgkmcnt(5)\n" \
  "v_mfma_f32_16x16x32_bf16 v[152:155], v[32:35], v[188:191], v[152:155]\n" "v_mfma_f32_16x16x32_bf16 v[156:159], v[36:39], v[188:191], v[156:159]\n" \
  "ds_read_b128 v[188:191], v217 offset:14336\n" "s_waitcnt lgkmcnt(5)\n" "v_mfma_f32_16x16x32_bf16 v[144:147], v[40:43], v[192:195], v[144:147]\n" \
  "v_mfma_f32_16x16x32_bf16 v[148:151], v[44:47], v[192:195], v[148:151]\n" "ds_read_b128 v[192:195], v212 offset:16384\n" "s_waitcnt lgkmcnt(5)\n" \
  "v_mfma_f32_16x16x32_bf16 v[152:155], v[40:43], v[196:199], v[152:155]\n" "v_mfma_f32_16x16x32_bf16 v[156:159], v[44:47], v[196:199], v[156:159]\n" \
  "ds_read_b128 v[196:199], v212 offset:18432\n" "s_waitcnt lgkmcnt(5)\n" "v_mfma_f32_16x16x32_bf16 v[144:147], v[48:51], v[176:179], v[144:147]\n" "s_waitcnt vmcnt(7)\n" "s_barrier\n" \
  "s_add_i32 s61, s56, 3\n" "s_mul_i32 s62, s61, 36864\n" "v_mfma_f32_16x16x32_bf16 v[148:151], v[52:55], v[176:179], v[148:151]\n" "ds_read_b128 v[176:179], v217 offset:16384\n" \
  "s_waitcnt lgkmcnt(5)\n" "v_mfma_f32_16x16x32_bf16 v[152:155], v[48:51], v[180:183], v[152:155]\n" "s_add_i32 m0, s57, 0\n" "s_add_i32 s63, s62, 0\n" \
  "buffer_load_dwordx4 v208, s[44:47], s63 offen lds\n" "v_mfma_f32_16x16x32_bf16 v[156:159], v[52:55], v[180:183], v[156:159]\n" "ds_read_b128 v[180:183], v217 offset:18432\n" \
  "s_waitcnt lgkmcnt(5)\n" "v_mfma_f32_16x16x32_bf16 v[144:147], v[56:59], v[184:187], v[144:147]\n" "v_mfma_f32_16x16x32_bf16 v[148:151], v[60:63], v[184:187], v[148:151]\n" \
  "ds_read_b128 v[184:187], v212 offset:20480\n" "s_waitcnt lgkmcnt(5)\n" "v_mfma_f32_16x16x32_bf16 v[152:155], v[56:59], v[188:191], v[152:155]\n" "s_add_i32 m0, s57, 8192\n" \
  "s_add_i32 s63, s62, 256\n" "buffer_load_dwordx4 v208, s[44:47], s63 offen lds\n" "v_mfma_f32_16x16x32_bf16 v[156:159], v[60:63], v[188:191], v[156:159]\n" \
  "ds_read_b128 v[188:191], v212 offset:22528\n" "s_waitcnt lgkmcnt(5)\n" "v_mfma_f32_16x16x32_bf16 v[144:147], v[64:67], v[192:195], v[144:147]\n" \
  "v_mfma_f32_16x16x32_bf16 v[148:151], v[68:71], v[192:195], v[148:151]\n" "ds_read_b128 v[192:195], v217 offset:20480\n" "s_waitcnt lgkmcnt(5)\n" \
  "v_mfma_f32_16x16x32_bf16 v[152:155], v[64:67], v[196:199], v[152:155]\n" "s_add_i32 m0, s57, 16384\n" "s_add_i32 s63, s62, 512\n" "buffer_load_dwordx4 v208, s[44:47], s63 offen lds\n" \
  "v_mfma_f32_16x16x32_bf16 v[156:159], v[68:71], v[196:199], v[156:159]\n" "ds_read_b128 v[196:199], v217 offset:22528\n" "s_waitcnt lgkmcnt(5)\n" \
  "v_mfma_f32_16x16x32_bf16 v[144:147], v[72:75], v[176:179], v[144:147]\n" "v_mfma_f32_16x16x32_bf16 v[148:151], v[76:79], v[176:179], v[148:151]\n" \
  "ds_read_b128 v[176:179], v212 offset:24576\n" "s_waitcnt lgkmcnt(5)\n" "v_mfma_f32_16x16x32_bf16 v[152:155], v[72:75], v[180:183], v[152:155]\n" "s_add_i32 m0, s57, 24576\n" \
  "s_add_i32 s63, s62, 768\n" "buffer_load_dwordx4 v208, s[44:47], s63 offen lds\n" "v_mfma_f32_16x16x32_bf16 v[156:159], v[76:79], v[180:183], v[156:159]\n" \
  "ds_read_b128 v[180:183], v212 offset:26624\n" "s_waitcnt lgkmcnt(5)\n" "v_mfma_f32_16x16x32_bf16 v[144:147], v[80:83], v[184:187], v[144:147]\n" \
  "v_mfma_f32_16x16x32_bf16 v[148:151], v[84:87], v[184:187], v[148:151]\n" "ds_read_b128 v[184:187], v217 offset:24576\n" "s_waitcnt lgkmcnt(5)\n" \
  "v_mfma_f32_16x16x32_bf16 v[152:155], v[80:83], v[188:191], v[152:155]\n" "s_add_i32 m0, s57, s54\n" "s_add_i32 s63, s62, 1024\n" "buffer_load_dwordx4 v209, s[44:47], s63 offen lds\n" \
  "v_mfma_f32_16x16x32_bf16 v[156:159], v[84:87], v[188:191], v[156:159]\n" "ds_read_b128 v[188:191], v217 offset:26624\n" "s_waitcnt lgkmcnt(5)\n" \
  "v_mfma_f32_16x16x32_bf16 v[144:147], v[88:91], v[192:195], v[144:147]\n" "v_mfma_f32_16x16x32_bf16 v[148:151], v[92:95], v[192:195], v[148:151]\n" \
  "ds_read_b128 v[192:195], v212 offset:28672\n" "s_waitcnt lgkmcnt(5)\n" "v_mfma_f32_16x16x32_bf16 v[152:155], v[88:91], v[196:199], v[152:155]\n" \
  "v_mfma_f32_16x16x32_bf16 v[156:159], v[92:95], v[196:199], v[156:159]\n" "ds_read_b128 v[196:199], v212 offset:30720\n" "s_waitcnt lgkmcnt(5)\n" \
  "v_mfma_f32_16x16x32_bf16 v[144:147], v[96:99], v[176:179], v[144:147]\n" "v_mfma_f32_16x16x32_bf16 v[148:151], v[100:103], v[176:179], v[148:151]\n" \
  "ds_read_b128 v[176:179], v217 offset:28672\n" "s_waitcnt lgkmcnt(5)\n" "v_mfma_f32_16x16x32_bf16 v[152:155], v[96:99], v[180:183], v[152:155]\n" \
  "v_mfma_f32_16x16x32_bf16 v[156:159], v[100:103], v[180:183], v[156:159]\n" "ds_read_b128 v[180:183], v217 offset:30720\n" "s_waitcnt lgkmcnt(5)\n" \
  "v_mfma_f32_16x16x32_bf16 v[144:147], v[104:107], v[184:187], v[144:147]\n" "v_mfma_f32_16x16x32_bf16 v[148:151], v[108:111], v[184:187], v[148:151]\n" \
  "ds_read_b128 v[184:187], v212 offset:32768\n" "s_waitcnt lgkmcnt(5)\n" "v_mfma_f32_16x16x32_bf16 v[152:155], v[104:107], v[188:191], v[152:155]\n" "s_lshl_b32 s58, s56, 19\n" \
  "s_lshl_b32 s60, s56, 11\n" "s_and_b32 s60, s60, 0x2000\n" "v_mfma_f32_16x16x32_bf16 v[156:159], v[108:111], v[188:191], v[156:159]\n" "ds_read_b128 v[188:191], v212 offset:34816\n" \
  "s_waitcnt lgkmcnt(5)\n" "v_mfma_f32_16x16x32_bf16 v[144:147], v[112:115], v[192:195], v[144:147]\n" "s_add_i32 s58, s58, s60\n" "s_and_b32 s60, s56, 3\n" "s_cselect_b32 s60, s64, s65\n" \
  "v_mfma_f32_16x16x32_bf16 v[148:151], v[116:119], v[192:195], v[148:151]\n" "ds_read_b128 v[192:195], v217 offset:32768\n" "s_waitcnt lgkmcnt(5)\n" \
  "v_mfma_f32_16x16x32_bf16 v[152:155], v[112:115], v[196:199], v[152:155]\n" "s_add_i32 s57, s57, s60\n" "s_add_i32 s56, s56, 1\n" \
  "v_mfma_f32_16x16x32_bf16 v[156:159], v[116:119], v[196:199], v[156:159]\n" "ds_read_b128 v[196:199], v217 offset:34816\n" "s_waitcnt lgkmcnt(5)\n" \
  "v_mfma_f32_16x16x32_bf16 v[144:147], v[120:123], v[176:179], v[144:147]\n" "v_mfma_f32_16x16x32_bf16 v[148:151], v[124:127], v[176:179], v[148:151]\n" "s_add_i32 s59, s59, 1\n" \
  "s_and_b32 s59, s59, 3\n" "s_cselect_b32 s60, s64, s65\n" "v_add_u32 v212, s60, v212\n" "v_add_u32 v217, s60, v217\n" "ds_read_b128 v[176:179], v212 offset:0\n" "s_waitcnt lgkmcnt(5)\n" \
  "v_mfma_f32_16x16x32_bf16 v[152:155], v[120:123], v[180:183], v[152:155]\n" "v_mfma_f32_16x16x32_bf16 v[156:159], v[124:127], v[180:183], v[156:159]\n" \
  "ds_read_b128 v[180:183], v212 offset:2048\n" "s_waitcnt lgkmcnt(5)\n" "v_mfma_f32_16x16x32_bf16 v[144:147], v[128:131], v[184:187], v[144:147]\n" \
  "v_mfma_f32_16x16x32_bf16 v[148:151], v[132:135], v[184:187], v[148:151]\n" "ds_read_b128 v[184:187], v217 offset:0\n" "s_waitcnt lgkmcnt(5)\n" \
  "v_mfma_f32_16x16x32_bf16 v[152:155], v[128:131], v[188:191], v[152:155]\n" "v_mfma_f32_16x16x32_bf16 v[156:159], v[132:135], v[188:191], v[156:159]\n" \
  "ds_read_b128 v[188:191], v217 offset:2048\n" "s_waitcnt lgkmcnt(5)\n" "v_mfma_f32_16x16x32_bf16 v[144:147], v[136:139], v[192:195], v[144:147]\n" "s_waitcnt vmcnt(5)\n" \
  "buffer_atomic_add v216, v215, s[48:51], s53 offen\n" "s_add_i32 s53, s53, 4\n" "v_mfma_f32_16x16x32_bf16 v[148:151], v[140:143], v[192:195], v[148:151]\n" \
  "ds_read_b128 v[192:195], v212 offset:4096\n" "s_waitcnt lgkmcnt(5)\n" "v_mfma_f32_16x16x32_bf16 v[152:155], v[136:139], v[196:199], v[152:155]\n" \
  "v_mfma_f32_16x16x32_bf16 v[156:159], v[140:143], v[196:199], v[156:159]\n" "s_branch ch_st2_loop\n" "ch_st2_end:\n" "s_nop 7\n" "v_cvt_pk_bf16_f32 v200, v160, v161\n" \
  "v_cvt_pk_bf16_f32 v202, v164, v165\n" "v_cvt_pk_bf16_f32 v201, v162, v163\n" "v_cvt_pk_bf16_f32 v203, v166, v167\n" "s_nop 1\n" "v_permlane16_swap_b32 v200, v202\n" \
  "v_permlane16_swap_b32 v201, v203\n" "buffer_store_dwordx4 v[200:203], v214, s[40:43], s58 offen offset:0 sc1\n" "v_cvt_pk_bf16_f32 v204, v168, v169\n" "v_cvt_pk_bf16_f32 v206, v172, v173\n" \
  "v_cvt_pk_bf16_f32 v205, v170, v171\n" "v_cvt_pk_bf16_f32 v207, v174, v175\n" "s_nop 1\n" "v_permlane16_swap_b32 v204, v206\n" "v_permlane16_swap_b32 v205, v207\n" \
  "buffer_store_dwordx4 v[204:207], v214, s[40:43], s58 offen offset:256 sc1\n" "s_waitcnt vmcnt(0) lgkmcnt(0)\n" "buffer_atomic_add v216, v215, s[48:51], s53 offen\n"
#define CH_HA \
  "s_setprio 2\n" "s_mov_b32 s56, 0\n" "s_mov_b32 s57, 0x3e0293ee\n" "s_mov_b32 s58, 0x40000\n" "s_mov_b32 s77, 0x84000\n" "s_mov_b32 s59, " XS(SPIN_CAP) "\n" "s_mov_b32 s68, 0x3f803f80\n" \
  "s_mov_b32 s69, s68\n" "s_mov_b32 s71, -18688\n" "s_barrier\n" "ds_read_b64 v[200:201], v189 offset:10248\n" "s_mov_b32 s60, 0\n" "s_lshl_b32 s61, s52, 11\n" "s_add_i32 s61, s61, s60\n" \
  "s_waitcnt lgkmcnt(0)\n" "v_add_u32 v200, s61, v200\n" "v_add_u32 v201, s60, v201\n" "ds_read_b128 v[240:243], v201\n" "ds_read_b128 v[244:247], v201 offset:4096\n" \
  "ds_read_b64_tr_b16 v[248:249], v200\n" "ds_read_b64_tr_b16 v[250:251], v200 offset:256\n" "ds_read_b64_tr_b16 v[252:253], v200 offset:1280\n" \
  "ds_read_b64_tr_b16 v[254:255], v200 offset:1024\n" "v_mov_b64 v[228:229], 0\n" "v_mov_b64 v[230:231], 0\n" "v_mov_b64 v[232:233], 0\n" "v_mov_b64 v[234:235], 0\n" "v_mov_b64 v[236:237], 0\n" \
  "v_mov_b64 v[238:239], 0\n" "s_waitcnt lgkmcnt(0)\n" ".p2align 6\n" "ch_ha_loop:\n" "s_nop 0\n" "s_barrier\n" ST0 "ds_read_b128 v[192:195], v185 offset:37120\n" \
  "ds_read_b128 v[196:199], v185 offset:38144\n" "ds_read_b128 v[200:203], v185 offset:39168\n" "ds_read_b128 v[204:207], v185 offset:40192\n" "ds_read_b128 v[208:211], v185 offset:41216\n" \
  "ds_read_b128 v[212:215], v185 offset:42240\n" "ds_read_b128 v[216:219], v185 offset:43264\n" "ds_read_b128 v[220:223], v185 offset:44288\n" "ds_read_b128 v[224:227], v185 offset:45312\n" \
  "s_nop 0\n" "s_waitcnt lgkmcnt(8)\n" "v_smfmac_f32_16x16x64_bf16 v[228:231], v[192:195], v[0:7], v184\n" "s_nop 0\n" "s_waitcnt lgkmcnt(7)\n" \
  "v_smfmac_f32_16x16x64_bf16 v[228:231], v[196:199], v[8:15], v184\n" "s_nop 0\n" "s_waitcnt lgkmcnt(6)\n" "v_smfmac_f32_16x16x64_bf16 v[228:231], v[200:203], v[16:23], v184\n" "s_nop 0\n" \
  "s_waitcnt lgkmcnt(5)\n" "v_smfmac_f32_16x16x64_bf16 v[228:231], v[204:207], v[24:31], v184\n" "s_nop 0\n" "s_waitcnt lgkmcnt(4)\n" \
  "v_smfmac_f32_16x16x64_bf16 v[228:231], v[208:211], v[32:39], v184\n" "s_nop 0\n" "s_waitcnt lgkmcnt(3)\n" "v_smfmac_f32_16x16x64_bf16 v[228:231], v[212:215], v[40:47], v184\n" "s_nop 0\n" \
  "s_waitcnt lgkmcnt(2)\n" "v_smfmac_f32_16x16x64_bf16 v[228:231], v[216:219], v[48:55], v184\n" "s_nop 0\n" "s_waitcnt lgkmcnt(1)\n" \
  "v_smfmac_f32_16x16x64_bf16 v[228:231], v[220:223], v[56:63], v184\n" "s_nop 0\n" "s_waitcnt lgkmcnt(0)\n" "v_smfmac_f32_16x16x64_bf16 v[228:231], v[224:227], v[64:71], v184\n" ST1 \
  "v_smfmac_f32_16x16x64_bf16 v[232:235], v[220:223], v[128:135], v184\n" ST20 "v_smfmac_f32_16x16x64_bf16 v[232:235], v[224:227], v[136:143], v184\n" ST21 \
  "v_smfmac_f32_16x16x64_bf16 v[232:235], v[212:215], v[112:119], v184\n" "v_add_f32_e64 v228, v228, v230\n" ST22 "v_smfmac_f32_16x16x64_bf16 v[232:235], v[216:219], v[120:127], v184\n" \
  "v_add_f32_e64 v229, v229, v231\n" ST23 "v_smfmac_f32_16x16x64_bf16 v[232:235], v[192:195], v[72:79], v184\n" "v_cmp_u_f32_e64 vcc, v228, v229\n" ST24 \
  "v_smfmac_f32_16x16x64_bf16 v[232:235], v[196:199], v[80:87], v184\n" "v_cvt_pk_bf16_f32 v228, v228, v229\n" ST25 "v_smfmac_f32_16x16x64_bf16 v[232:235], v[200:203], v[88:95], v184\n" \
  "ds_write_b32 v187, v228 offset:128\n" "v_mov_b32_e64 v229, s56\n" ST26 "v_smfmac_f32_16x16x64_bf16 v[232:235], v[204:207], v[96:103], v184\n" "s_cbranch_vccnz ch_ha_slow\n" "ch_ha_back:\n" \
  "v_lshrrev_b32 v231, 1, v186\n" ST27 "v_smfmac_f32_16x16x64_bf16 v[232:235], v[208:211], v[104:111], v184\n" "v_mov_b64_e64 v[220:221], s[68:69]\n" ST28 ST2 \
  "v_smfmac_f32_16x16x64_bf16 v[236:239], v[192:195], v[144:151], v184\n" "v_mov_b64_e64 v[222:223], s[68:69]\n" "v_smfmac_f32_16x16x64_bf16 v[236:239], v[196:199], v[152:159], v184\n" \
  "v_add_f32_e64 v232, v232, v234\n" "v_smfmac_f32_16x16x64_bf16 v[236:239], v[200:203], v[160:167], v184\n" "v_add_f32_e64 v233, v233, v235\n" \
  "v_smfmac_f32_16x16x64_bf16 v[236:239], v[204:207], v[168:175], v184\n" "v_cvt_pk_bf16_f32 v232, v232, v233\n" "v_smfmac_f32_16x16x64_bf16 v[236:239], v[208:211], v[176:183], v184\n" \
  "ds_write2st64_b32 v187, v232, v229 offset1:24\n" ST3 "v_mov_b32_e64 v235, 0\n" ".rept " XS(QD) "\n" "s_nop 0\n" "s_nop 0\n" ".endr\n" "ch_ha_p1:\n" "ds_read_b32 v228, v190\n" \
  "ds_read_b64_tr_b16 v[212:213], v186 offset:0\n" "ds_read_b64_tr_b16 v[214:215], v186 offset:512\n" "ds_read_b64_tr_b16 v[216:217], v186 offset:1024\n" \
  "ds_read_b64_tr_b16 v[218:219], v186 offset:1536\n" "s_nop 0\n" "s_waitcnt lgkmcnt(4)\n" "v_cmp_ne_u32 vcc, s56, v228\n" "s_waitcnt lgkmcnt(2)\n" \
  "v_mfma_f32_16x16x32_bf16 v[224:227], v[240:243], v[212:215], 0\n" "s_nop 0\n" "s_waitcnt lgkmcnt(0)\n" "v_mfma_f32_16x16x32_bf16 v[224:227], v[244:247], v[216:219], v[224:227]\n" \
  "s_cmp_lg_u32 vcc_lo, 0\n" "s_cbranch_scc1 ch_ha_p1\n" ST4 "s_setprio 0\n" "s_nop 4\n" "v_add_f32_dpp v244, v226, v224 row_ror:8 row_mask:0xf bank_mask:0x3\n" \
  "v_add_f32_dpp v244, v225, v227 row_ror:8 row_mask:0xf bank_mask:0xc\n" "ds_write2st64_b32 v188, v244, v229 offset0:16 offset1:20\n" ST5 "v_add_f32_e64 v232, v236, v238\n" \
  "v_add_f32 v233, v237, v239\n" ".rept " XS(KD) "\n" "s_nop 0\n" "s_nop 0\n" ".endr\n" "s_setprio " XS(P2P) "\n" "ch_ha_p2:\n" ST10 "ds_read_b32 v228, v190 offset:2048\n" \
  "ds_read_b64_tr_b16 v[192:193], v186 offset:2048\n" "ds_read_b64_tr_b16 v[194:195], v186 offset:2560\n" "ds_read_b64_tr_b16 v[196:197], v186 offset:3072\n" \
  "ds_read_b64_tr_b16 v[198:199], v186 offset:3584\n" "ds_read2st64_b32 v[200:201], v231 offset0:240 offset1:241\n" "ds_read2st64_b32 v[202:203], v231 offset0:242 offset1:243\n" "s_setprio 2\n" \
  "s_waitcnt lgkmcnt(6)\n" "v_cmp_ne_u32 vcc, s56, v228\n" "s_waitcnt lgkmcnt(4)\n" "v_mfma_f32_16x16x32_bf16 v[204:207], v[192:195], v[212:215], 0\n" "s_nop 0\n" "s_waitcnt lgkmcnt(2)\n" \
  "v_mfma_f32_16x16x32_bf16 v[204:207], v[196:199], v[216:219], v[204:207]\n" "s_cbranch_vccnz ch_ha_p2\n" ST6 "s_waitcnt lgkmcnt(0)\n" "v_max3_f32 v245, v200, v201, v202\n" "s_nop 0\n" \
  "s_nop 3\n" "v_add_f32_dpp v244, v206, v204 row_ror:8 row_mask:0xf bank_mask:0x3\n" "v_add_f32_dpp v244, v205, v207 row_ror:8 row_mask:0xf bank_mask:0xc\n" \
  "v_max3_f32 v245, v245, v203, v244\n" "s_nop 0\n" "s_nop 0\n" "v_max_f32_dpp v246, v245, v245 row_ror:8 row_mask:0xf bank_mask:0xf\n" \
  "v_max_f32_dpp v247, v245, v245 row_ror:8 row_mask:0xf bank_mask:0xf\n" "s_nop 1\n" "v_permlane16_swap_b32 v246, v247\n" "v_max_f32 v245, v246, v247\n" "v_max_f32 v247, v246, v247\n" \
  "s_nop 1\n" "v_permlane32_swap_b32 v245, v247\n" "v_max_f32 v245, v245, v247\n" "v_mul_f32 v245, s57, v245\n" "v_fma_f32 v244, v244, s57, -v245\n" "v_fma_f32 v200, v200, s57, -v245\n" \
  "v_exp_f32_e64 v244, v244\n" "v_fma_f32 v201, v201, s57, -v245\n" "ds_read_b128 v[208:211], v189 offset:14336\n" "v_exp_f32_e64 v200, v200\n" \
  "v_mov_b32_dpp v230, v244 row_ror:8 row_mask:0xf bank_mask:0xf\n" "v_exp_f32_e64 v201, v201\n" "v_cvt_pk_bf16_f32 v234, v244, v230\n" "v_fma_f32 v202, v202, s57, -v245\n" \
  "v_fma_f32 v203, v203, s57, -v245\n" "v_mfma_f32_16x16x16_bf16 v[240:243], v[220:221], v[234:235], 0\n" "v_exp_f32_e64 v202, v202\n" "v_exp_f32 v203, v203\n" "ch_ha_p3:\n" \
  "s_waitcnt lgkmcnt(0)\n" "v_mfma_f32_16x16x16_bf16 v[204:207], v[210:211], v[234:235], 0\n" "v_add_f32 v208, v208, v232\n" "v_add_f32 v209, v209, v233\n" \
  "v_cvt_pk_bf16_f32 v228, v208, v209\n" "v_cvt_pk_bf16_f32 v236, v200, v201\n" "v_cvt_pk_bf16_f32 v237, v202, v203\n" "v_mfma_f32_16x16x16_bf16 v[224:227], v[228:229], v[234:235], 0\n" \
  "v_mov_b32_dpp v238, v236 row_ror:8 row_mask:0xf bank_mask:0xf\n" "v_mov_b32_dpp v239, v237 row_ror:8 row_mask:0xf bank_mask:0xf\n" "v_cmp_ne_u32 vcc, s56, v211\n" "s_nop 0\n" ST7 \
  "v_mfma_f32_16x16x32_bf16 v[244:247], v[220:223], v[236:239], v[240:243]\n" "v_mfma_f32_16x16x32_bf16 v[204:207], v[252:255], v[236:239], v[204:207]\n" \
  "v_mfma_f32_16x16x32_bf16 v[224:227], v[248:251], v[236:239], v[224:227]\n" ST8 "s_cbranch_vccnz ch_ha_vlate\n" "v_rcp_f32 v230, v244\n" "s_cmp_eq_u32 s56, 63\n" "s_waitcnt vmcnt(0)\n" \
  "s_nop 0\n" "v_mul_f32 v204, v204, v230\n" "v_mul_f32_e64 v205, v205, v230\n" "v_mul_f32 v206, v206, v230\n" "v_mul_f32 v207, v207, v230\n" \
  "v_mul_f32_dpp v204, v224, v230 quad_perm:[0,1,2,3] row_mask:0xf bank_mask:0x3\n" "v_mul_f32_dpp v205, v225, v230 quad_perm:[0,1,2,3] row_mask:0xf bank_mask:0x3\n" \
  "v_mul_f32_dpp v206, v226, v230 quad_perm:[0,1,2,3] row_mask:0xf bank_mask:0x3\n" "v_mul_f32_dpp v207, v227, v230 quad_perm:[0,1,2,3] row_mask:0xf bank_mask:0x3\n" \
  "v_cvt_pk_bf16_f32 v224, v204, v205\n" "v_cvt_pk_bf16_f32 v225, v206, v207\n" "buffer_store_dwordx2 v[224:225], v191, s[40:43], 0 offen sc1\n" ST9 "s_cbranch_scc1 ch_ha_last\n" \
  "ch_ha_sent:\n" "s_memrealtime s[60:61]\n" "v_add_u32 v229, 64, v229\n" "ds_write_b64 v189, v[228:229] offset:14336\n" "ds_read_b96 v[252:254], v189 offset:10240\n" "s_setprio 0\n" \
  "s_add_i32 s70, s56, 64\n" "s_waitcnt lgkmcnt(0)\n" "s_lshl_b32 s60, s60, 6\n" "s_or_b32 s60, s60, s56\n" "v_mov_b32 v230, s60\n" "ds_write_b32 v188, v230 offset:46336\n" PTA \
  "s_lshl_b32 s64, s56, 13\n" "s_and_b32 s64, s64, 0x2000\n" "s_and_b32 s65, s52, 1\n" "v_add_u32 v252, s64, v252\n" "v_add_u32 v253, s64, v253\n" "v_xor_b32 v255, 0x80, v253\n" \
  "ds_read_b128 v[216:219], v252 offset:0\n" "ds_read_b128 v[220:223], v253 offset:0\n" "ds_read_b128 v[224:227], v253 offset:256\n" "ds_read_b128 v[228:231], v252 offset:512\n" \
  "ds_read_b128 v[232:235], v255 offset:512\n" "ds_read_b128 v[236:239], v255 offset:768\n" "ds_read_b128 v[240:243], v252 offset:4096\n" "ds_read_b128 v[244:247], v253 offset:4096\n" \
  "ds_read_b128 v[248:251], v253 offset:4352\n" "s_waitcnt lgkmcnt(6)\n" "v_mfma_f32_16x16x32_bf16 v[200:203], v[220:223], v[216:219], 0\n" \
  "v_mfma_f32_16x16x32_bf16 v[204:207], v[224:227], v[216:219], 0\n" "v_mfma_f32_16x16x32_bf16 v[208:211], v[192:195], v[216:219], 0\n" "ds_read_b128 v[216:219], v252 offset:4608\n" \
  "ds_read_b128 v[220:223], v255 offset:4608\n" "ds_read_b128 v[224:227], v255 offset:4864\n" "s_waitcnt lgkmcnt(6)\n" \
  "v_mfma_f32_16x16x32_bf16 v[200:203], v[232:235], v[228:231], v[200:203]\n" "v_mfma_f32_16x16x32_bf16 v[204:207], v[236:239], v[228:231], v[204:207]\n" \
  "v_mfma_f32_16x16x32_bf16 v[212:215], v[192:195], v[228:231], 0\n" "s_waitcnt lgkmcnt(3)\n" "v_mfma_f32_16x16x32_bf16 v[200:203], v[244:247], v[240:243], v[200:203]\n" \
  "v_mfma_f32_16x16x32_bf16 v[204:207], v[248:251], v[240:243], v[204:207]\n" "v_mfma_f32_16x16x32_bf16 v[208:211], v[196:199], v[240:243], v[208:211]\n" "s_waitcnt lgkmcnt(0)\n" \
  "v_mfma_f32_16x16x32_bf16 v[200:203], v[220:223], v[216:219], v[200:203]\n" "v_mfma_f32_16x16x32_bf16 v[204:207], v[224:227], v[216:219], v[204:207]\n" \
  "v_mfma_f32_16x16x32_bf16 v[212:215], v[196:199], v[216:219], v[212:215]\n" "s_lshr_b32 s61, s52, 1\n" "s_lshl_b32 s62, s61, 12\n" "s_add_i32 s62, s62, s64\n" "s_lshl_b32 s61, s61, 11\n" \
  "s_add_i32 s61, s61, 114688\n" "v_add_u32 v252, s62, v254\n" "v_mbcnt_lo_u32_b32 v254, -1, 0\n" "v_mbcnt_hi_u32_b32 v254, -1, v254\n" "v_lshl_add_u32 v253, v254, 4, s61\n" \
  "ds_read_b64_tr_b16 v[216:217], v252 offset:0\n" "ds_read_b64_tr_b16 v[218:219], v252 offset:256\n" "ds_read_b64_tr_b16 v[220:221], v252 offset:1024\n" \
  "ds_read_b64_tr_b16 v[222:223], v252 offset:1280\n" "ds_read_b64_tr_b16 v[224:225], v252 offset:2048\n" "ds_read_b64_tr_b16 v[226:227], v252 offset:2304\n" \
  "ds_read_b64_tr_b16 v[228:229], v252 offset:3072\n" "ds_read_b64_tr_b16 v[230:231], v252 offset:3328\n" "ch_ha_xv:\n" "ds_read_b128 v[232:235], v253 offset:14336\n" \
  "ds_read_b128 v[236:239], v253 offset:15360\n" "s_waitcnt lgkmcnt(0)\n" "v_cmp_ne_u32 vcc, s70, v233\n" "v_cmp_ne_u32 s[60:61], s70, v237\n" "s_or_b64 vcc, vcc, s[60:61]\n" \
  "s_cbranch_scc1 ch_ha_xv\n" "v_lshrrev_b32 v255, 3, v254\n" "v_and_b32 v255, 6, v255\n" "v_and_b32 v252, 15, v254\n" "v_sub_u32 v252, v252, v255\n" "v_lshl_add_u32 v252, s65, 4, v252\n" \
  "v_mov_b32 v253, 0xff800000\n" "v_add_f32 v208, v208, v214\n" "v_add_f32 v209, v209, v215\n" "v_cmp_le_i32 vcc, 0, v252\n" "v_cmp_le_i32 s[60:61], 8, v252\n" \
  "v_cmp_le_i32 s[62:63], 16, v252\n" "v_cmp_le_i32 s[66:67], 24, v252\n" "v_cndmask_b32 v200, v253, v200, vcc\n" "v_cndmask_b32 v201, v253, v201, s[60:61]\n" \
  "v_cndmask_b32 v202, v253, v202, s[62:63]\n" "v_cndmask_b32 v203, v253, v203, s[66:67]\n" "v_cmp_le_i32 vcc, 1, v252\n" "v_cmp_le_i32 s[60:61], 9, v252\n" "v_cmp_le_i32 s[62:63], 17, v252\n" \
  "v_cmp_le_i32 s[66:67], 25, v252\n" "v_cndmask_b32 v204, v253, v204, vcc\n" "v_cndmask_b32 v205, v253, v205, s[60:61]\n" "v_cndmask_b32 v206, v253, v206, s[62:63]\n" \
  "v_cndmask_b32 v207, v253, v207, s[66:67]\n" "v_max3_f32 v210, v200, v201, v202\n" "v_max3_f32 v210, v210, v203, v204\n" "v_max3_f32 v210, v210, v205, v206\n" \
  "v_max3_f32 v210, v210, v207, v208\n" "v_max_f32 v211, v210, v209\n" "v_max_f32 v210, v210, v209\n" "s_nop 1\n" "v_permlane16_swap_b32 v210, v211\n" "v_max_f32 v255, v210, v211\n" \
  "v_max_f32 v211, v210, v211\n" "s_nop 1\n" "v_permlane32_swap_b32 v255, v211\n" "v_max_f32 v210, v255, v211\n" "v_mul_f32 v210, s57, v210\n" "v_fma_f32 v200, v200, s57, -v210\n" \
  "v_fma_f32 v201, v201, s57, -v210\n" "v_fma_f32 v202, v202, s57, -v210\n" "v_fma_f32 v203, v203, s57, -v210\n" "v_fma_f32 v204, v204, s57, -v210\n" "v_fma_f32 v205, v205, s57, -v210\n" \
  "v_fma_f32 v206, v206, s57, -v210\n" "v_fma_f32 v207, v207, s57, -v210\n" "v_fma_f32 v208, v208, s57, -v210\n" "v_fma_f32 v209, v209, s57, -v210\n" "v_exp_f32 v200, v200\n" \
  "v_exp_f32 v201, v201\n" "v_exp_f32 v202, v202\n" "v_exp_f32 v203, v203\n" "v_exp_f32 v204, v204\n" "v_exp_f32 v205, v205\n" "v_exp_f32 v206, v206\n" "v_exp_f32 v207, v207\n" \
  "v_exp_f32 v208, v208\n" "v_exp_f32 v209, v209\n" "v_add_f32 v210, v200, v201\n" "v_add_f32 v210, v210, v202\n" "v_add_f32 v210, v210, v203\n" "v_add_f32 v210, v210, v204\n" \
  "v_add_f32 v210, v210, v205\n" "v_add_f32 v210, v210, v206\n" "v_add_f32 v210, v210, v207\n" "v_add_f32 v210, v210, v208\n" "v_add_f32 v211, v210, v209\n" "v_add_f32 v210, v210, v209\n" \
  "s_nop 1\n" "v_permlane16_swap_b32 v210, v211\n" "v_add_f32 v255, v210, v211\n" "v_add_f32 v211, v210, v211\n" "s_nop 1\n" "v_permlane32_swap_b32 v255, v211\n" "v_add_f32 v210, v255, v211\n" \
  "v_rcp_f32 v210, v210\n" "v_mov_b32 v241, 0\n" "v_mov_b64 v[242:243], 0\n" "v_mov_b32 v245, 0\n" "v_mov_b64 v[246:247], 0\n" "v_mul_f32 v200, v200, v210\n" "v_mul_f32 v201, v201, v210\n" \
  "v_mul_f32 v202, v202, v210\n" "v_mul_f32 v203, v203, v210\n" "v_mul_f32 v204, v204, v210\n" "v_mul_f32 v205, v205, v210\n" "v_mul_f32 v206, v206, v210\n" "v_mul_f32 v207, v207, v210\n" \
  "v_mul_f32 v208, v208, v210\n" "v_mul_f32 v209, v209, v210\n" "v_cvt_pk_bf16_f32 v248, v200, v201\n" "v_cvt_pk_bf16_f32 v249, v202, v203\n" "v_cvt_pk_bf16_f32 v250, v204, v205\n" \
  "v_cvt_pk_bf16_f32 v251, v206, v207\n" "v_cvt_pk_bf16_f32 v240, v208, v209\n" "v_cvt_pk_bf16_f32 v244, v209, v208\n" "s_waitcnt lgkmcnt(0)\n" \
  "v_mfma_f32_16x16x32_bf16 v[200:203], v[216:219], v[248:251], 0\n" "v_mfma_f32_16x16x32_bf16 v[204:207], v[220:223], v[248:251], 0\n" \
  "v_mfma_f32_16x16x32_bf16 v[208:211], v[224:227], v[248:251], 0\n" "v_mfma_f32_16x16x32_bf16 v[212:215], v[228:231], v[248:251], 0\n" \
  "v_mfma_f32_16x16x32_bf16 v[200:203], v[232:235], v[240:243], v[200:203]\n" "v_mfma_f32_16x16x32_bf16 v[204:207], v[234:237], v[244:247], v[204:207]\n" \
  "v_mfma_f32_16x16x32_bf16 v[208:211], v[236:239], v[240:243], v[208:211]\n" "v_mfma_f32_16x16x32_bf16 v[212:215], v[238:241], v[244:247], v[212:215]\n" "s_cmp_lt_u32 s56, " XS(SM0) "\n" \
  "s_cselect_b32 s60, 15, 8\n" "s_cselect_b32 s61, s53, s54\n" "s_cselect_b32 s62, 17, 10\n" "s_lshl_b32 s62, 1, s62\n" "s_lshl_b32 s63, s56, 19\n" "s_add_i32 s61, s61, s63\n" \
  "s_lshl_b32 s63, s56, 11\n" "s_andn2_b32 s63, 0x2000, s63\n" "s_add_i32 s64, s61, s63\n" "s_add_i32 s65, s64, s62\n" "v_bfe_u32 v255, v254, 4, 1\n" "v_lshrrev_b32 v252, 5, v254\n" \
  "v_lshl_add_u32 v252, v255, 1, v252\n" "v_lshlrev_b32 v252, s60, v252\n" "v_and_b32 v255, 15, v254\n" "v_lshl_add_u32 v224, v255, 4, v252\n" "v_cvt_pk_bf16_f32 v216, v200, v201\n" \
  "v_cvt_pk_bf16_f32 v217, v202, v203\n" "v_cvt_pk_bf16_f32 v218, v204, v205\n" "v_cvt_pk_bf16_f32 v219, v206, v207\n" "v_cvt_pk_bf16_f32 v220, v208, v209\n" \
  "v_cvt_pk_bf16_f32 v221, v210, v211\n" "v_cvt_pk_bf16_f32 v222, v212, v213\n" "v_cvt_pk_bf16_f32 v223, v214, v215\n" "s_nop 1\n" "v_permlane16_swap_b32 v216, v218\n" \
  "v_permlane16_swap_b32 v217, v219\n" "v_permlane16_swap_b32 v220, v222\n" "v_permlane16_swap_b32 v221, v223\n" "s_cmp_eq_u32 s56, 63\n" "s_cbranch_scc1 ch_ha_fin\n" \
  "ds_read_b64 v[200:201], v189 offset:10248\n" "v_and_b32 v204, 0x300, v189\n" "v_lshlrev_b32 v204, 2, v204\n" "v_add_u32 v204, 129036, v204\n" "ch_ha_w1:\n" "s_sleep 1\n" \
  "ds_read_b32 v202, v204\n" "s_waitcnt lgkmcnt(0)\n" "v_cmp_ne_u32 vcc, s70, v202\n" "s_lshl_b64 vcc, vcc, 16\n" "s_cbranch_scc1 ch_ha_w1\n" "s_lshl_b32 s60, s56, 13\n" \
  "s_andn2_b32 s60, 0x2000, s60\n" "s_lshl_b32 s61, s52, 11\n" "s_add_i32 s61, s61, s60\n" "s_waitcnt lgkmcnt(0)\n" "v_add_u32 v200, s61, v200\n" "v_add_u32 v201, s60, v201\n" \
  "ds_read_b128 v[240:243], v201\n" "ds_read_b128 v[244:247], v201 offset:4096\n" "ds_read_b64_tr_b16 v[248:249], v200\n" "ds_read_b64_tr_b16 v[250:251], v200 offset:256\n" \
  "ds_read_b64_tr_b16 v[252:253], v200 offset:1280\n" "ds_read_b64_tr_b16 v[254:255], v200 offset:1024\n" "v_mov_b64 v[228:229], 0\n" "v_mov_b64 v[230:231], 0\n" "v_mov_b64 v[232:233], 0\n" \
  "v_mov_b64 v[234:235], 0\n" "v_mov_b64 v[236:237], 0\n" "v_mov_b64 v[238:239], 0\n" "ch_ha_w2:\n" "s_sleep 1\n" "ds_read_b32 v200, v204\n" "s_waitcnt lgkmcnt(0)\n" \
  "v_cmp_ne_u32 vcc, s70, v200\n" "s_cbranch_vccnz ch_ha_w2\n" "s_sleep " XS(XSD) "\n" "buffer_store_dwordx4 v[216:219], v224, s[44:47], s64 offen sc1\n" \
  "buffer_store_dwordx4 v[220:223], v224, s[44:47], s65 offen sc1\n" "ch_ha_next:\n" "v_xor_b32 v200, s77, v191\n" "v_mov_b64 v[202:203], -1\n" "v_xor_b32 v191, s58, v191\n" \
  "buffer_store_dwordx2 v[202:203], v200, s[40:43], 0 offen sc1\n" "s_mov_b32 s60, s58\n" "s_xor_b32 s58, s58, s77\n" "s_mov_b32 s77, s60\n" "s_add_i32 s56, s56, 1\n" "s_setprio 2\n" \
  "s_branch ch_ha_loop\n" "ch_ha_vlate:\n" "ds_read_b128 v[208:211], v189 offset:14336\n" "s_branch ch_ha_p3\n" "ch_ha_slow:\n" "s_cmp_eq_u32 s56, 0\n" "s_cbranch_scc1 ch_ha_back\n" \
  "v_max3_u32 v230, v192, v193, v194\n" "v_max3_u32 v230, v230, v195, v196\n" "v_max3_u32 v230, v230, v197, v198\n" "v_max3_u32 v230, v230, v199, v200\n" "v_max3_u32 v230, v230, v201, v202\n" \
  "v_max3_u32 v230, v230, v203, v204\n" "v_max3_u32 v230, v230, v205, v206\n" "v_max3_u32 v230, v230, v207, v208\n" "v_max3_u32 v230, v230, v209, v210\n" "v_max3_u32 v230, v230, v211, v212\n" \
  "v_max3_u32 v230, v230, v213, v214\n" "v_max3_u32 v230, v230, v215, v216\n" "v_max3_u32 v230, v230, v217, v218\n" "v_max3_u32 v230, v230, v219, v220\n" "v_max3_u32 v230, v230, v221, v222\n" \
  "v_max3_u32 v230, v230, v223, v224\n" "v_max3_u32 v230, v230, v225, v226\n" "v_max_u32 v230, v230, v227\n" "v_cmp_eq_u32 vcc, -1, v230\n" "s_cbranch_vccz ch_ha_back\n" \
  "s_sub_u32 s59, s59, 1\n" "s_cselect_b32 s59, 0, s59\n" "s_cbranch_scc1 ch_ha_back\n" "s_nop 7\n" "v_mov_b64 v[228:229], 0\n" "v_mov_b64 v[230:231], 0\n" "v_mov_b64 v[232:233], 0\n" \
  "v_mov_b64 v[234:235], 0\n" "v_mov_b64 v[236:237], 0\n" "v_mov_b64 v[238:239], 0\n" "v_add_u32 v185, s71, v185\n" "s_sub_i32 s71, 0, s71\n" "s_branch ch_ha_loop\n" "ch_ha_last:\n" \
  "v_mov_b32 v230, 0\n" "s_mov_b64 exec, 1\n" "buffer_store_dword v184, v230, s[48:51], s55 offen sc1\n" "s_mov_b64 exec, -1\n" "s_branch ch_ha_sent\n" "ch_ha_fin:\n" \
  "buffer_store_dwordx4 v[216:219], v224, s[44:47], s64 offen sc1\n" "buffer_store_dwordx4 v[220:223], v224, s[44:47], s65 offen sc1\n" "v_mov_b32 v228, 0\n" "s_mov_b64 exec, 1\n" \
  "s_waitcnt vmcnt(0)\n" "buffer_store_dword v184, v228, s[48:51], s55 offen offset:512 sc1\n" "s_mov_b64 exec, -1\n"
#define CH_HB \
  "s_setprio 1\n" "s_mov_b32 s56, 0\n" "s_mov_b32 s59, " XS(SPIN_CAP) "\n" "s_mov_b32 s68, 0\n" "s_mov_b32 s58, " XS(DUR0) "*64\n" "s_add_i32 s70, s53, 9*0x8000\n" \
  "s_add_i32 s69, s53, 6*0x8000\n" "s_mov_b32 s71, -18688\n" "s_mov_b32 s77, 151808\n" "s_lshl_b32 s60, s52, 8\n" "s_add_i32 s60, s60, 118784\n" "v_mov_b32 v254, s60\n" \
  "ds_read_b128 v[176:179], v188 offset:0\n" "ds_read_b128 v[180:183], v188 offset:1024\n" "ds_read_b128 v[192:195], v188 offset:24576\n" "ds_read_b128 v[196:199], v188 offset:25600\n" \
  "ds_read_b128 v[200:203], v188 offset:49152\n" "ds_read_b128 v[204:207], v188 offset:50176\n" "v_mov_b64 v[244:245], 0\n" "v_mov_b64 v[246:247], 0\n" "v_mov_b64 v[248:249], 0\n" \
  "v_mov_b64 v[250:251], 0\n" "s_cmp_eq_u32 s52, 7\n" "s_cbranch_scc1 ch_hb_i1\n" "v_bfe_u32 v253, v187, 2, 8\n" "buffer_load_dword v190, v253, s[48:51], s55 offen sc1\n" \
  "s_mov_b32 s62, " XS(SPIN_CAP) "\n" "s_cmp_gt_u32 s68, s56\n" "s_cbranch_scc1 ch_hb_roa\n" "ch_hb_rla:\n" "s_waitcnt vmcnt(0)\n" "v_cmp_gt_u32 vcc, 8, v190\n" "s_ff1_i32_b64 s68, vcc\n" \
  "s_cmp_gt_u32 s68, s56\n" "s_cbranch_scc1 ch_hb_roa\n" "s_sleep 1\n" "buffer_load_dword v190, v253, s[48:51], s55 offen sc1\n" "s_sub_u32 s62, s62, 1\n" "s_cbranch_scc0 ch_hb_rla\n" \
  "ch_hb_roa:\n" "s_lshl_b32 s61, s56, 19\n" "s_add_i32 s61, s61, s53\n" "s_lshl_b32 s62, s56, 11\n" "s_and_b32 s62, s62, 0x2000\n" "s_add_i32 s61, s61, s62\n" "s_lshl_b32 s62, s56, 13\n" \
  "s_and_b32 s62, s62, 0x2000\n" "s_add_i32 m0, s62, s54\n" "s_add_i32 s63, s61, 0x1000\n" "buffer_load_dwordx4 v189, s[44:47], s61 offen offset:0 sc1 lds\n" \
  "buffer_load_dwordx4 v189, s[44:47], s61 offen offset:1024 sc1 lds\n" "buffer_load_dwordx4 v189, s[44:47], s61 offen offset:2048 sc1 lds\n" \
  "buffer_load_dwordx4 v189, s[44:47], s61 offen offset:3072 sc1 lds\n" "s_add_i32 m0, m0, 0x1000\n" "s_nop 0\n" "buffer_load_dwordx4 v189, s[44:47], s63 offen offset:0 sc1 lds\n" \
  "buffer_load_dwordx4 v189, s[44:47], s63 offen offset:1024 sc1 lds\n" "buffer_load_dwordx4 v189, s[44:47], s63 offen offset:2048 sc1 lds\n" \
  "buffer_load_dwordx4 v189, s[44:47], s63 offen offset:3072 sc1 lds\n" "s_waitcnt vmcnt(0)\n" "ch_hb_i1:\n" "s_barrier\n" "s_cmp_lg_u32 s52, 7\n" "s_cbranch_scc1 ch_hb_bar\n" \
  "s_memrealtime s[60:61]\n" "s_waitcnt lgkmcnt(0)\n" "s_add_i32 s57, s60, " XS(GL0) "\n" "s_lshl_b32 s57, s57, 6\n" "s_andn2_b32 s60, s58, 63\n" "s_add_i32 s60, s60, s57\n" \
  "s_add_i32 s60, s60, s56\n" "v_mov_b32 v252, s60\n" "buffer_store_dword v252, v191, s[40:43], 0 offen sc1\n" ".p2align 6\n" "ch_hb_bar:\n" "ch_hb_loop:\n" "v_and_b32 v253, 0x3f0, v187\n" \
  "v_bfe_u32 v252, v253, 2, 7\n" "v_lshl_add_u32 v252, v253, 3, v252\n" "v_add_u32 v252, s77, v252\n" "v_mov_b32 v255, s56\n" "s_waitcnt vmcnt(1)\n" "s_barrier\n" ST0 ".rept " XS(BD) "\n" \
  "s_nop 3\n" ".endr\n" ".p2align 3\n" "ds_read_b32 v252, v252\n" "ds_read_b32 v253, v185 offset:45312\n" "ds_read_b128 v[232:235], v185 offset:43264\n" \
  "ds_read_b128 v[236:239], v185 offset:44288\n" "ds_read_b128 v[240:243], v185 offset:45312\n" "ds_read_b128 v[208:211], v185 offset:37120\n" "ds_read_b128 v[212:215], v185 offset:38144\n" \
  "ds_read_b128 v[216:219], v185 offset:39168\n" "ds_read_b128 v[220:223], v185 offset:40192\n" "ds_read_b128 v[224:227], v185 offset:41216\n" "ds_read_b128 v[228:231], v185 offset:42240\n" \
  "s_setprio 3\n" "s_waitcnt lgkmcnt(6)\n" "v_smfmac_f32_16x16x64_bf16 v[244:247], v[232:235], v[176:183], v184\n" "v_max_u32_e64 v252, v252, v253\n" \
  "v_smfmac_f32_16x16x64_bf16 v[244:247], v[236:239], v[192:199], v184\n" "v_cmp_eq_u32_e64 vcc, -1, v252\n" "v_smfmac_f32_16x16x64_bf16 v[244:247], v[240:243], v[200:207], v184\n" "s_nop 0\n" \
  "s_setprio 1\n" ST30 "ds_read_b128 v[176:179], v188 offset:8192\n" "ds_read_b128 v[180:183], v188 offset:9216\n" "ds_read_b128 v[192:195], v188 offset:32768\n" \
  "ds_read_b128 v[196:199], v188 offset:33792\n" "ds_read_b128 v[200:203], v188 offset:57344\n" "ds_read_b128 v[204:207], v188 offset:58368\n" "s_cbranch_vccnz ch_hb_miss\n" "ch_hb_bk2:\n" \
  "s_waitcnt lgkmcnt(6)\n" "v_smfmac_f32_16x16x64_bf16 v[244:247], v[208:211], v[0:7], v184\n" "v_smfmac_f32_16x16x64_bf16 v[244:247], v[212:215], v[8:15], v184\n" \
  "v_smfmac_f32_16x16x64_bf16 v[244:247], v[216:219], v[16:23], v184\n" "v_smfmac_f32_16x16x64_bf16 v[244:247], v[220:223], v[24:31], v184\n" \
  "v_smfmac_f32_16x16x64_bf16 v[244:247], v[224:227], v[32:39], v184\n" "s_nop 0\n" "s_waitcnt lgkmcnt(0)\n" "v_smfmac_f32_16x16x64_bf16 v[244:247], v[228:231], v[40:47], v184\n" ST1 \
  "v_smfmac_f32_16x16x64_bf16 v[248:251], v[232:235], v[176:183], v184\n" "ds_read_b128 v[176:179], v188 offset:16384\n" ST20 \
  "v_smfmac_f32_16x16x64_bf16 v[248:251], v[236:239], v[192:199], v184\n" "ds_read_b128 v[180:183], v188 offset:17408\n" ST21 \
  "v_smfmac_f32_16x16x64_bf16 v[248:251], v[240:243], v[200:207], v184\n" "v_add_f32_e64 v252, v244, v246\n" ST22 "v_smfmac_f32_16x16x64_bf16 v[248:251], v[208:211], v[48:55], v184\n" \
  "ds_read_b128 v[192:195], v188 offset:40960\n" "v_add_f32_e64 v253, v245, v247\n" ST23 "v_smfmac_f32_16x16x64_bf16 v[248:251], v[212:215], v[56:63], v184\n" \
  "ds_read_b128 v[196:199], v188 offset:41984\n" "v_cmp_u_f32_e64 vcc, v252, v253\n" ST24 "v_smfmac_f32_16x16x64_bf16 v[248:251], v[216:219], v[64:71], v184\n" \
  "ds_read_b128 v[200:203], v187 offset:23552\n" "v_cvt_pk_bf16_f32 v252, v252, v253\n" ST25 "v_smfmac_f32_16x16x64_bf16 v[248:251], v[220:223], v[72:79], v184\n" \
  "ds_write_b32 v186, v252 offset:128\n" "v_mov_b64_e64 v[244:245], 0\n" ST26 "v_smfmac_f32_16x16x64_bf16 v[248:251], v[224:227], v[80:87], v184\n" "s_cbranch_vccnz ch_hb_slow\n" \
  "ch_hb_back:\n" "v_mov_b64 v[246:247], 0\n" ST27 "v_smfmac_f32_16x16x64_bf16 v[248:251], v[228:231], v[88:95], v184\n" "ds_read_b128 v[204:207], v187 offset:27648\n" ST28 ST2 \
  "v_smfmac_f32_16x16x64_bf16 v[244:247], v[208:211], v[96:103], v184\n" "v_smfmac_f32_16x16x64_bf16 v[244:247], v[212:215], v[104:111], v184\n" "v_add_f32 v252, v248, v250\n" \
  "v_add_f32 v253, v249, v251\n" "v_cvt_pk_bf16_f32 v252, v252, v253\n" "v_smfmac_f32_16x16x64_bf16 v[244:247], v[216:219], v[112:119], v184\n" "ds_write2st64_b32 v186, v252, v255 offset1:24\n" \
  ST3 "v_smfmac_f32_16x16x64_bf16 v[244:247], v[220:223], v[120:127], v184\n" "v_smfmac_f32_16x16x64_bf16 v[244:247], v[224:227], v[128:135], v184\n" \
  "v_smfmac_f32_16x16x64_bf16 v[244:247], v[228:231], v[136:143], v184\n" "s_waitcnt lgkmcnt(0)\n" "v_mov_b64 v[248:249], 0\n" \
  "v_smfmac_f32_16x16x64_bf16 v[244:247], v[232:235], v[176:183], v184\n" "v_mov_b64_e64 v[250:251], 0\n" "v_smfmac_f32_16x16x64_bf16 v[244:247], v[236:239], v[192:199], v184\n" \
  "v_smfmac_f32_16x16x64_bf16 v[244:247], v[240:243], v[200:207], v184\n" ST4 "v_smfmac_f32_16x16x64_bf16 v[248:251], v[228:231], v[144:151], v184\n" \
  "v_smfmac_f32_16x16x64_bf16 v[248:251], v[232:235], v[152:159], v184\n" "v_add_f32_e64 v252, v244, v246\n" "v_smfmac_f32_16x16x64_bf16 v[248:251], v[236:239], v[160:167], v184\n" \
  "v_add_f32_e64 v253, v245, v247\n" "v_smfmac_f32_16x16x64_bf16 v[248:251], v[240:243], v[168:175], v184\n" ST5 "v_cvt_pk_bf16_f32 v246, v253, v252\n" "v_mov_b32_e64 v247, s56\n" \
  "v_add_u32 v255, 64, v255\n" "s_nop 3\n" "v_add_f32 v244, v248, v250\n" "v_add_f32 v245, v249, v251\n" "ds_write_b128 v187, v[244:247] offset:10240\n" ST6 "s_setprio 0\n" \
  "s_cmp_eq_u32 s56, 63\n" "s_cbranch_scc1 ch_hb_fin\n" "s_sleep " XS(BSL) "\n" PTA "ch_hb_w0:\n" "s_sleep 1\n" "ds_read_b32 v252, v254 offset:41216\n" "s_waitcnt lgkmcnt(0)\n" \
  "v_readfirstlane_b32 s67, v252\n" "s_xor_b32 s61, s67, s56\n" "s_and_b32 s61, s61, 63\n" "s_cbranch_scc1 ch_hb_w0\n" "s_cmp_eq_u32 s52, 7\n" "s_cbranch_scc1 ch_hb_w7\n" \
  "s_add_i32 s56, s56, 1\n" "s_cmp_gt_u32 s68, s56\n" "s_cbranch_scc1 ch_hb_nf\n" "v_bfe_u32 v253, v187, 2, 8\n" "buffer_load_dword v190, v253, s[48:51], s55 offen sc1\n" "ch_hb_nf:\n" \
  "s_sleep " XS(RQD) "\n" "s_mov_b32 s62, " XS(SPIN_CAP) "\n" "s_cmp_gt_u32 s68, s56\n" "s_cbranch_scc1 ch_hb_roc\n" "ch_hb_rlc:\n" "s_waitcnt vmcnt(0)\n" "v_cmp_gt_u32 vcc, 8, v190\n" \
  "s_ff1_i32_b64 s68, vcc\n" "s_cmp_gt_u32 s68, s56\n" "s_cbranch_scc1 ch_hb_roc\n" "s_sleep 1\n" "buffer_load_dword v190, v253, s[48:51], s55 offen sc1\n" "s_sub_u32 s62, s62, 1\n" \
  "s_cbranch_scc0 ch_hb_rlc\n" "ch_hb_roc:\n" "s_lshl_b32 s61, s56, 19\n" "s_add_i32 s61, s61, s53\n" "s_lshl_b32 s62, s56, 11\n" "s_and_b32 s62, s62, 0x2000\n" "s_add_i32 s61, s61, s62\n" \
  "s_lshl_b32 s62, s56, 13\n" "s_and_b32 s62, s62, 0x2000\n" "s_add_i32 m0, s62, s54\n" "s_add_i32 s63, s61, 0x1000\n" "buffer_load_dwordx4 v189, s[44:47], s61 offen offset:0 sc1 lds\n" \
  "buffer_load_dwordx4 v189, s[44:47], s61 offen offset:1024 sc1 lds\n" "buffer_load_dwordx4 v189, s[44:47], s61 offen offset:2048 sc1 lds\n" \
  "buffer_load_dwordx4 v189, s[44:47], s61 offen offset:3072 sc1 lds\n" "s_add_i32 m0, m0, 0x1000\n" "s_nop 0\n" "buffer_load_dwordx4 v189, s[44:47], s63 offen offset:0 sc1 lds\n" \
  "buffer_load_dwordx4 v189, s[44:47], s63 offen offset:1024 sc1 lds\n" "buffer_load_dwordx4 v189, s[44:47], s63 offen offset:2048 sc1 lds\n" \
  "buffer_load_dwordx4 v189, s[44:47], s63 offen offset:3072 sc1 lds\n" "ds_read_b128 v[176:179], v188 offset:0\n" "ds_read_b128 v[180:183], v188 offset:1024\n" \
  "ds_read_b128 v[192:195], v188 offset:24576\n" "ds_read_b128 v[196:199], v188 offset:25600\n" "ds_read_b128 v[200:203], v188 offset:49152\n" "ds_read_b128 v[204:207], v188 offset:50176\n" \
  "v_mov_b64 v[244:245], 0\n" "v_mov_b64 v[246:247], 0\n" "v_mov_b64 v[248:249], 0\n" "v_mov_b64 v[250:251], 0\n" "s_waitcnt vmcnt(0)\n" "ds_write_b32 v187, v255 offset:10252\n" "s_setprio 1\n" \
  "s_branch ch_hb_bar\n" "ch_hb_w7:\n" "buffer_load_dword v208, v189, s[40:43], s70 offen sc1\n" "ds_read_b128 v[176:179], v188 offset:0\n" "ds_read_b128 v[180:183], v188 offset:1024\n" \
  "ds_read_b128 v[192:195], v188 offset:24576\n" "ds_read_b128 v[196:199], v188 offset:25600\n" "ds_read_b128 v[200:203], v188 offset:49152\n" "ds_read_b128 v[204:207], v188 offset:50176\n" \
  "v_mov_b64 v[244:245], 0\n" "v_mov_b64 v[246:247], 0\n" "v_mov_b64 v[248:249], 0\n" "v_mov_b64 v[250:251], 0\n" "s_or_b32 s60, s67, 63\n" "s_sub_i32 s60, s60, s57\n" \
  "s_min_u32 s60, s60, 300*64\n" "s_sub_i32 s60, s60, s58\n" "s_ashr_i32 s60, s60, " XS(DSH) "\n" "s_cmp_eq_u32 s56, 0\n" "s_cselect_b32 s60, 0, s60\n" "s_add_i32 s58, s58, s60\n" \
  "s_add_i32 s69, s69, 3*0x8000\n" "s_sub_i32 s60, s69, s53\n" "s_cmp_eq_u32 s60, 9*0x8000\n" "s_cselect_b32 s69, s53, s69\n" "s_cmp_lt_u32 s56, 2\n" "s_cselect_b32 s66, " XS(KE0) ", 0\n" \
  "s_add_i32 s66, s66, " XS(K1) "+" XS(K3) "\n" PSTEP_ "s_setprio 3\n" "s_movk_i32 s60, 0x1000\n" "ch_hb_anc:\n" "s_waitcnt vmcnt(0)\n" "v_subrev_u32 v209, s56, v208\n" \
  "v_and_b32 v209, 63, v209\n" "v_cmp_lt_u32 vcc, 1, v209\n" "s_cmp_eq_u32 vcc_lo, 0\n" "s_cbranch_scc1 ch_hb_ano\n" "ch_hb_anr:\n" "s_sub_u32 s60, s60, 1\n" "s_cbranch_scc1 ch_hb_ane\n" \
  "s_sleep 1\n" "buffer_load_dword v208, v189, s[40:43], s70 offen sc1\n" "s_branch ch_hb_anc\n" "ch_hb_ano:\n" "s_memrealtime s[62:63]\n" "v_cmp_eq_u32 vcc, 0, v209\n" "v_bfrev_b32 v210, 1\n" \
  "s_waitcnt lgkmcnt(0)\n" "s_lshl_b32 s61, s62, 6\n" "v_subrev_u32 v209, s61, v208\n" "v_cndmask_b32 v209, v210, v209, vcc\n" "s_mov_b32 exec_lo, 0\n" "v_mov_b32 v209, v208\n" \
  "s_mov_b32 exec_lo, -1\n" "s_nop 1\n" "v_max_i32_dpp v209, v209, v209 row_ror:8 row_mask:0xf bank_mask:0xf\n" "s_nop 1\n" \
  "v_max_i32_dpp v209, v209, v209 row_ror:4 row_mask:0xf bank_mask:0xf\n" "s_nop 1\n" "v_max_i32_dpp v209, v209, v209 quad_perm:[2,3,0,1] row_mask:0xf bank_mask:0xf\n" "s_nop 1\n" \
  "v_max_i32_dpp v209, v209, v209 quad_perm:[1,0,3,2] row_mask:0xf bank_mask:0xf\n" "s_nop 1\n" "v_readlane_b32 s61, v209, 0\n" "v_readlane_b32 s63, v209, 16\n" "v_readlane_b32 s64, v209, 32\n" \
  "v_readlane_b32 s65, v209, 48\n" "s_max_i32 s61, s61, s63\n" "s_ashr_i32 s61, s61, 6\n" "s_add_i32 s61, s61, s66\n" "s_max_i32 s64, s64, s65\n" "s_ashr_i32 s64, s64, 6\n" \
  "s_max_i32 s64, s64, 40\n" "s_min_i32 s64, s64, 100\n" "s_add_i32 s61, s61, s64\n" "s_add_i32 s61, s61, -3\n" "s_add_i32 s63, s61, s62\n" "s_add_i32 s57, s63, 3\n" "s_cmp_lt_i32 s61, -3\n" \
  "s_cselect_b32 s57, s62, s57\n" "s_cmp_gt_i32 s61, " XS(LIM2) "\n" "s_cbranch_scc1 ch_hb_anr\n" "s_cmp_le_i32 s61, 0\n" "s_cbranch_scc1 ch_hb_ane\n" "s_movk_i32 s60, 100\n" \
  "s_mov_b32 vcc_lo, s62\n" "ch_hb_an1:\n" ".rept 8\n" "s_memrealtime vcc\n" "s_sub_u32 s61, vcc_lo, s63\n" "s_cbranch_scc0 ch_hb_an2\n" ".endr\n" "s_sub_u32 s60, s60, 1\n" \
  "s_cbranch_scc0 ch_hb_an1\n" "ch_hb_an2:\n" "s_min_u32 s61, s61, 3\n" "s_mul_i32 s61, s61, " XS(CPT) "\n" "s_and_b32 s61, s61, -4\n" "s_waitcnt lgkmcnt(0)\n" "s_getpc_b64 vcc\n" \
  "s_add_i32 s61, s61, 16\n" "s_add_u32 vcc_lo, vcc_lo, s61\n" "s_addc_u32 vcc_hi, vcc_hi, 0\n" "s_setpc_b64 vcc\n" ".rept 3*" XS(CPT) "/4\n" "s_nop 0\n" ".endr\n" "ch_hb_ane:\n" ST12 \
  "s_add_i32 m0, s77, 4096\n" "s_add_i32 s60, s69, 0x8000\n" "buffer_load_dwordx4 v189, s[40:43], s60 offen offset:0 sc1 lds\n" \
  "buffer_load_dwordx4 v189, s[40:43], s60 offen offset:1024 sc1 lds\n" "buffer_load_dwordx4 v189, s[40:43], s60 offen offset:2048 sc1 lds\n" \
  "buffer_load_dwordx4 v189, s[40:43], s60 offen offset:3072 sc1 lds\n" "s_mov_b32 m0, s77\n" "s_add_i32 s61, s69, 0x10000\n" "buffer_load_dwordx4 v189, s[40:43], s69 offen offset:0 sc1 lds\n" \
  "buffer_load_dwordx4 v189, s[40:43], s69 offen offset:1024 sc1 lds\n" "buffer_load_dwordx4 v189, s[40:43], s69 offen offset:2048 sc1 lds\n" \
  "buffer_load_dwordx4 v189, s[40:43], s69 offen offset:3072 sc1 lds\n" "s_add_i32 m0, s77, 8192\n" "s_nop 0\n" "buffer_load_dwordx4 v190, s[40:43], s61 offen sc1 lds\n" ST13 \
  "ds_write_b32 v187, v255 offset:10252\n" "s_lshl_b32 s57, s57, 6\n" "s_andn2_b32 s60, s58, 63\n" "s_add_i32 s60, s60, s57\n" "s_add_i32 s60, s60, s56\n" "s_add_i32 s60, s60, 1\n" \
  "v_mov_b32 v252, s60\n" "buffer_store_dword v252, v191, s[40:43], 0 offen sc1\n" "s_xor_b32 s70, s70, 0x400\n" "s_add_i32 s56, s56, 1\n" "s_setprio 1\n" "s_branch ch_hb_loop\n" \
  "ch_hb_slow:\n" "s_cmp_eq_u32 s56, 0\n" "s_cbranch_scc1 ch_hb_back\n" "v_max3_u32 v253, v208, v209, v210\n" "v_max3_u32 v253, v253, v211, v212\n" "v_max3_u32 v253, v253, v213, v214\n" \
  "v_max3_u32 v253, v253, v215, v216\n" "v_max3_u32 v253, v253, v217, v218\n" "v_max3_u32 v253, v253, v219, v220\n" "v_max3_u32 v253, v253, v221, v222\n" "v_max3_u32 v253, v253, v223, v224\n" \
  "v_max3_u32 v253, v253, v225, v226\n" "v_max3_u32 v253, v253, v227, v228\n" "v_max3_u32 v253, v253, v229, v230\n" "v_max3_u32 v253, v253, v231, v232\n" "v_max3_u32 v253, v253, v233, v234\n" \
  "v_max3_u32 v253, v253, v235, v236\n" "v_max3_u32 v253, v253, v237, v238\n" "v_max3_u32 v253, v253, v239, v240\n" "v_max3_u32 v253, v253, v241, v242\n" "v_max_u32 v253, v253, v243\n" \
  "v_cmp_eq_u32 vcc, -1, v253\n" "s_cbranch_vccz ch_hb_back\n" "s_sub_u32 s59, s59, 1\n" "s_cselect_b32 s59, 0, s59\n" "s_cbranch_scc1 ch_hb_back\n" "s_waitcnt lgkmcnt(0)\n" "s_nop 7\n" \
  "ch_hb_redo:\n" CNTB "v_add_u32 v185, s71, v185\n" "s_add_i32 s77, s77, s71\n" "s_sub_i32 s71, 0, s71\n" "s_cmp_lg_u32 s52, 7\n" "s_cbranch_scc1 ch_hb_rd2\n" "s_memrealtime s[62:63]\n" \
  "s_add_i32 m0, s77, 4096\n" "s_add_i32 s60, s69, 0x8000\n" "buffer_load_dwordx4 v189, s[40:43], s60 offen offset:0 sc1 lds\n" \
  "buffer_load_dwordx4 v189, s[40:43], s60 offen offset:1024 sc1 lds\n" "buffer_load_dwordx4 v189, s[40:43], s60 offen offset:2048 sc1 lds\n" \
  "buffer_load_dwordx4 v189, s[40:43], s60 offen offset:3072 sc1 lds\n" "s_mov_b32 m0, s77\n" "s_add_i32 s61, s69, 0x10000\n" "buffer_load_dwordx4 v189, s[40:43], s69 offen offset:0 sc1 lds\n" \
  "buffer_load_dwordx4 v189, s[40:43], s69 offen offset:1024 sc1 lds\n" "buffer_load_dwordx4 v189, s[40:43], s69 offen offset:2048 sc1 lds\n" \
  "buffer_load_dwordx4 v189, s[40:43], s69 offen offset:3072 sc1 lds\n" "s_add_i32 m0, s77, 8192\n" "s_nop 0\n" "buffer_load_dwordx4 v190, s[40:43], s61 offen sc1 lds\n" \
  "s_waitcnt lgkmcnt(0)\n" "s_add_i32 s57, s62, " XS(GL1) "\n" "s_lshl_b32 s57, s57, 6\n" "s_andn2_b32 s60, s58, 63\n" "s_add_i32 s60, s60, s57\n" "s_add_i32 s60, s60, s56\n" \
  "v_mov_b32 v252, s60\n" "buffer_store_dword v252, v191, s[40:43], 0 offen sc1\n" "ch_hb_rd2:\n" "s_waitcnt lgkmcnt(0)\n" "s_nop 7\n" "ds_read_b128 v[176:179], v188 offset:0\n" \
  "ds_read_b128 v[180:183], v188 offset:1024\n" "ds_read_b128 v[192:195], v188 offset:24576\n" "ds_read_b128 v[196:199], v188 offset:25600\n" "ds_read_b128 v[200:203], v188 offset:49152\n" \
  "ds_read_b128 v[204:207], v188 offset:50176\n" "v_mov_b64 v[244:245], 0\n" "v_mov_b64 v[246:247], 0\n" "v_mov_b64 v[248:249], 0\n" "v_mov_b64 v[250:251], 0\n" "s_branch ch_hb_loop\n" \
  "ch_hb_miss:\n" "s_cmp_eq_u32 s56, 0\n" "s_cbranch_scc1 ch_hb_bk2\n" "s_sub_u32 s59, s59, 1\n" "s_cselect_b32 s59, 0, s59\n" "s_cbranch_scc0 ch_hb_redo\n" "s_branch ch_hb_bk2\n" \
  "ch_hb_fin:\n" "s_waitcnt vmcnt(0)\n"
#define CH_O5 \
  "s_mov_b32 s56, 0\n" "s_mov_b32 s59, " XS(SPIN_CAP) "\n" "s_mov_b32 s57, 0\n" "s_mov_b32 s68, 0\n" "s_mov_b32 s58, " XS(DUO0) "*64\n" "s_mov_b32 s69, 0x40000\n" "s_mov_b32 s70, 0x84000\n" \
  "ch_o5_loop:\n" "v_mov_b64 v[176:177], 0\n" "v_mov_b64 v[178:179], 0\n" "v_mov_b64 v[180:181], 0\n" "v_mov_b64 v[182:183], 0\n" "v_mov_b64 v[184:185], 0\n" "v_mov_b64 v[186:187], 0\n" \
  "v_mov_b64 v[188:189], 0\n" "v_mov_b64 v[190:191], 0\n" "v_mov_b64 v[192:193], 0\n" "v_mov_b64 v[194:195], 0\n" "s_cmp_lg_u32 s52, 7\n" "s_cbranch_scc1 ch_o5_go\n" "s_sleep " XS(RSL) "\n" \
  "v_mov_b32 v212, s58\n" "buffer_store_dword v212, v220, s[40:43], 0 offen sc1\n" "s_sleep " XS(OSL) "\n" "v_and_b32 v215, 0x1f0, v217\n" \
  "buffer_load_dword v214, v215, s[40:43], s54 offen sc1\n" "s_cmp_lt_u32 s56, 2\n" "s_cselect_b32 s66, " XS(KE1) ", 0\n" "s_add_i32 s66, s66, " XS(K1) "\n" "s_movk_i32 s60, 0x1000\n" \
  "ch_o5_anc:\n" "s_waitcnt vmcnt(0)\n" "v_subrev_u32 v212, s56, v214\n" "v_and_b32 v212, 63, v212\n" "v_cmp_lt_u32 vcc, 1, v212\n" "s_cmp_eq_u32 vcc_lo, 0\n" "s_cbranch_scc1 ch_o5_ano\n" \
  "ch_o5_anr:\n" "s_sub_u32 s60, s60, 1\n" "s_cbranch_scc1 ch_o5_ane\n" "s_sleep 1\n" "buffer_load_dword v214, v215, s[40:43], s54 offen sc1\n" "s_branch ch_o5_anc\n" "ch_o5_ano:\n" \
  "s_memrealtime s[62:63]\n" "v_cmp_eq_u32 vcc, 0, v212\n" "v_bfrev_b32 v213, 1\n" "s_waitcnt lgkmcnt(0)\n" "s_lshl_b32 s61, s62, 6\n" "v_subrev_u32 v212, s61, v214\n" \
  "v_cndmask_b32 v212, v213, v212, vcc\n" "s_mov_b32 exec_lo, 0\n" "v_mov_b32 v212, v214\n" "s_mov_b32 exec_lo, -1\n" "s_nop 1\n" \
  "v_max_i32_dpp v212, v212, v212 row_ror:8 row_mask:0xf bank_mask:0xf\n" "s_nop 1\n" "v_max_i32_dpp v212, v212, v212 row_ror:4 row_mask:0xf bank_mask:0xf\n" "s_nop 1\n" \
  "v_max_i32_dpp v212, v212, v212 quad_perm:[2,3,0,1] row_mask:0xf bank_mask:0xf\n" "s_nop 1\n" "v_max_i32_dpp v212, v212, v212 quad_perm:[1,0,3,2] row_mask:0xf bank_mask:0xf\n" "s_nop 1\n" \
  "v_readlane_b32 s61, v212, 0\n" "v_readlane_b32 s63, v212, 16\n" "s_max_i32 s61, s61, s63\n" "s_ashr_i32 s61, s61, 6\n" "s_add_i32 s61, s61, s66\n" "s_add_i32 s61, s61, -3\n" \
  "s_add_i32 s63, s61, s62\n" "s_add_i32 s68, s63, 3\n" "s_cmp_lt_i32 s61, -3\n" "s_cselect_b32 s68, s62, s68\n" "s_cmp_gt_i32 s61, " XS(LIM1) "\n" "s_cbranch_scc1 ch_o5_anr\n" \
  "s_cmp_le_i32 s61, 0\n" "s_cbranch_scc1 ch_o5_ane\n" "s_movk_i32 s60, 100\n" "s_mov_b32 vcc_lo, s62\n" "ch_o5_an1:\n" ".rept 8\n" "s_memrealtime vcc\n" "s_sub_u32 s61, vcc_lo, s63\n" \
  "s_cbranch_scc0 ch_o5_an2\n" ".endr\n" "s_sub_u32 s60, s60, 1\n" "s_cbranch_scc0 ch_o5_an1\n" "ch_o5_an2:\n" "s_min_u32 s61, s61, 3\n" "s_mul_i32 s61, s61, " XS(CPT) "\n" \
  "s_and_b32 s61, s61, -4\n" "s_waitcnt lgkmcnt(0)\n" "s_getpc_b64 vcc\n" "s_add_i32 s61, s61, 16\n" "s_add_u32 vcc_lo, vcc_lo, s61\n" "s_addc_u32 vcc_hi, vcc_hi, 0\n" "s_setpc_b64 vcc\n" \
  ".rept 3*" XS(CPT) "/4\n" "s_nop 0\n" ".endr\n" "ch_o5_ane:\n" "s_xor_b32 s54, s54, 0x400\n" ".p2align 3\n" "ch_o5_go:\n" "s_nop 0\n" "s_cmp_lt_u32 s52, 4\n" "s_barrier\n" ST0 \
  "s_cbranch_scc0 ch_o5_l2\n" "ch_o5_l1:\n" "buffer_load_dwordx4 v[160:163], v217, s[40:43], s53 offen offset:0 sc1\n" "s_nop " XS(OG) "\n" "s_nop 0\n" \
  "buffer_load_dwordx4 v[164:167], v217, s[40:43], s53 offen offset:1024 sc1\n" "s_nop " XS(OG) "\n" "s_nop 0\n" "buffer_load_dwordx4 v[168:171], v217, s[40:43], s53 offen offset:2048 sc1\n" \
  "s_nop " XS(OG) "\n" "s_nop 0\n" "buffer_load_dwordx4 v[172:175], v217, s[40:43], s53 offen offset:3072 sc1\n" "ch_o5_mul:\n" "s_nop 0\n" "s_waitcnt vmcnt(3)\n" ST1 \
  "v_smfmac_f32_16x16x64_bf16 v[176:179], v[160:163], v[0:7], v216\n" "v_cmp_eq_u32_e64 vcc, -1, v160\n" "v_smfmac_f32_16x16x64_bf16 v[180:183], v[160:163], v[8:15], v216\n" \
  "v_smfmac_f32_16x16x64_bf16 v[184:187], v[160:163], v[16:23], v216\n" "s_nop 0\n" "s_cbranch_vccnz ch_o5_slow2\n" "v_smfmac_f32_16x16x64_bf16 v[188:191], v[160:163], v[24:31], v216\n" \
  "v_smfmac_f32_16x16x64_bf16 v[192:195], v[160:163], v[32:39], v216\n" "s_nop 0\n" "s_waitcnt vmcnt(2)\n" "v_smfmac_f32_16x16x64_bf16 v[176:179], v[164:167], v[40:47], v216\n" \
  "v_cmp_eq_u32_e64 vcc, -1, v164\n" "v_smfmac_f32_16x16x64_bf16 v[180:183], v[164:167], v[48:55], v216\n" "v_smfmac_f32_16x16x64_bf16 v[184:187], v[164:167], v[56:63], v216\n" "s_nop 0\n" \
  "s_cbranch_vccnz ch_o5_slow2\n" "v_smfmac_f32_16x16x64_bf16 v[188:191], v[164:167], v[64:71], v216\n" "v_smfmac_f32_16x16x64_bf16 v[192:195], v[164:167], v[72:79], v216\n" "s_nop 0\n" \
  "s_waitcnt vmcnt(1)\n" "v_smfmac_f32_16x16x64_bf16 v[176:179], v[168:171], v[80:87], v216\n" "v_smfmac_f32_16x16x64_bf16 v[180:183], v[168:171], v[88:95], v216\n" \
  "v_smfmac_f32_16x16x64_bf16 v[184:187], v[168:171], v[96:103], v216\n" "v_smfmac_f32_16x16x64_bf16 v[188:191], v[168:171], v[104:111], v216\n" \
  "v_smfmac_f32_16x16x64_bf16 v[192:195], v[168:171], v[112:119], v216\n" "s_nop 0\n" "s_waitcnt vmcnt(0)\n" ST2 "v_smfmac_f32_16x16x64_bf16 v[176:179], v[172:175], v[120:127], v216\n" \
  "v_smfmac_f32_16x16x64_bf16 v[180:183], v[172:175], v[128:135], v216\n" "v_smfmac_f32_16x16x64_bf16 v[184:187], v[172:175], v[136:143], v216\n" \
  "v_smfmac_f32_16x16x64_bf16 v[188:191], v[172:175], v[144:151], v216\n" "v_smfmac_f32_16x16x64_bf16 v[192:195], v[172:175], v[152:159], v216\n" ST3 "v_add_f32 v196, v176, v177\n" \
  "v_add_f32 v200, v178, v179\n" "v_add_f32_dpp v200, v180, v181 quad_perm:[0,1,2,3] row_mask:0xf bank_mask:0x3\n" \
  "v_add_f32_dpp v196, v182, v183 quad_perm:[0,1,2,3] row_mask:0xf bank_mask:0xc\n" "v_add_f32 v197, v184, v185\n" "v_add_f32 v201, v186, v187\n" \
  "v_add_f32_dpp v201, v188, v189 quad_perm:[0,1,2,3] row_mask:0xf bank_mask:0x3\n" "v_add_f32_dpp v197, v190, v191 quad_perm:[0,1,2,3] row_mask:0xf bank_mask:0xc\n" \
  "v_add_f32 v202, v194, v195\n" "v_add_f32 v198, v192, v193\n" "v_add_f32_dpp v196, v200, v196 row_ror:8 row_mask:0xf bank_mask:0xf\n" \
  "v_add_f32_dpp v197, v201, v197 row_ror:8 row_mask:0xf bank_mask:0xf\n" "v_add_f32_dpp v198, v202, v198 row_ror:8 row_mask:0xf bank_mask:0xf\n" \
  "ds_write2st64_b32 v218, v196, v197 offset1:8\n" "ds_write_b32 v218, v198 offset:4096\n" "v_cmp_u_f32 vcc, v196, v196\n" "s_cbranch_vccnz ch_o5_slow\n" "ch_o5_back:\n" "s_cmp_gt_u32 s52, 2\n" \
  "s_waitcnt lgkmcnt(0)\n" "s_barrier\n" "s_cbranch_scc1 ch_o5_rest\n" ST4 "ds_read_b128 v[204:207], v219\n" "ds_read_b128 v[208:211], v219 offset:1024\n" "s_add_i32 s60, s57, 6*0x8000\n" \
  "s_cmp_ge_u32 s60, 9*0x8000\n" "s_cselect_b32 s61, 9*0x8000, 0\n" "s_sub_i32 s60, s60, s61\n" "v_mov_b32 v213, -1\n" ST7 "s_waitcnt lgkmcnt(1)\n" "v_add_f32 v204, v204, v205\n" \
  "v_add_f32 v206, v206, v207\n" "s_waitcnt lgkmcnt(0)\n" "v_add_f32 v208, v208, v209\n" "v_add_f32 v210, v210, v211\n" "v_add_f32 v208, v208, v204\n" "v_add_f32 v210, v210, v206\n" \
  "v_add_f32 v204, v208, v210\n" "v_add_f32 v205, v208, v210\n" "s_nop 1\n" "v_permlane32_swap_b32 v204, v205\n" "v_cvt_pk_bf16_f32 v204, v204, v205\n" ST6 \
  "buffer_store_dword v204, v220, s[40:43], s57 offen sc1\n" ST5 "s_sleep " XS(RSL) "\n" "buffer_store_dword v213, v220, s[40:43], s60 offen sc1\n" "ch_o5_nxt:\n" PTS PTA \
  "s_add_i32 s57, s57, 3*0x8000\n" "s_cmp_eq_u32 s57, 9*0x8000\n" "s_cselect_b32 s57, 0, s57\n" "s_xor_b32 s53, s53, s69\n" "s_mov_b32 s60, s69\n" "s_xor_b32 s69, s69, s70\n" \
  "s_mov_b32 s70, s60\n" "s_add_i32 s56, s56, 1\n" "s_cmp_lt_u32 s56, 63\n" "s_cbranch_scc1 ch_o5_loop\n" "s_branch ch_o5_fin\n" "ch_o5_tim:\n" "s_memrealtime s[60:61]\n" \
  "s_waitcnt lgkmcnt(0)\n" "s_sub_i32 s60, s60, s68\n" "s_lshl_b32 s60, s60, 6\n" "s_sub_i32 s60, s60, s58\n" "s_ashr_i32 s60, s60, " XS(DSO) "\n" "s_min_i32 s60, s60, " XS(DUP) "\n" \
  "s_add_i32 s58, s58, s60\n" "s_branch ch_o5_nxt\n" "ch_o5_tch:\n" "s_add_i32 s60, s56, 2\n" "s_cmp_eq_u32 s60, 64\n" "s_cbranch_scc1 ch_o5_nxt\n" "s_lshl_b32 s61, s60, 19\n" \
  "s_lshl_b32 s62, s60, 11\n" "s_and_b32 s62, s62, 0x2000\n" "s_add_i32 s61, s61, s62\n" "s_sleep " XS(TSL) "\n" "s_cmp_gt_u32 s68, s60\n" "s_cbranch_scc1 ch_o5_tk\n" \
  "buffer_load_dword v214, v219, s[48:51], 0 offen sc1\n" "s_waitcnt vmcnt(0)\n" "v_cmp_gt_u32 vcc, 8, v214\n" "s_ff1_i32_b64 s68, vcc\n" "s_cmp_gt_u32 s68, s60\n" "s_cbranch_scc0 ch_o5_nxt\n" \
  "ch_o5_tk:\n" "buffer_load_dword v214, v220, s[44:47], s61 offen sc1\n" "s_branch ch_o5_nxt\n" "ch_o5_rest:\n" "s_cmp_eq_u32 s52, 7\n" "s_cbranch_scc1 ch_o5_tim\n" "s_cmp_lg_u32 s52, 6\n" \
  "s_cbranch_scc1 ch_o5_tch\n" "s_and_b32 s60, s56, 31\n" "s_cmp_lg_u32 s60, s55\n" "s_cbranch_scc1 ch_o5_nxt\n" "s_sub_u32 s60, s56, 1\n" "s_cbranch_scc1 ch_o5_nxt\n" \
  "s_lshl_b32 s61, s60, 7\n" "s_add_i32 s62, s61, " XS(SY_CS0) "*4\n" "s_lshl_b32 s61, s60, 10\n" "s_add_i32 s63, s61, " XS(SY_CS0) "*4-48*7*128\n" "s_cmp_lt_u32 s60, 48\n" \
  "s_cselect_b32 s62, s62, s63\n" "s_cselect_b32 exec_lo, 1, 255\n" "s_mov_b32 exec_hi, 0\n" "s_sleep " XS(RSL) "\n" "v_lshlrev_b32 v212, 3, v217\n" \
  "buffer_store_dword v216, v212, s[48:51], s62 offen sc1\n" "s_mov_b64 exec, -1\n" "s_branch ch_o5_nxt\n" "ch_o5_l2:\n" ".rept " XS(OD) "\n" "s_nop 3\n" ".endr\n" "s_branch ch_o5_l1\n" \
  "ch_o5_slow:\n" "v_max3_u32 v212, v160, v161, v162\n" "v_max3_u32 v212, v212, v163, v164\n" "v_max3_u32 v212, v212, v165, v166\n" "v_max3_u32 v212, v212, v167, v168\n" \
  "v_max3_u32 v212, v212, v169, v170\n" "v_max3_u32 v212, v212, v171, v172\n" "v_max3_u32 v212, v212, v173, v174\n" "v_max_u32 v212, v212, v175\n" "v_cmp_eq_u32 vcc, -1, v212\n" \
  "s_cbranch_vccz ch_o5_back\n" "s_sub_u32 s59, s59, 1\n" "s_cselect_b32 s59, 0, s59\n" "s_cbranch_scc1 ch_o5_back\n" "ch_o5_slow2:\n" CNTO "s_waitcnt vmcnt(0)\n" "ch_o5_wait:\n" \
  "v_max3_u32 v212, v160, v161, v162\n" "v_max_u32 v212, v212, v163\n" "v_cmp_eq_u32 vcc, -1, v212\n" "s_cbranch_vccz ch_o5_h0\n" \
  "buffer_load_dwordx4 v[160:163], v217, s[40:43], s53 offen offset:0 sc1\n" "ch_o5_h0:\n" "v_max3_u32 v212, v164, v165, v166\n" "v_max_u32 v212, v212, v167\n" "v_cmp_eq_u32 vcc, -1, v212\n" \
  "s_cbranch_vccz ch_o5_h1\n" "buffer_load_dwordx4 v[164:167], v217, s[40:43], s53 offen offset:1024 sc1\n" "ch_o5_h1:\n" "v_max3_u32 v212, v168, v169, v170\n" "v_max_u32 v212, v212, v171\n" \
  "v_cmp_eq_u32 vcc, -1, v212\n" "s_cbranch_vccz ch_o5_h2\n" "buffer_load_dwordx4 v[168:171], v217, s[40:43], s53 offen offset:2048 sc1\n" "ch_o5_h2:\n" "v_max3_u32 v212, v172, v173, v174\n" \
  "v_max_u32 v212, v212, v175\n" "v_cmp_eq_u32 vcc, -1, v212\n" "s_cbranch_vccz ch_o5_h3\n" "buffer_load_dwordx4 v[172:175], v217, s[40:43], s53 offen offset:3072 sc1\n" "ch_o5_h3:\n" \
  "v_mov_b64 v[176:177], 0\n" "v_mov_b64 v[178:179], 0\n" "v_mov_b64 v[180:181], 0\n" "v_mov_b64 v[182:183], 0\n" "v_mov_b64 v[184:185], 0\n" "v_mov_b64 v[186:187], 0\n" \
  "v_mov_b64 v[188:189], 0\n" "v_mov_b64 v[190:191], 0\n" "v_mov_b64 v[192:193], 0\n" "v_mov_b64 v[194:195], 0\n" "s_waitcnt vmcnt(0)\n" "v_max3_u32 v212, v160, v161, v162\n" \
  "v_max3_u32 v212, v212, v163, v164\n" "v_max3_u32 v212, v212, v165, v166\n" "v_max3_u32 v212, v212, v167, v168\n" "v_max3_u32 v212, v212, v169, v170\n" "v_max3_u32 v212, v212, v171, v172\n" \
  "v_max3_u32 v212, v212, v173, v174\n" "v_max_u32 v212, v212, v175\n" "v_cmp_eq_u32 vcc, -1, v212\n" "s_cbranch_vccz ch_o5_mul\n" "s_sub_u32 s59, s59, 1\n" "s_cselect_b32 s59, 0, s59\n" \
  "s_cbranch_scc0 ch_o5_wait\n" "v_mov_b32 v160, 0\n" "v_mov_b32 v164, 0\n" "v_mov_b32 v168, 0\n" "v_mov_b32 v172, 0\n" "s_mov_b32 s59, 0\n" "s_nop 0\n" "s_branch ch_o5_mul\n" "ch_o5_fin:\n" \
  "s_waitcnt vmcnt(0)\n"
#define CH_O4 \
  "s_mov_b32 s56, 0\n" "s_mov_b32 s59, " XS(SPIN_CAP) "\n" "s_mov_b32 s57, 0\n" "s_mov_b32 s68, 0\n" "s_mov_b32 s58, " XS(DUO0) "*64\n" "s_mov_b32 s69, 0x40000\n" "s_mov_b32 s70, 0x84000\n" \
  "ch_o4_loop:\n" "v_mov_b64 v[176:177], 0\n" "v_mov_b64 v[178:179], 0\n" "v_mov_b64 v[180:181], 0\n" "v_mov_b64 v[182:183], 0\n" "v_mov_b64 v[184:185], 0\n" "v_mov_b64 v[186:187], 0\n" \
  "v_mov_b64 v[188:189], 0\n" "v_mov_b64 v[190:191], 0\n" "s_cmp_lg_u32 s52, 7\n" "s_cbranch_scc1 ch_o4_go\n" "s_sleep " XS(RSL) "\n" "v_mov_b32 v212, s58\n" \
  "buffer_store_dword v212, v220, s[40:43], 0 offen sc1\n" "s_sleep " XS(OSL) "\n" "v_and_b32 v215, 0x1f0, v217\n" "buffer_load_dword v214, v215, s[40:43], s54 offen sc1\n" \
  "s_cmp_lt_u32 s56, 2\n" "s_cselect_b32 s66, " XS(KE1) ", 0\n" "s_add_i32 s66, s66, " XS(K1) "\n" "s_movk_i32 s60, 0x1000\n" "ch_o4_anc:\n" "s_waitcnt vmcnt(0)\n" \
  "v_subrev_u32 v212, s56, v214\n" "v_and_b32 v212, 63, v212\n" "v_cmp_lt_u32 vcc, 1, v212\n" "s_cmp_eq_u32 vcc_lo, 0\n" "s_cbranch_scc1 ch_o4_ano\n" "ch_o4_anr:\n" "s_sub_u32 s60, s60, 1\n" \
  "s_cbranch_scc1 ch_o4_ane\n" "s_sleep 1\n" "buffer_load_dword v214, v215, s[40:43], s54 offen sc1\n" "s_branch ch_o4_anc\n" "ch_o4_ano:\n" "s_memrealtime s[62:63]\n" \
  "v_cmp_eq_u32 vcc, 0, v212\n" "v_bfrev_b32 v213, 1\n" "s_waitcnt lgkmcnt(0)\n" "s_lshl_b32 s61, s62, 6\n" "v_subrev_u32 v212, s61, v214\n" "v_cndmask_b32 v212, v213, v212, vcc\n" \
  "s_mov_b32 exec_lo, 0\n" "v_mov_b32 v212, v214\n" "s_mov_b32 exec_lo, -1\n" "s_nop 1\n" "v_max_i32_dpp v212, v212, v212 row_ror:8 row_mask:0xf bank_mask:0xf\n" "s_nop 1\n" \
  "v_max_i32_dpp v212, v212, v212 row_ror:4 row_mask:0xf bank_mask:0xf\n" "s_nop 1\n" "v_max_i32_dpp v212, v212, v212 quad_perm:[2,3,0,1] row_mask:0xf bank_mask:0xf\n" "s_nop 1\n" \
  "v_max_i32_dpp v212, v212, v212 quad_perm:[1,0,3,2] row_mask:0xf bank_mask:0xf\n" "s_nop 1\n" "v_readlane_b32 s61, v212, 0\n" "v_readlane_b32 s63, v212, 16\n" "s_max_i32 s61, s61, s63\n" \
  "s_ashr_i32 s61, s61, 6\n" "s_add_i32 s61, s61, s66\n" "s_add_i32 s61, s61, -3\n" "s_add_i32 s63, s61, s62\n" "s_add_i32 s68, s63, 3\n" "s_cmp_lt_i32 s61, -3\n" \
  "s_cselect_b32 s68, s62, s68\n" "s_cmp_gt_i32 s61, " XS(LIM1) "\n" "s_cbranch_scc1 ch_o4_anr\n" "s_cmp_le_i32 s61, 0\n" "s_cbranch_scc1 ch_o4_ane\n" "s_movk_i32 s60, 100\n" \
  "s_mov_b32 vcc_lo, s62\n" "ch_o4_an1:\n" ".rept 8\n" "s_memrealtime vcc\n" "s_sub_u32 s61, vcc_lo, s63\n" "s_cbranch_scc0 ch_o4_an2\n" ".endr\n" "s_sub_u32 s60, s60, 1\n" \
  "s_cbranch_scc0 ch_o4_an1\n" "ch_o4_an2:\n" "s_min_u32 s61, s61, 3\n" "s_mul_i32 s61, s61, " XS(CPT) "\n" "s_and_b32 s61, s61, -4\n" "s_waitcnt lgkmcnt(0)\n" "s_getpc_b64 vcc\n" \
  "s_add_i32 s61, s61, 16\n" "s_add_u32 vcc_lo, vcc_lo, s61\n" "s_addc_u32 vcc_hi, vcc_hi, 0\n" "s_setpc_b64 vcc\n" ".rept 3*" XS(CPT) "/4\n" "s_nop 0\n" ".endr\n" "ch_o4_ane:\n" \
  "s_xor_b32 s54, s54, 0x400\n" ".p2align 3\n" "ch_o4_go:\n" "s_nop 0\n" "s_cmp_lt_u32 s52, 4\n" "s_barrier\n" ST0 "s_cbranch_scc0 ch_o4_l2\n" "ch_o4_l1:\n" \
  "buffer_load_dwordx4 v[160:163], v217, s[40:43], s53 offen offset:0 sc1\n" "s_nop " XS(OG) "\n" "s_nop 0\n" "buffer_load_dwordx4 v[164:167], v217, s[40:43], s53 offen offset:1024 sc1\n" \
  "s_nop " XS(OG) "\n" "s_nop 0\n" "buffer_load_dwordx4 v[168:171], v217, s[40:43], s53 offen offset:2048 sc1\n" "s_nop " XS(OG) "\n" "s_nop 0\n" \
  "buffer_load_dwordx4 v[172:175], v217, s[40:43], s53 offen offset:3072 sc1\n" "ch_o4_mul:\n" "s_nop 0\n" "s_waitcnt vmcnt(3)\n" ST1 \
  "v_smfmac_f32_16x16x64_bf16 v[176:179], v[160:163], v[0:7], v216\n" "v_cmp_eq_u32_e64 vcc, -1, v160\n" "v_smfmac_f32_16x16x64_bf16 v[180:183], v[160:163], v[8:15], v216\n" \
  "v_smfmac_f32_16x16x64_bf16 v[184:187], v[160:163], v[16:23], v216\n" "s_nop 0\n" "s_cbranch_vccnz ch_o4_slow2\n" "v_smfmac_f32_16x16x64_bf16 v[188:191], v[160:163], v[24:31], v216\n" \
  "s_nop 0\n" "s_waitcnt vmcnt(2)\n" "v_smfmac_f32_16x16x64_bf16 v[176:179], v[164:167], v[40:47], v216\n" "v_cmp_eq_u32_e64 vcc, -1, v164\n" \
  "v_smfmac_f32_16x16x64_bf16 v[180:183], v[164:167], v[48:55], v216\n" "v_smfmac_f32_16x16x64_bf16 v[184:187], v[164:167], v[56:63], v216\n" "s_nop 0\n" "s_cbranch_vccnz ch_o4_slow2\n" \
  "v_smfmac_f32_16x16x64_bf16 v[188:191], v[164:167], v[64:71], v216\n" "s_nop 0\n" "s_waitcnt vmcnt(1)\n" "v_smfmac_f32_16x16x64_bf16 v[176:179], v[168:171], v[80:87], v216\n" \
  "v_smfmac_f32_16x16x64_bf16 v[180:183], v[168:171], v[88:95], v216\n" "v_smfmac_f32_16x16x64_bf16 v[184:187], v[168:171], v[96:103], v216\n" \
  "v_smfmac_f32_16x16x64_bf16 v[188:191], v[168:171], v[104:111], v216\n" "s_nop 0\n" "s_waitcnt vmcnt(0)\n" ST2 "v_smfmac_f32_16x16x64_bf16 v[176:179], v[172:175], v[120:127], v216\n" \
  "v_smfmac_f32_16x16x64_bf16 v[180:183], v[172:175], v[128:135], v216\n" "v_smfmac_f32_16x16x64_bf16 v[184:187], v[172:175], v[136:143], v216\n" \
  "v_smfmac_f32_16x16x64_bf16 v[188:191], v[172:175], v[144:151], v216\n" ST3 "v_add_f32 v196, v176, v177\n" "v_add_f32 v200, v178, v179\n" \
  "v_add_f32_dpp v200, v180, v181 quad_perm:[0,1,2,3] row_mask:0xf bank_mask:0x3\n" "v_add_f32_dpp v196, v182, v183 quad_perm:[0,1,2,3] row_mask:0xf bank_mask:0xc\n" \
  "v_add_f32_e64 v197, v184, v185\n" "v_add_f32 v201, v186, v187\n" "s_nop 1\n" "v_add_f32_dpp v201, v188, v189 quad_perm:[0,1,2,3] row_mask:0xf bank_mask:0x3\n" \
  "v_add_f32_dpp v197, v190, v191 quad_perm:[0,1,2,3] row_mask:0xf bank_mask:0xc\n" "v_add_f32_dpp v196, v200, v196 row_ror:8 row_mask:0xf bank_mask:0xf\n" \
  "v_add_f32_dpp v197, v201, v197 row_ror:8 row_mask:0xf bank_mask:0xf\n" "ds_write2st64_b32 v218, v196, v197 offset1:8\n" "v_cmp_u_f32 vcc, v196, v196\n" "s_cbranch_vccnz ch_o4_slow\n" \
  "ch_o4_back:\n" "s_cmp_gt_u32 s52, 2\n" "s_waitcnt lgkmcnt(0)\n" "s_barrier\n" "s_cbranch_scc1 ch_o4_rest\n" ST4 "ds_read_b128 v[204:207], v219\n" \
  "ds_read_b128 v[208:211], v219 offset:1024\n" "s_add_i32 s60, s57, 6*0x8000\n" "s_cmp_ge_u32 s60, 9*0x8000\n" "s_cselect_b32 s61, 9*0x8000, 0\n" "s_sub_i32 s60, s60, s61\n" \
  "v_mov_b32 v213, -1\n" ST7 "s_waitcnt lgkmcnt(1)\n" "v_add_f32 v204, v204, v205\n" "v_add_f32 v206, v206, v207\n" "s_waitcnt lgkmcnt(0)\n" "v_add_f32 v208, v208, v209\n" \
  "v_add_f32 v210, v210, v211\n" "v_add_f32 v208, v208, v204\n" "v_add_f32 v210, v210, v206\n" "v_add_f32 v204, v208, v210\n" "v_add_f32 v205, v208, v210\n" "s_nop 1\n" \
  "v_permlane32_swap_b32 v204, v205\n" "v_cvt_pk_bf16_f32 v204, v204, v205\n" ST6 "buffer_store_dword v204, v220, s[40:43], s57 offen sc1\n" ST5 "s_sleep " XS(RSL) "\n" \
  "buffer_store_dword v213, v220, s[40:43], s60 offen sc1\n" "ch_o4_nxt:\n" PTS PTA "s_add_i32 s57, s57, 3*0x8000\n" "s_cmp_eq_u32 s57, 9*0x8000\n" "s_cselect_b32 s57, 0, s57\n" \
  "s_xor_b32 s53, s53, s69\n" "s_mov_b32 s60, s69\n" "s_xor_b32 s69, s69, s70\n" "s_mov_b32 s70, s60\n" "s_add_i32 s56, s56, 1\n" "s_cmp_lt_u32 s56, 63\n" "s_cbranch_scc1 ch_o4_loop\n" \
  "s_branch ch_o4_fin\n" "ch_o4_tim:\n" "s_memrealtime s[60:61]\n" "s_waitcnt lgkmcnt(0)\n" "s_sub_i32 s60, s60, s68\n" "s_lshl_b32 s60, s60, 6\n" "s_sub_i32 s60, s60, s58\n" \
  "s_ashr_i32 s60, s60, " XS(DSO) "\n" "s_min_i32 s60, s60, " XS(DUP) "\n" "s_add_i32 s58, s58, s60\n" "s_branch ch_o4_nxt\n" "ch_o4_tch:\n" "s_add_i32 s60, s56, 2\n" "s_cmp_eq_u32 s60, 64\n" \
  "s_cbranch_scc1 ch_o4_nxt\n" "s_lshl_b32 s61, s60, 19\n" "s_lshl_b32 s62, s60, 11\n" "s_and_b32 s62, s62, 0x2000\n" "s_add_i32 s61, s61, s62\n" "s_sleep " XS(TSL) "\n" \
  "s_cmp_gt_u32 s68, s60\n" "s_cbranch_scc1 ch_o4_tk\n" "buffer_load_dword v214, v219, s[48:51], 0 offen sc1\n" "s_waitcnt vmcnt(0)\n" "v_cmp_gt_u32 vcc, 8, v214\n" "s_ff1_i32_b64 s68, vcc\n" \
  "s_cmp_gt_u32 s68, s60\n" "s_cbranch_scc0 ch_o4_nxt\n" "ch_o4_tk:\n" "buffer_load_dword v214, v220, s[44:47], s61 offen sc1\n" "s_branch ch_o4_nxt\n" "ch_o4_rest:\n" "s_cmp_eq_u32 s52, 7\n" \
  "s_cbranch_scc1 ch_o4_tim\n" "s_cmp_lg_u32 s52, 6\n" "s_cbranch_scc1 ch_o4_tch\n" "s_and_b32 s60, s56, 31\n" "s_cmp_lg_u32 s60, s55\n" "s_cbranch_scc1 ch_o4_nxt\n" "s_sub_u32 s60, s56, 1\n" \
  "s_cbranch_scc1 ch_o4_nxt\n" "s_lshl_b32 s61, s60, 7\n" "s_add_i32 s62, s61, " XS(SY_CS0) "*4\n" "s_lshl_b32 s61, s60, 10\n" "s_add_i32 s63, s61, " XS(SY_CS0) "*4-48*7*128\n" \
  "s_cmp_lt_u32 s60, 48\n" "s_cselect_b32 s62, s62, s63\n" "s_cselect_b32 exec_lo, 1, 255\n" "s_mov_b32 exec_hi, 0\n" "s_sleep " XS(RSL) "\n" "v_lshlrev_b32 v212, 3, v217\n" \
  "buffer_store_dword v216, v212, s[48:51], s62 offen sc1\n" "s_mov_b64 exec, -1\n" "s_branch ch_o4_nxt\n" "ch_o4_l2:\n" ".rept " XS(OD) "\n" "s_nop 3\n" ".endr\n" "s_branch ch_o4_l1\n" \
  "ch_o4_slow:\n" "v_max3_u32 v212, v160, v161, v162\n" "v_max3_u32 v212, v212, v163, v164\n" "v_max3_u32 v212, v212, v165, v166\n" "v_max3_u32 v212, v212, v167, v168\n" \
  "v_max3_u32 v212, v212, v169, v170\n" "v_max3_u32 v212, v212, v171, v172\n" "v_max3_u32 v212, v212, v173, v174\n" "v_max_u32 v212, v212, v175\n" "v_cmp_eq_u32 vcc, -1, v212\n" \
  "s_cbranch_vccz ch_o4_back\n" "s_sub_u32 s59, s59, 1\n" "s_cselect_b32 s59, 0, s59\n" "s_cbranch_scc1 ch_o4_back\n" "ch_o4_slow2:\n" CNTO "s_waitcnt vmcnt(0)\n" "ch_o4_wait:\n" \
  "v_max3_u32 v212, v160, v161, v162\n" "v_max_u32 v212, v212, v163\n" "v_cmp_eq_u32 vcc, -1, v212\n" "s_cbranch_vccz ch_o4_h0\n" \
  "buffer_load_dwordx4 v[160:163], v217, s[40:43], s53 offen offset:0 sc1\n" "ch_o4_h0:\n" "v_max3_u32 v212, v164, v165, v166\n" "v_max_u32 v212, v212, v167\n" "v_cmp_eq_u32 vcc, -1, v212\n" \
  "s_cbranch_vccz ch_o4_h1\n" "buffer_load_dwordx4 v[164:167], v217, s[40:43], s53 offen offset:1024 sc1\n" "ch_o4_h1:\n" "v_max3_u32 v212, v168, v169, v170\n" "v_max_u32 v212, v212, v171\n" \
  "v_cmp_eq_u32 vcc, -1, v212\n" "s_cbranch_vccz ch_o4_h2\n" "buffer_load_dwordx4 v[168:171], v217, s[40:43], s53 offen offset:2048 sc1\n" "ch_o4_h2:\n" "v_max3_u32 v212, v172, v173, v174\n" \
  "v_max_u32 v212, v212, v175\n" "v_cmp_eq_u32 vcc, -1, v212\n" "s_cbranch_vccz ch_o4_h3\n" "buffer_load_dwordx4 v[172:175], v217, s[40:43], s53 offen offset:3072 sc1\n" "ch_o4_h3:\n" \
  "v_mov_b64 v[176:177], 0\n" "v_mov_b64 v[178:179], 0\n" "v_mov_b64 v[180:181], 0\n" "v_mov_b64 v[182:183], 0\n" "v_mov_b64 v[184:185], 0\n" "v_mov_b64 v[186:187], 0\n" \
  "v_mov_b64 v[188:189], 0\n" "v_mov_b64 v[190:191], 0\n" "s_waitcnt vmcnt(0)\n" "v_max3_u32 v212, v160, v161, v162\n" "v_max3_u32 v212, v212, v163, v164\n" \
  "v_max3_u32 v212, v212, v165, v166\n" "v_max3_u32 v212, v212, v167, v168\n" "v_max3_u32 v212, v212, v169, v170\n" "v_max3_u32 v212, v212, v171, v172\n" "v_max3_u32 v212, v212, v173, v174\n" \
  "v_max_u32 v212, v212, v175\n" "v_cmp_eq_u32 vcc, -1, v212\n" "s_cbranch_vccz ch_o4_mul\n" "s_sub_u32 s59, s59, 1\n" "s_cselect_b32 s59, 0, s59\n" "s_cbranch_scc0 ch_o4_wait\n" \
  "v_mov_b32 v160, 0\n" "v_mov_b32 v164, 0\n" "v_mov_b32 v168, 0\n" "v_mov_b32 v172, 0\n" "s_mov_b32 s59, 0\n" "s_nop 0\n" "s_branch ch_o4_mul\n" "ch_o4_fin:\n" "s_waitcnt vmcnt(0)\n"
typedef __attribute__((ext_vector_type(16))) unsigned u32x16;
typedef __attribute__((ext_vector_type(32))) unsigned u32x32;
#define C_R0 114688
#define C_IMG (C_R0 + 37120)
__device__ __forceinline__ void count_votes(const u32 fv, int& xk, int& xb) {
  xk = 0; xb = X_W2;
#pragma unroll
  for (int i = 0; i < 3; i++) {
    const u32 m = (u32)__ballot((fv >> i) & 1u);
    const int a = i == 1 ? __popc(m & 0xF0000u) > 2 : __popc(m & 255u) > 4, b = i == 1 ? __popc(m & 0xF00000u) > 2 : __popc(m & 0xFF00u) > 4;
    xk |= a << (12 + i); xb |= (a ^ b) << (12 + i);
  }
}
#define TG_CLAIM2(n) do { SB0(); VMW(n); asm volatile("" : "+v"(st.ep)); const int t_ = TIDX; \
    st.l = ((u32)__builtin_amdgcn_readfirstlane((int)st.ep[0]) + 16u) & ~15u; st.h = (u32)__builtin_amdgcn_readfirstlane((int)st.ep[1]); \
    st.old = (u32x2){st.l, st.h}; \
    __builtin_amdgcn_raw_buffer_store_b64(st.old, RSS, (t_ & 63) ? PJ_OOB : 0, st.duty >= 0 ? TG_CF(st.duty) * 8 : PJ_OOB, 16); \
    asm volatile("s_nop 4\n\tbuffer_atomic_swap_x2 %0, %1, %2, 0 offen sc0" : "+{v[252:253]}"(st.old) : "v"(t_ == 448 ? TG_CL(st.slot) * 8 : PJ_OOB), "s"(RSS) : "memory"); SB0(); } while (0)
__device__ __forceinline__ u32x4 rs4(const void* q, u32 bytes) {
  return (u32x4){(u32)(uintptr_t)q, (u32)((uintptr_t)q >> 32) & 0xFFFFu, bytes, 0x00020000u};
}
__device__ __forceinline__ u32x16 sargs(u32x4 a, u32x4 b, u32x4 c, int d0, int d1, int d2, int d3) {
  u32x16 r;
#pragma unroll
  for (int i = 0; i < 4; i++) { r[i] = (u32)__builtin_amdgcn_readfirstlane((int)a[i]); r[4 + i] = (u32)__builtin_amdgcn_readfirstlane((int)b[i]); r[8 + i] = (u32)__builtin_amdgcn_readfirstlane((int)c[i]); }
  r[12] = (u32)__builtin_amdgcn_readfirstlane(d0); r[13] = (u32)__builtin_amdgcn_readfirstlane(d1); r[14] = (u32)__builtin_amdgcn_readfirstlane(d2); r[15] = (u32)__builtin_amdgcn_readfirstlane(d3);
  return r;
}
#define CV8(a) "v" #a "0", "v" #a "1", "v" #a "2", "v" #a "3", "v" #a "4", "v" #a "5", "v" #a "6", "v" #a "7"
#define CV10(a) CV8(a), "v" #a "8", "v" #a "9"
#define CS10(a) "s" #a "0", "s" #a "1", "s" #a "2", "s" #a "3", "s" #a "4", "s" #a "5", "s" #a "6", "s" #a "7", "s" #a "8", "s" #a "9"
#define CLS "s56", "s57", "s58", "s59", CS10(6), CS10(7), "vcc", "scc", "memory"
#define WIN() ((char*)(((uintptr_t)p.ws + O_XW + 0x1FFFFFu) & ~(uintptr_t)0x1FFFFFu))
#define AWAIT_ALL(fk) do { const int io_ = lane < 24 ? TG_VOTE(lane) : lane < 32 ? (fk) : TG_INIT(lane - 32); int it_ = 0; fv = TG_LD(io_); fp = TG_LD(lane); \
    __syncthreads(); TG_CHECK(151552); \
    while (!__all(TG_OK(fv)) && ++it_ < SPIN_CAP) { __builtin_amdgcn_s_sleep(1); fv = TG_LD(io_); } \
    count_votes(fv[0], xk, xb); TG_KPW(fp, 2); } while (0)
__device__ __forceinline__ void head_body(const Params& p, Start& st, char* smem, const int h, const int wv) {
  TG_BEGIN();
  const int wid = wv, w = wv & 3;
  u32x4 va;
  const __amdgpu_buffer_rsrc_t rq = RSRC(p.BWq, 576 * 8192), rk = RSRC(p.BWk, 576 * 8192), rv = RSRC(p.BWv, 576 * 8192);
  {
    const int tid = TIDX, lane = tid & 63, l15 = lane & 15, lq = lane >> 4;
    const int mo = ((l15 >> 2) * 2 + (l15 & 1)) * 1152 + lq * 32 + (l15 & 2) * 8;
    DMA16(RSRC(p.Bm, 8 * 1152), C_IMG + wid * 1024, mo, wid * 128, 0);
    DMA16(RSRC(p.Bm, 8 * 1152), C_IMG + 8192, mo, 1024, 0);
    va[0] = (wid * 4 + lq) * 8192 + h * 256 + (((l15 >> 1) ^ ((lq & 1) | (w << 1))) << 5) + (lane & 1) * 16;
    auto req = [&](const int r) {
#pragma unroll
      for (int i = 0; i < 6; i++) DMA16(i < 2 ? rq : i < 4 ? rk : rv, r * 49152 + i * 8192 + wid * 1024, va[0], (r * 64 + (i & 1) * 32) * 8192, 0);
      SB0();
    };
    req(0); req(1);
    const int r2 = l15 >> 2;
    va[1] = ((lq >> 1) * 16 + (lq & 1) * 4 + (r2 >> 1) * 8 + (r2 & 1)) * 256 + (((2 * w) ^ ((r2 & 1) | ((lq & 1) << 1) | ((r2 >> 1) << 2))) << 5) + (lane & 3) * 8;
    va[2] = w * 2048 + lane * 16; va[3] = C_R0 + tid * 16;
    TG_CLAIM2(14);
    req(2);
  }
  u32x32 w0, w1, w2, w3, w4; u32x16 w5; u32x8 w6;
  int t_;
#define W_OUT "={v[0:31]}"(w0), "={v[32:63]}"(w1), "={v[64:95]}"(w2), "={v[96:127]}"(w3), "={v[128:159]}"(w4), "={v[160:175]}"(w5)
#define W_IN "{v[0:31]}"(w0), "{v[32:63]}"(w1), "{v[64:95]}"(w2), "{v[96:127]}"(w3), "{v[128:159]}"(w4), "{v[160:175]}"(w5)
#define S_OPS "+{v[248:251]}"(va), "+{v[252:253]}"(st.old), [t] "=&s"(t_) : [lw] "s"(wid * 1024), [r0] "s"(rq), [r1] "s"(rk), [r2] "s"(rv)
  int xk, xb, kp;
  u32x2 fv, fp;
  PRDECL(h);
  asm volatile("s_cmp_lt_u32 %[lw], 0x1000\n s_cbranch_scc0 ch_s1\n" CH_SA "s_branch ch_s2\n ch_s1:\n" CH_SB "ch_s2:\n"
               : W_OUT, "={v[176:183]}"(w6), S_OPS : "v184", "v185", "v186", "v187", "v188", "v189", CV10(19), "v244", "v245", "scc", "memory");
  {
    const int tid = TIDX, lane = tid & 63, l15 = lane & 15, lq = lane >> 4;
    TG_NOTE(151552);
#pragma unroll
    for (int i = 0; i < 2; i++) *(u32x4*)(smem + C_R0 + 2048 + i * 8192 + tid * 16) = (u32x4){~0u, ~0u, ~0u, ~0u};
    if (wid < 4) *(u32x4*)(smem + C_R0 + 10240 + tid * 16) = (u32x4){(u32)(81920 + lq * 1024 + (w & 1) * 256 + l15 * 16),
        (u32)(65536 + lq * 1024 + (((l15 >> 2) & 1) | ((l15 & 3) << 1) | (l15 & 8)) * 16),
        (u32)(98304 + (lane & 2) * 256 + ((lq & 1) | ((l15 >> 2) << 1) | (((lq ^ lane) & 2) << 2)) * 16 + (lane & 1) * 8),
        (u32)(65536 + lq * 1024 + (lane & 2) * 256 + (((l15 >> 2) & 1) | (w << 1) | ((l15 ^ (l15 << 2)) & 8) | ((l15 & 1) << 4)) * 16)};
    *(u32*)(smem + C_R0 + 46336 + (tid & 255) * 4) = ~0u;
    GMAX(85); AWAIT_ALL(TG_VOTE(0)); GMAX(69);
    const int wa = C_R0 + (w >> 1) * 1024 + ((l15 >> 2) & 1) * 512 + (w & 1) * 256 + (l15 & 3) * 32;
    const bool lo = wid < 4;
    u32x8 vb = {(l15 & 2) ? 0xEEEEu : 0x4444u, (u32)(C_R0 + lane * 16),
                (u32)(lo ? C_R0 + lane * 8 : wa + 2048 + lq * 8 + (l15 >> 3) * 4),
                (u32)(lo ? wa + (l15 >> 3) * 16 + lq * 4 : C_R0 + tid * 16),
                (u32)(lo ? C_R0 + tid * 4 : lane * 16 + w * 2048),
                (u32)(lo ? C_R0 + tid * 16 : w >= 2 ? lane * 16 : (lane & 32) * 16 + (((lane >> 4) & 1) | ((lane & 1) << 1) | ((((lane >> 3) ^ (lane >> 5)) & 1) << 2) | ((lane & 6) << 2)) * 16),
                (u32)(lo ? C_R0 + (lane < 32 ? 6144 + (lane & 16) * 64 + (lane & 8) * 36 : 3072 + 16 + (lane & 24) * 36) : (lq * 4 + (l15 & 2) + (l15 >> 3)) * 128 + (((l15 >> 2) & 1) * 2 + (l15 & 1)) * 16),
                (u32)(lo ? (h >> 2) * 32768 + (OGEO((l15 >> 2) & 1) ^ xk) + (h & 3) * 1024 + w * 256 + (l15 & 3) * 64 + (l15 >> 3) * 32 + lq * 8
                         : lane < 4 ? ((41 * 32768 + 4096) ^ XGEO(lane >> 1) ^ xk) + h * 16 + (lane & 1) * 1024 : PJ_OOB)};
    static_assert(S_OB == S_QB, "one descriptor");
    u32x16 sa = sargs(rs4(WIN(), X_W2), rs4(BIGP(kp, S_QB), 3 * ACB + KVO), rs4(p.ws, O_XW), wid,
                      lo ? ((w >> 1) * 16 + (w & 1)) * RP + h * 256 : w == 3 ? (32 * 32768 + 4096) ^ XGEO(SEP ? 0 : h >> 4) ^ xk : (w == 2 ? 0 : (w + 1) * ACB + KVO) + h * RP,
                      lo ? ((w & 1) * 16 + (h >> 1)) * RP + ((h & 1) * 4 + (w >> 1) * 2) * 1024 : w == 0 ? 65536 : w == 1 ? 98304 : 81920,
                      lo ? SY_SEG(62, h * 4 + w) * 4 : SY_BLK(w, h >> 1, 0) * 4);
    asm volatile(PRO "s_cmp_lt_u32 s52, 4\n s_cbranch_scc0 ch_h1\n" CH_HA "s_branch ch_hb_fin\n ch_h1:\n" CH_HB PRE
                 : "+{s[40:55]}"(sa), "+{v[184:191]}"(vb), "+{v[176:183]}"(w6) PROUT : W_IN
                 : "v192", "v193", "v194", "v195", "v196", "v197", "v198", "v199", CV10(20), CV10(21), CV10(22), CV10(23), CV10(24), "v250", "v251", "v252", "v253", CLS);
  }
  PRFIN(h);
  if (TIDX == 64 && h == 0) st4_sc1(WSP(u32, 0) + TG_EP * 2, ld4_sc1(WSP(u32, 0) + TG_EP * 2) + 16u);
}
__device__ __forceinline__ void stream_body(const Params& p, Start& st, char* smem, const int m, const int cb, int& kp, const int wv) {
  TG_BEGIN();
  const int wid = wv;
  u32x8 va;
  const char* const W = (const char*)(__attribute__((address_space(1))) const char*)((const __attribute__((address_space(4))) u64*)__builtin_amdgcn_kernarg_segment_ptr())[(0x8A9 >> (m * 4)) & 15];
  const __amdgpu_buffer_rsrc_t rx = RSRC(p.Bx, 2048 * 1152), rw = RSRC(W, 576 * 8192);
  {
    const int tid = TIDX, lane = tid & 63, l15 = lane & 15, lq = lane >> 4;
    va[1] = ((wid & 3) * 8 + (lane >> 3)) * 1152 + (((lane & 7) ^ (lane >> 3)) << 4);
    va[0] = va[1] + (wid >> 2) * 128;
    va[2] = (wid * 2 + (lane >> 5)) * 8192 + cb * 512 + ((((lane >> 1) & 15) ^ (((wid & 1) * 2 + (lane >> 5)) | (wid & 4))) << 5) + (lane & 1) * 16;
#pragma unroll
    for (int t = 0; t < 5; t++) DMA16(rx, t * 8192 + (t == 4 ? wid & 3 : wid) * 1024, va[t == 4], t * 256, 0);
    auto req = [&](const int r) {
#pragma unroll
      for (int t = 0; t < 4; t++) DMA16(rw, (r + 1) * 36864 + t * 8192 + wid * 1024, va[2], ((8 - r) * 64 + t * 16) * 8192, 0);
      SB0();
    };
    req(0); req(1);
    va[3] = (lq * 8 + (l15 >> 2)) * 512 + (((wid * 2) ^ ((l15 >> 2) | ((lq & 1) << 2))) << 5) + (lane & 3) * 8;
    va[4] = l15 * 128 + ((lq ^ (l15 & 7)) << 4);
    va[5] = lane * 8;
    va[6] = (cb * 2 + (wid >> 2)) * RP + ((wid & 3) * 4 + (lq & 1) * 2 + (lq >> 1)) * 512 + l15 * 16;
    va[7] = lane ? PJ_OOB : 0;
    TG_CLAIM2(13);
    req(2);
  }
  u32x32 w0, w1, w2, w3; u32x16 w4, acc; u32x2 kv;
  u32x16 sa = sargs(rs4(W, 576 * 8192), rs4(p.Bx, 2048 * 1152), rs4(p.ws, O_XW), wid * 1024, SY_BLK(m, cb, 0) * 4, 32768 - (wid >> 2) * 4096, 0);
#define ST_CL "v176", "v177", "v178", "v179", CV10(18), CV10(19), CV8(20), "v216", "v217", CLS
  asm volatile(CH_ST1 : "={v[0:31]}"(w0), "={v[32:63]}"(w1), "={v[64:95]}"(w2), "={v[96:127]}"(w3), "={v[128:143]}"(w4), "={v[144:159]}"(acc), "={v[250:251]}"(kv),
               "+{v[252:253]}"(st.old) : "{v[208:215]}"(va), "{s[40:55]}"(sa) : ST_CL);
  TG_NOTE(151552);
  CFENCE(); __builtin_amdgcn_s_barrier(); CFENCE();
  TG_CHECK(151552);
  TG_KPW(kv, 2);
  GMAX(86);
  const u32x4 ro = rs4(BIGP(kp, m == 2 ? S_QB : S_KB + m * ACB), ACB);
#pragma unroll
  for (int i = 0; i < 4; i++) sa[i] = (u32)__builtin_amdgcn_readfirstlane((int)ro[i]);
  asm volatile(CH_ST2 : "+{v[144:159]}"(acc), "+{v[208:215]}"(va), "+{s[40:55]}"(sa)
               : "{v[0:31]}"(w0), "{v[32:63]}"(w1), "{v[64:95]}"(w2), "{v[96:127]}"(w3), "{v[128:143]}"(w4) : CV10(16), "v170", "v171", "v172", "v173", "v174", "v175", ST_CL);
}
__device__ __forceinline__ void outp_body(const Params& p, Start& st, char* smem, const int tg, const int j, const int wv) {
  TG_BEGIN();
  const int wid = wv, hw = wv ^ (tg * OHX) ^ ((j & 1) * OHR);
  u32x4 va; int vt;
  const __amdgpu_buffer_rsrc_t ro = RSRC(p.BWo, 4096 * 1152);
  {
    const int tid = TIDX, lane = tid & 63, l15 = lane & 15, lq = lane >> 4, r2 = l15 >> 2;
    va[0] = (hw * 512 + (lane >> 2)) * 1152 + j * 64 + (((lane & 3) ^ lq) << 4);
#pragma unroll
    for (int i = 0; i < 16; i++) DMA16(ro, wid * 16384 + i * 1024, va[0], i * 16 * 1152, 0);
    SB0();
    va[1] = j < 8 ? (hw * 512 + lane) * 1152 + 1024 + j * 16 : PJ_OOB;
    const int dim = (lq >> 1) * 32 + (lane & 2) * 8 + (r2 >> 1) * 8 + (lq & 1) * 4 + (r2 & 1);
    va[2] = wid * 16384 + dim * 64 + (((lq & 1) | (r2 & 2)) << 4) + (lane & 1) * 8;
    va[3] = wid * 16384 + dim * 16 + (lane & 1) * 8;
    vt = j < 8 ? PJ_OOB : (wid * 512 + (j & 7) * 64 + lane) * 1152 + 1024;
    TG_CLAIM2(16);
  }
  u32x32 w0, w1, w2, w3, w4;
  int t_, xk, xb, kp;
  u32x2 fv, fp;
  PRDECL(32 + tg * 16 + j);
  asm volatile(CH_SO : "={v[0:31]}"(w0), "={v[32:63]}"(w1), "={v[64:95]}"(w2), "={v[96:127]}"(w3), "={v[128:159]}"(w4), "+{v[216:219]}"(va), "+{v[252:253]}"(st.old), [t] "=&s"(t_)
               : [lw] "s"(wid * 16384), [r] "s"(ro), "{v220}"(vt) : "v200", "v221", "scc", "memory");
  const int tid = TIDX, lane = tid & 63, l = lane & 31;
  TG_NOTE(151552);
  GMAX(84);
  AWAIT_ALL(TG_VOTE(0));
  const int cp = SEP && lane >= 32 ? PJ_OOB : (32 * 32768 + 4096) ^ XGEO(lane >> 5) ^ xk;
  u32x8 vb = {(lane & 1) ? 0xEEEEu : 0x4444u, (u32)(lane * 16), (u32)((wid >> 2) * 1024 + ((lane & 1) * 32 + (lane >> 5) * 16 + (lane & 8) + ((lane >> 2) & 4) + ((lane >> 1) & 3)) * 16 + ((wid + ((lane >> 2) & 2) + (lane & 1)) & 3) * 4),
              (u32)(wid > 2 ? SY_BLK(wid - 3, tg * 8 + (j >> 1), lane) * 4 : wid * 2048 + (wid < 2 ? lane : (lane & 0x27) + (lane & 8) * 2) * 16),
              (u32)(wid == 7 ? (lane < 4 ? ((41 * 32768 + 4096) ^ XGEO(lane >> 1) ^ xk) + (32 + tg * 16 + j) * 16 + (lane & 1) * 1024 : PJ_OOB) : wid > 2 ? (wid == 5 ? 0 : (wid - 2) * ACB + KVO) + (tg * 16 + j) * RP + lane * 128 : wid < 2 ? cp + (j >> 3) * 32768 + (j & 7) * 512 + wid * 256 + tg * 128 + l * 4 : j < 8 ? cp + 65536 + (j * 2 + tg) * 128 + l * 4 : PJ_OOB), 0u, 0u, 0u};
  u32x16 sa = sargs(rs4(WIN(), X_W2), rs4(BIGP(kp, S_QB), 3 * ACB + KVO), rs4(p.ws, O_XW), wid, hw * 32768 + (OGEO(tg) ^ xk), (41 * 32768 + 4096) ^ XGEO(SEP ? 1 : tg) ^ xk, tg * 16 + j);
  asm volatile(PRO "s_bitcmp1_b32 s55, 3\n s_cbranch_scc1 ch_o1\n" CH_O5 "s_branch ch_o4_fin\n ch_o1:\n" CH_O4 PRE : "+{s[40:55]}"(sa), "+{v[216:223]}"(vb) PROUT : "{v[0:31]}"(w0), "{v[32:63]}"(w1), "{v[64:95]}"(w2), "{v[96:127]}"(w3), "{v[128:159]}"(w4)
               : CV10(16), CV10(17), CV10(18), CV10(19), CV10(20), "v210", "v211", "v212", "v213", "v214", "v215", "v224", "v225", CLS);
  PRFIN(32 + tg * 16 + j);
}

__device__ __forceinline__ void wait_ge(const u32* w, u32 val, const int wv) {
  if (TIDX == 0) {
    int it = 0;
    while (ld4_sc1(w) < val && ++it < SPIN_CAP) __builtin_amdgcn_s_sleep(WPS);
  }
}
__device__ __forceinline__ void acquire_all(const int wv) {
  if (TIDX == 0) {
    __builtin_amdgcn_fence(__ATOMIC_ACQUIRE, "agent");
    VMW0();
  }
  __syncthreads();
}

#define KROT 0
#define OPD 4
#define PPD 2
#define WWM 2
#define WWN 2
#define NWM 4
#define NWN 1
#define WPD 2
#define WAD false
#define NAD false
#define KPX 0
#define NAW 0
#define NBW 10
#define NAM 4
#define NBM 2
#define NKV 8
#define CFG 1
#define TMASK 0x71
#define SBM 256
#define SPD 4
#define SMASK 0x00
#define TU 10
#define PORD 0x354
#define SDL 0
#define PWV 1
#define NPF 2
#define NPS 4
#define BST 0
#define NPR 3
#define QUIET 0
#define WDL 0
#define TGAP 6
#define TOUCH 1
__host__ __device__ __forceinline__ int decode_task(int q, int u, int& a, int& b) {
  if (q < 5) {
    const int nw = q < 4 ? NAW : NBW, nm = q < 4 ? NAM : NBM, n = q < 4 ? 4 : SM0 / 4;
    if (u < nw * 3) { a = u / 3; b = u % 3; }
    else if ((u -= nw * 3) < nm * 6) { a = nw + u / 6; b = 3 + u % 6; }
    else { u -= nm * 6; a = nw + nm + u / 9; b = 9 + u % 9; }
    if (a >= n) return -1;
    if (q < 4) a += q * 4;
    return q < 4 ? 2 : 3;
  }
  if (u >= 108) return -1;
  a = (u / 36 + SM0) % 3; b = u % 36; return 4;
}

#define TP(code)
#define TPC(ty, a, b) TP(((ty) << 16) | ((a) << 8) | (b))
#if defined(__HIP_DEVICE_COMPILE__) && !defined(KEEP_PK)
#define NOPK __attribute__((target("no-packed-fp32-ops")))
#else
#define NOPK
#endif
#define P0V 2
__device__ __forceinline__ Params prm() {
  const __attribute__((address_space(4))) u64* a = (const __attribute__((address_space(4))) u64*)__builtin_amdgcn_kernarg_segment_ptr();
  asm volatile("" : "+s"(a));
#define GP(T, x) ((T*)(__attribute__((address_space(1))) T*)(x))
  Params p;
  p.Ax = GP(const u16, a[0]); p.Ac = GP(const u16, a[1]); p.Awq = GP(const u16, a[2]); p.Awk = GP(const u16, a[3]); p.Awv = GP(const u16, a[4]); p.Awo = GP(const u16, a[5]);
  p.Bx = GP(const u16, a[6]); p.Bm = GP(const u16, a[7]); p.BWq = GP(const u16, a[8]); p.BWk = GP(const u16, a[9]); p.BWv = GP(const u16, a[10]); p.BWo = GP(const u16, a[11]);
  p.out = GP(u16, a[12]); p.ws = GP(char, a[13]); p.big = GP(char, a[14]);
  return p;
}
#define p prm()
__device__ __forceinline__ void mega_body(char* smem) {
  int* const s_task = (int*)(smem + 151552);
  const int wv = __builtin_amdgcn_readfirstlane(threadIdx.x >> 6);
  if (blockIdx.x == 0 && TIDX == 0) asm volatile("s_nop 0" ::: "memory");
  STS(0);
#define tid TIDX
  const int xcc = (int)(__builtin_amdgcn_s_getreg(20 | (3 << 11)) & 7u);
  Start st;
  const int bid = blockIdx.x;
  const int slot = bid >= 256 ? bid & 255 : xcc * 32 + (bid >> 3);
  const int x = (slot >> 5) & 3, j = slot & 31;
  st.slot = slot; st.duty = bid >= 200 && bid < 256 && (bid & 7) == 2 ? bid - 202 + wv : -1;
  const int wk = slot >> 7;
  const int role = x == 3 ? 64 + j : x == 2 ? 96 + j : SEP ? x * 32 + j : (j & 16) * 2 + x * 16 + (j & 15);
  int kp = 0, pre = 0, q = wk ? x : 4;
  u32* qh = WSP(u32, 0) + SY_QH(q);
  if (!wk && role < 64) {
    if (role < 32) head_body(p, st, smem, role, wv); else outp_body(p, st, smem, (role >> 4) & 1, role & 15, wv);
    GMAX(65); GMAX(68);
    return;
  } else {
    TPC(10, 0, 0);
    auto touch = [&](const int mask, const int b, const int rot) {
#pragma unroll
      for (int m = 0; m < 7; m++) {
        if (!((mask >> m) & 1)) continue;
        const int bytes = m == 2 ? 2048 * 1024 : (m == 3 || m == 4 ? 512 * 8192 : (m == 0 || m == 5 ? 2048 * 1152 : 576 * 8192));
        __builtin_amdgcn_raw_ptr_buffer_load_lds(RSRC(m == 0 ? p.Bx : m == 1 ? p.BWq : m == 2 ? p.Ac : m == 3 ? p.Awk : m == 4 ? p.Awv : m == 5 ? p.Ax : p.Awq, bytes),
                                                 (LDSP void*)(smem + C_BUFB + wv * 256), 4, (tid & 63) * 128, (rot * ((wv + m * 5) & 7) * 128 + b) * 8192, 0, 0);
      }
    };
    if (!wk && role >= 112) {
      const Params p0 = p;
#undef p
#define p p0
      TG_BEGIN();
      const int v = role & 15;
      const __amdgpu_buffer_rsrc_t rsX = RSRC((((uintptr_t)p.ws + O_XW + 0x1FFFFFu) & ~(uintptr_t)0x1FFFFFu), X_END);
      const int ty = v & 3, vi = v >> 2;
      {
        const __amdgpu_buffer_rsrc_t r = ty ? rsX : RSRC(BIGP(0, vi * 0x3000000), 32768);
        const int o = (ty ? 63 * 32768 : 0) + v * 256 + (wv & 1) * 128;
        u32* const bs = (u32*)smem;
        if (tid < 8) bs[tid] = ~0u;
        auto probe = [&](const int k) {
          __syncthreads();
          if (wv < PWV) {
            const u32 t0 = (u32)__builtin_readcyclecounter();
            __builtin_amdgcn_raw_buffer_store_b32(0u, r, k * 4096, o, 16);
            VMW0();
            const u32 t = (u32)__builtin_readcyclecounter() - t0;
            if ((tid & 63) == 0) __hip_atomic_fetch_min(bs + k, t, __ATOMIC_RELAXED, __HIP_MEMORY_SCOPE_WORKGROUP);
          }
        };
        if (ty) probe(0);
        TG_CLAIM(0);
        if (ty) probe(2);
        VMW0(); TG_NOTE(151552);
        __syncthreads();
        TG_CHECK(151552);
#pragma unroll 1
        for (int n = ty ? 2 : 0; n < (ty < 2 ? 2 * NPR : NPS); n++) probe((n & 1) * 2);
        __syncthreads();
        const int sd = __builtin_amdgcn_readfirstlane(bs[2] < bs[0] ? 2 : 0);
        int kb = sd;
        if (ty >= 2) {
#pragma unroll 1
          for (int n = 1; n < 4 * NPF; n++) probe(sd | (n & 1) | ((n & 2) << 1));
          __syncthreads();
          const u32 l0 = bs[sd], l1 = bs[sd | 1], l4 = bs[sd | 4], l5 = bs[sd | 5];
          kb = sd | (l1 + l5 < l0 + l4 ? 1 : 0) | (l4 + l5 < l0 + l1 ? 4 : 0);
        }
        kb ^= 5 ^ (XGEO(xcc) >> 12) ^ ((xcc >> 2) << 1);
        if (tid < 2) TG_ST(ty == 0 ? TG_KP(vi * 2 + tid) : ty == 1 ? TG_VOTE(16 + tid * 4 + vi) : TG_VOTE(tid * 8 + vi * 2 + (ty & 1)),
                           ty ? (u32)kb : (u32)(((kb >> 1) ^ KPX ^ 1) & 1));
      }
      TPC(14, 0, 0);
      {
        u32x2 fv; int it = 0;
        while (fv = TG_LD(TG_VOTE(16 + (tid & 7))), !__all(TG_OK(fv)) && ++it < SPIN_CAP) __builtin_amdgcn_s_sleep(4);
        TPC(15, 0, 0);
        int xk, xb;
        count_votes(fv[0], xk, xb);
        const int cv = (xk & 8192) + tid * 16;
#pragma unroll
        for (int i = 0; i < 5; i++) {
          const int n = i * 16 + v;
          u32 z0 = ~0u; asm volatile("" : "+v"(z0));
          __builtin_amdgcn_raw_buffer_store_b128((u32x4){z0, z0, z0, z0}, rsX, n < 68 ? cv + (n < 48 ? n : n + 16) * 16384 : PJ_OOB, 0, 16);
        }
        VMW0();
        __syncthreads();
        if (tid < 2) TG_ST(TG_INIT(v + tid * 16), 0u);
        u32x2 kv = TG_LD(tid & 63);
        TG_KPW(kv, 4);
      }
#undef p
#define p prm()
      TPC(11, 0, 0);
      __syncthreads();
    }
    TPC(12, wk, 0);
    if (wk) {
      kp = -1;
      TG_BEGIN();
      __builtin_amdgcn_raw_ptr_buffer_load_lds(RSRC(p.BWo, 576 * 8192), (LDSP void*)(smem + C_BUFB + wv * 256), 4, (tid & 63) * 128, (wv * 128 + (slot & 127)) * 8192, 0, 0);
      for (int i = 0; i < WDL; i++) __builtin_amdgcn_s_sleep(32);
      const int hd = x * 8 + (j >> 3) * 2 + ((j & 4) == 0);
      bf8 qr[8]; u32 vfl;
      proj_slot([&]() __attribute__((always_inline)) { touch(TMASK, slot & 127, 1); }, p, st, 3, PORD, (j & 7) * 2, 0, x * 4 + (j >> 3), kp, hd, qr, vfl, smem, wv);
      TPC(0, j & 7, hd);
      fa::attn_pair(p, BIGP(kp, S_KA + hd * 256), BIGP(kp, S_VA + hd * 256), hd, j & 7, kp, qr, vfl, smem, wv, qh, pre);
      if (tid < 2) __hip_atomic_fetch_add(WSP(u32, 0) + SY_CC((j & 7) ^ (tid * 7)), 1u, __ATOMIC_RELAXED, __HIP_MEMORY_SCOPE_AGENT);
      GMAX(80);
    } else if (role < 112) {
      stream_body(p, st, smem, (role >> 4) - 4, role & 15, kp, wv);
      GMAX(82);
    } else {
      kp = __builtin_amdgcn_readfirstlane(kp);
      for (int i = 0; i < TSD; i++) __builtin_amdgcn_s_sleep(32);
#pragma unroll
      for (int i = 0; i < 5; i++) touch(SMASK, (i * 8 + wv) * 16 + (j & 15), 0);
#pragma unroll 1
      for (int u = j & 15; u < 128; u += 16) {
        TPC(13, 0, 0);
        u32x4 sv[9];
        const __amdgpu_buffer_rsrc_t r = RSRC((u & 64) ? p.Awo : p.BWo, 4096 * 1152);
#pragma unroll
        for (int i = 0; i < 9; i++) { sv[i] = __builtin_amdgcn_raw_buffer_load_b128(r, tid * 16, (u & 63) * 64 * 1152 + i * 8192, 2); if (WOP) { SB0(); __builtin_amdgcn_s_sleep(WOP); SB0(); } }
        wof_task(p, sv, (u >> 6) ^ 1, u & 63, kp, smem, wv);
      }
      VMW0();
      __syncthreads();
      if (tid == 0) __hip_atomic_fetch_add(WSP(u32, 0) + SY_CW, 8u, __ATOMIC_RELAXED, __HIP_MEMORY_SCOPE_AGENT);
      GMAX(83);
    }
    {
      if (!wk) {
        for (int i = 0; i < BST * (role & 31); i++) __builtin_amdgcn_s_sleep(4);
        CLAIM();
      }
    }
  }
  while (true) {
    if (tid == 0) *s_task = pre;
    __syncthreads();
    const int t = __builtin_amdgcn_readfirstlane(*s_task);
    __syncthreads();
    int a, b;
    const int ty = decode_task(q, t, a, b);
    if (ty < 0) {
      if (q == 5) break;
      if (!wk && QUIET) break;
      q = q < 4 ? 4 : 5; qh = WSP(u32, 0) + SY_QH(q);
      CLAIM();
      continue;
    }
    TPC(ty, a, b);
    if (ty < 4) {
      const int nt0 = b < 3 ? b * 12 : b < 9 ? (b - 3) * 6 : (b - 9) * 4;
      wait_ge(WSP(u32, 0) + SY_CW, 128u, wv);
      if (ty == 2) wait_ge(WSP(u32, 0) + SY_CC(a >> 1), 32u, wv);
      else for (int i = 0; i < 4; i++) wait_ge(WSP(u32, 0) + SY_CS(a * 4 + i), 4u, wv);
      __syncthreads();
      TPC(ty + 20, a, b);
      const char* A = BIGP(kp, ty == 2 ? S_CA + ROWO(a * 128) : S_OB + OROW(a * 128));
      u16* C = p.out + (size_t)a * 128 * 1152 + (ty - 2) * 576 + nt0 * 16;
      if (b < 3) out_tile<4, 6, 2, 4>(A, BIGP(kp, S_WF(ty - 2)), nt0, C, smem, wv, qh, pre);
      else if (b < 9) out_tile<4, 3, 2, 5>(A, BIGP(kp, S_WF(ty - 2)), nt0, C, smem, wv, qh, pre);
      else out_tile<4, 2, 2, 6>(A, BIGP(kp, S_WF(ty - 2)), nt0, C, smem, wv, qh, pre);
      GMAX(64 + ty);
    } else {
      GMAX(81);
      small_out_task(p, a, b & 1, b >> 1, kp, smem, wv, qh, pre);
    }
  }
  TPC(99, 0, 0);
}

#undef tid
#undef p
__global__ NOPK void __launch_bounds__(512, 2) mega(Params p_) {
  __shared__ __attribute__((aligned(16))) char smem[LDS_ALL];
#define CODE_KB 76
#define CPM 8
  int t;
  asm volatile("s_load_dword s8, %1, 0\n\ts_load_dword s9, %1, 0x40\n\t"
               "s_getpc_b64 s[4:5]\n\ts_and_b32 s4, s4, 0xffffff00\n\ts_mov_b32 s6, %4\n\ts_mov_b32 s7, 0x20000\n\ts_mov_b32 m0, %5\n\ts_and_b32 s3, %2, %6\n\ts_lshl_b32 s3, s3, 13\n\t"
               "v_lshlrev_b32 %0, 7, %3\n\tbuffer_load_dword %0, s[4:7], s3 offen lds\n\ts_waitcnt lgkmcnt(0)"
               : "=&v"(t) : "s"(__builtin_amdgcn_kernarg_segment_ptr()), "s"(blockIdx.x), "v"(threadIdx.x), "n"(CODE_KB * 1024), "n"(150528), "n"(CPM)
               : "s3", "s4", "s5", "s6", "s7", "s8", "s9", "scc", "memory");
  mega_body(smem);
}
extern "C" void kernel_launch(void* const* d_in, const int* in_sizes, int n_in, void* d_out, int out_size, void* d_ws,
                              size_t ws_size, hipStream_t stream) {
  Params p;
  p.Ax = (const u16*)d_in[0]; p.Ac = (const u16*)d_in[1]; p.Awq = (const u16*)d_in[2]; p.Awk = (const u16*)d_in[3];
  p.Awv = (const u16*)d_in[4]; p.Awo = (const u16*)d_in[5]; p.Bx = (const u16*)d_in[6]; p.Bm = (const u16*)d_in[7];
  p.BWq = (const u16*)d_in[8]; p.BWk = (const u16*)d_in[9]; p.BWv = (const u16*)d_in[10]; p.BWo = (const u16*)d_in[11];
  p.out = (u16*)d_out;
  p.ws = (char*)d_ws;
  p.big = (char*)d_ws + O_BIG + 0x200000;
  mega<<<256, 512, 0, stream>>>(p);
}
```

```cpp
#include <hip/hip_runtime.h>
#include <hip/hip_bf16.h>
#include <stdint.h>
#include <type_traits>

typedef unsigned short u16;
typedef unsigned int u32;
typedef unsigned long long u64;
typedef __attribute__((ext_vector_type(8))) short bf8;
typedef __attribute__((ext_vector_type(4))) short bf4;
typedef __attribute__((ext_vector_type(4))) float f4;
typedef __attribute__((ext_vector_type(2))) float f2v;
typedef __attribute__((ext_vector_type(2))) __bf16 bf2v;
typedef __attribute__((ext_vector_type(4))) unsigned u32x4;
typedef __attribute__((ext_vector_type(2))) unsigned u32x2;
typedef float f32x16 __attribute__((ext_vector_type(16)));
typedef __attribute__((ext_vector_type(8))) unsigned u32x8;
typedef __attribute__((ext_vector_type(8))) __bf16 bh8;
typedef __attribute__((ext_vector_type(16))) __bf16 bh16;
#define LDSP __attribute__((address_space(3)))

__device__ __forceinline__ u32 pk2(float a, float b) {
  f2v x = {a, b};
  bf2v y = __builtin_convertvector(x, bf2v);
  return __builtin_bit_cast(u32, y);
}
__device__ __forceinline__ u32 umin(u32 a, u32 b) { return a < b ? a : b; }
__device__ __forceinline__ u32 umax(u32 a, u32 b) { return a > b ? a : b; }
__device__ __forceinline__ float rbf(float f) { return __uint_as_float(pk2(f, f) << 16); }
#define MFMA(a, b, c) __builtin_amdgcn_mfma_f32_16x16x32_bf16(a, b, c, 0, 0, 0)
#define MFMA16(a, b, c) __builtin_amdgcn_mfma_f32_16x16x16bf16_1k(a, b, c, 0, 0, 0)
#define SMF(a, b, c, idx) __builtin_amdgcn_smfmac_f32_16x16x64_bf16(__builtin_bit_cast(bh8, a), __builtin_bit_cast(bh16, b), c, idx, 0, 0)
__device__ __forceinline__ u32x8 ilv(bf4 a, bf4 b, bf4 c, bf4 d) {
  const u32x2 x = __builtin_bit_cast(u32x2, a), y = __builtin_bit_cast(u32x2, b), z = __builtin_bit_cast(u32x2, c), w = __builtin_bit_cast(u32x2, d);
  return (u32x8){x[0], x[1], y[0], y[1], z[0], z[1], w[0], w[1]};
}
__device__ __forceinline__ u32x8 cat16(bf8 a, bf8 b) {
  const u32x4 x = __builtin_bit_cast(u32x4, a), y = __builtin_bit_cast(u32x4, b);
  return (u32x8){x[0], x[1], x[2], x[3], y[0], y[1], y[2], y[3]};
}
__device__ __forceinline__ float swadd(float a, float b) {
  auto x = __builtin_amdgcn_permlane32_swap(__float_as_uint(a), __float_as_uint(b), false, false);
  return __uint_as_float(x[0]) + __uint_as_float(x[1]);
}
typedef __attribute__((ext_vector_type(2))) _Float16 h2v;
__device__ __forceinline__ u32x4 enc8(const float* v, u32 tag) {
  float m = fmaxf(fmaxf(fabsf(v[0]), fabsf(v[1])), fabsf(v[2]));
  m = fmaxf(fmaxf(m, fabsf(v[3])), fabsf(v[4]));
  m = fmaxf(fmaxf(m, fabsf(v[5])), fabsf(v[6]));
  m = fmaxf(m, fabsf(v[7]));
  m = m >= 0x1p-23f ? fmaxf(m, 0x1p-15f) : m;
  const u32 code = __float_as_uint(m) >> 27;
  const float inv = __uint_as_float((253u << 23) - (code << 27));
  u32x4 w;
#pragma unroll
  for (int i = 0; i < 4; i++) {
    f2v x = {v[2 * i], v[2 * i + 1]};
    x *= inv;
    w[i] = __builtin_bit_cast(u32, __builtin_convertvector(x, h2v));
  }
  const u32 t = code | (code << 15);
  w[0] = (w[0] & ~1u) | (tag & 1u);
  w[2] = (w[2] & ~1u) | (tag & 1u);
  w[1] = (w[1] & ~0x10001u) | (t & 0x10001u);
  w[3] = (w[3] & ~0x10001u) | ((t >> 2) & 0x10001u);
  return w;
}
#define FMIX_LO(acc, w, S) asm("v_fma_mix_f32 %0, %1, %2, %0 op_sel_hi:[1,0,0]" : "+v"(acc) : "v"(w), "v"(S))
#define FMIX_HI(acc, w, S) asm("v_fma_mix_f32 %0, %1, %2, %0 op_sel:[1,0,0] op_sel_hi:[1,0,0]" : "+v"(acc) : "v"(w), "v"(S))
__device__ __forceinline__ bf4 trrd(const char* p) {
  return __builtin_amdgcn_ds_read_tr16_b64_v4i16((LDSP bf4*)(p));
}
__device__ __forceinline__ bf8 cat8(bf4 a, bf4 b) { return __builtin_shufflevector(a, b, 0, 1, 2, 3, 4, 5, 6, 7); }
__device__ __forceinline__ void st8_sc1(void* p, u64 v) { __hip_atomic_store((u64*)p, v, __ATOMIC_RELAXED, __HIP_MEMORY_SCOPE_AGENT); }
__device__ __forceinline__ u32 ld4_sc1(const u32* p) { return __hip_atomic_load(p, __ATOMIC_RELAXED, __HIP_MEMORY_SCOPE_AGENT); }
__device__ __forceinline__ void st4_sc1(u32* p, u32 v) { __hip_atomic_store(p, v, __ATOMIC_RELAXED, __HIP_MEMORY_SCOPE_AGENT); }
#define VMW0() asm volatile("s_waitcnt vmcnt(0)" ::: "memory")
#define CFENCE() asm volatile("" ::: "memory")
#define VMW0C() do { __builtin_amdgcn_s_waitcnt(0x0F70); asm volatile("" ::: "memory"); } while (0)
#define SPIN_CAP (1 << 22)
#define XPS 16
#define SPS 4
#define SP2 4
#define WPS 8
__device__ __forceinline__ int tid_now(int wv) {
  int l; asm volatile("v_mbcnt_lo_u32_b32 %0, -1, 0\n\tv_mbcnt_hi_u32_b32 %0, -1, %0" : "=v"(l));
  return wv * 64 + l;
}
#define TIDX tid_now(wv)
#define CLAIM() do { if (TIDX == 0) pre = (int)__hip_atomic_fetch_add(qh, 1u, __ATOMIC_RELAXED, __HIP_MEMORY_SCOPE_AGENT); } while (0)

#define TSD 6
#define TFL 5
struct Params {
  const u16 *Ax, *Ac, *Awq, *Awk, *Awv, *Awo, *Bx, *Bm, *BWq, *BWk, *BWv, *BWo;
  u16* out;
  char* ws;
  char* big;
};
#define TG_KP(i) (i)
#define TG_CF(i) (8 + (i))
#define TG_VOTE(i) (64 + (i))
#define TG_INIT(i) (96 + (i))
#define TG_FK(i) (128 + (i))
#define TG_CL(i) (256 + (i))
#define TG_EP 512
#define SY0 2048
#define SY_QH(q) (SY0 + (q) * 32)
#define SY_CC(rb) (SY0 + 224 + (rb) * 32)
#define SY_CW (SY0 + 480)
#define SY_SEG(s, c) (SY_CS(62) + ((s) - 62) * 128 + (c))
#define SY_KMF(s) (SY0 + 704 + (s) * 32)
#define SY_CS(s) (SY0 + 2496 + ((s) < 48 ? (s) : 48 + ((s) - 48) * 8) * 32)
#define BLKS 1
#define SY_BLK(mi, cb, g) (SY0 + 8128 + ((mi) * 16 + (cb)) * 64 + (g))
#define SY_WORDS (SY0 + 56 * 256)

#define AL256(x) (((x) + 255) & ~255)
#define O_SYNC_END (SY_WORDS * 4)
#define RP 16384
#define ACB (2048 * RP)
#define ROWO(r) ((r) * RP + (((r) >> 7) & 1) * 8192)
#define S_QA 0
#define S_KA (1 * ACB)
#define S_VA (2 * ACB)
#define S_CA S_QA
#define S_QB (3 * ACB)
#define KVO 0x200000
#define S_KB (4 * ACB + KVO)
#define S_VB (5 * ACB + KVO)
#define S_OB S_QB
#define OBN 1
#define S_OL S_OB
#define OROW(r) (ROWO(r) ^ 8192)
#define S_WF(m) (6 * ACB + (8 << 20) + (m) * (12 << 20))
#define WFO(kc, f) (ROWO((kc) * 9 + ((f) >> 3)) + ((f) & 7) * 1024)
#define O_BIG (20 << 20)
#define BIG0 ((char*)((uintptr_t)p.big & ~(uintptr_t)0x1FFFFFu))
#define BIGP(kp, off) (BIG0 + (u32)((kp) * 0x200000 + (off)))
#define O_KM AL256(O_SYNC_END)
#define O_VM (O_KM + 64 * 8 * 4096 * 2)
#define O_XW (O_VM + 64 * 8 * 4096 * 2)
#define XGEO(x) ((((x) & 1) << 14) | (((x) & 2) << 11))
#define XBLK(n) ((n) * 32768 + (((n) & 16) << 10) + (((n) & 32) << 7))
#define X_M2 (56 * 32768)
#define X_ANN (62 * 32768)
#define X_W2 (1 << 21)
#define X_END (2 * X_W2)
#define WSP(T, off) ((T*)(p.ws + (off)))

#define XS0 8
#define SM0 52
#define LDS_TOTAL (131072 + 8192 + 16384)
#define LDS_ALL 163840

#define SB0() __builtin_amdgcn_sched_barrier(0)
#define RSRC(ptr, bytes) __builtin_amdgcn_make_buffer_rsrc((void*)(ptr), 0, (bytes), 0x00020000)
__device__ __forceinline__ bf8 ldsc1(__amdgpu_buffer_rsrc_t r, int off) {
  return __builtin_bit_cast(bf8, __builtin_amdgcn_raw_buffer_load_b128(r, off, 0, 16));
}
__device__ __forceinline__ u32x4 pair16(u32 a0, u32 a1, u32 b0, u32 b1) {
  auto r0 = __builtin_amdgcn_permlane16_swap(a0, b0, false, false);
  auto r1 = __builtin_amdgcn_permlane16_swap(a1, b1, false, false);
  return (u32x4){r0[0], r1[0], r0[1], r1[1]};
}
__device__ __forceinline__ int fsw(int k) { return (k & 3) | (((k >> 3) & 1) << 2); }

#define PJ_OOB 0x7F000000
#define VMW(n) asm volatile("s_waitcnt vmcnt(%0)" :: "n"(n) : "memory")
#define BARV(n) do { VMW(n); __builtin_amdgcn_s_barrier(); CFENCE(); } while (0)
#define BARL(n) do { asm volatile("s_waitcnt vmcnt(%0) lgkmcnt(0)" :: "n"(n) : "memory"); __builtin_amdgcn_s_barrier(); CFENCE(); } while (0)
#define DMA16(rs, dst, vo, so, aux) __builtin_amdgcn_raw_ptr_buffer_load_lds(rs, (LDSP void*)(smem + (dst)), 16, vo, so, 0, aux)
#define TRA(dst, a, off) asm volatile("ds_read_b64_tr_b16 %0, %1 offset:%2" : "=&v"(dst) : "v"(a), "i"(off) : "memory")
#define RDA(dst, a, off) asm volatile("ds_read_b128 %0, %1 offset:%2" : "=&v"(dst) : "v"(a), "i"(off) : "memory")
struct Start { u32x2 ep, old; u32 l, h; int slot, duty; };
#define RSS RSRC(p.ws, SY_WORDS * 4)
#define TG_OK(x) (((((x)[0] ^ st.l) >> 4) | ((x)[1] ^ st.h)) == 0u)
#define TG_LD(i) __builtin_bit_cast(u32x2, __hip_atomic_load(WSP(u64, 0) + (i), __ATOMIC_RELAXED, __HIP_MEMORY_SCOPE_AGENT))
#define TG_ST(i, v) st8_sc1(WSP(u64, 0) + (i), (u64)(st.l | (v)) | ((u64)st.h << 32))
#define TG_BEGIN() do { \
    __builtin_amdgcn_raw_buffer_store_b128((u32x4){0u, 0u, 0u, 0u}, RSS, (TIDX & 63) * 16, st.duty >= 0 ? SY0 * 4 + st.duty * 1024 : PJ_OOB, 16); \
    SB0(); asm volatile("s_nop 1" ::: "memory"); SB0(); st.ep = TG_LD(TG_EP); SB0(); } while (0)
#define TG_CLAIM(n) do { SB0(); VMW(n); asm volatile("" : "+v"(st.ep)); const int t_ = TIDX; \
    st.l = ((u32)__builtin_amdgcn_readfirstlane((int)st.ep[0]) + 16u) & ~15u; st.h = (u32)__builtin_amdgcn_readfirstlane((int)st.ep[1]); \
    st.old = (u32x2){st.l, st.h}; \
    __builtin_amdgcn_raw_buffer_store_b64(st.old, RSS, (t_ & 63) ? PJ_OOB : 0, st.duty >= 0 ? TG_CF(st.duty) * 8 : PJ_OOB, 16); \
    asm volatile("s_nop 4\n\tbuffer_atomic_swap_x2 %0, %1, %2, 0 offen sc0" : "+v"(st.old) : "v"(t_ == 448 ? TG_CL(st.slot) * 8 : PJ_OOB), "s"(RSS) : "memory"); SB0(); } while (0)
#define TG_NOTE(off) do { if (wv == 7) { asm volatile("" : "+v"(st.old)); \
    asm volatile("ds_write_b32 %0, %1\n\ts_waitcnt lgkmcnt(0)" :: "v"((int)(uintptr_t)smem + (off)), "v"(__builtin_amdgcn_readfirstlane(TG_OK(st.old) ? 1 : 0)) : "memory"); } } while (0)
#define TG_CHECK(off) do { int x_, y_; asm volatile("ds_read_b32 %0, %2\n\ts_waitcnt lgkmcnt(0)\n\tv_readfirstlane_b32 %1, %0\n\ts_cmp_eq_u32 %1, 0\n\ts_cbranch_scc1 1f\n\t" \
      "s_waitcnt vmcnt(0)\n\ts_barrier\n\ts_mov_b64 exec, -1\n\ts_mov_b64 vcc, %3\n\ts_mov_b32 m0, %4\n\ts_mov_b64 s[0:1], vcc\n\ts_and_b32 s2, m0, 0x1ff\n\ts_lshr_b32 s3, m0, 10\n\t" \
      "v_mbcnt_lo_u32_b32 v0, -1, 0\n\tv_mbcnt_hi_u32_b32 v0, -1, v0\n\tv_add_u32 v0, s3, v0\n\t" \
      "s_getpc_b64 s[4:5]\n\ts_add_u32 s4, s4, _Z4mega6Params@rel32@lo+4\n\ts_addc_u32 s5, s5, _Z4mega6Params@rel32@hi+12\n\ts_setpc_b64 s[4:5]\n1:" \
      : "=&v"(x_), "=&s"(y_) : "v"((int)(uintptr_t)smem + (off)), "s"(__builtin_amdgcn_kernarg_segment_ptr()), "s"((wv << 16) | 256 | ((st.slot + 1) & 255)) : "scc", "memory"); } while (0)
#define TG_KPW(kv, sl) do { if (!__all(TG_OK(kv))) { int it_ = 0; \
      do { __builtin_amdgcn_s_sleep(sl); kv = TG_LD(TIDX & 63); } while (!__all(TG_OK(kv)) && ++it_ < SPIN_CAP); } \
    kp = __builtin_amdgcn_readfirstlane(__popc((u32)__ballot((kv)[0] & 1u) & 255u) > 4); } while (0)
#define FKS(i)
#define C_BUFA 131072
#define C_BUFB (131072 + 16384)
#define AT_GK ((wid * 4 + (lane >> 4)) * RP + (lane & 15) * 16)
#define AT_GV (AT_GK ^ (((wid & 1) * 2 + (lane >> 5)) * 64))
#define AT_RS(off) RSRC(BIGP(kp, (off) + hd * 256), ACB - 8192 + 256)
constexpr int pj_pack(int S, int ph) { return S == 8 ? (ph == 1 ? 0x01 : ph == 2 ? 0x11 : ph == 3 ? 0x6423 : 0) : S == 0 ? (ph == 0 ? 0x7533 : ph == 1 ? 0xA8EC4 : ph == 2 ? 0xB9FD4 : 0) : 0; }
constexpr int pj_store(int S, int ph) { return ph == 0 ? 0 : S == 8 ? (ph > 1 ? (ph - 2) * 16 + 1 : 0) : S < 2 ? (S * 3 + 1 + ph) * 16 + 1
                                               : S < 6 && ph > 1 ? (S * 2 + 2 + ph) * 16 + 1 : 0; }
template <class F>
__device__ __forceinline__ void proj_slot(F early, const Params& p, Start& st, const int nu, const int ml, const int rt0, const int rs, const int cb, int& kp, const int hd,
                                          bf8* qr, u32& vfl, char* smem, const int wv) {
  const int tid = TIDX, lane = tid & 63, wid = wv;
  const int l15 = lane & 15, lq = lane >> 4, q4 = l15 >> 2, p4 = lane & 3, wr = wid >> 2, wc = wid & 3;
  const __amdgpu_buffer_rsrc_t rc = RSRC(p.ws, 0x10000000), rd = RSRC(BIG0, 0xE200000);

  int vB = ((wid * 4 + lq) * 4096 + (((l15 >> 1) ^ (lq | (((wid >> 1) & 1) << 2))) * 16 + (lane & 1) * 8)) * 2, vB1 = vB + 32 * 8192;
  int ao = (wr * 64 + l15) * 128 + ((lq ^ (l15 & 7)) << 4);
  int bo = (int)(uintptr_t)smem + 65536 + (lq * 8 + q4) * 256 + (((wc * 2) ^ (q4 | ((lq & 1) << 2))) << 5) + p4 * 8;
  int vD = 0, sp = C_BUFA + tid * 16;
  asm volatile("" : "+v"(sp));
  __amdgpu_buffer_rsrc_t rsA, rsB;
  int k2, vA, vA1, sa, sb, da = 128, xb = 256, yb = 64 * 8192, mn;
  u64 pa, pb;
  const int nb = (rt0 < 8 ? 16 - rt0 : rt0 + 2) * 4;
  auto unit = [&](const int u, const int part) __attribute__((always_inline)) {
    if (part == 0) {
      const __attribute__((address_space(4))) u64* a = (const __attribute__((address_space(4))) u64*)__builtin_amdgcn_kernarg_segment_ptr();
      mn = ((ml >> (u * 4)) & 15) * 4;
      pa = a[(0x110666 >> mn) & 15]; pb = a[(0x4328A9 >> mn) & 15];
    } else if (part == 1) {
      k2 = mn < 16 ? 1152 : 1024;
      sa = ((rt0 + u * rs) & 15) * 128 * k2 - 128; sb = mn == 12 ? (hd & 1) * 256 : 0;
      asm volatile("" : "+s"(k2), "+s"(sa), "+s"(sb));
    } else if (part == 2) {
      vA = TIDX & 63; vA1 = ((vA & 7) ^ (vA >> 3)) * 16; vA >>= 3;
      asm volatile("" : "+v"(vA), "+v"(vA1));
    } else if (part == 3) {
      vA = ((wid >> 1) * 32 + (wid & 1) * 8 + vA) * k2 + vA1; vA1 = vA + 16 * k2;
      asm volatile("" : "+v"(vA), "+v"(vA1));
    } else {
      rsA = RSRC((const char*)(__attribute__((address_space(1))) const char*)pa, 2048 * k2);
      rsB = RSRC((const char*)(__attribute__((address_space(1))) const char*)pb + cb * 512, k2 * 4096 - cb * 512);
      asm volatile("" : "+s"(rsA), "+s"(rsB));
    }
  };
#define PJ_ATT() do { const int lane = TIDX & 63; u32 f_, g_; int it_ = 0; \
    asm volatile("ds_read_b32 %0, %2\n\tds_read_b32 %1, %2 offset:256\n\ts_waitcnt lgkmcnt(0)" : "=&v"(f_), "=&v"(g_) : "v"((int)(uintptr_t)smem + C_BUFA + lane * 4) : "memory"); \
    while (!__all((lane >= nb || f_ >= 8u) && (lane >= 6 || g_ >= 8u)) && ++it_ < SPIN_CAP) { \
      f_ = ld4_sc1(WSP(u32, 0) + SY_BLK(4, cb, lane)); g_ = ld4_sc1(WSP(u32, 0) + SY_BLK(5, cb, lane)); VMW0C(); } \
    rsA = AT_RS(S_VA); rsB = AT_RS(S_KA); vA = AT_GV; vA1 = vA + 32 * RP; vB = AT_GK; vB1 = vB + 32 * RP; \
    k2 = RP / 2; da = yb = ROWO(128); xb = 64 * RP; sa = -ROWO(128); sb = 0; } while (0)
#define PJ_STAGE(dst, rs, v0, v1, so, aux) do { const int so_ = (so); \
    __builtin_amdgcn_raw_ptr_buffer_load_lds(rs, (LDSP void*)(smem + (dst) + wid * 1024), 16, v0, so_, 0, aux); \
    __builtin_amdgcn_raw_ptr_buffer_load_lds(rs, (LDSP void*)(smem + (dst) + wid * 1024 + 8192), 16, v1, so_, 0, aux); } while (0)
#define PJ_SA(c, dst, so) PJ_STAGE(dst, rsA, vA, vA1, so, (c) ? 16 : 0)
#define PJ_SB(c, dst, so) PJ_STAGE(dst, rsB, vB, vB1, so, (c) ? 16 : 0)
#define PJ_LDA(h) do { _Pragma("unroll") for (int m = 0; m < 4; m++) _Pragma("unroll") for (int k = 0; k < 2; k++) \
    At[m][k] = *(const bf8*)(smem + (ao ^ (k * 64)) + (h) * 16384 + m * 2048); } while (0)
#define PJ_TR(dst, a, off) asm volatile("ds_read_b64_tr_b16 %0, %1 offset:%2" : "=&v"(dst) : "v"(a), "i"(off) : "memory")
#define PJ_LDB(D, h) do { const int b1_ = bo ^ 32; _Pragma("unroll") for (int n = 0; n < 2; n++) _Pragma("unroll") for (int k = 0; k < 2; k++) { \
    bf4 lo_, hi_; PJ_TR(lo_, n ? b1_ : bo, (h) * 16384 + k * 8192); PJ_TR(hi_, n ? b1_ : bo, (h) * 16384 + k * 8192 + 1024); D[n][k] = cat8(lo_, hi_); } } while (0)
#define PJ_WV(n) asm volatile("s_waitcnt vmcnt(%0)" :: "n"(n) : "memory")
#define PJ_WL(n) asm volatile("s_waitcnt lgkmcnt(" #n ")" ::: "memory")
#define PJ_WL0 do { __builtin_amdgcn_s_waitcnt(0xC07F); asm volatile("" ::: "memory"); } while (0)
#define PJ_BAR __builtin_amdgcn_s_barrier()
#define PJ_SCHED __builtin_amdgcn_sched_barrier(0)
#define PJ_NOP do { PJ_SCHED; asm volatile("s_nop 7"); PJ_SCHED; } while (0)
#define PJ_P2(z, n, r, f) pk2(acc[(z) >> 3][(z) & 1][((z) >> 1) & 3][n][r] f, acc[(z) >> 3][(z) & 1][((z) >> 1) & 3][n][(r) + 1] f)
#define PJ_PK(z, f) pair16(PJ_P2(z, 0, 0, f), PJ_P2(z, 0, 2, f), PJ_P2(z, 1, 0, f), PJ_P2(z, 1, 2, f))
#define QSC * (1.4426950408889634f * 0.08838834764831845f)
#define PJ_QW(a) do { const int l_ = TIDX & 63; char* const w_ = smem + C_BUFA + (wc * 4 + ((l_ >> 4) & 1) * 2 + (l_ >> 5)) * 512 + wr * 256 + (l_ & 15) * 16; \
    _Pragma("unroll") for (int q = 0; q < 4; q++) *(u32x4*)(w_ + q * 8192) = PJ_PK((a) * 8 + q * 2, QSC); } while (0)
#define PJ_QR() do { const char* a_ = smem + C_BUFA + wc * 8192 + (TIDX & 63) * 16; _Pragma("unroll") for (int i = 0; i < 8; i++) qr[i] = *(const bf8*)(a_ + i * 1024); } while (0)
#define PJ_DS(v, z) do { PJ_SCHED; __builtin_amdgcn_raw_buffer_store_b128(v, rd, vD + ((z) & 1) * 256, (dco ^ (((z) >> 3) * 8192)) + ((z) >> 1) * 32 * RP, 16); \
    asm volatile("s_nop 1"); PJ_SCHED; } while (0)
#define PJ_PUB(a, n) do { if (wid == 4) { const int l_ = TIDX & 63; __builtin_amdgcn_raw_buffer_store_b32(8u, rc, l_ < (n) ? (l_ + (a)) * 4 : PJ_OOB, dfl, 16); } } while (0)
#define PJ_ST()
#define PJ_CV(z, j) asm volatile("v_cvt_pk_bf16_f32 %0, %1, %2" : "=v"(pk[z][j]) \
    : "v"(acc[(z) >> 3][(z) & 1][((z) >> 1) & 3][(j) >> 1][((j) & 1) * 2]), "v"(acc[(z) >> 3][(z) & 1][((z) >> 1) & 3][(j) >> 1][((j) & 1) * 2 + 1]))
#define PJ_DP(z) PJ_DS(((u32x4){pk[z][0], pk[z][1], pk[z][2], pk[z][3]}), z)
#define PJ_OUT(S, ph) do { constexpr int ps_ = pj_store(S, ph), pn_ = pj_store(S, (ph) % 3 + 1) >> 4; if (ps_ & 15) PJ_DP(ps_ >> 4); \
    if (pn_ >= 12) { const u32x4 y_ = *(const u32x4*)(smem + sp + (pn_ - 12) * 8192); _Pragma("unroll") for (int i = 0; i < 4; i++) pk[pn_][i] = y_[i]; } } while (0)
#define PJ_MMA(S, ai, bj, D) do { PJ_SCHED; __builtin_amdgcn_s_setprio(1); \
    constexpr int pp_ = pj_pack(S, (ai) * 2 + (bj)); \
    _Pragma("unroll") for (int i = 0; i < 16; i++) { const int k = i >> 3, m = (i >> 1) & 3, n = i & 1; \
      if (k == 0 && (S) == 0) asm volatile("v_mfma_f32_16x16x32_bf16 %0, %1, %2, 0" : "+v"(acc[ai][bj][m][n]) : "v"(D[n][k]), "v"(At[m][k])); \
      else asm volatile("v_mfma_f32_16x16x32_bf16 %0, %1, %2, %0" : "+v"(acc[ai][bj][m][n]) : "v"(D[n][k]), "v"(At[m][k])); \
      if (i < (pp_ & 15) * 3) { PJ_SCHED; const int z = (pp_ >> (4 + i / 3 * 4)) & 15; \
          \
        if (i % 3 == 0) { PJ_CV(z, 0); PJ_CV(z, 2); PJ_CV(z, 1); } \
        else { if (i % 3 == 1) PJ_CV(z, 3); \
          asm volatile("v_permlane16_swap_b32 %0, %1" : "+v"(pk[z][i % 3 - 1]), "+v"(pk[z][i % 3 + 1])); \
          if (i % 3 == 2 && z >= 12) *(u32x4*)(smem + sp + (z - 12) * 8192) = (u32x4){pk[z][0], pk[z][1], pk[z][2], pk[z][3]}; } \
        PJ_SCHED; } \
      if ((S) == 7 && (ai) + (bj) == 0 && i % 3 == 0 && i < 15) { PJ_SCHED; unit(u + 1 < nu ? u + 1 : u, i / 3); PJ_SCHED; } \
      if ((S) == 8 && (ai) + (bj) == 0 && i % 3 == 0 && i < 12) { PJ_SCHED; dest(u, i / 3); PJ_SCHED; } } \
    __builtin_amdgcn_s_setprio(0); PJ_SCHED; } while (0)
#define PJ_STEP(S) do { PJ_ST(); \
    PJ_LDB(B0, 0); PJ_SCHED; \
    if ((S) == 6 && own && wid == 0) { _Pragma("unroll") for (int i = 0; i < 2; i++) \
      __builtin_amdgcn_raw_ptr_buffer_load_lds(rc, (LDSP void*)((LDSP char*)smem + (C_BUFA + i * 256)), 4, (TIDX & 63) * 4, SY_BLK(4 + i, cb, 0) * 4, 0, 16); } \
    PJ_SCHED; PJ_LDA(0); PJ_SA((S) == 8, (bsel ^ 32768) + 16384, sa + 128 * k2); \
    PJ_WL(8); PJ_BAR; PJ_WL0; PJ_MMA(S, 0, 0, B0); PJ_BAR; PJ_SCHED; \
    if ((S) == 7 && own) PJ_ATT(); \
    PJ_SCHED; PJ_LDB(B1, 1); PJ_SB((S) >= 7, 65536 + bsel, sb); PJ_OUT(S, 1); \
    PJ_BAR; PJ_WL0; PJ_MMA(S, 0, 1, B1); PJ_BAR; \
    if ((S) == 8 && own) { PJ_QW(0); PJ_SCHED; } \
    PJ_LDA(1); sa += (S) >= 7 ? da : 128; PJ_SA((S) >= 7, bsel, sa); PJ_OUT(S, 2); \
    PJ_BAR; PJ_WL0; PJ_MMA(S, 1, 0, B0); PJ_BAR; PJ_SCHED; \
    PJ_SB((S) >= 7, 65536 + bsel + 16384, sb ^ ((S) >= 7 ? xb : 256)); sb += (S) >= 7 ? yb : 64 * 8192; PJ_OUT(S, 3); \
      \
    PJ_WV(6 + (pj_store(S, 1) & 15) + (pj_store(S, 2) & 15) + (pj_store(S, 3) & 15)); \
    PJ_BAR; \
    if ((S) != 6 && (S) != 8) PJ_PUB((S) < 3 || (S) == 7 ? (S) : (S) + 1, (S) == 2 ? 2 : 1); \
    PJ_MMA(S, 1, 1, B1); PJ_BAR; \
    bsel ^= 32768; ao ^= 32768; bo ^= 32768; } while (0)
  f4 acc[2][2][4][2];
#pragma unroll
  for (int a = 0; a < 16; a++) { acc[a >> 3][(a >> 2) & 1][a & 3][0] = (f4){0, 0, 0, 0}; acc[a >> 3][(a >> 2) & 1][a & 3][1] = (f4){0, 0, 0, 0}; }
  bf8 At[4][2], B0[2][2], B1[2][2];
  u32 pk[16][4];
#pragma unroll
  for (int i = 0; i < 4; i++) { pk[2][i] = 0u; pk[4][i] = 0u; pk[6][i] = 0u; }
#pragma unroll
  for (int i = 0; i < 5; i++) unit(0, i);
  int bsel = hd >= 0 ? 32768 : 0;
  ao ^= bsel; bo ^= bsel;
  PJ_SB(0, 65536 + bsel, 0); PJ_SA(0, bsel, sa + 128);
  PJ_SB(0, 65536 + bsel + 16384, 256); PJ_SA(0, bsel + 16384, sa + 128 + 128 * k2);
  if (kp < 0) {
    TG_CLAIM(9);
    u32x2 kv = TG_LD(lane);
    VMW0(); TG_NOTE(151552);
    __syncthreads();
    TG_CHECK(151552);
    TG_KPW(kv, 2);
  }
  if (wr == 1) PJ_BAR;
  PJ_WV(4); PJ_BAR;
  PJ_SB(0, 65536 + (bsel ^ 32768), 64 * 8192); PJ_SA(0, bsel ^ 32768, sa + 256);
  PJ_SB(0, 65536 + (bsel ^ 32768) + 16384, 64 * 8192 + 256);
  PJ_WV(6); PJ_BAR;
  early();
  sa += 256; sb = 128 * 8192;
  int dco = PJ_OOB, dfl = PJ_OOB, md, vE;
  auto dest = [&](const int u, const int part) __attribute__((always_inline)) {
    if (part == 0) {
      md = (ml >> (u * 4)) & 15;
      dfl = SY_BLK(md, cb, ((rt0 + u * rs) & 15) * 4) * 4;
      asm volatile("" : "+s"(md), "+s"(dfl));
    } else if (part == 1) {
      dco = md == 3 ? PJ_OOB : (kp + (md < 2)) * 0x200000 + ((0x210354 >> (md * 4)) & 15) * ACB + cb * 512 + ((rt0 + u * rs) & 15) * 128 * RP;
      asm volatile("" : "+s"(dco));
    } else if (part == 2) {
      vD = TIDX; vE = ((vD >> 3) & 2) + ((vD >> 5) & 1) + wc * 4; vD &= 15;
      asm volatile("" : "+v"(vD), "+v"(vE));
    } else {
      vD = vD * (md < 3 ? RP : 16) + (vE * (md < 3 ? 16 : 2 * RP) + wr * (md < 3 ? 16 * RP : RP));
      asm volatile("" : "+v"(vD));
    }
  };
  static_assert(KVO == 0x200000, "places");
#pragma unroll 1
  for (int u = 0; u < nu; u++) {
    const bool own = hd >= 0 && u + 1 == nu;
    PJ_STEP(0); PJ_STEP(1); PJ_STEP(2); PJ_STEP(3); PJ_STEP(4); PJ_STEP(5);
    int more = k2 - 1024; asm volatile("" : "+s"(more));
    if (more) PJ_STEP(6);
    PJ_STEP(7); PJ_STEP(8);
  }
  PJ_ST();
  PJ_NOP;
  if (hd >= 0) {
    vfl = (TIDX & 63) * 4;
    if (wid == 0) asm volatile("s_nop 4\n\tbuffer_load_dword %0, %0, %1, %2 offen sc1" : "+v"(vfl) : "s"(rc), "s"(SY_BLK(5, cb, 0) * 4) : "memory");
    if (wr == 0) PJ_QR();
    dco = kp * 0x200000 + S_QA + (hd ^ 1) * 256 + rt0 * 128 * RP;
#pragma unroll
    for (int z = 0; z < 16; z += 2) PJ_DS(PJ_PK(z + 1, QSC), z);
    if (wr == 0) { PJ_WL0; PJ_BAR; }
    PJ_SCHED; PJ_QW(1); PJ_SCHED;
    if (wr == 1) { PJ_WL0; PJ_BAR; asm volatile("" ::: "memory"); PJ_QR(); }
    return;
  }
#pragma unroll
  for (int z = 2; z < 16; z++) PJ_DS(PJ_PK(z, ), z);
  PJ_WV(0);
  if (wr == 0) PJ_BAR;
  PJ_BAR;
  PJ_PUB(0, 8);
  PJ_ST();
}

namespace fa {
#define ABS(i)
#define ABT()
#define EPS 3
#define RTH 65536.f
constexpr int TS = 16384, KR = 4 * TS;
#define SBAR() __builtin_amdgcn_sched_barrier(0)
#define MF32(a, b, c) __builtin_amdgcn_mfma_f32_32x32x16_bf16(a, b, c, 0, 0, 0)
#define FMX3(a, b, c) __builtin_elementwise_maximum(__builtin_elementwise_maximum(a, b), c)
__device__ __forceinline__ void attn_pair(const Params& p, const char* __restrict__ Kh, const char* __restrict__ Vh, const int hd, const int R, const int kp, bf8* qr, u32 vfl, char* lds, const int wv, u32* qh, int& pre) {
  const int tid = TIDX, wid = wv, lane = tid & 63, grp = wid >> 2;
  const int n = R * 4 + 4;
  float l_reg = 0.f;
  f32x16 o[4] = {}, s0, s1, cm;
  int ko = KR + lane * 16;
  int vo = ((((lane >> 3) & 2) | ((lane >> 1) & 1)) * (512 + 64) + ((lane >> 2) & 3) * 16 + (lane & 1) * 8) ^ ((lane >> 5) * 64), vp;
  const __amdgpu_buffer_rsrc_t rk = RSRC(Kh, ACB - 8192 + 256), rv = RSRC(Vh, ACB - 8192 + 256);
  const int gk = AT_GK, gv = AT_GV;
  u32* const qf = WSP(u32, 0) + SY_BLK(3, hd >> 1, 0);
  int t = 0, rl = 1, ks = ROWO(192), vs;
#define DMA(rs, go, dst, so) __builtin_amdgcn_raw_ptr_buffer_load_lds(rs, (LDSP void*)((LDSP char*)lds + ((dst) + wid * 1024)), 16, go, so, 0, 16)
#define NEXT_TILES() do { const int u_ = t + 4 < n ? t + 4 : t + 4 - n; vs = ks; ks = t < 32 ? ROWO(u_ * 64) : PJ_OOB; asm volatile("" : "+s"(ks)); } while (0)
#define ISSUE(i) DMA((i) < 2 ? rk : rv, (i) < 2 ? gk : gv, ((i) < 2 ? KR + (t & 3) * TS : ((t + 3) & 3) * TS) + ((i) & 1) * 8192, ((i) < 2 ? ks : vs) + ((i) & 1) * 32 * RP)
#define PBAR() do { SBAR(); asm volatile("" ::: "memory"); __builtin_amdgcn_s_barrier(); asm volatile("" ::: "memory"); SBAR(); } while (0)
#define SEND() do { SBAR(); int x_, y_; \
    asm volatile("s_waitcnt vmcnt(12)\n\ts_cmp_lg_u32 %0, 0\n\ts_mov_b32 %0, 0\n\ts_cbranch_scc1 1f\n\ts_waitcnt vmcnt(4)\n1:\n\ts_cmp_lg_u32 %3, 1\n\ts_cbranch_scc1 2f\n\t" \
      "v_mov_b32 %1, %5\n\tv_mov_b32 %2, 1\n\ts_mov_b64 exec, 1\n\ts_nop 1\n\tglobal_atomic_add %1, %2, %4\n\ts_mov_b64 exec, -1\n2:\n\ts_barrier" \
      : "+&s"(rl), "=&v"(x_), "=&v"(y_) : "s"(t), "s"(qf), "s"(R * 32) : "scc", "memory"); \
    SBAR(); } while (0)
#define QO_RS(b) RSRC(BIGP(kp, S_QA + ROWO(((b) ? 7 - R : R) * 256 + wid * 32) + hd * 256), 32 * RP)
#define QO_VO() (((TIDX & 63) >> 4) * RP + (TIDX & 15) * 16)
#define LQ1(i) "s_add_i32 %8, %8, 0x10000\n\tbuffer_load_dwordx4 %" #i ", %9, %10, %8 offen sc1\n\t"
#define QRS "+v"(qr[0]), "+v"(qr[1]), "+v"(qr[2]), "+v"(qr[3]), "+v"(qr[4]), "+v"(qr[5]), "+v"(qr[6]), "+v"(qr[7])
#define LDQ() asm volatile("s_mov_b32 %8, 0xffff0000\n\ts_nop 3\n\t" LQ1(0) LQ1(1) LQ1(2) LQ1(3) LQ1(4) LQ1(5) LQ1(6) LQ1(7) \
    : QRS, "=&s"(rl) : "v"(QO_VO()), "s"(QO_RS(1)) : "scc", "memory")
#define LDK(d0) do { const char* a_ = lds + ko + (d0) * 1024; \
    kf[(d0) % 3][0] = *(const bf8*)a_; kf[(d0) % 3][1] = *(const bf8*)(a_ + 8192); } while (0)
#define LDV(i) do { const int a_ = ((i) & 3) * 2048 + (((i) >> 2) & 1) * 256 + ((i) >> 3) * 8192; vf[(i) & 3] = cat8(TRL(vo + a_), TRL(vp + a_)); } while (0)
#define TRL(o) __builtin_amdgcn_ds_read_tr16_b64_v4i16((LDSP bf4*)((LDSP char*)lds + (o)))
#define QK(NEXT, is) do { _Pragma("unroll") for (int d0 = 0; d0 < 8; ++d0) { \
      if (d0 + 2 < 8) LDK(d0 + 2); else { NEXT(2 * (d0 - 6)); NEXT(2 * (d0 - 6) + 1); } \
      SBAR(); \
      s0 = MF32(kf[d0 % 3][0], qr[d0], d0 ? s0 : (is) ? cm : f32x16{}); s1 = MF32(kf[d0 % 3][1], qr[d0], d0 ? s1 : (is) ? cm : f32x16{}); \
      SBAR(); \
      if ((is) && d0 < 4) ISSUE(d0); } } while (0)
#define NOV(i)
#define TR1(r, c) "s_bfm_b32 exec_lo, " #c ", 0\n\ts_bfm_b32 exec_hi, " #c "+4, 0\n\tv_mov_b32 %" #r ", 0xff800000\n\t"
#define TRI(S) do { float y[16]; _Pragma("unroll") for (int r = 0; r < 16; ++r) y[r] = S[r]; \
      asm volatile(TR1(0, 0) TR1(1, 1) TR1(2, 2) TR1(3, 3) TR1(4, 8) TR1(5, 9) TR1(6, 10) TR1(7, 11) TR1(8, 16) TR1(9, 17) TR1(10, 18) TR1(11, 19) \
          TR1(12, 24) TR1(13, 25) TR1(14, 26) TR1(15, 27) "s_mov_b64 exec, -1" \
          : "+v"(y[0]), "+v"(y[1]), "+v"(y[2]), "+v"(y[3]), "+v"(y[4]), "+v"(y[5]), "+v"(y[6]), "+v"(y[7]), \
            "+v"(y[8]), "+v"(y[9]), "+v"(y[10]), "+v"(y[11]), "+v"(y[12]), "+v"(y[13]), "+v"(y[14]), "+v"(y[15])); \
      _Pragma("unroll") for (int r = 0; r < 16; ++r) S[r] = y[r]; } while (0)
#define MASK() do { SBAR(); asm volatile("s_nop 7\n\ts_nop 3" : "+v"(s0), "+v"(s1)); \
      if (wid & 1) TRI(s1); \
      else { TRI(s0); _Pragma("unroll") for (int r = 0; r < 16; ++r) s1[r] = -__builtin_inff(); } \
      asm volatile("" : "+v"(s0), "+v"(s1)); SBAR(); } while (0)
#define RENEW(floor) do { \
      float mx = FMX3(s0[0], s0[1], s1[0]), my = FMX3(s0[2], s0[3], s1[1]); \
      _Pragma("unroll") for (int r = 4; r < 16; r += 4) { mx = FMX3(mx, s0[r], s0[r + 1]); my = FMX3(my, s0[r + 2], s0[r + 3]); } \
      _Pragma("unroll") for (int r = 2; r < 14; r += 4) { mx = FMX3(mx, s1[r], s1[r + 1]); my = FMX3(my, s1[r + 2], s1[r + 3]); } \
      mx = FMX3(FMX3(mx, s1[14], s1[15]), my, floor); \
      auto rr = __builtin_amdgcn_permlane32_swap(__float_as_uint(mx), __float_as_uint(mx), false, false); \
      up = __builtin_elementwise_maximum(__uint_as_float(rr[0]), __uint_as_float(rr[1])); \
      _Pragma("unroll") for (int r = 0; r < 16; ++r) { s0[r] -= up; s1[r] -= up; } } while (0)
#define SETCM(v) do { const float v_ = (v); _Pragma("unroll") for (int r = 0; r < 16; ++r) { float w_ = v_; asm volatile("" : "+v"(w_)); cm[r] = w_; } } while (0)
  bf8 kf[3][2], vf[4], pa[4];
  float up;
  int b = 0, e;
  auto tile = [&](auto LASTC) __attribute__((always_inline)) {
    constexpr bool LAST = decltype(LASTC)::value;
    ABT();
    if (LAST && grp > b) LDQ();
    if (LAST) MASK();
    float sa;
    u32 pw[16];
#define EXPS() do { float x[32], sb; \
      _Pragma("unroll") for (int r = 0; r < 18; ++r) { \
        SBAR(); \
        if (r < 16) { x[2 * r] = __builtin_amdgcn_exp2f(r < 8 ? s0[2 * r] : s1[2 * r - 16]); x[2 * r + 1] = __builtin_amdgcn_exp2f(r < 8 ? s0[2 * r + 1] : s1[2 * r - 15]); } \
        if (r >= 2) { \
          sa = r == 2 ? x[0] : sa + x[2 * r - 4]; \
          sb = r == 2 ? x[1] : sb + x[2 * r - 3]; \
          pw[r - 2] = pk2(x[2 * r - 4], x[2 * r - 3]); asm volatile("" : "+v"(sa), "+v"(pw[r - 2])); } } \
      SBAR(); \
      sa += sb; } while (0)
    EXPS();
    if (__builtin_expect(__ballot(!(sa < RTH)) != 0, 0)) {
      RENEW(0.f);
      SETCM(cm[0] - up);
      const float alpha = __builtin_amdgcn_exp2f(-up);
      l_reg *= alpha;
#pragma unroll
      for (int d = 0; d < 4; ++d) o[d] *= alpha;
      EXPS();
    }
    l_reg += sa;
#pragma unroll
    for (int i = 0; i < 4; ++i) pa[i] = __builtin_bit_cast(bf8, (u32x4){pw[4 * i], pw[4 * i + 1], pw[4 * i + 2], pw[4 * i + 3]});
    asm volatile("" : "+v"(pa[0]), "+v"(pa[1]), "+v"(pa[2]), "+v"(pa[3]), "+v"(l_reg));
    if constexpr (!LAST) ko ^= (t & 1) ? 3 * TS : TS;
    vp = vo ^ 128; asm volatile("" : "+v"(vp));
    NEXT_TILES();
    SBAR();
    if constexpr (!LAST) { LDK(0); LDK(1); } else { LDV(0); LDV(1); LDV(2); LDV(3); }
    SEND();
    __builtin_amdgcn_s_setprio(1);
    if constexpr (!LAST) QK(LDV, true);
#pragma unroll
    for (int i = 0; i < 16; ++i) {
      SBAR();
      o[i & 3] = MF32(vf[i & 3], pa[i >> 2], o[i & 3]);
      SBAR();
      if (i + 4 < 16) LDV(i + 4);
      if (LAST && i < 4) ISSUE(i);
    }
    __builtin_amdgcn_s_setprio(0);
    if constexpr (!LAST) { vo ^= (t & 1) ? 3 * TS : TS; PBAR(); }
  };
  ABS(40);
#pragma unroll 1
  while (true) {
    e = (b ? 32 : n - 4) + (wid >> 1);
    __builtin_amdgcn_s_setprio(1);
    LDK(0); LDK(1);
    QK(NOV, false);
    __builtin_amdgcn_s_setprio(0);
    if (wid + b == 0) {
      const int nb = (R < 4 ? 8 - R : R + 1) * 8;
      int it = 0;
      asm volatile("s_waitcnt vmcnt(8)" : "+v"(vfl) :: "memory");
      while (!__all(lane >= nb || vfl >= 8u) && ++it < SPIN_CAP) { vfl = ld4_sc1(qf + 2 * 16 * 64 + lane); VMW0C(); }
    }
    PBAR();
    if (t == e) MASK();
    RENEW(-__builtin_inff());
    SETCM(-up);
#pragma unroll 1
    for (; t < e; t++) tile(std::false_type{});
    tile(std::true_type{});
    {
      const float inv = __builtin_amdgcn_rcpf(swadd(l_reg, l_reg));
      const __amdgpu_buffer_rsrc_t ro = QO_RS(b);
      const int v = QO_VO();
#pragma unroll
      for (int i = 0; i < 8; ++i) {
        if (i == EPS && wid < 6) PBAR();
        const int g = (i & 1) * 8;
        u32 w[4];
#pragma unroll
        for (int k = 0; k < 4; k++) w[k] = pk2(o[i >> 1][g + 2 * k] * inv, o[i >> 1][g + 2 * k + 1] * inv);
        auto y0 = __builtin_amdgcn_permlane32_swap(w[0], w[2], false, false);
        auto y1 = __builtin_amdgcn_permlane32_swap(w[1], w[3], false, false);
        SBAR();
        __builtin_amdgcn_raw_buffer_store_b128((u32x4){y0[0], y1[0], y0[1], y1[1]}, ro, v, rl, 16);
        asm volatile("s_add_i32 %0, %0, 0x10000\n\ts_nop 0" : "+s"(rl) :: "scc");
        SBAR();
      }
      SBAR();
    }
#pragma unroll 1
    while ((++t & 3) != 0) {
      if (t != e + 1) PBAR();
      ABT();
      if (b == 0) {
        if (wid == 0 && (t & 3) == 2) {
          int it = 0;
          while (ld4_sc1(qf + (7 - R) * 8) < 8u && ++it < SPIN_CAP) __builtin_amdgcn_s_sleep(2);
          VMW0C();
        }
        if (!grp && (t & 3) == 3) LDQ();
      }
      NEXT_TILES();
      SEND();
#pragma unroll
      for (int i = 0; i < 4; i++) ISSUE(i);
    }
    if (b) break;
    b = 1;
    l_reg = 0.f;
#pragma unroll
    for (int i = 0; i < 4; i++) o[i] = f32x16{};
    ko &= ~(3 * TS); vo &= ~(3 * TS);
    asm volatile("s_waitcnt vmcnt(12)\n\ts_cmp_lg_u32 %8, 0\n\ts_cbranch_scc1 1f\n\ts_waitcnt vmcnt(4)\n1:"
        : QRS : "s"(grp) : "scc", "memory");
  }
  ABS(41);
  CLAIM();
  if (!grp) __builtin_amdgcn_s_barrier();
  VMW0();
  __syncthreads();
}
#undef DMA
#undef ISSUE
#undef LDK
#undef LDV
#undef QK
#undef NOV
#undef MASK
}

#define WOP 0
__device__ __forceinline__ void wof_task(const Params& p, const u32x4* st, const int m, const int kc, const int kp, char* smem, const int wv) {
  const int tid = TIDX, lane = tid & 63, wid = wv;
  const int l15 = lane & 15, lq = lane >> 4, q4 = l15 >> 2, p4 = lane & 3;
  const __amdgpu_buffer_rsrc_t rw = RSRC(BIGP(kp, S_WF(m)), 576 * RP);
#pragma unroll
  for (int i = 0; i < 9; i++) {
    const int id = tid + 512 * i, row = id / 72, ch = id % 72;
    *(u32x4*)(smem + row * 1280 + (((ch >> 1) ^ fsw(row)) << 5) + ((ch & 1) << 4)) = st[i];
  }
  asm volatile("s_waitcnt lgkmcnt(0)" ::: "memory"); __builtin_amdgcn_s_barrier(); asm volatile("" ::: "memory");
#pragma unroll
  for (int i = 0; i < 9; i++) {
    const int f = wid * 9 + i, nt = f >> 1, k2 = f & 1;
    const int r0 = k2 * 32 + lq * 8 + q4, r1 = r0 + 4;
    bf4 lo = trrd(smem + r0 * 1280 + ((nt ^ fsw(r0)) << 5) + p4 * 8);
    bf4 hi = trrd(smem + r1 * 1280 + ((nt ^ fsw(r1)) << 5) + p4 * 8);
    __builtin_amdgcn_raw_buffer_store_b128(__builtin_bit_cast(u32x4, cat8(lo, hi)), rw, lane * 16 + WFO(kc, f), 0, 16);
    if (WOP) { SB0(); __builtin_amdgcn_s_sleep(WOP); SB0(); }
  }
  asm volatile("s_waitcnt lgkmcnt(0)" ::: "memory"); __builtin_amdgcn_s_barrier(); asm volatile("" ::: "memory");
}

template <int MT, int NT, int WM, int S>
__device__ __forceinline__ void out_tile(const char* A, const char* W, const int nt0, u16* __restrict__ C, char* smem, const int wv, u32* qh, int& pre) {
  constexpr int WN = 4 / WM, BI = NT * WN / 2, STG = 16384 + NT * WN * 2048, NV = 4 + BI;
  const int tid = TIDX, lane = tid & 63, wid = wv & 3, l15 = lane & 15, lq = lane >> 4, wr = wid / WN, wc = wid % WN;
  if (wv >= 4) {
    const __amdgpu_buffer_rsrc_t ra = RSRC(A, 128 * RP), rw = RSRC(W, 576 * RP);
    const int va = (wid * 32 + lq) * RP + l15 * 16, vw = lane * 16;
    auto fill = [&](const int k, const int bo) {
      const int in = k < 64 ? 0 : PJ_OOB;
#pragma unroll
      for (int i = 0; i < 4; i++) __builtin_amdgcn_raw_ptr_buffer_load_lds(ra, (LDSP void*)(smem + bo + (wid * 4 + i) * 1024), 16, va, ((k >> 1) * 256 + ((k & 1) * 16 + i * 4) * RP) | in, 0, 16);
#pragma unroll
      for (int i = 0; i < BI; i++) { const int f = nt0 * 2 + wid * BI + i;
        __builtin_amdgcn_raw_ptr_buffer_load_lds(rw, (LDSP void*)(smem + bo + 16384 + (wid * BI + i) * 1024), 16, vw, WFO(k, f) | in, 0, 16); }
    };
#define OT_SYNC(n) do { asm volatile("s_waitcnt vmcnt(%0)" :: "n"(n) : "memory"); __builtin_amdgcn_s_barrier(); asm volatile("" ::: "memory"); } while (0)
#pragma unroll
    for (int u = 0; u < S; u++) fill(u, u * STG);
    OT_SYNC((S - 1) * NV);
    int bo = 0;
#pragma unroll 1
    for (int k = 0; k < 64; k++) {
      OT_SYNC((S - 2) * NV);
      fill(k + S, bo);
      bo = bo + STG == S * STG ? 0 : bo + STG;
    }
  } else {
    const int oa = wr * MT * 2048 + lq * 512 + l15 * 16, ob = 16384 + wc * NT * 2048 + lane * 16;
    f4 acc[MT][NT];
#pragma unroll
    for (int i = 0; i < MT; i++)
#pragma unroll
      for (int j = 0; j < NT; j++) acc[i][j] = (f4){0, 0, 0, 0};
    auto rd = [&](const int bo, const int ks, bf8* f, const int i) {
      if (i < NT) f[i] = *(const bf8*)(smem + (ob + bo) + i * 2048 + ks * 1024);
      else f[i] = *(const bf8*)(smem + (oa + bo) + ks * 2048 + ((i - NT) >> 1) * 4096 + ((i - NT) & 1) * 256);
    };
    auto mm = [&](bf8* f, const int bo, const int ks, bf8* g) {
#pragma unroll
      for (int u = 0; u < MT * NT; u++) {
        if (u < MT + NT) rd(bo, ks, g, u);
        SB0();
        acc[u / NT][u % NT] = MFMA(f[u % NT], f[NT + u / NT], acc[u / NT][u % NT]);
        SB0();
      }
    };
    bf8 f0[MT + NT], f1[MT + NT];
    __builtin_amdgcn_s_barrier(); asm volatile("" ::: "memory");
#pragma unroll
    for (int i = 0; i < MT + NT; i++) rd(0, 0, f0, i);
    int bo = 0;
    __builtin_amdgcn_s_setprio(1);
#pragma unroll 1
    for (int k = 0; k < 64; k++) {
      mm(f0, bo, 1, f1);
      asm volatile("" ::: "memory"); __builtin_amdgcn_s_waitcnt(0xC07F); __builtin_amdgcn_s_barrier(); asm volatile("" ::: "memory");
      bo = bo + STG == S * STG ? 0 : bo + STG;
      mm(f1, bo, 0, f0);
    }
    __builtin_amdgcn_s_setprio(0);
    CLAIM();
#pragma unroll
    for (int i = 0; i < MT; i++)
#pragma unroll
      for (int j = 0; j < NT; j += 2) {
        u16* const q = C + (size_t)(wr * (MT * 16) + i * 16 + l15) * 1152 + wc * (NT * 16) + j * 16;
        if (j + 1 == NT) *(u32x2*)(q + lq * 4) = (u32x2){pk2(acc[i][j][0], acc[i][j][1]), pk2(acc[i][j][2], acc[i][j][3])};
        else *(u32x4*)(q + (lq & 1) * 16 + (lq >> 1) * 8) =
            pair16(pk2(acc[i][j][0], acc[i][j][1]), pk2(acc[i][j][2], acc[i][j][3]), pk2(acc[i][(j + 1) % NT][0], acc[i][(j + 1) % NT][1]), pk2(acc[i][(j + 1) % NT][2], acc[i][(j + 1) % NT][3]));
      }
  }
  VMW0();
  __syncthreads();
}

__device__ __forceinline__ void small_out_task(const Params& p, const int set, int hf, int np, const int kp, char* smem, const int wv, u32* qh, int& pre) {
  const int tid = TIDX, lane = tid & 63, wid = wv;
  const int l15 = lane & 15, lq = lane >> 4;
  const __amdgpu_buffer_rsrc_t rw = RSRC(BIGP(kp, S_WF(1)), 576 * RP), ro = RSRC(BIGP(kp, S_OL), ACB);
  if (wid == 0) {
    int it = 0;
    while (ld4_sc1(WSP(u32, 0) + SY_CW) < 128u && ++it < SPIN_CAP) __builtin_amdgcn_s_sleep(8);
  }
  __syncthreads();
  bf8 bfr[2][16];
#pragma unroll
  for (int k = 0; k < 32; k++) bfr[(k >> 1) & 1][(k >> 2) * 2 + (k & 1)] = ldsc1(rw, WFO(wid * 8 + (k >> 2), np * 4 + (k & 3)) + lane * 16);
  int va = (hf * 16 + wid * 2) * RP + lane * 16;
  static_assert(SM0 >= 48 && SM0 < 60, "late segments");
  int u = 0;
#pragma unroll 1
  for (int s = SM0 + (set + 63 - SM0) % 3;; s += 3) {
    if (s > 63) s = 63;
    if (wid == 0) {
      int it = 0;
      const u32* f = WSP(u32, 0) + SY_CS(s < 62 ? s : 61) + (np & 7) * 32;
      while (ld4_sc1(f) < 4u && ++it < SPIN_CAP) __builtin_amdgcn_s_sleep(SPS);
      if (s >= 62) { u32x2 y; while (y = TG_LD(SY_SEG(s, 0) / 2 + lane), !__all(y[0] != 0u && y[1] != 0u) && ++it < SPIN_CAP) __builtin_amdgcn_s_sleep(SP2); }
    }
    CFENCE(); __builtin_amdgcn_s_barrier(); CFENCE();
    const bool mine = s < 63 || (l15 >> 3) == set;
    if (!mine) va = PJ_OOB;
    bf8 a[16];
#pragma unroll
    for (int k = 0; k < 16; k++) a[k] = __builtin_bit_cast(bf8, __builtin_amdgcn_raw_buffer_load_b128(ro, va + (k >> 3) * RP + (k & 7) * 1024, OROW(s * 32), 16));
    SB0();
    f4 acc[2] = {(f4){0, 0, 0, 0}, (f4){0, 0, 0, 0}};
#pragma unroll
    for (int k = 0; k < 16; k++) { acc[0] = MFMA(bfr[0][k], a[k], acc[0]); acc[1] = MFMA(bfr[1][k], a[k], acc[1]); }
    *(f4*)(smem + u + tid * 16) = acc[0];
    *(f4*)(smem + u + 8192 + tid * 16) = acc[1];
    if (s >= 62) CLAIM();
    asm volatile("s_waitcnt lgkmcnt(0)" ::: "memory"); __builtin_amdgcn_s_barrier(); CFENCE();
    if (wid >= 6) {
      f4 t = (f4){0, 0, 0, 0};
#pragma unroll
      for (int w = 0; w < 8; w++) t += *(const f4*)(smem + u + (wid & 1) * 8192 + (w * 64 + lane) * 16);
      if (mine) *(u64*)(p.out + (size_t)(s * 32 + hf * 16 + l15) * 1152 + 576 + np * 32 + (wid & 1) * 16 + lq * 4) = (u64)pk2(t[0], t[1]) | ((u64)pk2(t[2], t[3]) << 32);
    }
    if (s >= 62) break;
    u ^= 16384;
  }
  __syncthreads();
}

#define XS_(x) #x
#define XS(x) XS_(x)
#define GMAX(i)
#define PSTEP_
#define CNTB
#define CNTO
#define PTA
#define PRO
#define PRE
#define PROUT
#define PRDECL(c)
#define PRFIN(c)
#define PT_PROF 99
#define PTS
#define ST0
#define STX "s_memtime s[74:75]\n"
#define ST1
#define ST2
#define ST3
#define ST4
#define ST5
#define ST6
#define ST7
#define ST8
#define ST9
#define ST10
#define ST11
#define ST12
#define ST13
#define ST14
#define ST15
#define ST16
#define ST17
#define ST18
#define ST19
#define ST20
#define ST21
#define ST22
#define ST23
#define ST24
#define ST25
#define ST26
#define ST27
#define ST28
#define ST29
#define ST30
#define ST31
#define ST32
#define ST33
#define ST34
#define ST35
#define ST36
#define ST37
#define ST38
#define ST39
#define ST40
#define ST41
#define ST42
#define ST43
#define ST44
#define ST45
#define ST46
#define ST47
#define ST48
#define ST49
#define STS(i)
#define CPT 22
#define K1 7
#define K2 72
#define KE0 18
#define KE1 4
#define GL0 20
#define DUR0 110
#define DSH 2
#define LIM1 30
#define LIM2 70
#define OD 12
#define OG 12
#define RQD 13
#define GL1 0
#define K3 8
#define DUO0 66
#define DSO 2
#define DUP 64
#define XSD 0
#define TCH2 0
#define TSL 14
#define P2P 2
#define OHX 4
#define SEP 0
#define OGEO(g) (SEP ? XGEO(1) ^ ((g) * 4096) : XGEO(g))
#define TCH (1 - SEP)
#define OHR 3
#define ODL 0
#define HAUX 0
#define SAUX 0
#define HNT
#define SNT
#define HPX 0
#define ANS 0
#define EXD 32
#define EXM 8
#define EXU 4
#define XWT 1
#define NAP 14
#define QDL 4
#define RQX 6
#define QD 1
#define KD 10
#define BD 4
#define BW
#define BSL 4
#define OSL 12
#define RSL 8
#define SY_KMF0 (SY0 + 704)
#define SY_CS0 (SY0 + 2496)
#define CH_SA \
  "v_add_u32 v244, 0, v249\n" "v_xor_b32 v245, 32, v244\n" "s_waitcnt vmcnt(14)\n" "s_barrier\n" "ds_read_b64_tr_b16 v[0:1], v245 offset:0\n" "ds_read_b64_tr_b16 v[2:3], v245 offset:512\n" \
  "ds_read_b64_tr_b16 v[4:5], v245 offset:8192\n" "ds_read_b64_tr_b16 v[6:7], v245 offset:8704\n" "ds_read_b64_tr_b16 v[72:73], v244 offset:0\n" "ds_read_b64_tr_b16 v[74:75], v244 offset:512\n" \
  "ds_read_b64_tr_b16 v[76:77], v244 offset:8192\n" "ds_read_b64_tr_b16 v[78:79], v244 offset:8704\n" "ds_read_b64_tr_b16 v[144:145], v244 offset:32768\n" \
  "ds_read_b64_tr_b16 v[146:147], v244 offset:33280\n" "ds_read_b64_tr_b16 v[148:149], v244 offset:40960\n" "ds_read_b64_tr_b16 v[150:151], v244 offset:41472\n" "s_waitcnt lgkmcnt(0)\n" \
  "s_barrier\n" "s_add_i32 m0, %[lw], 0\n" "s_mov_b32 %[t], 1572864\n" "buffer_load_dwordx4 v248, %[r0], %[t] offen " XS(HNT) " lds\n" "s_add_i32 m0, %[lw], 8192\n" "s_mov_b32 %[t], 1835008\n" \
  "buffer_load_dwordx4 v248, %[r0], %[t] offen " XS(HNT) " lds\n" "s_add_i32 m0, %[lw], 16384\n" "s_mov_b32 %[t], 1572864\n" "buffer_load_dwordx4 v248, %[r1], %[t] offen " XS(HNT) " lds\n" \
  "s_add_i32 m0, %[lw], 24576\n" "s_mov_b32 %[t], 1835008\n" "buffer_load_dwordx4 v248, %[r1], %[t] offen " XS(HNT) " lds\n" "s_add_i32 m0, %[lw], 32768\n" "s_mov_b32 %[t], 1572864\n" \
  "buffer_load_dwordx4 v248, %[r2], %[t] offen " XS(HNT) " lds\n" "s_add_i32 m0, %[lw], 40960\n" "s_mov_b32 %[t], 1835008\n" "buffer_load_dwordx4 v248, %[r2], %[t] offen " XS(HNT) " lds\n" \
  "v_add_u32 v244, 49152, v249\n" "v_xor_b32 v245, 32, v244\n" "s_waitcnt vmcnt(14)\n" "s_barrier\n" "ds_read_b64_tr_b16 v[8:9], v245 offset:0\n" \
  "ds_read_b64_tr_b16 v[10:11], v245 offset:512\n" "ds_read_b64_tr_b16 v[12:13], v245 offset:8192\n" "ds_read_b64_tr_b16 v[14:15], v245 offset:8704\n" \
  "ds_read_b64_tr_b16 v[80:81], v244 offset:0\n" "ds_read_b64_tr_b16 v[82:83], v244 offset:512\n" "ds_read_b64_tr_b16 v[84:85], v244 offset:8192\n" \
  "ds_read_b64_tr_b16 v[86:87], v244 offset:8704\n" "ds_read_b64_tr_b16 v[152:153], v244 offset:32768\n" "ds_read_b64_tr_b16 v[154:155], v244 offset:33280\n" \
  "ds_read_b64_tr_b16 v[156:157], v244 offset:40960\n" "ds_read_b64_tr_b16 v[158:159], v244 offset:41472\n" "s_waitcnt lgkmcnt(0)\n" "s_barrier\n" "s_add_i32 m0, %[lw], 49152\n" \
  "s_mov_b32 %[t], 2097152\n" "buffer_load_dwordx4 v248, %[r0], %[t] offen " XS(HNT) " lds\n" "s_add_i32 m0, %[lw], 57344\n" "s_mov_b32 %[t], 2359296\n" \
  "buffer_load_dwordx4 v248, %[r0], %[t] offen " XS(HNT) " lds\n" "s_add_i32 m0, %[lw], 65536\n" "s_mov_b32 %[t], 2097152\n" "buffer_load_dwordx4 v248, %[r1], %[t] offen " XS(HNT) " lds\n" \
  "s_add_i32 m0, %[lw], 73728\n" "s_mov_b32 %[t], 2359296\n" "buffer_load_dwordx4 v248, %[r1], %[t] offen " XS(HNT) " lds\n" "s_add_i32 m0, %[lw], 81920\n" "s_mov_b32 %[t], 2097152\n" \
  "buffer_load_dwordx4 v248, %[r2], %[t] offen " XS(HNT) " lds\n" "s_add_i32 m0, %[lw], 90112\n" "s_mov_b32 %[t], 2359296\n" "buffer_load_dwordx4 v248, %[r2], %[t] offen " XS(HNT) " lds\n" \
  "v_add_u32 v244, 98304, v249\n" "v_xor_b32 v245, 32, v244\n" "s_waitcnt vmcnt(12)\n" "s_barrier\n" "ds_read_b64_tr_b16 v[16:17], v245 offset:0\n" \
  "ds_read_b64_tr_b16 v[18:19], v245 offset:512\n" "ds_read_b64_tr_b16 v[20:21], v245 offset:8192\n" "ds_read_b64_tr_b16 v[22:23], v245 offset:8704\n" \
  "ds_read_b64_tr_b16 v[88:89], v244 offset:0\n" "ds_read_b64_tr_b16 v[90:91], v244 offset:512\n" "ds_read_b64_tr_b16 v[92:93], v244 offset:8192\n" \
  "ds_read_b64_tr_b16 v[94:95], v244 offset:8704\n" "ds_read_b64_tr_b16 v[160:161], v244 offset:32768\n" "ds_read_b64_tr_b16 v[162:163], v244 offset:33280\n" \
  "ds_read_b64_tr_b16 v[164:165], v244 offset:40960\n" "ds_read_b64_tr_b16 v[166:167], v244 offset:41472\n" "s_waitcnt lgkmcnt(0)\n" "s_barrier\n" "s_add_i32 m0, %[lw], 98304\n" \
  "s_mov_b32 %[t], 2621440\n" "buffer_load_dwordx4 v248, %[r0], %[t] offen " XS(HNT) " lds\n" "s_add_i32 m0, %[lw], 106496\n" "s_mov_b32 %[t], 2883584\n" \
  "buffer_load_dwordx4 v248, %[r0], %[t] offen " XS(HNT) " lds\n" "s_add_i32 m0, %[lw], 114688\n" "s_mov_b32 %[t], 2621440\n" "buffer_load_dwordx4 v248, %[r1], %[t] offen " XS(HNT) " lds\n" \
  "s_add_i32 m0, %[lw], 122880\n" "s_mov_b32 %[t], 2883584\n" "buffer_load_dwordx4 v248, %[r1], %[t] offen " XS(HNT) " lds\n" "s_add_i32 m0, %[lw], 131072\n" "s_mov_b32 %[t], 2621440\n" \
  "buffer_load_dwordx4 v248, %[r2], %[t] offen " XS(HNT) " lds\n" "s_add_i32 m0, %[lw], 139264\n" "s_mov_b32 %[t], 2883584\n" "buffer_load_dwordx4 v248, %[r2], %[t] offen " XS(HNT) " lds\n" \
  "v_add_u32 v244, 0, v249\n" "v_xor_b32 v245, 32, v244\n" "s_waitcnt vmcnt(12)\n" "s_barrier\n" "ds_read_b64_tr_b16 v[24:25], v245 offset:0\n" "ds_read_b64_tr_b16 v[26:27], v245 offset:512\n" \
  "ds_read_b64_tr_b16 v[28:29], v245 offset:8192\n" "ds_read_b64_tr_b16 v[30:31], v245 offset:8704\n" "ds_read_b64_tr_b16 v[96:97], v244 offset:0\n" \
  "ds_read_b64_tr_b16 v[98:99], v244 offset:512\n" "ds_read_b64_tr_b16 v[100:101], v244 offset:8192\n" "ds_read_b64_tr_b16 v[102:103], v244 offset:8704\n" \
  "ds_read_b64_tr_b16 v[168:169], v244 offset:32768\n" "ds_read_b64_tr_b16 v[170:171], v244 offset:33280\n" "ds_read_b64_tr_b16 v[172:173], v244 offset:40960\n" \
  "ds_read_b64_tr_b16 v[174:175], v244 offset:41472\n" "s_waitcnt lgkmcnt(0)\n" "s_barrier\n" "s_add_i32 m0, %[lw], 0\n" "s_mov_b32 %[t], 3145728\n" \
  "buffer_load_dwordx4 v248, %[r0], %[t] offen " XS(HNT) " lds\n" "s_add_i32 m0, %[lw], 8192\n" "s_mov_b32 %[t], 3407872\n" "buffer_load_dwordx4 v248, %[r0], %[t] offen " XS(HNT) " lds\n" \
  "s_add_i32 m0, %[lw], 16384\n" "s_mov_b32 %[t], 3145728\n" "buffer_load_dwordx4 v248, %[r1], %[t] offen " XS(HNT) " lds\n" "s_add_i32 m0, %[lw], 24576\n" "s_mov_b32 %[t], 3407872\n" \
  "buffer_load_dwordx4 v248, %[r1], %[t] offen " XS(HNT) " lds\n" "s_add_i32 m0, %[lw], 32768\n" "s_mov_b32 %[t], 3145728\n" "buffer_load_dwordx4 v248, %[r2], %[t] offen " XS(HNT) " lds\n" \
  "s_add_i32 m0, %[lw], 40960\n" "s_mov_b32 %[t], 3407872\n" "buffer_load_dwordx4 v248, %[r2], %[t] offen " XS(HNT) " lds\n" "v_add_u32 v244, 49152, v249\n" "v_xor_b32 v245, 32, v244\n" \
  "s_waitcnt vmcnt(12)\n" "s_barrier\n" "ds_read_b64_tr_b16 v[32:33], v245 offset:0\n" "ds_read_b64_tr_b16 v[34:35], v245 offset:512\n" "ds_read_b64_tr_b16 v[36:37], v245 offset:8192\n" \
  "ds_read_b64_tr_b16 v[38:39], v245 offset:8704\n" "ds_read_b64_tr_b16 v[104:105], v244 offset:0\n" "ds_read_b64_tr_b16 v[106:107], v244 offset:512\n" \
  "ds_read_b64_tr_b16 v[108:109], v244 offset:8192\n" "ds_read_b64_tr_b16 v[110:111], v244 offset:8704\n" "ds_read_b64_tr_b16 v[176:177], v244 offset:32768\n" \
  "ds_read_b64_tr_b16 v[178:179], v244 offset:33280\n" "ds_read_b64_tr_b16 v[180:181], v244 offset:40960\n" "ds_read_b64_tr_b16 v[182:183], v244 offset:41472\n" "s_waitcnt lgkmcnt(0)\n" \
  "s_barrier\n" "s_add_i32 m0, %[lw], 49152\n" "s_mov_b32 %[t], 3670016\n" "buffer_load_dwordx4 v248, %[r0], %[t] offen " XS(HNT) " lds\n" "s_add_i32 m0, %[lw], 57344\n" \
  "s_mov_b32 %[t], 3932160\n" "buffer_load_dwordx4 v248, %[r0], %[t] offen " XS(HNT) " lds\n" "s_add_i32 m0, %[lw], 65536\n" "s_mov_b32 %[t], 3670016\n" \
  "buffer_load_dwordx4 v248, %[r1], %[t] offen " XS(HNT) " lds\n" "s_add_i32 m0, %[lw], 73728\n" "s_mov_b32 %[t], 3932160\n" "buffer_load_dwordx4 v248, %[r1], %[t] offen " XS(HNT) " lds\n" \
  "s_add_i32 m0, %[lw], 81920\n" "s_mov_b32 %[t], 3670016\n" "buffer_load_dwordx4 v248, %[r2], %[t] offen " XS(HNT) " lds\n" "s_add_i32 m0, %[lw], 90112\n" "s_mov_b32 %[t], 3932160\n" \
  "buffer_load_dwordx4 v248, %[r2], %[t] offen " XS(HNT) " lds\n" "v_add_u32 v244, 98304, v249\n" "v_xor_b32 v245, 32, v244\n" "s_waitcnt vmcnt(12)\n" "s_barrier\n" \
  "ds_read_b64_tr_b16 v[40:41], v245 offset:0\n" "ds_read_b64_tr_b16 v[42:43], v245 offset:512\n" "ds_read_b64_tr_b16 v[44:45], v245 offset:8192\n" \
  "ds_read_b64_tr_b16 v[46:47], v245 offset:8704\n" "ds_read_b64_tr_b16 v[112:113], v244 offset:0\n" "ds_read_b64_tr_b16 v[114:115], v244 offset:512\n" \
  "ds_read_b64_tr_b16 v[116:117], v244 offset:8192\n" "ds_read_b64_tr_b16 v[118:119], v244 offset:8704\n" "s_waitcnt lgkmcnt(0)\n" "s_barrier\n" "s_add_i32 m0, %[lw], 98304\n" \
  "s_mov_b32 %[t], 4194304\n" "buffer_load_dwordx4 v248, %[r0], %[t] offen " XS(HNT) " lds\n" "s_add_i32 m0, %[lw], 106496\n" "s_mov_b32 %[t], 4456448\n" \
  "buffer_load_dwordx4 v248, %[r0], %[t] offen " XS(HNT) " lds\n" "s_add_i32 m0, %[lw], 114688\n" "s_mov_b32 %[t], 4194304\n" "buffer_load_dwordx4 v248, %[r1], %[t] offen " XS(HNT) " lds\n" \
  "s_add_i32 m0, %[lw], 122880\n" "s_mov_b32 %[t], 4456448\n" "buffer_load_dwordx4 v248, %[r1], %[t] offen " XS(HNT) " lds\n" "s_add_i32 m0, %[lw], 131072\n" "s_mov_b32 %[t], 4194304\n" \
  "buffer_load_dwordx4 v248, %[r2], %[t] offen " XS(HNT) " lds\n" "s_add_i32 m0, %[lw], 139264\n" "s_mov_b32 %[t], 4456448\n" "buffer_load_dwordx4 v248, %[r2], %[t] offen " XS(HNT) " lds\n" \
  "v_add_u32 v244, 0, v249\n" "v_xor_b32 v245, 32, v244\n" "s_waitcnt vmcnt(12)\n" "s_barrier\n" "ds_read_b64_tr_b16 v[48:49], v245 offset:0\n" "ds_read_b64_tr_b16 v[50:51], v245 offset:512\n" \
  "ds_read_b64_tr_b16 v[52:53], v245 offset:8192\n" "ds_read_b64_tr_b16 v[54:55], v245 offset:8704\n" "ds_read_b64_tr_b16 v[120:121], v244 offset:0\n" \
  "ds_read_b64_tr_b16 v[122:123], v244 offset:512\n" "ds_read_b64_tr_b16 v[124:125], v244 offset:8192\n" "ds_read_b64_tr_b16 v[126:127], v244 offset:8704\n" "s_waitcnt lgkmcnt(0)\n" \
  "s_barrier\n" "v_add_u32 v244, 49152, v249\n" "v_xor_b32 v245, 32, v244\n" "s_waitcnt vmcnt(6)\n" "s_barrier\n" "ds_read_b64_tr_b16 v[56:57], v245 offset:0\n" \
  "ds_read_b64_tr_b16 v[58:59], v245 offset:512\n" "ds_read_b64_tr_b16 v[60:61], v245 offset:8192\n" "ds_read_b64_tr_b16 v[62:63], v245 offset:8704\n" \
  "ds_read_b64_tr_b16 v[128:129], v244 offset:0\n" "ds_read_b64_tr_b16 v[130:131], v244 offset:512\n" "ds_read_b64_tr_b16 v[132:133], v244 offset:8192\n" \
  "ds_read_b64_tr_b16 v[134:135], v244 offset:8704\n" "s_waitcnt lgkmcnt(0)\n" "s_barrier\n" "v_add_u32 v244, 98304, v249\n" "v_xor_b32 v245, 32, v244\n" "s_waitcnt vmcnt(0)\n" "s_barrier\n" \
  "ds_read_b64_tr_b16 v[64:65], v245 offset:0\n" "ds_read_b64_tr_b16 v[66:67], v245 offset:512\n" "ds_read_b64_tr_b16 v[68:69], v245 offset:8192\n" \
  "ds_read_b64_tr_b16 v[70:71], v245 offset:8704\n" "ds_read_b64_tr_b16 v[136:137], v244 offset:0\n" "ds_read_b64_tr_b16 v[138:139], v244 offset:512\n" \
  "ds_read_b64_tr_b16 v[140:141], v244 offset:8192\n" "ds_read_b64_tr_b16 v[142:143], v244 offset:8704\n" "s_waitcnt lgkmcnt(0)\n" "s_barrier\n"
#define CH_SB \
  "v_add_u32 v244, 0, v249\n" "v_xor_b32 v245, 32, v244\n" "s_waitcnt vmcnt(14)\n" "s_barrier\n" "ds_read_b64_tr_b16 v[0:1], v245 offset:16384\n" \
  "ds_read_b64_tr_b16 v[2:3], v245 offset:16896\n" "ds_read_b64_tr_b16 v[4:5], v245 offset:24576\n" "ds_read_b64_tr_b16 v[6:7], v245 offset:25088\n" \
  "ds_read_b64_tr_b16 v[48:49], v244 offset:16384\n" "ds_read_b64_tr_b16 v[50:51], v244 offset:16896\n" "ds_read_b64_tr_b16 v[52:53], v244 offset:24576\n" \
  "ds_read_b64_tr_b16 v[54:55], v244 offset:25088\n" "ds_read_b64_tr_b16 v[96:97], v245 offset:32768\n" "ds_read_b64_tr_b16 v[98:99], v245 offset:33280\n" \
  "ds_read_b64_tr_b16 v[100:101], v245 offset:40960\n" "ds_read_b64_tr_b16 v[102:103], v245 offset:41472\n" "s_waitcnt lgkmcnt(0)\n" "s_barrier\n" "s_add_i32 m0, %[lw], 0\n" \
  "s_mov_b32 %[t], 1572864\n" "buffer_load_dwordx4 v248, %[r0], %[t] offen " XS(HNT) " lds\n" "s_add_i32 m0, %[lw], 8192\n" "s_mov_b32 %[t], 1835008\n" \
  "buffer_load_dwordx4 v248, %[r0], %[t] offen " XS(HNT) " lds\n" "s_add_i32 m0, %[lw], 16384\n" "s_mov_b32 %[t], 1572864\n" "buffer_load_dwordx4 v248, %[r1], %[t] offen " XS(HNT) " lds\n" \
  "s_add_i32 m0, %[lw], 24576\n" "s_mov_b32 %[t], 1835008\n" "buffer_load_dwordx4 v248, %[r1], %[t] offen " XS(HNT) " lds\n" "s_add_i32 m0, %[lw], 32768\n" "s_mov_b32 %[t], 1572864\n" \
  "buffer_load_dwordx4 v248, %[r2], %[t] offen " XS(HNT) " lds\n" "s_add_i32 m0, %[lw], 40960\n" "s_mov_b32 %[t], 1835008\n" "buffer_load_dwordx4 v248, %[r2], %[t] offen " XS(HNT) " lds\n" \
  "v_add_u32 v244, 49152, v249\n" "v_xor_b32 v245, 32, v244\n" "s_waitcnt vmcnt(14)\n" "s_barrier\n" "ds_read_b64_tr_b16 v[8:9], v245 offset:16384\n" \
  "ds_read_b64_tr_b16 v[10:11], v245 offset:16896\n" "ds_read_b64_tr_b16 v[12:13], v245 offset:24576\n" "ds_read_b64_tr_b16 v[14:15], v245 offset:25088\n" \
  "ds_read_b64_tr_b16 v[56:57], v244 offset:16384\n" "ds_read_b64_tr_b16 v[58:59], v244 offset:16896\n" "ds_read_b64_tr_b16 v[60:61], v244 offset:24576\n" \
  "ds_read_b64_tr_b16 v[62:63], v244 offset:25088\n" "ds_read_b64_tr_b16 v[104:105], v245 offset:32768\n" "ds_read_b64_tr_b16 v[106:107], v245 offset:33280\n" \
  "ds_read_b64_tr_b16 v[108:109], v245 offset:40960\n" "ds_read_b64_tr_b16 v[110:111], v245 offset:41472\n" "s_waitcnt lgkmcnt(0)\n" "s_barrier\n" "s_add_i32 m0, %[lw], 49152\n" \
  "s_mov_b32 %[t], 2097152\n" "buffer_load_dwordx4 v248, %[r0], %[t] offen " XS(HNT) " lds\n" "s_add_i32 m0, %[lw], 57344\n" "s_mov_b32 %[t], 2359296\n" \
  "buffer_load_dwordx4 v248, %[r0], %[t] offen " XS(HNT) " lds\n" "s_add_i32 m0, %[lw], 65536\n" "s_mov_b32 %[t], 2097152\n" "buffer_load_dwordx4 v248, %[r1], %[t] offen " XS(HNT) " lds\n" \
  "s_add_i32 m0, %[lw], 73728\n" "s_mov_b32 %[t], 2359296\n" "buffer_load_dwordx4 v248, %[r1], %[t] offen " XS(HNT) " lds\n" "s_add_i32 m0, %[lw], 81920\n" "s_mov_b32 %[t], 2097152\n" \
  "buffer_load_dwordx4 v248, %[r2], %[t] offen " XS(HNT) " lds\n" "s_add_i32 m0, %[lw], 90112\n" "s_mov_b32 %[t], 2359296\n" "buffer_load_dwordx4 v248, %[r2], %[t] offen " XS(HNT) " lds\n" \
  "v_add_u32 v244, 98304, v249\n" "v_xor_b32 v245, 32, v244\n" "s_waitcnt vmcnt(12)\n" "s_barrier\n" "ds_read_b64_tr_b16 v[16:17], v245 offset:16384\n" \
  "ds_read_b64_tr_b16 v[18:19], v245 offset:16896\n" "ds_read_b64_tr_b16 v[20:21], v245 offset:24576\n" "ds_read_b64_tr_b16 v[22:23], v245 offset:25088\n" \
  "ds_read_b64_tr_b16 v[64:65], v244 offset:16384\n" "ds_read_b64_tr_b16 v[66:67], v244 offset:16896\n" "ds_read_b64_tr_b16 v[68:69], v244 offset:24576\n" \
  "ds_read_b64_tr_b16 v[70:71], v244 offset:25088\n" "ds_read_b64_tr_b16 v[112:113], v245 offset:32768\n" "ds_read_b64_tr_b16 v[114:115], v245 offset:33280\n" \
  "ds_read_b64_tr_b16 v[116:117], v245 offset:40960\n" "ds_read_b64_tr_b16 v[118:119], v245 offset:41472\n" "s_waitcnt lgkmcnt(0)\n" "s_barrier\n" "s_add_i32 m0, %[lw], 98304\n" \
  "s_mov_b32 %[t], 2621440\n" "buffer_load_dwordx4 v248, %[r0], %[t] offen " XS(HNT) " lds\n" "s_add_i32 m0, %[lw], 106496\n" "s_mov_b32 %[t], 2883584\n" \
  "buffer_load_dwordx4 v248, %[r0], %[t] offen " XS(HNT) " lds\n" "s_add_i32 m0, %[lw], 114688\n" "s_mov_b32 %[t], 2621440\n" "buffer_load_dwordx4 v248, %[r1], %[t] offen " XS(HNT) " lds\n" \
  "s_add_i32 m0, %[lw], 122880\n" "s_mov_b32 %[t], 2883584\n" "buffer_load_dwordx4 v248, %[r1], %[t] offen " XS(HNT) " lds\n" "s_add_i32 m0, %[lw], 131072\n" "s_mov_b32 %[t], 2621440\n" \
  "buffer_load_dwordx4 v248, %[r2], %[t] offen " XS(HNT) " lds\n" "s_add_i32 m0, %[lw], 139264\n" "s_mov_b32 %[t], 2883584\n" "buffer_load_dwordx4 v248, %[r2], %[t] offen " XS(HNT) " lds\n" \
  "v_add_u32 v244, 0, v249\n" "v_xor_b32 v245, 32, v244\n" "s_waitcnt vmcnt(12)\n" "s_barrier\n" "ds_read_b64_tr_b16 v[24:25], v245 offset:16384\n" \
  "ds_read_b64_tr_b16 v[26:27], v245 offset:16896\n" "ds_read_b64_tr_b16 v[28:29], v245 offset:24576\n" "ds_read_b64_tr_b16 v[30:31], v245 offset:25088\n" \
  "ds_read_b64_tr_b16 v[72:73], v244 offset:16384\n" "ds_read_b64_tr_b16 v[74:75], v244 offset:16896\n" "ds_read_b64_tr_b16 v[76:77], v244 offset:24576\n" \
  "ds_read_b64_tr_b16 v[78:79], v244 offset:25088\n" "ds_read_b64_tr_b16 v[120:121], v245 offset:32768\n" "ds_read_b64_tr_b16 v[122:123], v245 offset:33280\n" \
  "ds_read_b64_tr_b16 v[124:125], v245 offset:40960\n" "ds_read_b64_tr_b16 v[126:127], v245 offset:41472\n" "s_waitcnt lgkmcnt(0)\n" "s_barrier\n" "s_add_i32 m0, %[lw], 0\n" \
  "s_mov_b32 %[t], 3145728\n" "buffer_load_dwordx4 v248, %[r0], %[t] offen " XS(HNT) " lds\n" "s_add_i32 m0, %[lw], 8192\n" "s_mov_b32 %[t], 3407872\n" \
  "buffer_load_dwordx4 v248, %[r0], %[t] offen " XS(HNT) " lds\n" "s_add_i32 m0, %[lw], 16384\n" "s_mov_b32 %[t], 3145728\n" "buffer_load_dwordx4 v248, %[r1], %[t] offen " XS(HNT) " lds\n" \
  "s_add_i32 m0, %[lw], 24576\n" "s_mov_b32 %[t], 3407872\n" "buffer_load_dwordx4 v248, %[r1], %[t] offen " XS(HNT) " lds\n" "s_add_i32 m0, %[lw], 32768\n" "s_mov_b32 %[t], 3145728\n" \
  "buffer_load_dwordx4 v248, %[r2], %[t] offen " XS(HNT) " lds\n" "s_add_i32 m0, %[lw], 40960\n" "s_mov_b32 %[t], 3407872\n" "buffer_load_dwordx4 v248, %[r2], %[t] offen " XS(HNT) " lds\n" \
  "v_add_u32 v244, 49152, v249\n" "v_xor_b32 v245, 32, v244\n" "s_waitcnt vmcnt(12)\n" "s_barrier\n" "ds_read_b64_tr_b16 v[32:33], v245 offset:16384\n" \
  "ds_read_b64_tr_b16 v[34:35], v245 offset:16896\n" "ds_read_b64_tr_b16 v[36:37], v245 offset:24576\n" "ds_read_b64_tr_b16 v[38:39], v245 offset:25088\n" \
  "ds_read_b64_tr_b16 v[80:81], v244 offset:16384\n" "ds_read_b64_tr_b16 v[82:83], v244 offset:16896\n" "ds_read_b64_tr_b16 v[84:85], v244 offset:24576\n" \
  "ds_read_b64_tr_b16 v[86:87], v244 offset:25088\n" "ds_read_b64_tr_b16 v[128:129], v245 offset:32768\n" "ds_read_b64_tr_b16 v[130:131], v245 offset:33280\n" \
  "ds_read_b64_tr_b16 v[132:133], v245 offset:40960\n" "ds_read_b64_tr_b16 v[134:135], v245 offset:41472\n" "s_waitcnt lgkmcnt(0)\n" "s_barrier\n" "s_add_i32 m0, %[lw], 49152\n" \
  "s_mov_b32 %[t], 3670016\n" "buffer_load_dwordx4 v248, %[r0], %[t] offen " XS(HNT) " lds\n" "s_add_i32 m0, %[lw], 57344\n" "s_mov_b32 %[t], 3932160\n" \
  "buffer_load_dwordx4 v248, %[r0], %[t] offen " XS(HNT) " lds\n" "s_add_i32 m0, %[lw], 65536\n" "s_mov_b32 %[t], 3670016\n" "buffer_load_dwordx4 v248, %[r1], %[t] offen " XS(HNT) " lds\n" \
  "s_add_i32 m0, %[lw], 73728\n" "s_mov_b32 %[t], 3932160\n" "buffer_load_dwordx4 v248, %[r1], %[t] offen " XS(HNT) " lds\n" "s_add_i32 m0, %[lw], 81920\n" "s_mov_b32 %[t], 3670016\n" \
  "buffer_load_dwordx4 v248, %[r2], %[t] offen " XS(HNT) " lds\n" "s_add_i32 m0, %[lw], 90112\n" "s_mov_b32 %[t], 3932160\n" "buffer_load_dwordx4 v248, %[r2], %[t] offen " XS(HNT) " lds\n" \
  "v_add_u32 v244, 98304, v249\n" "v_xor_b32 v245, 32, v244\n" "s_waitcnt vmcnt(12)\n" "s_barrier\n" "ds_read_b64_tr_b16 v[40:41], v245 offset:16384\n" \
  "ds_read_b64_tr_b16 v[42:43], v245 offset:16896\n" "ds_read_b64_tr_b16 v[44:45], v245 offset:24576\n" "ds_read_b64_tr_b16 v[46:47], v245 offset:25088\n" \
  "ds_read_b64_tr_b16 v[88:89], v244 offset:16384\n" "ds_read_b64_tr_b16 v[90:91], v244 offset:16896\n" "ds_read_b64_tr_b16 v[92:93], v244 offset:24576\n" \
  "ds_read_b64_tr_b16 v[94:95], v244 offset:25088\n" "ds_read_b64_tr_b16 v[136:137], v245 offset:32768\n" "ds_read_b64_tr_b16 v[138:139], v245 offset:33280\n" \
  "ds_read_b64_tr_b16 v[140:141], v245 offset:40960\n" "ds_read_b64_tr_b16 v[142:143], v245 offset:41472\n" "ds_read_b64_tr_b16 v[144:145], v244 offset:32768\n" \
  "ds_read_b64_tr_b16 v[146:147], v244 offset:33280\n" "ds_read_b64_tr_b16 v[148:149], v244 offset:40960\n" "ds_read_b64_tr_b16 v[150:151], v244 offset:41472\n" "s_waitcnt lgkmcnt(0)\n" \
  "s_barrier\n" "s_add_i32 m0, %[lw], 98304\n" "s_mov_b32 %[t], 4194304\n" "buffer_load_dwordx4 v248, %[r0], %[t] offen " XS(HNT) " lds\n" "s_add_i32 m0, %[lw], 106496\n" \
  "s_mov_b32 %[t], 4456448\n" "buffer_load_dwordx4 v248, %[r0], %[t] offen " XS(HNT) " lds\n" "s_add_i32 m0, %[lw], 114688\n" "s_mov_b32 %[t], 4194304\n" \
  "buffer_load_dwordx4 v248, %[r1], %[t] offen " XS(HNT) " lds\n" "s_add_i32 m0, %[lw], 122880\n" "s_mov_b32 %[t], 4456448\n" "buffer_load_dwordx4 v248, %[r1], %[t] offen " XS(HNT) " lds\n" \
  "s_add_i32 m0, %[lw], 131072\n" "s_mov_b32 %[t], 4194304\n" "buffer_load_dwordx4 v248, %[r2], %[t] offen " XS(HNT) " lds\n" "s_add_i32 m0, %[lw], 139264\n" "s_mov_b32 %[t], 4456448\n" \
  "buffer_load_dwordx4 v248, %[r2], %[t] offen " XS(HNT) " lds\n" "v_add_u32 v244, 0, v249\n" "v_xor_b32 v245, 32, v244\n" "s_waitcnt vmcnt(12)\n" "s_barrier\n" \
  "ds_read_b64_tr_b16 v[176:177], v245 offset:16384\n" "ds_read_b64_tr_b16 v[178:179], v245 offset:16896\n" "ds_read_b64_tr_b16 v[180:181], v245 offset:24576\n" \
  "ds_read_b64_tr_b16 v[182:183], v245 offset:25088\n" "ds_read_b64_tr_b16 v[184:185], v244 offset:16384\n" "ds_read_b64_tr_b16 v[186:187], v244 offset:16896\n" \
  "ds_read_b64_tr_b16 v[188:189], v244 offset:24576\n" "ds_read_b64_tr_b16 v[190:191], v244 offset:25088\n" "ds_read_b64_tr_b16 v[192:193], v245 offset:32768\n" \
  "ds_read_b64_tr_b16 v[194:195], v245 offset:33280\n" "ds_read_b64_tr_b16 v[196:197], v245 offset:40960\n" "ds_read_b64_tr_b16 v[198:199], v245 offset:41472\n" \
  "ds_read_b64_tr_b16 v[152:153], v244 offset:32768\n" "ds_read_b64_tr_b16 v[154:155], v244 offset:33280\n" "ds_read_b64_tr_b16 v[156:157], v244 offset:40960\n" \
  "ds_read_b64_tr_b16 v[158:159], v244 offset:41472\n" "s_waitcnt lgkmcnt(0)\n" "s_barrier\n" "ds_write_b128 v250, v[176:179] offset:0\n" "ds_write_b128 v250, v[180:183] offset:1024\n" \
  "ds_write_b128 v250, v[184:187] offset:8192\n" "ds_write_b128 v250, v[188:191] offset:9216\n" "ds_write_b128 v250, v[192:195] offset:16384\n" "ds_write_b128 v250, v[196:199] offset:17408\n" \
  "s_waitcnt lgkmcnt(0)\n" "v_add_u32 v244, 49152, v249\n" "v_xor_b32 v245, 32, v244\n" "s_waitcnt vmcnt(6)\n" "s_barrier\n" "ds_read_b64_tr_b16 v[176:177], v245 offset:16384\n" \
  "ds_read_b64_tr_b16 v[178:179], v245 offset:16896\n" "ds_read_b64_tr_b16 v[180:181], v245 offset:24576\n" "ds_read_b64_tr_b16 v[182:183], v245 offset:25088\n" \
  "ds_read_b64_tr_b16 v[184:185], v244 offset:16384\n" "ds_read_b64_tr_b16 v[186:187], v244 offset:16896\n" "ds_read_b64_tr_b16 v[188:189], v244 offset:24576\n" \
  "ds_read_b64_tr_b16 v[190:191], v244 offset:25088\n" "ds_read_b64_tr_b16 v[192:193], v245 offset:32768\n" "ds_read_b64_tr_b16 v[194:195], v245 offset:33280\n" \
  "ds_read_b64_tr_b16 v[196:197], v245 offset:40960\n" "ds_read_b64_tr_b16 v[198:199], v245 offset:41472\n" "ds_read_b64_tr_b16 v[160:161], v244 offset:32768\n" \
  "ds_read_b64_tr_b16 v[162:163], v244 offset:33280\n" "ds_read_b64_tr_b16 v[164:165], v244 offset:40960\n" "ds_read_b64_tr_b16 v[166:167], v244 offset:41472\n" "s_waitcnt lgkmcnt(0)\n" \
  "s_barrier\n" "v_add_u32 v250, 24576, v250\n" "ds_write_b128 v250, v[176:179] offset:0\n" "ds_write_b128 v250, v[180:183] offset:1024\n" "ds_write_b128 v250, v[184:187] offset:8192\n" \
  "ds_write_b128 v250, v[188:191] offset:9216\n" "ds_write_b128 v250, v[192:195] offset:16384\n" "ds_write_b128 v250, v[196:199] offset:17408\n" "s_waitcnt lgkmcnt(0)\n" \
  "v_add_u32 v244, 98304, v249\n" "v_xor_b32 v245, 32, v244\n" "s_waitcnt vmcnt(0)\n" "s_barrier\n" "ds_read_b64_tr_b16 v[176:177], v245 offset:16384\n" \
  "ds_read_b64_tr_b16 v[178:179], v245 offset:16896\n" "ds_read_b64_tr_b16 v[180:181], v245 offset:24576\n" "ds_read_b64_tr_b16 v[182:183], v245 offset:25088\n" \
  "ds_read_b64_tr_b16 v[184:185], v244 offset:16384\n" "ds_read_b64_tr_b16 v[186:187], v244 offset:16896\n" "ds_read_b64_tr_b16 v[188:189], v244 offset:24576\n" \
  "ds_read_b64_tr_b16 v[190:191], v244 offset:25088\n" "ds_read_b64_tr_b16 v[192:193], v245 offset:32768\n" "ds_read_b64_tr_b16 v[194:195], v245 offset:33280\n" \
  "ds_read_b64_tr_b16 v[196:197], v245 offset:40960\n" "ds_read_b64_tr_b16 v[198:199], v245 offset:41472\n" "ds_read_b64_tr_b16 v[168:169], v244 offset:32768\n" \
  "ds_read_b64_tr_b16 v[170:171], v244 offset:33280\n" "ds_read_b64_tr_b16 v[172:173], v244 offset:40960\n" "ds_read_b64_tr_b16 v[174:175], v244 offset:41472\n" "s_waitcnt lgkmcnt(0)\n" \
  "s_barrier\n" "v_add_u32 v250, 24576, v250\n" "ds_write_b128 v250, v[176:179] offset:0\n" "ds_write_b128 v250, v[180:183] offset:1024\n" "ds_write_b128 v250, v[184:187] offset:8192\n" \
  "ds_write_b128 v250, v[188:191] offset:9216\n" "ds_write_b128 v251, v[192:195] offset:23552\n" "ds_write_b128 v251, v[196:199] offset:27648\n" "s_waitcnt lgkmcnt(0)\n"
#define CH_SO \
  "s_waitcnt vmcnt(10)\n" "ds_read_b64_tr_b16 v[0:1], v218 offset:0\n" "ds_read_b64_tr_b16 v[2:3], v218 offset:128\n" "ds_read_b64_tr_b16 v[4:5], v218 offset:4096\n" \
  "ds_read_b64_tr_b16 v[6:7], v218 offset:4224\n" "v_xor_b32 v200, 16, v218\n" "ds_read_b64_tr_b16 v[8:9], v200 offset:0\n" "ds_read_b64_tr_b16 v[10:11], v200 offset:128\n" \
  "ds_read_b64_tr_b16 v[12:13], v200 offset:4096\n" "ds_read_b64_tr_b16 v[14:15], v200 offset:4224\n" "v_xor_b32 v200, 32, v218\n" "ds_read_b64_tr_b16 v[16:17], v200 offset:0\n" \
  "ds_read_b64_tr_b16 v[18:19], v200 offset:128\n" "ds_read_b64_tr_b16 v[20:21], v200 offset:4096\n" "ds_read_b64_tr_b16 v[22:23], v200 offset:4224\n" "v_xor_b32 v200, 48, v218\n" \
  "ds_read_b64_tr_b16 v[24:25], v200 offset:0\n" "ds_read_b64_tr_b16 v[26:27], v200 offset:128\n" "ds_read_b64_tr_b16 v[28:29], v200 offset:4096\n" \
  "ds_read_b64_tr_b16 v[30:31], v200 offset:4224\n" "s_waitcnt lgkmcnt(0)\n" "s_add_i32 m0, %[lw], 0\n" "s_mov_b32 %[t], 294912\n" "buffer_load_dwordx4 v216, %[r], %[t] offen lds\n" \
  "s_add_i32 m0, %[lw], 1024\n" "s_mov_b32 %[t], 313344\n" "buffer_load_dwordx4 v216, %[r], %[t] offen lds\n" "s_add_i32 m0, %[lw], 2048\n" "s_mov_b32 %[t], 331776\n" \
  "buffer_load_dwordx4 v216, %[r], %[t] offen lds\n" "s_add_i32 m0, %[lw], 3072\n" "s_mov_b32 %[t], 350208\n" "buffer_load_dwordx4 v216, %[r], %[t] offen lds\n" "s_add_i32 m0, %[lw], 4096\n" \
  "s_mov_b32 %[t], 368640\n" "buffer_load_dwordx4 v216, %[r], %[t] offen lds\n" "s_add_i32 m0, %[lw], 5120\n" "s_mov_b32 %[t], 387072\n" "buffer_load_dwordx4 v216, %[r], %[t] offen lds\n" \
  "s_add_i32 m0, %[lw], 6144\n" "s_mov_b32 %[t], 405504\n" "buffer_load_dwordx4 v216, %[r], %[t] offen lds\n" "s_add_i32 m0, %[lw], 7168\n" "s_mov_b32 %[t], 423936\n" \
  "buffer_load_dwordx4 v216, %[r], %[t] offen lds\n" "s_waitcnt vmcnt(8)\n" "buffer_load_dword v221, v220, %[r], 0 offen\n" "ds_read_b64_tr_b16 v[40:41], v218 offset:8192\n" \
  "ds_read_b64_tr_b16 v[42:43], v218 offset:8320\n" "ds_read_b64_tr_b16 v[44:45], v218 offset:12288\n" "ds_read_b64_tr_b16 v[46:47], v218 offset:12416\n" "v_xor_b32 v200, 16, v218\n" \
  "ds_read_b64_tr_b16 v[48:49], v200 offset:8192\n" "ds_read_b64_tr_b16 v[50:51], v200 offset:8320\n" "ds_read_b64_tr_b16 v[52:53], v200 offset:12288\n" \
  "ds_read_b64_tr_b16 v[54:55], v200 offset:12416\n" "v_xor_b32 v200, 32, v218\n" "ds_read_b64_tr_b16 v[56:57], v200 offset:8192\n" "ds_read_b64_tr_b16 v[58:59], v200 offset:8320\n" \
  "ds_read_b64_tr_b16 v[60:61], v200 offset:12288\n" "ds_read_b64_tr_b16 v[62:63], v200 offset:12416\n" "v_xor_b32 v200, 48, v218\n" "ds_read_b64_tr_b16 v[64:65], v200 offset:8192\n" \
  "ds_read_b64_tr_b16 v[66:67], v200 offset:8320\n" "ds_read_b64_tr_b16 v[68:69], v200 offset:12288\n" "ds_read_b64_tr_b16 v[70:71], v200 offset:12416\n" "s_waitcnt lgkmcnt(0)\n" \
  "s_add_i32 m0, %[lw], 8192\n" "s_mov_b32 %[t], 442368\n" "buffer_load_dwordx4 v216, %[r], %[t] offen lds\n" "s_add_i32 m0, %[lw], 9216\n" "s_mov_b32 %[t], 460800\n" \
  "buffer_load_dwordx4 v216, %[r], %[t] offen lds\n" "s_add_i32 m0, %[lw], 10240\n" "s_mov_b32 %[t], 479232\n" "buffer_load_dwordx4 v216, %[r], %[t] offen lds\n" "s_add_i32 m0, %[lw], 11264\n" \
  "s_mov_b32 %[t], 497664\n" "buffer_load_dwordx4 v216, %[r], %[t] offen lds\n" "s_add_i32 m0, %[lw], 12288\n" "s_mov_b32 %[t], 516096\n" "buffer_load_dwordx4 v216, %[r], %[t] offen lds\n" \
  "s_add_i32 m0, %[lw], 13312\n" "s_mov_b32 %[t], 534528\n" "buffer_load_dwordx4 v216, %[r], %[t] offen lds\n" "s_add_i32 m0, %[lw], 14336\n" "s_mov_b32 %[t], 552960\n" \
  "buffer_load_dwordx4 v216, %[r], %[t] offen lds\n" "s_add_i32 m0, %[lw], 15360\n" "s_mov_b32 %[t], 571392\n" "buffer_load_dwordx4 v216, %[r], %[t] offen lds\n" "s_waitcnt vmcnt(8)\n" \
  "ds_read_b64_tr_b16 v[80:81], v218 offset:0\n" "ds_read_b64_tr_b16 v[82:83], v218 offset:128\n" "ds_read_b64_tr_b16 v[84:85], v218 offset:4096\n" \
  "ds_read_b64_tr_b16 v[86:87], v218 offset:4224\n" "v_xor_b32 v200, 16, v218\n" "ds_read_b64_tr_b16 v[88:89], v200 offset:0\n" "ds_read_b64_tr_b16 v[90:91], v200 offset:128\n" \
  "ds_read_b64_tr_b16 v[92:93], v200 offset:4096\n" "ds_read_b64_tr_b16 v[94:95], v200 offset:4224\n" "v_xor_b32 v200, 32, v218\n" "ds_read_b64_tr_b16 v[96:97], v200 offset:0\n" \
  "ds_read_b64_tr_b16 v[98:99], v200 offset:128\n" "ds_read_b64_tr_b16 v[100:101], v200 offset:4096\n" "ds_read_b64_tr_b16 v[102:103], v200 offset:4224\n" "v_xor_b32 v200, 48, v218\n" \
  "ds_read_b64_tr_b16 v[104:105], v200 offset:0\n" "ds_read_b64_tr_b16 v[106:107], v200 offset:128\n" "ds_read_b64_tr_b16 v[108:109], v200 offset:4096\n" \
  "ds_read_b64_tr_b16 v[110:111], v200 offset:4224\n" "s_waitcnt lgkmcnt(0)\n" "s_add_i32 m0, %[lw], 0\n" "s_mov_b32 %[t], 0\n" "buffer_load_dwordx4 v217, %[r], %[t] offen lds\n" \
  "s_add_i32 m0, %[lw], 1024\n" "s_mov_b32 %[t], 73728\n" "buffer_load_dwordx4 v217, %[r], %[t] offen lds\n" "s_add_i32 m0, %[lw], 2048\n" "s_mov_b32 %[t], 147456\n" \
  "buffer_load_dwordx4 v217, %[r], %[t] offen lds\n" "s_add_i32 m0, %[lw], 3072\n" "s_mov_b32 %[t], 221184\n" "buffer_load_dwordx4 v217, %[r], %[t] offen lds\n" "s_add_i32 m0, %[lw], 4096\n" \
  "s_mov_b32 %[t], 294912\n" "buffer_load_dwordx4 v217, %[r], %[t] offen lds\n" "s_add_i32 m0, %[lw], 5120\n" "s_mov_b32 %[t], 368640\n" "buffer_load_dwordx4 v217, %[r], %[t] offen lds\n" \
  "s_add_i32 m0, %[lw], 6144\n" "s_mov_b32 %[t], 442368\n" "buffer_load_dwordx4 v217, %[r], %[t] offen lds\n" "s_add_i32 m0, %[lw], 7168\n" "s_mov_b32 %[t], 516096\n" \
  "buffer_load_dwordx4 v217, %[r], %[t] offen lds\n" "s_waitcnt vmcnt(8)\n" "ds_read_b64_tr_b16 v[120:121], v218 offset:8192\n" "ds_read_b64_tr_b16 v[122:123], v218 offset:8320\n" \
  "ds_read_b64_tr_b16 v[124:125], v218 offset:12288\n" "ds_read_b64_tr_b16 v[126:127], v218 offset:12416\n" "v_xor_b32 v200, 16, v218\n" "ds_read_b64_tr_b16 v[128:129], v200 offset:8192\n" \
  "ds_read_b64_tr_b16 v[130:131], v200 offset:8320\n" "ds_read_b64_tr_b16 v[132:133], v200 offset:12288\n" "ds_read_b64_tr_b16 v[134:135], v200 offset:12416\n" "v_xor_b32 v200, 32, v218\n" \
  "ds_read_b64_tr_b16 v[136:137], v200 offset:8192\n" "ds_read_b64_tr_b16 v[138:139], v200 offset:8320\n" "ds_read_b64_tr_b16 v[140:141], v200 offset:12288\n" \
  "ds_read_b64_tr_b16 v[142:143], v200 offset:12416\n" "v_xor_b32 v200, 48, v218\n" "ds_read_b64_tr_b16 v[144:145], v200 offset:8192\n" "ds_read_b64_tr_b16 v[146:147], v200 offset:8320\n" \
  "ds_read_b64_tr_b16 v[148:149], v200 offset:12288\n" "ds_read_b64_tr_b16 v[150:151], v200 offset:12416\n" "s_waitcnt lgkmcnt(0)\n" "s_waitcnt vmcnt(0)\n" \
  "ds_read_b64_tr_b16 v[32:33], v219 offset:0\n" "ds_read_b64_tr_b16 v[34:35], v219 offset:32\n" "ds_read_b64_tr_b16 v[36:37], v219 offset:1024\n" \
  "ds_read_b64_tr_b16 v[38:39], v219 offset:1056\n" "ds_read_b64_tr_b16 v[72:73], v219 offset:2048\n" "ds_read_b64_tr_b16 v[74:75], v219 offset:2080\n" \
  "ds_read_b64_tr_b16 v[76:77], v219 offset:3072\n" "ds_read_b64_tr_b16 v[78:79], v219 offset:3104\n" "ds_read_b64_tr_b16 v[112:113], v219 offset:4096\n" \
  "ds_read_b64_tr_b16 v[114:115], v219 offset:4128\n" "ds_read_b64_tr_b16 v[116:117], v219 offset:5120\n" "ds_read_b64_tr_b16 v[118:119], v219 offset:5152\n" \
  "ds_read_b64_tr_b16 v[152:153], v219 offset:6144\n" "ds_read_b64_tr_b16 v[154:155], v219 offset:6176\n" "ds_read_b64_tr_b16 v[156:157], v219 offset:7168\n" \
  "ds_read_b64_tr_b16 v[158:159], v219 offset:7200\n" "s_waitcnt lgkmcnt(0)\n"
#define CH_ST1 \
  "v_xor_b32 v217, 64, v212\n" "v_add_u32 v200, 36864, v211\n" "v_xor_b32 v201, 32, v200\n" "s_waitcnt vmcnt(10)\n" "s_barrier\n" "ds_read_b64_tr_b16 v[128:129], v200 offset:0\n" \
  "ds_read_b64_tr_b16 v[130:131], v200 offset:2048\n" "ds_read_b64_tr_b16 v[132:133], v201 offset:0\n" "ds_read_b64_tr_b16 v[134:135], v201 offset:2048\n" \
  "ds_read_b64_tr_b16 v[136:137], v200 offset:16384\n" "ds_read_b64_tr_b16 v[138:139], v200 offset:18432\n" "ds_read_b64_tr_b16 v[140:141], v201 offset:16384\n" \
  "ds_read_b64_tr_b16 v[142:143], v201 offset:18432\n" "ds_read_b128 v[176:179], v212 offset:32768\n" "ds_read_b128 v[180:183], v212 offset:34816\n" \
  "ds_read_b128 v[184:187], v217 offset:32768\n" "ds_read_b128 v[188:191], v217 offset:34816\n" "s_waitcnt lgkmcnt(0)\n" "s_barrier\n" "s_add_i32 m0, s52, 36864\n" "s_mov_b32 s63, 2621440\n" \
  "buffer_load_dwordx4 v210, s[40:43], s63 offen " XS(SNT) " lds\n" "s_add_i32 m0, s52, 45056\n" "s_mov_b32 s63, 2752512\n" "buffer_load_dwordx4 v210, s[40:43], s63 offen " XS(SNT) " lds\n" \
  "s_add_i32 m0, s52, 53248\n" "s_mov_b32 s63, 2883584\n" "buffer_load_dwordx4 v210, s[40:43], s63 offen " XS(SNT) " lds\n" "s_add_i32 m0, s52, 61440\n" "s_mov_b32 s63, 3014656\n" \
  "buffer_load_dwordx4 v210, s[40:43], s63 offen " XS(SNT) " lds\n" "v_mfma_f32_16x16x32_bf16 v[144:147], v[128:131], v[176:179], 0\n" \
  "v_mfma_f32_16x16x32_bf16 v[148:151], v[132:135], v[176:179], 0\n" "v_mfma_f32_16x16x32_bf16 v[152:155], v[128:131], v[180:183], 0\n" \
  "v_mfma_f32_16x16x32_bf16 v[156:159], v[132:135], v[180:183], 0\n" "v_mfma_f32_16x16x32_bf16 v[144:147], v[136:139], v[184:187], v[144:147]\n" \
  "v_mfma_f32_16x16x32_bf16 v[148:151], v[140:143], v[184:187], v[148:151]\n" "v_mfma_f32_16x16x32_bf16 v[152:155], v[136:139], v[188:191], v[152:155]\n" \
  "v_mfma_f32_16x16x32_bf16 v[156:159], v[140:143], v[188:191], v[156:159]\n" "v_add_u32 v200, 73728, v211\n" "v_xor_b32 v201, 32, v200\n" "s_waitcnt vmcnt(10)\n" "s_barrier\n" \
  "ds_read_b64_tr_b16 v[112:113], v200 offset:0\n" "ds_read_b64_tr_b16 v[114:115], v200 offset:2048\n" "ds_read_b64_tr_b16 v[116:117], v201 offset:0\n" \
  "ds_read_b64_tr_b16 v[118:119], v201 offset:2048\n" "ds_read_b64_tr_b16 v[120:121], v200 offset:16384\n" "ds_read_b64_tr_b16 v[122:123], v200 offset:18432\n" \
  "ds_read_b64_tr_b16 v[124:125], v201 offset:16384\n" "ds_read_b64_tr_b16 v[126:127], v201 offset:18432\n" "ds_read_b128 v[176:179], v212 offset:28672\n" \
  "ds_read_b128 v[180:183], v212 offset:30720\n" "ds_read_b128 v[184:187], v217 offset:28672\n" "ds_read_b128 v[188:191], v217 offset:30720\n" "s_waitcnt lgkmcnt(0)\n" "s_barrier\n" \
  "s_add_i32 m0, s52, 73728\n" "s_mov_b32 s63, 2097152\n" "buffer_load_dwordx4 v210, s[40:43], s63 offen " XS(SNT) " lds\n" "s_add_i32 m0, s52, 81920\n" "s_mov_b32 s63, 2228224\n" \
  "buffer_load_dwordx4 v210, s[40:43], s63 offen " XS(SNT) " lds\n" "s_add_i32 m0, s52, 90112\n" "s_mov_b32 s63, 2359296\n" "buffer_load_dwordx4 v210, s[40:43], s63 offen " XS(SNT) " lds\n" \
  "s_add_i32 m0, s52, 98304\n" "s_mov_b32 s63, 2490368\n" "buffer_load_dwordx4 v210, s[40:43], s63 offen " XS(SNT) " lds\n" \
  "v_mfma_f32_16x16x32_bf16 v[144:147], v[112:115], v[176:179], v[144:147]\n" "v_mfma_f32_16x16x32_bf16 v[148:151], v[116:119], v[176:179], v[148:151]\n" \
  "v_mfma_f32_16x16x32_bf16 v[152:155], v[112:115], v[180:183], v[152:155]\n" "v_mfma_f32_16x16x32_bf16 v[156:159], v[116:119], v[180:183], v[156:159]\n" \
  "v_mfma_f32_16x16x32_bf16 v[144:147], v[120:123], v[184:187], v[144:147]\n" "v_mfma_f32_16x16x32_bf16 v[148:151], v[124:127], v[184:187], v[148:151]\n" \
  "v_mfma_f32_16x16x32_bf16 v[152:155], v[120:123], v[188:191], v[152:155]\n" "v_mfma_f32_16x16x32_bf16 v[156:159], v[124:127], v[188:191], v[156:159]\n" "v_add_u32 v200, 110592, v211\n" \
  "v_xor_b32 v201, 32, v200\n" "s_waitcnt vmcnt(8)\n" "s_barrier\n" "ds_read_b64_tr_b16 v[96:97], v200 offset:0\n" "ds_read_b64_tr_b16 v[98:99], v200 offset:2048\n" \
  "ds_read_b64_tr_b16 v[100:101], v201 offset:0\n" "ds_read_b64_tr_b16 v[102:103], v201 offset:2048\n" "ds_read_b64_tr_b16 v[104:105], v200 offset:16384\n" \
  "ds_read_b64_tr_b16 v[106:107], v200 offset:18432\n" "ds_read_b64_tr_b16 v[108:109], v201 offset:16384\n" "ds_read_b64_tr_b16 v[110:111], v201 offset:18432\n" \
  "ds_read_b128 v[176:179], v212 offset:24576\n" "ds_read_b128 v[180:183], v212 offset:26624\n" "ds_read_b128 v[184:187], v217 offset:24576\n" "ds_read_b128 v[188:191], v217 offset:26624\n" \
  "s_waitcnt lgkmcnt(0)\n" "s_barrier\n" "s_add_i32 m0, s52, 110592\n" "s_mov_b32 s63, 1572864\n" "buffer_load_dwordx4 v210, s[40:43], s63 offen " XS(SNT) " lds\n" "s_add_i32 m0, s52, 118784\n" \
  "s_mov_b32 s63, 1703936\n" "buffer_load_dwordx4 v210, s[40:43], s63 offen " XS(SNT) " lds\n" "s_add_i32 m0, s52, 126976\n" "s_mov_b32 s63, 1835008\n" \
  "buffer_load_dwordx4 v210, s[40:43], s63 offen " XS(SNT) " lds\n" "s_add_i32 m0, s52, 135168\n" "s_mov_b32 s63, 1966080\n" "buffer_load_dwordx4 v210, s[40:43], s63 offen " XS(SNT) " lds\n" \
  "v_mfma_f32_16x16x32_bf16 v[144:147], v[96:99], v[176:179], v[144:147]\n" "v_mfma_f32_16x16x32_bf16 v[148:151], v[100:103], v[176:179], v[148:151]\n" \
  "v_mfma_f32_16x16x32_bf16 v[152:155], v[96:99], v[180:183], v[152:155]\n" "v_mfma_f32_16x16x32_bf16 v[156:159], v[100:103], v[180:183], v[156:159]\n" \
  "v_mfma_f32_16x16x32_bf16 v[144:147], v[104:107], v[184:187], v[144:147]\n" "v_mfma_f32_16x16x32_bf16 v[148:151], v[108:111], v[184:187], v[148:151]\n" \
  "v_mfma_f32_16x16x32_bf16 v[152:155], v[104:107], v[188:191], v[152:155]\n" "v_mfma_f32_16x16x32_bf16 v[156:159], v[108:111], v[188:191], v[156:159]\n" "v_add_u32 v200, 36864, v211\n" \
  "v_xor_b32 v201, 32, v200\n" "s_waitcnt vmcnt(8)\n" "s_barrier\n" "ds_read_b64_tr_b16 v[80:81], v200 offset:0\n" "ds_read_b64_tr_b16 v[82:83], v200 offset:2048\n" \
  "ds_read_b64_tr_b16 v[84:85], v201 offset:0\n" "ds_read_b64_tr_b16 v[86:87], v201 offset:2048\n" "ds_read_b64_tr_b16 v[88:89], v200 offset:16384\n" \
  "ds_read_b64_tr_b16 v[90:91], v200 offset:18432\n" "ds_read_b64_tr_b16 v[92:93], v201 offset:16384\n" "ds_read_b64_tr_b16 v[94:95], v201 offset:18432\n" \
  "ds_read_b128 v[176:179], v212 offset:20480\n" "ds_read_b128 v[180:183], v212 offset:22528\n" "ds_read_b128 v[184:187], v217 offset:20480\n" "ds_read_b128 v[188:191], v217 offset:22528\n" \
  "s_waitcnt lgkmcnt(0)\n" "s_barrier\n" "s_add_i32 m0, s52, 36864\n" "s_mov_b32 s63, 1048576\n" "buffer_load_dwordx4 v210, s[40:43], s63 offen " XS(SNT) " lds\n" "s_add_i32 m0, s52, 45056\n" \
  "s_mov_b32 s63, 1179648\n" "buffer_load_dwordx4 v210, s[40:43], s63 offen " XS(SNT) " lds\n" "s_add_i32 m0, s52, 53248\n" "s_mov_b32 s63, 1310720\n" \
  "buffer_load_dwordx4 v210, s[40:43], s63 offen " XS(SNT) " lds\n" "s_add_i32 m0, s52, 61440\n" "s_mov_b32 s63, 1441792\n" "buffer_load_dwordx4 v210, s[40:43], s63 offen " XS(SNT) " lds\n" \
  "v_mfma_f32_16x16x32_bf16 v[144:147], v[80:83], v[176:179], v[144:147]\n" "v_mfma_f32_16x16x32_bf16 v[148:151], v[84:87], v[176:179], v[148:151]\n" \
  "v_mfma_f32_16x16x32_bf16 v[152:155], v[80:83], v[180:183], v[152:155]\n" "v_mfma_f32_16x16x32_bf16 v[156:159], v[84:87], v[180:183], v[156:159]\n" \
  "v_mfma_f32_16x16x32_bf16 v[144:147], v[88:91], v[184:187], v[144:147]\n" "v_mfma_f32_16x16x32_bf16 v[148:151], v[92:95], v[184:187], v[148:151]\n" \
  "v_mfma_f32_16x16x32_bf16 v[152:155], v[88:91], v[188:191], v[152:155]\n" "v_mfma_f32_16x16x32_bf16 v[156:159], v[92:95], v[188:191], v[156:159]\n" "v_add_u32 v200, 73728, v211\n" \
  "v_xor_b32 v201, 32, v200\n" "s_waitcnt vmcnt(8)\n" "s_barrier\n" "ds_read_b64_tr_b16 v[64:65], v200 offset:0\n" "ds_read_b64_tr_b16 v[66:67], v200 offset:2048\n" \
  "ds_read_b64_tr_b16 v[68:69], v201 offset:0\n" "ds_read_b64_tr_b16 v[70:71], v201 offset:2048\n" "ds_read_b64_tr_b16 v[72:73], v200 offset:16384\n" \
  "ds_read_b64_tr_b16 v[74:75], v200 offset:18432\n" "ds_read_b64_tr_b16 v[76:77], v201 offset:16384\n" "ds_read_b64_tr_b16 v[78:79], v201 offset:18432\n" \
  "ds_read_b128 v[176:179], v212 offset:16384\n" "ds_read_b128 v[180:183], v212 offset:18432\n" "ds_read_b128 v[184:187], v217 offset:16384\n" "ds_read_b128 v[188:191], v217 offset:18432\n" \
  "s_waitcnt lgkmcnt(0)\n" "s_barrier\n" "s_add_i32 m0, s52, 73728\n" "s_mov_b32 s63, 524288\n" "buffer_load_dwordx4 v210, s[40:43], s63 offen " XS(SNT) " lds\n" "s_add_i32 m0, s52, 81920\n" \
  "s_mov_b32 s63, 655360\n" "buffer_load_dwordx4 v210, s[40:43], s63 offen " XS(SNT) " lds\n" "s_add_i32 m0, s52, 90112\n" "s_mov_b32 s63, 786432\n" \
  "buffer_load_dwordx4 v210, s[40:43], s63 offen " XS(SNT) " lds\n" "s_add_i32 m0, s52, 98304\n" "s_mov_b32 s63, 917504\n" "buffer_load_dwordx4 v210, s[40:43], s63 offen " XS(SNT) " lds\n" \
  "v_mfma_f32_16x16x32_bf16 v[144:147], v[64:67], v[176:179], v[144:147]\n" "v_mfma_f32_16x16x32_bf16 v[148:151], v[68:71], v[176:179], v[148:151]\n" \
  "v_mfma_f32_16x16x32_bf16 v[152:155], v[64:67], v[180:183], v[152:155]\n" "v_mfma_f32_16x16x32_bf16 v[156:159], v[68:71], v[180:183], v[156:159]\n" \
  "v_mfma_f32_16x16x32_bf16 v[144:147], v[72:75], v[184:187], v[144:147]\n" "v_mfma_f32_16x16x32_bf16 v[148:151], v[76:79], v[184:187], v[148:151]\n" \
  "v_mfma_f32_16x16x32_bf16 v[152:155], v[72:75], v[188:191], v[152:155]\n" "v_mfma_f32_16x16x32_bf16 v[156:159], v[76:79], v[188:191], v[156:159]\n" "v_add_u32 v200, 110592, v211\n" \
  "v_xor_b32 v201, 32, v200\n" "s_waitcnt vmcnt(8)\n" "s_barrier\n" "ds_read_b64_tr_b16 v[48:49], v200 offset:0\n" "ds_read_b64_tr_b16 v[50:51], v200 offset:2048\n" \
  "ds_read_b64_tr_b16 v[52:53], v201 offset:0\n" "ds_read_b64_tr_b16 v[54:55], v201 offset:2048\n" "ds_read_b64_tr_b16 v[56:57], v200 offset:16384\n" \
  "ds_read_b64_tr_b16 v[58:59], v200 offset:18432\n" "ds_read_b64_tr_b16 v[60:61], v201 offset:16384\n" "ds_read_b64_tr_b16 v[62:63], v201 offset:18432\n" \
  "ds_read_b128 v[176:179], v212 offset:12288\n" "ds_read_b128 v[180:183], v212 offset:14336\n" "ds_read_b128 v[184:187], v217 offset:12288\n" "ds_read_b128 v[188:191], v217 offset:14336\n" \
  "s_waitcnt lgkmcnt(0)\n" "s_barrier\n" "s_add_i32 m0, s52, 110592\n" "s_mov_b32 s63, 0\n" "buffer_load_dwordx4 v210, s[40:43], s63 offen " XS(SNT) " lds\n" "s_add_i32 m0, s52, 118784\n" \
  "s_mov_b32 s63, 131072\n" "buffer_load_dwordx4 v210, s[40:43], s63 offen " XS(SNT) " lds\n" "s_add_i32 m0, s52, 126976\n" "s_mov_b32 s63, 262144\n" \
  "buffer_load_dwordx4 v210, s[40:43], s63 offen " XS(SNT) " lds\n" "s_add_i32 m0, s52, 135168\n" "s_mov_b32 s63, 393216\n" "buffer_load_dwordx4 v210, s[40:43], s63 offen " XS(SNT) " lds\n" \
  "v_mfma_f32_16x16x32_bf16 v[144:147], v[48:51], v[176:179], v[144:147]\n" "v_mfma_f32_16x16x32_bf16 v[148:151], v[52:55], v[176:179], v[148:151]\n" \
  "v_mfma_f32_16x16x32_bf16 v[152:155], v[48:51], v[180:183], v[152:155]\n" "v_mfma_f32_16x16x32_bf16 v[156:159], v[52:55], v[180:183], v[156:159]\n" \
  "v_mfma_f32_16x16x32_bf16 v[144:147], v[56:59], v[184:187], v[144:147]\n" "v_mfma_f32_16x16x32_bf16 v[148:151], v[60:63], v[184:187], v[148:151]\n" \
  "v_mfma_f32_16x16x32_bf16 v[152:155], v[56:59], v[188:191], v[152:155]\n" "v_mfma_f32_16x16x32_bf16 v[156:159], v[60:63], v[188:191], v[156:159]\n" "v_add_u32 v200, 36864, v211\n" \
  "v_xor_b32 v201, 32, v200\n" "s_waitcnt vmcnt(8)\n" "s_barrier\n" "ds_read_b64_tr_b16 v[32:33], v200 offset:0\n" "ds_read_b64_tr_b16 v[34:35], v200 offset:2048\n" \
  "ds_read_b64_tr_b16 v[36:37], v201 offset:0\n" "ds_read_b64_tr_b16 v[38:39], v201 offset:2048\n" "ds_read_b64_tr_b16 v[40:41], v200 offset:16384\n" \
  "ds_read_b64_tr_b16 v[42:43], v200 offset:18432\n" "ds_read_b64_tr_b16 v[44:45], v201 offset:16384\n" "ds_read_b64_tr_b16 v[46:47], v201 offset:18432\n" \
  "ds_read_b128 v[176:179], v212 offset:8192\n" "ds_read_b128 v[180:183], v212 offset:10240\n" "ds_read_b128 v[184:187], v217 offset:8192\n" "ds_read_b128 v[188:191], v217 offset:10240\n" \
  "s_waitcnt lgkmcnt(0)\n" "s_barrier\n" "buffer_load_dwordx2 v[250:251], v213, s[48:51], 0 offen sc1\n" "s_add_i32 s61, s52, 36864\n" "s_mov_b32 s62, 36864\n" "s_add_i32 m0, s61, 0\n" \
  "s_add_i32 s63, s62, 0\n" "buffer_load_dwordx4 v208, s[44:47], s63 offen lds\n" "s_add_i32 m0, s61, 8192\n" "s_add_i32 s63, s62, 256\n" "buffer_load_dwordx4 v208, s[44:47], s63 offen lds\n" \
  "s_add_i32 m0, s61, 16384\n" "s_add_i32 s63, s62, 512\n" "buffer_load_dwordx4 v208, s[44:47], s63 offen lds\n" "s_add_i32 m0, s61, 24576\n" "s_add_i32 s63, s62, 768\n" \
  "buffer_load_dwordx4 v208, s[44:47], s63 offen lds\n" "s_add_i32 m0, s61, s54\n" "s_add_i32 s63, s62, 1024\n" "buffer_load_dwordx4 v209, s[44:47], s63 offen lds\n" \
  "v_mfma_f32_16x16x32_bf16 v[144:147], v[32:35], v[176:179], v[144:147]\n" "v_mfma_f32_16x16x32_bf16 v[148:151], v[36:39], v[176:179], v[148:151]\n" \
  "v_mfma_f32_16x16x32_bf16 v[152:155], v[32:35], v[180:183], v[152:155]\n" "v_mfma_f32_16x16x32_bf16 v[156:159], v[36:39], v[180:183], v[156:159]\n" \
  "v_mfma_f32_16x16x32_bf16 v[144:147], v[40:43], v[184:187], v[144:147]\n" "v_mfma_f32_16x16x32_bf16 v[148:151], v[44:47], v[184:187], v[148:151]\n" \
  "v_mfma_f32_16x16x32_bf16 v[152:155], v[40:43], v[188:191], v[152:155]\n" "v_mfma_f32_16x16x32_bf16 v[156:159], v[44:47], v[188:191], v[156:159]\n" "v_add_u32 v200, 73728, v211\n" \
  "v_xor_b32 v201, 32, v200\n" "s_waitcnt vmcnt(10)\n" "s_barrier\n" "ds_read_b64_tr_b16 v[16:17], v200 offset:0\n" "ds_read_b64_tr_b16 v[18:19], v200 offset:2048\n" \
  "ds_read_b64_tr_b16 v[20:21], v201 offset:0\n" "ds_read_b64_tr_b16 v[22:23], v201 offset:2048\n" "ds_read_b64_tr_b16 v[24:25], v200 offset:16384\n" \
  "ds_read_b64_tr_b16 v[26:27], v200 offset:18432\n" "ds_read_b64_tr_b16 v[28:29], v201 offset:16384\n" "ds_read_b64_tr_b16 v[30:31], v201 offset:18432\n" \
  "ds_read_b128 v[176:179], v212 offset:4096\n" "ds_read_b128 v[180:183], v212 offset:6144\n" "ds_read_b128 v[184:187], v217 offset:4096\n" "ds_read_b128 v[188:191], v217 offset:6144\n" \
  "s_waitcnt lgkmcnt(0)\n" "s_barrier\n" "s_add_i32 s61, s52, 73728\n" "s_mov_b32 s62, 73728\n" "s_add_i32 m0, s61, 0\n" "s_add_i32 s63, s62, 0\n" \
  "buffer_load_dwordx4 v208, s[44:47], s63 offen lds\n" "s_add_i32 m0, s61, 8192\n" "s_add_i32 s63, s62, 256\n" "buffer_load_dwordx4 v208, s[44:47], s63 offen lds\n" \
  "s_add_i32 m0, s61, 16384\n" "s_add_i32 s63, s62, 512\n" "buffer_load_dwordx4 v208, s[44:47], s63 offen lds\n" "s_add_i32 m0, s61, 24576\n" "s_add_i32 s63, s62, 768\n" \
  "buffer_load_dwordx4 v208, s[44:47], s63 offen lds\n" "s_add_i32 m0, s61, s54\n" "s_add_i32 s63, s62, 1024\n" "buffer_load_dwordx4 v209, s[44:47], s63 offen lds\n" \
  "v_mfma_f32_16x16x32_bf16 v[144:147], v[16:19], v[176:179], v[144:147]\n" "v_mfma_f32_16x16x32_bf16 v[148:151], v[20:23], v[176:179], v[148:151]\n" \
  "v_mfma_f32_16x16x32_bf16 v[152:155], v[16:19], v[180:183], v[152:155]\n" "v_mfma_f32_16x16x32_bf16 v[156:159], v[20:23], v[180:183], v[156:159]\n" \
  "v_mfma_f32_16x16x32_bf16 v[144:147], v[24:27], v[184:187], v[144:147]\n" "v_mfma_f32_16x16x32_bf16 v[148:151], v[28:31], v[184:187], v[148:151]\n" \
  "v_mfma_f32_16x16x32_bf16 v[152:155], v[24:27], v[188:191], v[152:155]\n" "v_mfma_f32_16x16x32_bf16 v[156:159], v[28:31], v[188:191], v[156:159]\n" "v_add_u32 v200, 110592, v211\n" \
  "v_xor_b32 v201, 32, v200\n" "s_waitcnt vmcnt(11)\n" "s_barrier\n" "ds_read_b64_tr_b16 v[0:1], v200 offset:0\n" "ds_read_b64_tr_b16 v[2:3], v200 offset:2048\n" \
  "ds_read_b64_tr_b16 v[4:5], v201 offset:0\n" "ds_read_b64_tr_b16 v[6:7], v201 offset:2048\n" "ds_read_b64_tr_b16 v[8:9], v200 offset:16384\n" \
  "ds_read_b64_tr_b16 v[10:11], v200 offset:18432\n" "ds_read_b64_tr_b16 v[12:13], v201 offset:16384\n" "ds_read_b64_tr_b16 v[14:15], v201 offset:18432\n" \
  "ds_read_b128 v[176:179], v212 offset:0\n" "ds_read_b128 v[180:183], v212 offset:2048\n" "ds_read_b128 v[184:187], v217 offset:0\n" "ds_read_b128 v[188:191], v217 offset:2048\n" \
  "s_waitcnt lgkmcnt(0)\n" "s_barrier\n" "s_add_i32 s61, s52, 110592\n" "s_mov_b32 s62, 110592\n" "s_add_i32 m0, s61, 0\n" "s_add_i32 s63, s62, 0\n" \
  "buffer_load_dwordx4 v208, s[44:47], s63 offen lds\n" "s_add_i32 m0, s61, 8192\n" "s_add_i32 s63, s62, 256\n" "buffer_load_dwordx4 v208, s[44:47], s63 offen lds\n" \
  "s_add_i32 m0, s61, 16384\n" "s_add_i32 s63, s62, 512\n" "buffer_load_dwordx4 v208, s[44:47], s63 offen lds\n" "s_add_i32 m0, s61, 24576\n" "s_add_i32 s63, s62, 768\n" \
  "buffer_load_dwordx4 v208, s[44:47], s63 offen lds\n" "s_add_i32 m0, s61, s54\n" "s_add_i32 s63, s62, 1024\n" "buffer_load_dwordx4 v209, s[44:47], s63 offen lds\n" \
  "v_mfma_f32_16x16x32_bf16 v[144:147], v[0:3], v[176:179], v[144:147]\n" "v_mfma_f32_16x16x32_bf16 v[148:151], v[4:7], v[176:179], v[148:151]\n" \
  "v_mfma_f32_16x16x32_bf16 v[152:155], v[0:3], v[180:183], v[152:155]\n" "v_mfma_f32_16x16x32_bf16 v[156:159], v[4:7], v[180:183], v[156:159]\n" \
  "v_mfma_f32_16x16x32_bf16 v[144:147], v[8:11], v[184:187], v[144:147]\n" "v_mfma_f32_16x16x32_bf16 v[148:151], v[12:15], v[184:187], v[148:151]\n" \
  "v_mfma_f32_16x16x32_bf16 v[152:155], v[8:11], v[188:191], v[152:155]\n" "v_mfma_f32_16x16x32_bf16 v[156:159], v[12:15], v[188:191], v[156:159]\n" "s_waitcnt vmcnt(15)\n" "s_nop 7\n"
#define CH_ST2 \
  "s_mov_b32 s56, 1\n" "s_mov_b32 s57, s52\n" "s_mov_b32 s58, 0\n" "s_mov_b32 s59, 0\n" "s_mov_b32 s64, 36864\n" "s_mov_b32 s65, -110592\n" "v_mov_b32 v216, 1\n" "v_xor_b32 v217, 64, v212\n" \
  "s_add_i32 s59, s59, 1\n" "s_and_b32 s59, s59, 3\n" "s_cselect_b32 s60, s64, s65\n" "v_add_u32 v212, s60, v212\n" "v_add_u32 v217, s60, v217\n" "s_waitcnt vmcnt(10)\n" "s_barrier\n" \
  "ds_read_b128 v[176:179], v212 offset:0\n" "ds_read_b128 v[180:183], v212 offset:2048\n" "ds_read_b128 v[184:187], v217 offset:0\n" "ds_read_b128 v[188:191], v217 offset:2048\n" \
  "ds_read_b128 v[192:195], v212 offset:4096\n" ".p2align 3\n" "ch_st2_loop:\n" "ds_read_b128 v[196:199], v212 offset:6144\n" "s_waitcnt lgkmcnt(5)\n" \
  "v_mfma_f32_16x16x32_bf16 v[160:163], v[0:3], v[176:179], 0\n" "v_mfma_f32_16x16x32_bf16 v[164:167], v[4:7], v[176:179], 0\n" "ds_read_b128 v[176:179], v217 offset:4096\n" \
  "s_waitcnt lgkmcnt(5)\n" "v_mfma_f32_16x16x32_bf16 v[168:171], v[0:3], v[180:183], 0\n" "v_cvt_pk_bf16_f32 v200, v144, v145\n" "v_mfma_f32_16x16x32_bf16 v[172:175], v[4:7], v[180:183], 0\n" \
  "v_cvt_pk_bf16_f32 v202, v148, v149\n" "ds_read_b128 v[180:183], v217 offset:6144\n" "s_waitcnt lgkmcnt(5)\n" "v_mfma_f32_16x16x32_bf16 v[160:163], v[8:11], v[184:187], v[160:163]\n" \
  "v_cvt_pk_bf16_f32 v201, v146, v147\n" "v_mfma_f32_16x16x32_bf16 v[164:167], v[12:15], v[184:187], v[164:167]\n" "v_cvt_pk_bf16_f32 v203, v150, v151\n" \
  "ds_read_b128 v[184:187], v212 offset:8192\n" "s_waitcnt lgkmcnt(5)\n" "v_mfma_f32_16x16x32_bf16 v[168:171], v[8:11], v[188:191], v[168:171]\n" \
  "v_mfma_f32_16x16x32_bf16 v[172:175], v[12:15], v[188:191], v[172:175]\n" "v_permlane16_swap_b32 v200, v202\n" "ds_read_b128 v[188:191], v212 offset:10240\n" "s_waitcnt lgkmcnt(5)\n" \
  "v_mfma_f32_16x16x32_bf16 v[160:163], v[16:19], v[192:195], v[160:163]\n" "v_permlane16_swap_b32 v201, v203\n" "v_mfma_f32_16x16x32_bf16 v[164:167], v[20:23], v[192:195], v[164:167]\n" \
  "buffer_store_dwordx4 v[200:203], v214, s[40:43], s58 offen offset:0 sc1\n" "ds_read_b128 v[192:195], v217 offset:8192\n" "s_waitcnt lgkmcnt(5)\n" \
  "v_mfma_f32_16x16x32_bf16 v[168:171], v[16:19], v[196:199], v[168:171]\n" "v_cvt_pk_bf16_f32 v204, v152, v153\n" "v_mfma_f32_16x16x32_bf16 v[172:175], v[20:23], v[196:199], v[172:175]\n" \
  "v_cvt_pk_bf16_f32 v206, v156, v157\n" "ds_read_b128 v[196:199], v217 offset:10240\n" "s_waitcnt lgkmcnt(5)\n" "v_mfma_f32_16x16x32_bf16 v[160:163], v[24:27], v[176:179], v[160:163]\n" \
  "v_cvt_pk_bf16_f32 v205, v154, v155\n" "v_mfma_f32_16x16x32_bf16 v[164:167], v[28:31], v[176:179], v[164:167]\n" "v_cvt_pk_bf16_f32 v207, v158, v159\n" \
  "ds_read_b128 v[176:179], v212 offset:12288\n" "s_waitcnt lgkmcnt(5)\n" "v_mfma_f32_16x16x32_bf16 v[168:171], v[24:27], v[180:183], v[168:171]\n" \
  "v_mfma_f32_16x16x32_bf16 v[172:175], v[28:31], v[180:183], v[172:175]\n" "v_permlane16_swap_b32 v204, v206\n" "ds_read_b128 v[180:183], v212 offset:14336\n" "s_waitcnt lgkmcnt(5)\n" \
  "v_mfma_f32_16x16x32_bf16 v[160:163], v[32:35], v[184:187], v[160:163]\n" "v_permlane16_swap_b32 v205, v207\n" "v_mfma_f32_16x16x32_bf16 v[164:167], v[36:39], v[184:187], v[164:167]\n" \
  "buffer_store_dwordx4 v[204:207], v214, s[40:43], s58 offen offset:256 sc1\n" "ds_read_b128 v[184:187], v217 offset:12288\n" "s_waitcnt lgkmcnt(5)\n" \
  "v_mfma_f32_16x16x32_bf16 v[168:171], v[32:35], v[188:191], v[168:171]\n" "v_mfma_f32_16x16x32_bf16 v[172:175], v[36:39], v[188:191], v[172:175]\n" \
  "ds_read_b128 v[188:191], v217 offset:14336\n" "s_waitcnt lgkmcnt(5)\n" "v_mfma_f32_16x16x32_bf16 v[160:163], v[40:43], v[192:195], v[160:163]\n" \
  "v_mfma_f32_16x16x32_bf16 v[164:167], v[44:47], v[192:195], v[164:167]\n" "ds_read_b128 v[192:195], v212 offset:16384\n" "s_waitcnt lgkmcnt(5)\n" \
  "v_mfma_f32_16x16x32_bf16 v[168:171], v[40:43], v[196:199], v[168:171]\n" "v_mfma_f32_16x16x32_bf16 v[172:175], v[44:47], v[196:199], v[172:175]\n" \
  "ds_read_b128 v[196:199], v212 offset:18432\n" "s_waitcnt lgkmcnt(5)\n" "v_mfma_f32_16x16x32_bf16 v[160:163], v[48:51], v[176:179], v[160:163]\n" "s_waitcnt vmcnt(7)\n" "s_barrier\n" \
  "s_add_i32 s61, s56, 3\n" "s_mul_i32 s62, s61, 36864\n" "v_mfma_f32_16x16x32_bf16 v[164:167], v[52:55], v[176:179], v[164:167]\n" "ds_read_b128 v[176:179], v217 offset:16384\n" \
  "s_waitcnt lgkmcnt(5)\n" "v_mfma_f32_16x16x32_bf16 v[168:171], v[48:51], v[180:183], v[168:171]\n" "s_add_i32 m0, s57, 0\n" "s_add_i32 s63, s62, 0\n" \
  "buffer_load_dwordx4 v208, s[44:47], s63 offen lds\n" "v_mfma_f32_16x16x32_bf16 v[172:175], v[52:55], v[180:183], v[172:175]\n" "ds_read_b128 v[180:183], v217 offset:18432\n" \
  "s_waitcnt lgkmcnt(5)\n" "v_mfma_f32_16x16x32_bf16 v[160:163], v[56:59], v[184:187], v[160:163]\n" "v_mfma_f32_16x16x32_bf16 v[164:167], v[60:63], v[184:187], v[164:167]\n" \
  "ds_read_b128 v[184:187], v212 offset:20480\n" "s_waitcnt lgkmcnt(5)\n" "v_mfma_f32_16x16x32_bf16 v[168:171], v[56:59], v[188:191], v[168:171]\n" "s_add_i32 m0, s57, 8192\n" \
  "s_add_i32 s63, s62, 256\n" "buffer_load_dwordx4 v208, s[44:47], s63 offen lds\n" "v_mfma_f32_16x16x32_bf16 v[172:175], v[60:63], v[188:191], v[172:175]\n" \
  "ds_read_b128 v[188:191], v212 offset:22528\n" "s_waitcnt lgkmcnt(5)\n" "v_mfma_f32_16x16x32_bf16 v[160:163], v[64:67], v[192:195], v[160:163]\n" \
  "v_mfma_f32_16x16x32_bf16 v[164:167], v[68:71], v[192:195], v[164:167]\n" "ds_read_b128 v[192:195], v217 offset:20480\n" "s_waitcnt lgkmcnt(5)\n" \
  "v_mfma_f32_16x16x32_bf16 v[168:171], v[64:67], v[196:199], v[168:171]\n" "s_add_i32 m0, s57, 16384\n" "s_add_i32 s63, s62, 512\n" "buffer_load_dwordx4 v208, s[44:47], s63 offen lds\n" \
  "v_mfma_f32_16x16x32_bf16 v[172:175], v[68:71], v[196:199], v[172:175]\n" "ds_read_b128 v[196:199], v217 offset:22528\n" "s_waitcnt lgkmcnt(5)\n" \
  "v_mfma_f32_16x16x32_bf16 v[160:163], v[72:75], v[176:179], v[160:163]\n" "v_mfma_f32_16x16x32_bf16 v[164:167], v[76:79], v[176:179], v[164:167]\n" \
  "ds_read_b128 v[176:179], v212 offset:24576\n" "s_waitcnt lgkmcnt(5)\n" "v_mfma_f32_16x16x32_bf16 v[168:171], v[72:75], v[180:183], v[168:171]\n" "s_add_i32 m0, s57, 24576\n" \
  "s_add_i32 s63, s62, 768\n" "buffer_load_dwordx4 v208, s[44:47], s63 offen lds\n" "v_mfma_f32_16x16x32_bf16 v[172:175], v[76:79], v[180:183], v[172:175]\n" \
  "ds_read_b128 v[180:183], v212 offset:26624\n" "s_waitcnt lgkmcnt(5)\n" "v_mfma_f32_16x16x32_bf16 v[160:163], v[80:83], v[184:187], v[160:163]\n" \
  "v_mfma_f32_16x16x32_bf16 v[164:167], v[84:87], v[184:187], v[164:167]\n" "ds_read_b128 v[184:187], v217 offset:24576\n" "s_waitcnt lgkmcnt(5)\n" \
  "v_mfma_f32_16x16x32_bf16 v[168:171], v[80:83], v[188:191], v[168:171]\n" "s_add_i32 m0, s57, s54\n" "s_add_i32 s63, s62, 1024\n" "buffer_load_dwordx4 v209, s[44:47], s63 offen lds\n" \
  "v_mfma_f32_16x16x32_bf16 v[172:175], v[84:87], v[188:191], v[172:175]\n" "ds_read_b128 v[188:191], v217 offset:26624\n" "s_waitcnt lgkmcnt(5)\n" \
  "v_mfma_f32_16x16x32_bf16 v[160:163], v[88:91], v[192:195], v[160:163]\n" "v_mfma_f32_16x16x32_bf16 v[164:167], v[92:95], v[192:195], v[164:167]\n" \
  "ds_read_b128 v[192:195], v212 offset:28672\n" "s_waitcnt lgkmcnt(5)\n" "v_mfma_f32_16x16x32_bf16 v[168:171], v[88:91], v[196:199], v[168:171]\n" \
  "v_mfma_f32_16x16x32_bf16 v[172:175], v[92:95], v[196:199], v[172:175]\n" "ds_read_b128 v[196:199], v212 offset:30720\n" "s_waitcnt lgkmcnt(5)\n" \
  "v_mfma_f32_16x16x32_bf16 v[160:163], v[96:99], v[176:179], v[160:163]\n" "v_mfma_f32_16x16x32_bf16 v[164:167], v[100:103], v[176:179], v[164:167]\n" \
  "ds_read_b128 v[176:179], v217 offset:28672\n" "s_waitcnt lgkmcnt(5)\n" "v_mfma_f32_16x16x32_bf16 v[168:171], v[96:99], v[180:183], v[168:171]\n" \
  "v_mfma_f32_16x16x32_bf16 v[172:175], v[100:103], v[180:183], v[172:175]\n" "ds_read_b128 v[180:183], v217 offset:30720\n" "s_waitcnt lgkmcnt(5)\n" \
  "v_mfma_f32_16x16x32_bf16 v[160:163], v[104:107], v[184:187], v[160:163]\n" "v_mfma_f32_16x16x32_bf16 v[164:167], v[108:111], v[184:187], v[164:167]\n" \
  "ds_read_b128 v[184:187], v212 offset:32768\n" "s_waitcnt lgkmcnt(5)\n" "v_mfma_f32_16x16x32_bf16 v[168:171], v[104:107], v[188:191], v[168:171]\n" "s_lshl_b32 s58, s56, 19\n" \
  "s_lshl_b32 s60, s56, 11\n" "s_and_b32 s60, s60, 0x2000\n" "v_mfma_f32_16x16x32_bf16 v[172:175], v[108:111], v[188:191], v[172:175]\n" "ds_read_b128 v[188:191], v212 offset:34816\n" \
  "s_waitcnt lgkmcnt(5)\n" "v_mfma_f32_16x16x32_bf16 v[160:163], v[112:115], v[192:195], v[160:163]\n" "s_add_i32 s58, s58, s60\n" "s_and_b32 s60, s56, 3\n" "s_cselect_b32 s60, s64, s65\n" \
  "v_mfma_f32_16x16x32_bf16 v[164:167], v[116:119], v[192:195], v[164:167]\n" "ds_read_b128 v[192:195], v217 offset:32768\n" "s_waitcnt lgkmcnt(5)\n" \
  "v_mfma_f32_16x16x32_bf16 v[168:171], v[112:115], v[196:199], v[168:171]\n" "s_add_i32 s57, s57, s60\n" "s_add_i32 s56, s56, 1\n" \
  "v_mfma_f32_16x16x32_bf16 v[172:175], v[116:119], v[196:199], v[172:175]\n" "ds_read_b128 v[196:199], v217 offset:34816\n" "s_waitcnt lgkmcnt(5)\n" \
  "v_mfma_f32_16x16x32_bf16 v[160:163], v[120:123], v[176:179], v[160:163]\n" "v_mfma_f32_16x16x32_bf16 v[164:167], v[124:127], v[176:179], v[164:167]\n" "s_add_i32 s59, s59, 1\n" \
  "s_and_b32 s59, s59, 3\n" "s_cselect_b32 s60, s64, s65\n" "v_add_u32 v212, s60, v212\n" "v_add_u32 v217, s60, v217\n" "ds_read_b128 v[176:179], v212 offset:0\n" "s_waitcnt lgkmcnt(5)\n" \
  "v_mfma_f32_16x16x32_bf16 v[168:171], v[120:123], v[180:183], v[168:171]\n" "v_mfma_f32_16x16x32_bf16 v[172:175], v[124:127], v[180:183], v[172:175]\n" \
  "ds_read_b128 v[180:183], v212 offset:2048\n" "s_waitcnt lgkmcnt(5)\n" "v_mfma_f32_16x16x32_bf16 v[160:163], v[128:131], v[184:187], v[160:163]\n" \
  "v_mfma_f32_16x16x32_bf16 v[164:167], v[132:135], v[184:187], v[164:167]\n" "ds_read_b128 v[184:187], v217 offset:0\n" "s_waitcnt lgkmcnt(5)\n" \
  "v_mfma_f32_16x16x32_bf16 v[168:171], v[128:131], v[188:191], v[168:171]\n" "v_mfma_f32_16x16x32_bf16 v[172:175], v[132:135], v[188:191], v[172:175]\n" \
  "ds_read_b128 v[188:191], v217 offset:2048\n" "s_waitcnt lgkmcnt(5)\n" "v_mfma_f32_16x16x32_bf16 v[160:163], v[136:139], v[192:195], v[160:163]\n" "s_waitcnt vmcnt(5)\n" \
  "buffer_atomic_add v216, v215, s[48:51], s53 offen\n" "s_add_i32 s53, s53, 4\n" "v_mfma_f32_16x16x32_bf16 v[164:167], v[140:143], v[192:195], v[164:167]\n" \
  "ds_read_b128 v[192:195], v212 offset:4096\n" "s_waitcnt lgkmcnt(5)\n" "v_mfma_f32_16x16x32_bf16 v[168:171], v[136:139], v[196:199], v[168:171]\n" \
  "v_mfma_f32_16x16x32_bf16 v[172:175], v[140:143], v[196:199], v[172:175]\n" "s_cmp_eq_u32 s56, 64\n" "s_cbranch_scc1 ch_st2_end\n" "ds_read_b128 v[196:199], v212 offset:6144\n" \
  "s_waitcnt lgkmcnt(5)\n" "v_mfma_f32_16x16x32_bf16 v[144:147], v[0:3], v[176:179], 0\n" "v_mfma_f32_16x16x32_bf16 v[148:151], v[4:7], v[176:179], 0\n" \
  "ds_read_b128 v[176:179], v217 offset:4096\n" "s_waitcnt lgkmcnt(5)\n" "v_mfma_f32_16x16x32_bf16 v[152:155], v[0:3], v[180:183], 0\n" "v_cvt_pk_bf16_f32 v200, v160, v161\n" \
  "v_mfma_f32_16x16x32_bf16 v[156:159], v[4:7], v[180:183], 0\n" "v_cvt_pk_bf16_f32 v202, v164, v165\n" "ds_read_b128 v[180:183], v217 offset:6144\n" "s_waitcnt lgkmcnt(5)\n" \
  "v_mfma_f32_16x16x32_bf16 v[144:147], v[8:11], v[184:187], v[144:147]\n" "v_cvt_pk_bf16_f32 v201, v162, v163\n" "v_mfma_f32_16x16x32_bf16 v[148:151], v[12:15], v[184:187], v[148:151]\n" \
  "v_cvt_pk_bf16_f32 v203, v166, v167\n" "ds_read_b128 v[184:187], v212 offset:8192\n" "s_waitcnt lgkmcnt(5)\n" "v_mfma_f32_16x16x32_bf16 v[152:155], v[8:11], v[188:191], v[152:155]\n" \
  "v_mfma_f32_16x16x32_bf16 v[156:159], v[12:15], v[188:191], v[156:159]\n" "v_permlane16_swap_b32 v200, v202\n" "ds_read_b128 v[188:191], v212 offset:10240\n" "s_waitcnt lgkmcnt(5)\n" \
  "v_mfma_f32_16x16x32_bf16 v[144:147], v[16:19], v[192:195], v[144:147]\n" "v_permlane16_swap_b32 v201, v203\n" "v_mfma_f32_16x16x32_bf16 v[148:151], v[20:23], v[192:195], v[148:151]\n" \
  "buffer_store_dwordx4 v[200:203], v214, s[40:43], s58 offen offset:0 sc1\n" "ds_read_b128 v[192:195], v217 offset:8192\n" "s_waitcnt lgkmcnt(5)\n" \
  "v_mfma_f32_16x16x32_bf16 v[152:155], v[16:19], v[196:199], v[152:155]\n" "v_cvt_pk_bf16_f32 v204, v168, v169\n" "v_mfma_f32_16x16x32_bf16 v[156:159], v[20:23], v[196:199], v[156:159]\n" \
  "v_cvt_pk_bf16_f32 v206, v172, v173\n" "ds_read_b128 v[196:199], v217 offset:10240\n" "s_waitcnt lgkmcnt(5)\n" "v_mfma_f32_16x16x32_bf16 v[144:147], v[24:27], v[176:179], v[144:147]\n" \
  "v_cvt_pk_bf16_f32 v205, v170, v171\n" "v_mfma_f32_16x16x32_bf16 v[148:151], v[28:31], v[176:179], v[148:151]\n" "v_cvt_pk_bf16_f32 v207, v174, v175\n" \
  "ds_read_b128 v[176:179], v212 offset:12288\n" "s_waitcnt lgkmcnt(5)\n" "v_mfma_f32_16x16x32_bf16 v[152:155], v[24:27], v[180:183], v[152:155]\n" \
  "v_mfma_f32_16x16x32_bf16 v[156:159], v[28:31], v[180:183], v[156:159]\n" "v_permlane16_swap_b32 v204, v206\n" "ds_read_b128 v[180:183], v212 offset:14336\n" "s_waitcnt lgkmcnt(5)\n" \
  "v_mfma_f32_16x16x32_bf16 v[144:147], v[32:35], v[184:187], v[144:147]\n" "v_permlane16_swap_b32 v205, v207\n" "v_mfma_f32_16x16x32_bf16 v[148:151], v[36:39], v[184:187], v[148:151]\n" \
  "buffer_store_dwordx4 v[204:207], v214, s[40:43], s58 offen offset:256 sc1\n" "ds_read_b128 v[184:187], v217 offset:12288\n" "s_waitcnt lgkmcnt(5)\n" \
  "v_mfma_f32_16x16x32_bf16 v[152:155], v[32:35], v[188:191], v[152:155]\n" "v_mfma_f32_16x16x32_bf16 v[156:159], v[36:39], v[188:191], v[156:159]\n" \
  "ds_read_b128 v[188:191], v217 offset:14336\n" "s_waitcnt lgkmcnt(5)\n" "v_mfma_f32_16x16x32_bf16 v[144:147], v[40:43], v[192:195], v[144:147]\n" \
  "v_mfma_f32_16x16x32_bf16 v[148:151], v[44:47], v[192:195], v[148:151]\n" "ds_read_b128 v[192:195], v212 offset:16384\n" "s_waitcnt lgkmcnt(5)\n" \
  "v_mfma_f32_16x16x32_bf16 v[152:155], v[40:43], v[196:199], v[152:155]\n" "v_mfma_f32_16x16x32_bf16 v[156:159], v[44:47], v[196:199], v[156:159]\n" \
  "ds_read_b128 v[196:199], v212 offset:18432\n" "s_waitcnt lgkmcnt(5)\n" "v_mfma_f32_16x16x32_bf16 v[144:147], v[48:51], v[176:179], v[144:147]\n" "s_waitcnt vmcnt(7)\n" "s_barrier\n" \
  "s_add_i32 s61, s56, 3\n" "s_mul_i32 s62, s61, 36864\n" "v_mfma_f32_16x16x32_bf16 v[148:151], v[52:55], v[176:179], v[148:151]\n" "ds_read_b128 v[176:179], v217 offset:16384\n" \
  "s_waitcnt lgkmcnt(5)\n" "v_mfma_f32_16x16x32_bf16 v[152:155], v[48:51], v[180:183], v[152:155]\n" "s_add_i32 m0, s57, 0\n" "s_add_i32 s63, s62, 0\n" \
  "buffer_load_dwordx4 v208, s[44:47], s63 offen lds\n" "v_mfma_f32_16x16x32_bf16 v[156:159], v[52:55], v[180:183], v[156:159]\n" "ds_read_b128 v[180:183], v217 offset:18432\n" \
  "s_waitcnt lgkmcnt(5)\n" "v_mfma_f32_16x16x32_bf16 v[144:147], v[56:59], v[184:187], v[144:147]\n" "v_mfma_f32_16x16x32_bf16 v[148:151], v[60:63], v[184:187], v[148:151]\n" \
  "ds_read_b128 v[184:187], v212 offset:20480\n" "s_waitcnt lgkmcnt(5)\n" "v_mfma_f32_16x16x32_bf16 v[152:155], v[56:59], v[188:191], v[152:155]\n" "s_add_i32 m0, s57, 8192\n" \
  "s_add_i32 s63, s62, 256\n" "buffer_load_dwordx4 v208, s[44:47], s63 offen lds\n" "v_mfma_f32_16x16x32_bf16 v[156:159], v[60:63], v[188:191], v[156:159]\n" \
  "ds_read_b128 v[188:191], v212 offset:22528\n" "s_waitcnt lgkmcnt(5)\n" "v_mfma_f32_16x16x32_bf16 v[144:147], v[64:67], v[192:195], v[144:147]\n" \
  "v_mfma_f32_16x16x32_bf16 v[148:151], v[68:71], v[192:195], v[148:151]\n" "ds_read_b128 v[192:195], v217 offset:20480\n" "s_waitcnt lgkmcnt(5)\n" \
  "v_mfma_f32_16x16x32_bf16 v[152:155], v[64:67], v[196:199], v[152:155]\n" "s_add_i32 m0, s57, 16384\n" "s_add_i32 s63, s62, 512\n" "buffer_load_dwordx4 v208, s[44:47], s63 offen lds\n" \
  "v_mfma_f32_16x16x32_bf16 v[156:159], v[68:71], v[196:199], v[156:159]\n" "ds_read_b128 v[196:199], v217 offset:22528\n" "s_waitcnt lgkmcnt(5)\n" \
  "v_mfma_f32_16x16x32_bf16 v[144:147], v[72:75], v[176:179], v[144:147]\n" "v_mfma_f32_16x16x32_bf16 v[148:151], v[76:79], v[176:179], v[148:151]\n" \
  "ds_read_b128 v[176:179], v212 offset:24576\n" "s_waitcnt lgkmcnt(5)\n" "v_mfma_f32_16x16x32_bf16 v[152:155], v[72:75], v[180:183], v[152:155]\n" "s_add_i32 m0, s57, 24576\n" \
  "s_add_i32 s63, s62, 768\n" "buffer_load_dwordx4 v208, s[44:47], s63 offen lds\n" "v_mfma_f32_16x16x32_bf16 v[156:159], v[76:79], v[180:183], v[156:159]\n" \
  "ds_read_b128 v[180:183], v212 offset:26624\n" "s_waitcnt lgkmcnt(5)\n" "v_mfma_f32_16x16x32_bf16 v[144:147], v[80:83], v[184:187], v[144:147]\n" \
  "v_mfma_f32_16x16x32_bf16 v[148:151], v[84:87], v[184:187], v[148:151]\n" "ds_read_b128 v[184:187], v217 offset:24576\n" "s_waitcnt lgkmcnt(5)\n" \
  "v_mfma_f32_16x16x32_bf16 v[152:155], v[80:83], v[188:191], v[152:155]\n" "s_add_i32 m0, s57, s54\n" "s_add_i32 s63, s62, 1024\n" "buffer_load_dwordx4 v209, s[44:47], s63 offen lds\n" \
  "v_mfma_f32_16x16x32_bf16 v[156:159], v[84:87], v[188:191], v[156:159]\n" "ds_read_b128 v[188:191], v217 offset:26624\n" "s_waitcnt lgkmcnt(5)\n" \
  "v_mfma_f32_16x16x32_bf16 v[144:147], v[88:91], v[192:195], v[144:147]\n" "v_mfma_f32_16x16x32_bf16 v[148:151], v[92:95], v[192:195], v[148:151]\n" \
  "ds_read_b128 v[192:195], v212 offset:28672\n" "s_waitcnt lgkmcnt(5)\n" "v_mfma_f32_16x16x32_bf16 v[152:155], v[88:91], v[196:199], v[152:155]\n" \
  "v_mfma_f32_16x16x32_bf16 v[156:159], v[92:95], v[196:199], v[156:159]\n" "ds_read_b128 v[196:199], v212 offset:30720\n" "s_waitcnt lgkmcnt(5)\n" \
  "v_mfma_f32_16x16x32_bf16 v[144:147], v[96:99], v[176:179], v[144:147]\n" "v_mfma_f32_16x16x32_bf16 v[148:151], v[100:103], v[176:179], v[148:151]\n" \
  "ds_read_b128 v[176:179], v217 offset:28672\n" "s_waitcnt lgkmcnt(5)\n" "v_mfma_f32_16x16x32_bf16 v[152:155], v[96:99], v[180:183], v[152:155]\n" \
  "v_mfma_f32_16x16x32_bf16 v[156:159], v[100:103], v[180:183], v[156:159]\n" "ds_read_b128 v[180:183], v217 offset:30720\n" "s_waitcnt lgkmcnt(5)\n" \
  "v_mfma_f32_16x16x32_bf16 v[144:147], v[104:107], v[184:187], v[144:147]\n" "v_mfma_f32_16x16x32_bf16 v[148:151], v[108:111], v[184:187], v[148:151]\n" \
  "ds_read_b128 v[184:187], v212 offset:32768\n" "s_waitcnt lgkmcnt(5)\n" "v_mfma_f32_16x16x32_bf16 v[152:155], v[104:107], v[188:191], v[152:155]\n" "s_lshl_b32 s58, s56, 19\n" \
  "s_lshl_b32 s60, s56, 11\n" "s_and_b32 s60, s60, 0x2000\n" "v_mfma_f32_16x16x32_bf16 v[156:159], v[108:111], v[188:191], v[156:159]\n" "ds_read_b128 v[188:191], v212 offset:34816\n" \
  "s_waitcnt lgkmcnt(5)\n" "v_mfma_f32_16x16x32_bf16 v[144:147], v[112:115], v[192:195], v[144:147]\n" "s_add_i32 s58, s58, s60\n" "s_and_b32 s60, s56, 3\n" "s_cselect_b32 s60, s64, s65\n" \
  "v_mfma_f32_16x16x32_bf16 v[148:151], v[116:119], v[192:195], v[148:151]\n" "ds_read_b128 v[192:195], v217 offset:32768\n" "s_waitcnt lgkmcnt(5)\n" \
  "v_mfma_f32_16x16x32_bf16 v[152:155], v[112:115], v[196:199], v[152:155]\n" "s_add_i32 s57, s57, s60\n" "s_add_i32 s56, s56, 1\n" \
  "v_mfma_f32_16x16x32_bf16 v[156:159], v[116:119], v[196:199], v[156:159]\n" "ds_read_b128 v[196:199], v217 offset:34816\n" "s_waitcnt lgkmcnt(5)\n" \
  "v_mfma_f32_16x16x32_bf16 v[144:147], v[120:123], v[176:179], v[144:147]\n" "v_mfma_f32_16x16x32_bf16 v[148:151], v[124:127], v[176:179], v[148:151]\n" "s_add_i32 s59, s59, 1\n" \
  "s_and_b32 s59, s59, 3\n" "s_cselect_b32 s60, s64, s65\n" "v_add_u32 v212, s60, v212\n" "v_add_u32 v217, s60, v217\n" "ds_read_b128 v[176:179], v212 offset:0\n" "s_waitcnt lgkmcnt(5)\n" \
  "v_mfma_f32_16x16x32_bf16 v[152:155], v[120:123], v[180:183], v[152:155]\n" "v_mfma_f32_16x16x32_bf16 v[156:159], v[124:127], v[180:183], v[156:159]\n" \
  "ds_read_b128 v[180:183], v212 offset:2048\n" "s_waitcnt lgkmcnt(5)\n" "v_mfma_f32_16x16x32_bf16 v[144:147], v[128:131], v[184:187], v[144:147]\n" \
  "v_mfma_f32_16x16x32_bf16 v[148:151], v[132:135], v[184:187], v[148:151]\n" "ds_read_b128 v[184:187], v217 offset:0\n" "s_waitcnt lgkmcnt(5)\n" \
  "v_mfma_f32_16x16x32_bf16 v[152:155], v[128:131], v[188:191], v[152:155]\n" "v_mfma_f32_16x16x32_bf16 v[156:159], v[132:135], v[188:191], v[156:159]\n" \
  "ds_read_b128 v[188:191], v217 offset:2048\n" "s_waitcnt lgkmcnt(5)\n" "v_mfma_f32_16x16x32_bf16 v[144:147], v[136:139], v[192:195], v[144:147]\n" "s_waitcnt vmcnt(5)\n" \
  "buffer_atomic_add v216, v215, s[48:51], s53 offen\n" "s_add_i32 s53, s53, 4\n" "v_mfma_f32_16x16x32_bf16 v[148:151], v[140:143], v[192:195], v[148:151]\n" \
  "ds_read_b128 v[192:195], v212 offset:4096\n" "s_waitcnt lgkmcnt(5)\n" "v_mfma_f32_16x16x32_bf16 v[152:155], v[136:139], v[196:199], v[152:155]\n" \
  "v_mfma_f32_16x16x32_bf16 v[156:159], v[140:143], v[196:199], v[156:159]\n" "s_branch ch_st2_loop\n" "ch_st2_end:\n" "s_nop 7\n" "v_cvt_pk_bf16_f32 v200, v160, v161\n" \
  "v_cvt_pk_bf16_f32 v202, v164, v165\n" "v_cvt_pk_bf16_f32 v201, v162, v163\n" "v_cvt_pk_bf16_f32 v203, v166, v167\n" "s_nop 1\n" "v_permlane16_swap_b32 v200, v202\n" \
  "v_permlane16_swap_b32 v201, v203\n" "buffer_store_dwordx4 v[200:203], v214, s[40:43], s58 offen offset:0 sc1\n" "v_cvt_pk_bf16_f32 v204, v168, v169\n" "v_cvt_pk_bf16_f32 v206, v172, v173\n" \
  "v_cvt_pk_bf16_f32 v205, v170, v171\n" "v_cvt_pk_bf16_f32 v207, v174, v175\n" "s_nop 1\n" "v_permlane16_swap_b32 v204, v206\n" "v_permlane16_swap_b32 v205, v207\n" \
  "buffer_store_dwordx4 v[204:207], v214, s[40:43], s58 offen offset:256 sc1\n" "s_waitcnt vmcnt(0) lgkmcnt(0)\n" "buffer_atomic_add v216, v215, s[48:51], s53 offen\n"
#define CH_HA \
  "s_setprio 2\n" "s_mov_b32 s56, 0\n" "s_mov_b32 s57, 0x3e0293ee\n" "s_mov_b32 s58, 0x40000\n" "s_mov_b32 s77, 0x84000\n" "s_mov_b32 s59, " XS(SPIN_CAP) "\n" "s_mov_b32 s68, 0x3f803f80\n" \
  "s_mov_b32 s69, s68\n" "s_mov_b32 s71, -18688\n" "s_barrier\n" "ds_read_b64 v[200:201], v189 offset:10248\n" "s_mov_b32 s60, 0\n" "s_lshl_b32 s61, s52, 11\n" "s_add_i32 s61, s61, s60\n" \
  "s_waitcnt lgkmcnt(0)\n" "v_add_u32 v200, s61, v200\n" "v_add_u32 v201, s60, v201\n" "ds_read_b128 v[240:243], v201\n" "ds_read_b128 v[244:247], v201 offset:4096\n" \
  "ds_read_b64_tr_b16 v[248:249], v200\n" "ds_read_b64_tr_b16 v[250:251], v200 offset:256\n" "ds_read_b64_tr_b16 v[252:253], v200 offset:1280\n" \
  "ds_read_b64_tr_b16 v[254:255], v200 offset:1024\n" "v_mov_b64 v[228:229], 0\n" "v_mov_b64 v[230:231], 0\n" "v_mov_b64 v[232:233], 0\n" "v_mov_b64 v[234:235], 0\n" "v_mov_b64 v[236:237], 0\n" \
  "v_mov_b64 v[238:239], 0\n" "s_waitcnt lgkmcnt(0)\n" ".p2align 6\n" "ch_ha_loop:\n" "s_nop 0\n" "s_barrier\n" ST0 "ds_read_b128 v[192:195], v185 offset:37120\n" \
  "ds_read_b128 v[196:199], v185 offset:37632\n" "ds_read_b128 v[200:203], v185 offset:38144\n" "ds_read_b128 v[204:207], v185 offset:38656\n" "ds_read_b128 v[208:211], v185 offset:39168\n" \
  "ds_read_b128 v[212:215], v185 offset:39680\n" "ds_read_b128 v[216:219], v185 offset:40192\n" "ds_read_b128 v[220:223], v185 offset:40704\n" "ds_read_b128 v[224:227], v185 offset:41216\n" \
  "s_nop 0\n" "s_waitcnt lgkmcnt(8)\n" "v_smfmac_f32_16x16x64_bf16 v[228:231], v[192:195], v[0:7], v184\n" "s_nop 0\n" "s_waitcnt lgkmcnt(7)\n" \
  "v_smfmac_f32_16x16x64_bf16 v[228:231], v[196:199], v[8:15], v184\n" "s_nop 0\n" "s_waitcnt lgkmcnt(6)\n" "v_smfmac_f32_16x16x64_bf16 v[228:231], v[200:203], v[16:23], v184\n" "s_nop 0\n" \
  "s_waitcnt lgkmcnt(5)\n" "v_smfmac_f32_16x16x64_bf16 v[228:231], v[204:207], v[24:31], v184\n" "s_nop 0\n" "s_waitcnt lgkmcnt(4)\n" \
  "v_smfmac_f32_16x16x64_bf16 v[228:231], v[208:211], v[32:39], v184\n" "s_nop 0\n" "s_waitcnt lgkmcnt(3)\n" "v_smfmac_f32_16x16x64_bf16 v[228:231], v[212:215], v[40:47], v184\n" "s_nop 0\n" \
  "s_waitcnt lgkmcnt(2)\n" "v_smfmac_f32_16x16x64_bf16 v[228:231], v[216:219], v[48:55], v184\n" "s_nop 0\n" "s_waitcnt lgkmcnt(1)\n" \
  "v_smfmac_f32_16x16x64_bf16 v[228:231], v[220:223], v[56:63], v184\n" "s_nop 0\n" "s_waitcnt lgkmcnt(0)\n" "v_smfmac_f32_16x16x64_bf16 v[228:231], v[224:227], v[64:71], v184\n" ST1 \
  "v_smfmac_f32_16x16x64_bf16 v[232:235], v[220:223], v[128:135], v184\n" ST20 "v_smfmac_f32_16x16x64_bf16 v[232:235], v[224:227], v[136:143], v184\n" ST21 \
  "v_smfmac_f32_16x16x64_bf16 v[232:235], v[212:215], v[112:119], v184\n" "v_add_f32_e64 v228, v228, v230\n" ST22 "v_smfmac_f32_16x16x64_bf16 v[232:235], v[216:219], v[120:127], v184\n" \
  "v_add_f32_e64 v229, v229, v231\n" ST23 "v_smfmac_f32_16x16x64_bf16 v[232:235], v[192:195], v[72:79], v184\n" "v_cmp_u_f32_e64 vcc, v228, v229\n" ST24 \
  "v_smfmac_f32_16x16x64_bf16 v[232:235], v[196:199], v[80:87], v184\n" "v_cvt_pk_bf16_f32 v228, v228, v229\n" ST25 "v_smfmac_f32_16x16x64_bf16 v[232:235], v[200:203], v[88:95], v184\n" \
  "ds_write_b32 v187, v228 offset:128\n" "v_mov_b32_e64 v229, s56\n" ST26 "v_smfmac_f32_16x16x64_bf16 v[232:235], v[204:207], v[96:103], v184\n" "s_cbranch_vccnz ch_ha_slow\n" "ch_ha_back:\n" \
  "v_lshrrev_b32 v231, 1, v186\n" ST27 "v_smfmac_f32_16x16x64_bf16 v[232:235], v[208:211], v[104:111], v184\n" "v_mov_b64_e64 v[220:221], s[68:69]\n" ST28 ST2 \
  "v_smfmac_f32_16x16x64_bf16 v[236:239], v[192:195], v[144:151], v184\n" "v_mov_b64_e64 v[222:223], s[68:69]\n" "v_smfmac_f32_16x16x64_bf16 v[236:239], v[196:199], v[152:159], v184\n" \
  "v_add_f32_e64 v232, v232, v234\n" "v_smfmac_f32_16x16x64_bf16 v[236:239], v[200:203], v[160:167], v184\n" "v_add_f32_e64 v233, v233, v235\n" \
  "v_smfmac_f32_16x16x64_bf16 v[236:239], v[204:207], v[168:175], v184\n" "v_cvt_pk_bf16_f32 v232, v232, v233\n" "v_smfmac_f32_16x16x64_bf16 v[236:239], v[208:211], v[176:183], v184\n" \
  "ds_write2st64_b32 v187, v232, v229 offset1:24\n" ST3 "v_mov_b32_e64 v235, 0\n" ".rept " XS(QD) "\n" "s_nop 0\n" "s_nop 0\n" ".endr\n" "ch_ha_p1:\n" "ds_read_b32 v228, v190\n" \
  "ds_read_b64_tr_b16 v[212:213], v186 offset:0\n" "ds_read_b64_tr_b16 v[214:215], v186 offset:512\n" "ds_read_b64_tr_b16 v[216:217], v186 offset:1024\n" \
  "ds_read_b64_tr_b16 v[218:219], v186 offset:1536\n" "s_nop 0\n" "s_waitcnt lgkmcnt(4)\n" "v_cmp_ne_u32 vcc, s56, v228\n" "s_waitcnt lgkmcnt(2)\n" \
  "v_mfma_f32_16x16x32_bf16 v[224:227], v[240:243], v[212:215], 0\n" "s_nop 0\n" "s_waitcnt lgkmcnt(0)\n" "v_mfma_f32_16x16x32_bf16 v[224:227], v[244:247], v[216:219], v[224:227]\n" \
  "s_cmp_lg_u32 vcc_lo, 0\n" "s_cbranch_scc1 ch_ha_p1\n" ST4 "s_setprio 0\n" "s_nop 4\n" "v_add_f32_dpp v244, v226, v224 row_ror:8 row_mask:0xf bank_mask:0x3\n" \
  "v_add_f32_dpp v244, v225, v227 row_ror:8 row_mask:0xf bank_mask:0xc\n" "ds_write2st64_b32 v188, v244, v229 offset0:16 offset1:20\n" ST5 "v_add_f32_e64 v232, v236, v238\n" \
  "v_add_f32 v233, v237, v239\n" ".rept " XS(KD) "\n" "s_nop 0\n" "s_nop 0\n" ".endr\n" "s_setprio " XS(P2P) "\n" "ch_ha_p2:\n" ST10 "ds_read_b32 v228, v190 offset:2048\n" \
  "ds_read_b64_tr_b16 v[192:193], v186 offset:2048\n" "ds_read_b64_tr_b16 v[194:195], v186 offset:2560\n" "ds_read_b64_tr_b16 v[196:197], v186 offset:3072\n" \
  "ds_read_b64_tr_b16 v[198:199], v186 offset:3584\n" "ds_read2st64_b32 v[200:201], v231 offset0:240 offset1:241\n" "ds_read2st64_b32 v[202:203], v231 offset0:242 offset1:243\n" "s_setprio 2\n" \
  "s_waitcnt lgkmcnt(6)\n" "v_cmp_ne_u32 vcc, s56, v228\n" "s_waitcnt lgkmcnt(4)\n" "v_mfma_f32_16x16x32_bf16 v[204:207], v[192:195], v[212:215], 0\n" "s_nop 0\n" "s_waitcnt lgkmcnt(2)\n" \
  "v_mfma_f32_16x16x32_bf16 v[204:207], v[196:199], v[216:219], v[204:207]\n" "s_cbranch_vccnz ch_ha_p2\n" ST6 "s_waitcnt lgkmcnt(0)\n" "v_max3_f32 v245, v200, v201, v202\n" "s_nop 0\n" \
  "s_nop 3\n" "v_add_f32_dpp v244, v206, v204 row_ror:8 row_mask:0xf bank_mask:0x3\n" "v_add_f32_dpp v244, v205, v207 row_ror:8 row_mask:0xf bank_mask:0xc\n" \
  "v_max3_f32 v245, v245, v203, v244\n" "s_nop 0\n" "s_nop 0\n" "v_max_f32_dpp v246, v245, v245 row_ror:8 row_mask:0xf bank_mask:0xf\n" \
  "v_max_f32_dpp v247, v245, v245 row_ror:8 row_mask:0xf bank_mask:0xf\n" "s_nop 1\n" "v_permlane16_swap_b32 v246, v247\n" "v_max_f32 v245, v246, v247\n" "v_max_f32 v247, v246, v247\n" \
  "s_nop 1\n" "v_permlane32_swap_b32 v245, v247\n" "v_max_f32 v245, v245, v247\n" "v_mul_f32 v245, s57, v245\n" "v_fma_f32 v244, v244, s57, -v245\n" "v_fma_f32 v200, v200, s57, -v245\n" \
  "v_exp_f32_e64 v244, v244\n" "v_fma_f32 v201, v201, s57, -v245\n" "ds_read_b128 v[208:211], v189 offset:14336\n" "v_exp_f32_e64 v200, v200\n" \
  "v_mov_b32_dpp v230, v244 row_ror:8 row_mask:0xf bank_mask:0xf\n" "v_exp_f32_e64 v201, v201\n" "v_cvt_pk_bf16_f32 v234, v244, v230\n" "v_fma_f32 v202, v202, s57, -v245\n" \
  "v_fma_f32 v203, v203, s57, -v245\n" "v_mfma_f32_16x16x16_bf16 v[240:243], v[220:221], v[234:235], 0\n" "v_exp_f32_e64 v202, v202\n" "v_exp_f32 v203, v203\n" "ch_ha_p3:\n" \
  "s_waitcnt lgkmcnt(0)\n" "v_mfma_f32_16x16x16_bf16 v[204:207], v[210:211], v[234:235], 0\n" "v_add_f32 v208, v208, v232\n" "v_add_f32 v209, v209, v233\n" \
  "v_cvt_pk_bf16_f32 v228, v208, v209\n" "v_cvt_pk_bf16_f32 v236, v200, v201\n" "v_cvt_pk_bf16_f32 v237, v202, v203\n" "v_mfma_f32_16x16x16_bf16 v[224:227], v[228:229], v[234:235], 0\n" \
  "v_mov_b32_dpp v238, v236 row_ror:8 row_mask:0xf bank_mask:0xf\n" "v_mov_b32_dpp v239, v237 row_ror:8 row_mask:0xf bank_mask:0xf\n" "v_cmp_ne_u16 vcc, s56, v211\n" "s_nop 0\n" ST7 \
  "v_mfma_f32_16x16x32_bf16 v[244:247], v[220:223], v[236:239], v[240:243]\n" "v_mfma_f32_16x16x32_bf16 v[204:207], v[252:255], v[236:239], v[204:207]\n" \
  "v_mfma_f32_16x16x32_bf16 v[224:227], v[248:251], v[236:239], v[224:227]\n" ST8 "s_cbranch_vccnz ch_ha_vlate\n" "v_rcp_f32 v230, v244\n" "s_cmp_eq_u32 s56, 63\n" "s_waitcnt vmcnt(0)\n" \
  "s_nop 0\n" "v_mul_f32 v204, v204, v230\n" "v_mul_f32_e64 v205, v205, v230\n" "v_mul_f32 v206, v206, v230\n" "v_mul_f32 v207, v207, v230\n" \
  "v_mul_f32_dpp v204, v224, v230 quad_perm:[0,1,2,3] row_mask:0xf bank_mask:0x3\n" "v_mul_f32_dpp v205, v225, v230 quad_perm:[0,1,2,3] row_mask:0xf bank_mask:0x3\n" \
  "v_mul_f32_dpp v206, v226, v230 quad_perm:[0,1,2,3] row_mask:0xf bank_mask:0x3\n" "v_mul_f32_dpp v207, v227, v230 quad_perm:[0,1,2,3] row_mask:0xf bank_mask:0x3\n" \
  "v_cvt_pk_bf16_f32 v224, v204, v205\n" "v_cvt_pk_bf16_f32 v225, v206, v207\n" "buffer_store_dwordx2 v[224:225], v191, s[40:43], 0 offen sc1\n" ST9 "s_cbranch_scc1 ch_ha_last\n" \
  "ch_ha_sent:\n" "s_memrealtime s[60:61]\n" "v_add_u32 v229, 64, v229\n" "ds_write_b64 v189, v[228:229] offset:14336\n" "ds_read_b96 v[252:254], v189 offset:10240\n" "s_setprio 0\n" \
  "s_add_i32 s70, s56, 64\n" "s_waitcnt lgkmcnt(0)\n" "s_lshl_b32 s60, s60, 6\n" "s_or_b32 s60, s60, s56\n" "v_mov_b32 v230, s60\n" "ds_write_b32 v188, v230 offset:46336\n" PTA \
  "s_lshl_b32 s64, s56, 13\n" "s_and_b32 s64, s64, 0x2000\n" "s_and_b32 s65, s52, 1\n" "v_add_u32 v252, s64, v252\n" "v_add_u32 v253, s64, v253\n" "v_xor_b32 v255, 0x80, v253\n" \
  "ds_read_b128 v[216:219], v252 offset:0\n" "ds_read_b128 v[220:223], v253 offset:0\n" "ds_read_b128 v[224:227], v253 offset:256\n" "ds_read_b128 v[228:231], v252 offset:512\n" \
  "ds_read_b128 v[232:235], v255 offset:512\n" "ds_read_b128 v[236:239], v255 offset:768\n" "ds_read_b128 v[240:243], v252 offset:4096\n" "ds_read_b128 v[244:247], v253 offset:4096\n" \
  "ds_read_b128 v[248:251], v253 offset:4352\n" "s_waitcnt lgkmcnt(6)\n" "v_mfma_f32_16x16x32_bf16 v[200:203], v[220:223], v[216:219], 0\n" \
  "v_mfma_f32_16x16x32_bf16 v[204:207], v[224:227], v[216:219], 0\n" "v_mfma_f32_16x16x32_bf16 v[208:211], v[192:195], v[216:219], 0\n" "ds_read_b128 v[216:219], v252 offset:4608\n" \
  "ds_read_b128 v[220:223], v255 offset:4608\n" "ds_read_b128 v[224:227], v255 offset:4864\n" "s_waitcnt lgkmcnt(6)\n" \
  "v_mfma_f32_16x16x32_bf16 v[200:203], v[232:235], v[228:231], v[200:203]\n" "v_mfma_f32_16x16x32_bf16 v[204:207], v[236:239], v[228:231], v[204:207]\n" \
  "v_mfma_f32_16x16x32_bf16 v[212:215], v[192:195], v[228:231], 0\n" "s_waitcnt lgkmcnt(3)\n" "v_mfma_f32_16x16x32_bf16 v[200:203], v[244:247], v[240:243], v[200:203]\n" \
  "v_mfma_f32_16x16x32_bf16 v[204:207], v[248:251], v[240:243], v[204:207]\n" "v_mfma_f32_16x16x32_bf16 v[208:211], v[196:199], v[240:243], v[208:211]\n" "s_waitcnt lgkmcnt(0)\n" \
  "v_mfma_f32_16x16x32_bf16 v[200:203], v[220:223], v[216:219], v[200:203]\n" "v_mfma_f32_16x16x32_bf16 v[204:207], v[224:227], v[216:219], v[204:207]\n" \
  "v_mfma_f32_16x16x32_bf16 v[212:215], v[196:199], v[216:219], v[212:215]\n" "s_lshr_b32 s61, s52, 1\n" "s_lshl_b32 s62, s61, 12\n" "s_add_i32 s62, s62, s64\n" "s_lshl_b32 s61, s61, 11\n" \
  "s_add_i32 s61, s61, 114688\n" "v_add_u32 v252, s62, v254\n" "v_mbcnt_lo_u32_b32 v254, -1, 0\n" "v_mbcnt_hi_u32_b32 v254, -1, v254\n" "v_lshl_add_u32 v253, v254, 4, s61\n" \
  "ds_read_b64_tr_b16 v[216:217], v252 offset:0\n" "ds_read_b64_tr_b16 v[218:219], v252 offset:256\n" "ds_read_b64_tr_b16 v[220:221], v252 offset:1024\n" \
  "ds_read_b64_tr_b16 v[222:223], v252 offset:1280\n" "ds_read_b64_tr_b16 v[224:225], v252 offset:2048\n" "ds_read_b64_tr_b16 v[226:227], v252 offset:2304\n" \
  "ds_read_b64_tr_b16 v[228:229], v252 offset:3072\n" "ds_read_b64_tr_b16 v[230:231], v252 offset:3328\n" "ch_ha_xv:\n" "ds_read_b128 v[232:235], v253 offset:14336\n" \
  "ds_read_b128 v[236:239], v253 offset:15360\n" "s_waitcnt lgkmcnt(0)\n" "v_cmp_ne_u32 vcc, s70, v233\n" "v_cmp_ne_u32 s[60:61], s70, v237\n" "s_or_b64 vcc, vcc, s[60:61]\n" \
  "s_cbranch_scc1 ch_ha_xv\n" "v_lshrrev_b32 v255, 3, v254\n" "v_and_b32 v255, 6, v255\n" "v_and_b32 v252, 15, v254\n" "v_sub_u32 v252, v252, v255\n" "v_lshl_add_u32 v252, s65, 4, v252\n" \
  "v_mov_b32 v253, 0xff800000\n" "v_add_f32 v208, v208, v214\n" "v_add_f32 v209, v209, v215\n" "v_cmp_le_i32 vcc, 0, v252\n" "v_cmp_le_i32 s[60:61], 8, v252\n" \
  "v_cmp_le_i32 s[62:63], 16, v252\n" "v_cmp_le_i32 s[66:67], 24, v252\n" "v_cndmask_b32 v200, v253, v200, vcc\n" "v_cndmask_b32 v201, v253, v201, s[60:61]\n" \
  "v_cndmask_b32 v202, v253, v202, s[62:63]\n" "v_cndmask_b32 v203, v253, v203, s[66:67]\n" "v_cmp_le_i32 vcc, 1, v252\n" "v_cmp_le_i32 s[60:61], 9, v252\n" "v_cmp_le_i32 s[62:63], 17, v252\n" \
  "v_cmp_le_i32 s[66:67], 25, v252\n" "v_cndmask_b32 v204, v253, v204, vcc\n" "v_cndmask_b32 v205, v253, v205, s[60:61]\n" "v_cndmask_b32 v206, v253, v206, s[62:63]\n" \
  "v_cndmask_b32 v207, v253, v207, s[66:67]\n" "v_max3_f32 v210, v200, v201, v202\n" "v_max3_f32 v210, v210, v203, v204\n" "v_max3_f32 v210, v210, v205, v206\n" \
  "v_max3_f32 v210, v210, v207, v208\n" "v_max_f32 v211, v210, v209\n" "v_max_f32 v210, v210, v209\n" "s_nop 1\n" "v_permlane16_swap_b32 v210, v211\n" "v_max_f32 v255, v210, v211\n" \
  "v_max_f32 v211, v210, v211\n" "s_nop 1\n" "v_permlane32_swap_b32 v255, v211\n" "v_max_f32 v210, v255, v211\n" "v_mul_f32 v210, s57, v210\n" "v_fma_f32 v200, v200, s57, -v210\n" \
  "v_fma_f32 v201, v201, s57, -v210\n" "v_fma_f32 v202, v202, s57, -v210\n" "v_fma_f32 v203, v203, s57, -v210\n" "v_fma_f32 v204, v204, s57, -v210\n" "v_fma_f32 v205, v205, s57, -v210\n" \
  "v_fma_f32 v206, v206, s57, -v210\n" "v_fma_f32 v207, v207, s57, -v210\n" "v_fma_f32 v208, v208, s57, -v210\n" "v_fma_f32 v209, v209, s57, -v210\n" "v_exp_f32 v200, v200\n" \
  "v_exp_f32 v201, v201\n" "v_exp_f32 v202, v202\n" "v_exp_f32 v203, v203\n" "v_exp_f32 v204, v204\n" "v_exp_f32 v205, v205\n" "v_exp_f32 v206, v206\n" "v_exp_f32 v207, v207\n" \
  "v_exp_f32 v208, v208\n" "v_exp_f32 v209, v209\n" "v_add_f32 v210, v200, v201\n" "v_add_f32 v210, v210, v202\n" "v_add_f32 v210, v210, v203\n" "v_add_f32 v210, v210, v204\n" \
  "v_add_f32 v210, v210, v205\n" "v_add_f32 v210, v210, v206\n" "v_add_f32 v210, v210, v207\n" "v_add_f32 v210, v210, v208\n" "v_add_f32 v211, v210, v209\n" "v_add_f32 v210, v210, v209\n" \
  "s_nop 1\n" "v_permlane16_swap_b32 v210, v211\n" "v_add_f32 v255, v210, v211\n" "v_add_f32 v211, v210, v211\n" "s_nop 1\n" "v_permlane32_swap_b32 v255, v211\n" "v_add_f32 v210, v255, v211\n" \
  "v_rcp_f32 v210, v210\n" "v_mov_b32 v241, 0\n" "v_mov_b64 v[242:243], 0\n" "v_mov_b32 v245, 0\n" "v_mov_b64 v[246:247], 0\n" "v_mul_f32 v200, v200, v210\n" "v_mul_f32 v201, v201, v210\n" \
  "v_mul_f32 v202, v202, v210\n" "v_mul_f32 v203, v203, v210\n" "v_mul_f32 v204, v204, v210\n" "v_mul_f32 v205, v205, v210\n" "v_mul_f32 v206, v206, v210\n" "v_mul_f32 v207, v207, v210\n" \
  "v_mul_f32 v208, v208, v210\n" "v_mul_f32 v209, v209, v210\n" "v_cvt_pk_bf16_f32 v248, v200, v201\n" "v_cvt_pk_bf16_f32 v249, v202, v203\n" "v_cvt_pk_bf16_f32 v250, v204, v205\n" \
  "v_cvt_pk_bf16_f32 v251, v206, v207\n" "v_cvt_pk_bf16_f32 v240, v208, v209\n" "v_cvt_pk_bf16_f32 v244, v209, v208\n" "s_waitcnt lgkmcnt(0)\n" \
  "v_mfma_f32_16x16x32_bf16 v[200:203], v[216:219], v[248:251], 0\n" "v_mfma_f32_16x16x32_bf16 v[204:207], v[220:223], v[248:251], 0\n" \
  "v_mfma_f32_16x16x32_bf16 v[208:211], v[224:227], v[248:251], 0\n" "v_mfma_f32_16x16x32_bf16 v[212:215], v[228:231], v[248:251], 0\n" \
  "v_mfma_f32_16x16x32_bf16 v[200:203], v[232:235], v[240:243], v[200:203]\n" "v_mfma_f32_16x16x32_bf16 v[204:207], v[234:237], v[244:247], v[204:207]\n" \
  "v_mfma_f32_16x16x32_bf16 v[208:211], v[236:239], v[240:243], v[208:211]\n" "v_mfma_f32_16x16x32_bf16 v[212:215], v[238:241], v[244:247], v[212:215]\n" "s_cmp_lt_u32 s56, " XS(SM0) "\n" \
  "s_cselect_b32 s60, 15, 8\n" "s_cselect_b32 s61, s53, s54\n" "s_cselect_b32 s62, 17, 10\n" "s_lshl_b32 s62, 1, s62\n" "s_lshl_b32 s63, s56, 19\n" "s_add_i32 s61, s61, s63\n" \
  "s_lshl_b32 s63, s56, 11\n" "s_andn2_b32 s63, 0x2000, s63\n" "s_add_i32 s64, s61, s63\n" "s_add_i32 s65, s64, s62\n" "v_bfe_u32 v255, v254, 4, 1\n" "v_lshrrev_b32 v252, 5, v254\n" \
  "v_lshl_add_u32 v252, v255, 1, v252\n" "v_lshlrev_b32 v252, s60, v252\n" "v_and_b32 v255, 15, v254\n" "v_lshl_add_u32 v224, v255, 4, v252\n" "v_cvt_pk_bf16_f32 v216, v200, v201\n" \
  "v_cvt_pk_bf16_f32 v217, v202, v203\n" "v_cvt_pk_bf16_f32 v218, v204, v205\n" "v_cvt_pk_bf16_f32 v219, v206, v207\n" "v_cvt_pk_bf16_f32 v220, v208, v209\n" \
  "v_cvt_pk_bf16_f32 v221, v210, v211\n" "v_cvt_pk_bf16_f32 v222, v212, v213\n" "v_cvt_pk_bf16_f32 v223, v214, v215\n" "s_nop 1\n" "v_permlane16_swap_b32 v216, v218\n" \
  "v_permlane16_swap_b32 v217, v219\n" "v_permlane16_swap_b32 v220, v222\n" "v_permlane16_swap_b32 v221, v223\n" "s_cmp_eq_u32 s56, 63\n" "s_cbranch_scc1 ch_ha_fin\n" ST14 \
  "v_xor_b32 v205, s77, v191\n" "v_mov_b64 v[206:207], -1\n" "v_xor_b32 v191, s58, v191\n" "buffer_store_dwordx2 v[206:207], v205, s[40:43], 0 offen sc1\n" "s_mov_b32 s60, s58\n" \
  "s_xor_b32 s58, s58, s77\n" "s_mov_b32 s77, s60\n" "v_mov_b64 v[228:229], 0\n" "v_mov_b64 v[230:231], 0\n" "v_mov_b64 v[232:233], 0\n" "v_mov_b64 v[234:235], 0\n" "v_mov_b64 v[236:237], 0\n" \
  "v_mov_b64 v[238:239], 0\n" "ds_read_b64 v[200:201], v189 offset:10248\n" "v_and_b32 v204, 0x300, v189\n" "v_lshlrev_b32 v204, 2, v204\n" "v_add_u32 v204, 129038, v204\n" "ch_ha_w1:\n" \
  "ds_read_u16 v202, v204\n" "s_waitcnt lgkmcnt(0)\n" "v_cmp_ne_u32 vcc, s70, v202\n" "s_cmp_lg_u32 vcc_lo, 0\n" "s_cbranch_scc1 ch_ha_w1\n" "s_lshl_b32 s60, s56, 13\n" \
  "s_andn2_b32 s60, 0x2000, s60\n" "s_lshl_b32 s61, s52, 11\n" "s_add_i32 s61, s61, s60\n" "s_waitcnt lgkmcnt(0)\n" "v_add_u32 v200, s61, v200\n" "v_add_u32 v201, s60, v201\n" \
  "ds_read_b128 v[240:243], v201\n" "ds_read_b128 v[244:247], v201 offset:4096\n" "ds_read_b64_tr_b16 v[248:249], v200\n" "ds_read_b64_tr_b16 v[250:251], v200 offset:256\n" \
  "ds_read_b64_tr_b16 v[252:253], v200 offset:1280\n" "ds_read_b64_tr_b16 v[254:255], v200 offset:1024\n" ST15 ".if " XS(XWT) "\n" "ch_ha_w2:\n" "ds_read_u16 v200, v204\n" \
  "s_waitcnt lgkmcnt(0)\n" "v_cmp_ne_u32 vcc, s70, v200\n" "s_lshr_b32 s60, vcc_hi, 16\n" "s_cbranch_scc1 ch_ha_w2\n" ".endif\n" "s_sleep " XS(XSD) "\n" \
  "buffer_store_dwordx4 v[216:219], v224, s[44:47], s64 offen sc1\n" "buffer_store_dwordx4 v[220:223], v224, s[44:47], s65 offen sc1\n" "ch_ha_next:\n" ST16 "s_add_i32 s56, s56, 1\n" \
  "s_setprio 2\n" "s_branch ch_ha_loop\n" "ch_ha_vlate:\n" "ds_read_b128 v[208:211], v189 offset:14336\n" "s_branch ch_ha_p3\n" "ch_ha_slow:\n" "s_cmp_eq_u32 s56, 0\n" \
  "s_cbranch_scc1 ch_ha_back\n" "v_max3_u32 v230, v192, v193, v194\n" "v_max3_u32 v230, v230, v195, v196\n" "v_max3_u32 v230, v230, v197, v198\n" "v_max3_u32 v230, v230, v199, v200\n" \
  "v_max3_u32 v230, v230, v201, v202\n" "v_max3_u32 v230, v230, v203, v204\n" "v_max3_u32 v230, v230, v205, v206\n" "v_max3_u32 v230, v230, v207, v208\n" "v_max3_u32 v230, v230, v209, v210\n" \
  "v_max3_u32 v230, v230, v211, v212\n" "v_max3_u32 v230, v230, v213, v214\n" "v_max3_u32 v230, v230, v215, v216\n" "v_max3_u32 v230, v230, v217, v218\n" "v_max3_u32 v230, v230, v219, v220\n" \
  "v_max3_u32 v230, v230, v221, v222\n" "v_max3_u32 v230, v230, v223, v224\n" "v_max3_u32 v230, v230, v225, v226\n" "v_max_u32 v230, v230, v227\n" "v_cmp_eq_u32 vcc, -1, v230\n" \
  "s_cbranch_vccz ch_ha_back\n" "s_sub_u32 s59, s59, 1\n" "s_cselect_b32 s59, 0, s59\n" "s_cbranch_scc1 ch_ha_back\n" "s_nop 7\n" "v_mov_b64 v[228:229], 0\n" "v_mov_b64 v[230:231], 0\n" \
  "v_mov_b64 v[232:233], 0\n" "v_mov_b64 v[234:235], 0\n" "v_mov_b64 v[236:237], 0\n" "v_mov_b64 v[238:239], 0\n" "v_add_u32 v185, s71, v185\n" "s_sub_i32 s71, 0, s71\n" "s_branch ch_ha_loop\n" \
  "ch_ha_last:\n" "v_mov_b32 v230, 0\n" "s_mov_b64 exec, 1\n" "buffer_store_dword v184, v230, s[48:51], s55 offen sc1\n" "s_mov_b64 exec, -1\n" "s_branch ch_ha_sent\n" "ch_ha_fin:\n" \
  "buffer_store_dwordx4 v[216:219], v224, s[44:47], s64 offen sc1\n" "buffer_store_dwordx4 v[220:223], v224, s[44:47], s65 offen sc1\n" "v_mov_b32 v228, 0\n" "s_mov_b64 exec, 1\n" \
  "s_waitcnt vmcnt(0)\n" "buffer_store_dword v184, v228, s[48:51], s55 offen offset:512 sc1\n" "s_mov_b64 exec, -1\n"
#define CH_HB \
  "s_setprio 1\n" "s_mov_b32 s56, 0\n" "s_mov_b32 s59, " XS(SPIN_CAP) "\n" "s_mov_b32 s68, 0\n" "s_mov_b32 s58, " XS(DUR0) "*64\n" "s_add_i32 s70, s53, 9*0x8000\n" \
  "s_add_i32 s69, s53, 6*0x8000\n" "s_mov_b32 s71, -18688\n" "s_mov_b32 s77, 151808\n" "s_lshl_b32 s60, s52, 8\n" "s_add_i32 s60, s60, 118784\n" "v_mov_b32 v254, s60\n" "s_mov_b32 s67, 0\n" \
  "s_cmp_eq_u32 s52, 7\n" "s_cselect_b32 s70, s70, 0x7f000000\n" "ds_read_b128 v[176:179], v188 offset:0\n" "ds_read_b128 v[180:183], v188 offset:1024\n" \
  "ds_read_b128 v[192:195], v188 offset:24576\n" "ds_read_b128 v[196:199], v188 offset:25600\n" "ds_read_b128 v[200:203], v188 offset:49152\n" "ds_read_b128 v[204:207], v188 offset:50176\n" \
  "v_mov_b64 v[244:245], 0\n" "v_mov_b64 v[246:247], 0\n" "v_mov_b64 v[248:249], 0\n" "v_mov_b64 v[250:251], 0\n" "s_cmp_eq_u32 s52, 7\n" "s_cbranch_scc1 ch_hb_i1\n" \
  "v_bfe_u32 v253, v187, 2, 8\n" "buffer_load_dword v190, v253, s[48:51], s55 offen sc1\n" "s_mov_b32 s62, " XS(SPIN_CAP) "\n" "s_cmp_gt_u32 s68, s56\n" "s_cbranch_scc1 ch_hb_roa\n" \
  "ch_hb_rla:\n" "s_waitcnt vmcnt(0)\n" "v_cmp_gt_u32 vcc, 8, v190\n" "s_ff1_i32_b64 s68, vcc\n" "s_cmp_gt_u32 s68, s56\n" "s_cbranch_scc1 ch_hb_roa\n" "s_sleep 1\n" \
  "buffer_load_dword v190, v253, s[48:51], s55 offen sc1\n" "s_sub_u32 s62, s62, 1\n" "s_cbranch_scc0 ch_hb_rla\n" "ch_hb_roa:\n" "s_lshl_b32 s61, s56, 19\n" "s_add_i32 s61, s61, s53\n" \
  "s_lshl_b32 s62, s56, 11\n" "s_and_b32 s62, s62, 0x2000\n" "s_add_i32 s61, s61, s62\n" "s_lshl_b32 s62, s56, 13\n" "s_and_b32 s62, s62, 0x2000\n" "s_add_i32 m0, s62, s54\n" \
  "s_add_i32 s63, s61, 0x1000\n" "buffer_load_dwordx4 v189, s[44:47], s61 offen offset:0 sc1 lds\n" "buffer_load_dwordx4 v189, s[44:47], s61 offen offset:1024 sc1 lds\n" \
  "buffer_load_dwordx4 v189, s[44:47], s61 offen offset:2048 sc1 lds\n" "buffer_load_dwordx4 v189, s[44:47], s61 offen offset:3072 sc1 lds\n" "s_add_i32 m0, m0, 0x1000\n" "s_nop 0\n" \
  "buffer_load_dwordx4 v189, s[44:47], s63 offen offset:0 sc1 lds\n" "buffer_load_dwordx4 v189, s[44:47], s63 offen offset:1024 sc1 lds\n" \
  "buffer_load_dwordx4 v189, s[44:47], s63 offen offset:2048 sc1 lds\n" "buffer_load_dwordx4 v189, s[44:47], s63 offen offset:3072 sc1 lds\n" "s_waitcnt vmcnt(0)\n" "ch_hb_i1:\n" "s_barrier\n" \
  "s_cmp_lg_u32 s52, 7\n" "s_cbranch_scc1 ch_hb_bar\n" "s_memrealtime s[60:61]\n" "s_mov_b32 s54, " XS(KE0) "*64\n" "s_waitcnt lgkmcnt(0)\n" "s_add_i32 s57, s60, " XS(GL0) "\n" \
  "s_lshl_b32 s57, s57, 6\n" "s_andn2_b32 s60, s58, 63\n" "s_add_i32 s60, s60, s57\n" "s_add_i32 s60, s60, s56\n" "v_mov_b32 v252, s60\n" \
  "buffer_store_dword v252, v191, s[40:43], 0 offen sc1\n" ".p2align 6\n" "ch_hb_bar:\n" "ch_hb_loop:\n" "v_and_b32 v253, 0x3f0, v187\n" "v_bfe_u32 v252, v253, 2, 7\n" \
  "v_lshl_add_u32 v252, v253, 3, v252\n" "v_and_b32 v253, 0x200, v253\n" "v_add3_u32 v252, s77, v252, v253\n" "v_mov_b32 v255, s56\n" "s_waitcnt vmcnt(1)\n" "s_barrier\n" ST0 \
  ".rept " XS(BD) "\n" "s_nop 3\n" ".endr\n" ".p2align 3\n" "ds_read_b32 v252, v252\n" "ds_read_b32 v253, v185 offset:41216\n" "ds_read_b128 v[232:235], v185 offset:40192\n" \
  "ds_read_b128 v[236:239], v185 offset:40704\n" "ds_read_b128 v[240:243], v185 offset:41216\n" "ds_read_b128 v[208:211], v185 offset:37120\n" "ds_read_b128 v[212:215], v185 offset:37632\n" \
  "ds_read_b128 v[216:219], v185 offset:38144\n" "ds_read_b128 v[220:223], v185 offset:38656\n" "ds_read_b128 v[224:227], v185 offset:39168\n" "ds_read_b128 v[228:231], v185 offset:39680\n" \
  "s_setprio 3\n" "s_waitcnt lgkmcnt(6)\n" "v_smfmac_f32_16x16x64_bf16 v[244:247], v[232:235], v[176:183], v184\n" "v_max_u32_e64 v252, v252, v253\n" \
  "v_smfmac_f32_16x16x64_bf16 v[244:247], v[236:239], v[192:199], v184\n" "v_cmp_eq_u32_e64 vcc, -1, v252\n" "v_smfmac_f32_16x16x64_bf16 v[244:247], v[240:243], v[200:207], v184\n" "s_nop 0\n" \
  "s_setprio 1\n" ST30 "ds_read_b128 v[176:179], v188 offset:8192\n" "ds_read_b128 v[180:183], v188 offset:9216\n" "ds_read_b128 v[192:195], v188 offset:32768\n" \
  "ds_read_b128 v[196:199], v188 offset:33792\n" "ds_read_b128 v[200:203], v188 offset:57344\n" "ds_read_b128 v[204:207], v188 offset:58368\n" "s_cbranch_vccnz ch_hb_miss\n" "ch_hb_bk2:\n" \
  "s_waitcnt lgkmcnt(6)\n" "v_smfmac_f32_16x16x64_bf16 v[244:247], v[208:211], v[0:7], v184\n" "v_smfmac_f32_16x16x64_bf16 v[244:247], v[212:215], v[8:15], v184\n" \
  "v_smfmac_f32_16x16x64_bf16 v[244:247], v[216:219], v[16:23], v184\n" "v_smfmac_f32_16x16x64_bf16 v[244:247], v[220:223], v[24:31], v184\n" \
  "v_smfmac_f32_16x16x64_bf16 v[244:247], v[224:227], v[32:39], v184\n" "s_nop 0\n" "s_waitcnt lgkmcnt(0)\n" "v_smfmac_f32_16x16x64_bf16 v[244:247], v[228:231], v[40:47], v184\n" ST1 \
  "v_smfmac_f32_16x16x64_bf16 v[248:251], v[232:235], v[176:183], v184\n" "ds_read_b128 v[176:179], v188 offset:16384\n" ST20 \
  "v_smfmac_f32_16x16x64_bf16 v[248:251], v[236:239], v[192:199], v184\n" "ds_read_b128 v[180:183], v188 offset:17408\n" ST21 \
  "v_smfmac_f32_16x16x64_bf16 v[248:251], v[240:243], v[200:207], v184\n" "v_add_f32_e64 v252, v244, v246\n" ST22 "v_smfmac_f32_16x16x64_bf16 v[248:251], v[208:211], v[48:55], v184\n" \
  "ds_read_b128 v[192:195], v188 offset:40960\n" "v_add_f32_e64 v253, v245, v247\n" ST23 "v_smfmac_f32_16x16x64_bf16 v[248:251], v[212:215], v[56:63], v184\n" \
  "ds_read_b128 v[196:199], v188 offset:41984\n" "v_cmp_u_f32_e64 vcc, v252, v253\n" ST24 "v_smfmac_f32_16x16x64_bf16 v[248:251], v[216:219], v[64:71], v184\n" \
  "ds_read_b128 v[200:203], v187 offset:23552\n" "v_cvt_pk_bf16_f32 v252, v252, v253\n" ST25 "v_smfmac_f32_16x16x64_bf16 v[248:251], v[220:223], v[72:79], v184\n" \
  "ds_write_b32 v186, v252 offset:128\n" "v_mov_b64_e64 v[244:245], 0\n" ST26 "v_smfmac_f32_16x16x64_bf16 v[248:251], v[224:227], v[80:87], v184\n" "s_cbranch_vccnz ch_hb_slow\n" \
  "ch_hb_back:\n" "v_mov_b64 v[246:247], 0\n" ST27 "v_smfmac_f32_16x16x64_bf16 v[248:251], v[228:231], v[88:95], v184\n" "ds_read_b128 v[204:207], v187 offset:27648\n" ST28 ST2 \
  "v_smfmac_f32_16x16x64_bf16 v[244:247], v[208:211], v[96:103], v184\n" "v_smfmac_f32_16x16x64_bf16 v[244:247], v[212:215], v[104:111], v184\n" "v_add_f32 v252, v248, v250\n" \
  "v_add_f32 v253, v249, v251\n" "v_cvt_pk_bf16_f32 v252, v252, v253\n" "v_smfmac_f32_16x16x64_bf16 v[244:247], v[216:219], v[112:119], v184\n" "ds_write2st64_b32 v186, v252, v255 offset1:24\n" \
  ST3 "v_smfmac_f32_16x16x64_bf16 v[244:247], v[220:223], v[120:127], v184\n" "v_mov_b64_e64 v[248:249], 0\n" "v_smfmac_f32_16x16x64_bf16 v[244:247], v[224:227], v[128:135], v184\n" \
  "v_mov_b64_e64 v[250:251], 0\n" "v_smfmac_f32_16x16x64_bf16 v[244:247], v[228:231], v[136:143], v184\n" "s_waitcnt lgkmcnt(0)\n" "v_add_u32 v255, 64, v255\n" \
  "v_smfmac_f32_16x16x64_bf16 v[248:251], v[228:231], v[144:151], v184\n" "v_smfmac_f32_16x16x64_bf16 v[244:247], v[232:235], v[176:183], v184\n" \
  "v_smfmac_f32_16x16x64_bf16 v[248:251], v[232:235], v[152:159], v184\n" "v_smfmac_f32_16x16x64_bf16 v[244:247], v[236:239], v[192:199], v184\n" \
  "v_smfmac_f32_16x16x64_bf16 v[248:251], v[236:239], v[160:167], v184\n" "v_smfmac_f32_16x16x64_bf16 v[244:247], v[240:243], v[200:207], v184\n" \
  "v_smfmac_f32_16x16x64_bf16 v[248:251], v[240:243], v[168:175], v184\n" ST5 "s_nop 4\n" "v_add_f32 v252, v244, v246\n" "v_add_f32 v253, v245, v247\n" "v_mov_b32 v247, s56\n" \
  "v_cvt_pk_bf16_f32 v246, v253, v252\n" "v_add_f32 v244, v248, v250\n" "v_add_f32 v245, v249, v251\n" "ds_write_b128 v187, v[244:247] offset:10240\n" ST6 "s_setprio 0\n" \
  "s_cmp_eq_u32 s56, 63\n" "s_cbranch_scc1 ch_hb_fin\n" "buffer_load_dword v208, v189, s[40:43], s70 offen sc1\n" "s_sleep " XS(BSL) "\n" PTA "s_cmp_eq_u32 s52, 7\n" "s_cbranch_scc1 ch_hb_w7\n" \
  "s_add_i32 s56, s56, 1\n" "s_cmp_gt_u32 s68, s56\n" "s_cbranch_scc1 ch_hb_nf\n" "v_bfe_u32 v253, v187, 2, 8\n" "buffer_load_dword v190, v253, s[48:51], s55 offen sc1\n" "ch_hb_nf:\n" \
  "s_sleep " XS(RQD) "\n" "s_cmp_lg_u32 s56, s67\n" "s_cbranch_scc1 ch_hb_nx\n" "s_sleep " XS(RQX) "\n" "ch_hb_nx:\n" "s_cmp_lg_u32 s52, 6\n" "s_cbranch_scc1 ch_hb_nq\n" "s_sleep " XS(QDL) "\n" \
  "ch_hb_nq:\n" ST14 "s_mov_b32 s62, " XS(SPIN_CAP) "\n" "s_cmp_gt_u32 s68, s56\n" "s_cbranch_scc1 ch_hb_roc\n" "ch_hb_rlc:\n" "s_waitcnt vmcnt(0)\n" "v_cmp_gt_u32 vcc, 8, v190\n" \
  "s_ff1_i32_b64 s68, vcc\n" "s_cmp_gt_u32 s68, s56\n" "s_cbranch_scc1 ch_hb_roc\n" "s_sleep 1\n" "buffer_load_dword v190, v253, s[48:51], s55 offen sc1\n" "s_sub_u32 s62, s62, 1\n" \
  "s_cbranch_scc0 ch_hb_rlc\n" "ch_hb_roc:\n" "s_lshl_b32 s61, s56, 19\n" "s_add_i32 s61, s61, s53\n" "s_lshl_b32 s62, s56, 11\n" "s_and_b32 s62, s62, 0x2000\n" "s_add_i32 s61, s61, s62\n" \
  "s_lshl_b32 s62, s56, 13\n" "s_and_b32 s62, s62, 0x2000\n" "s_add_i32 m0, s62, s54\n" "s_add_i32 s63, s61, 0x1000\n" "buffer_load_dwordx4 v189, s[44:47], s61 offen offset:0 sc1 lds\n" \
  "buffer_load_dwordx4 v189, s[44:47], s61 offen offset:1024 sc1 lds\n" "buffer_load_dwordx4 v189, s[44:47], s61 offen offset:2048 sc1 lds\n" \
  "buffer_load_dwordx4 v189, s[44:47], s61 offen offset:3072 sc1 lds\n" "s_add_i32 m0, m0, 0x1000\n" "s_nop 0\n" "buffer_load_dwordx4 v189, s[44:47], s63 offen offset:0 sc1 lds\n" \
  "buffer_load_dwordx4 v189, s[44:47], s63 offen offset:1024 sc1 lds\n" "buffer_load_dwordx4 v189, s[44:47], s63 offen offset:2048 sc1 lds\n" \
  "buffer_load_dwordx4 v189, s[44:47], s63 offen offset:3072 sc1 lds\n" ST15 "ds_read_b128 v[176:179], v188 offset:0\n" "ds_read_b128 v[180:183], v188 offset:1024\n" \
  "ds_read_b128 v[192:195], v188 offset:24576\n" "ds_read_b128 v[196:199], v188 offset:25600\n" "ds_read_b128 v[200:203], v188 offset:49152\n" "ds_read_b128 v[204:207], v188 offset:50176\n" \
  "v_mov_b64 v[244:245], 0\n" "v_mov_b64 v[246:247], 0\n" "v_mov_b64 v[248:249], 0\n" "v_mov_b64 v[250:251], 0\n" "s_waitcnt vmcnt(0)\n" ST16 "s_waitcnt vmcnt(0)\n" \
  "ds_write_b16 v187, v255 offset:10254\n" "s_setprio 1\n" "s_branch ch_hb_bar\n" "ch_hb_w7:\n" "s_setprio 1\n" "ds_read_b128 v[176:179], v188 offset:0\n" \
  "ds_read_b128 v[180:183], v188 offset:1024\n" "ds_read_b128 v[192:195], v188 offset:24576\n" "ds_read_b128 v[196:199], v188 offset:25600\n" "ds_read_b128 v[200:203], v188 offset:49152\n" \
  "ds_read_b128 v[204:207], v188 offset:50176\n" "v_mov_b64 v[244:245], 0\n" "v_mov_b64 v[246:247], 0\n" "v_mov_b64 v[248:249], 0\n" "v_mov_b64 v[250:251], 0\n" "s_mov_b32 s55, s57\n" \
  "s_add_i32 s69, s69, 3*0x8000\n" "s_sub_i32 s60, s69, s53\n" "s_cmp_eq_u32 s60, 9*0x8000\n" "s_cselect_b32 s69, s53, s69\n" "s_lshr_b32 s66, s54, 6\n" "s_sub_i32 s54, s54, " XS(EXD) "\n" \
  "s_max_i32 s54, s54, 0\n" "s_min_i32 s54, s54, " XS(EXM) "*64\n" "s_add_i32 s66, s66, " XS(K1) "+" XS(K3) "\n" PSTEP_ "s_movk_i32 s60, 0x1000\n" "s_mov_b32 s76, 0\n" "ch_hb_anc:\n" \
  "s_waitcnt vmcnt(0)\n" "v_subrev_u32 v209, s56, v208\n" "v_and_b32 v209, 63, v209\n" "v_cmp_lt_u32 vcc, 1, v209\n" "s_cmp_eq_u32 vcc_lo, 0\n" "s_cbranch_scc1 ch_hb_ano\n" "ch_hb_anr:\n" \
  "s_sub_u32 s60, s60, 1\n" "s_cbranch_scc1 ch_hb_ano\n" "s_sleep 1\n" "buffer_load_dword v208, v189, s[40:43], s70 offen sc1\n" "s_branch ch_hb_anc\n" "ch_hb_ano:\n" "s_memrealtime s[62:63]\n" \
  "v_cmp_eq_u32 vcc, 0, v209\n" "v_bfrev_b32 v210, 1\n" "s_waitcnt lgkmcnt(0)\n" "s_lshl_b32 s61, s62, 6\n" "v_subrev_u32 v209, s61, v208\n" "v_cndmask_b32 v209, v210, v209, vcc\n" \
  "s_mov_b32 exec_lo, 0\n" "v_mov_b32 v209, v208\n" "s_mov_b32 exec_lo, -1\n" "s_nop 1\n" "v_max_i32_dpp v209, v209, v209 row_ror:8 row_mask:0xf bank_mask:0xf\n" "s_nop 1\n" \
  "v_max_i32_dpp v209, v209, v209 row_ror:4 row_mask:0xf bank_mask:0xf\n" "s_nop 1\n" "v_max_i32_dpp v209, v209, v209 quad_perm:[2,3,0,1] row_mask:0xf bank_mask:0xf\n" "s_nop 1\n" \
  "v_max_i32_dpp v209, v209, v209 quad_perm:[1,0,3,2] row_mask:0xf bank_mask:0xf\n" "s_nop 1\n" "v_readlane_b32 s61, v209, 0\n" "v_readlane_b32 s63, v209, 16\n" "v_readlane_b32 s64, v209, 32\n" \
  "v_readlane_b32 s65, v209, 48\n" "s_max_i32 s61, s61, s63\n" "s_ashr_i32 s61, s61, 6\n" "s_add_i32 s61, s61, s66\n" "s_max_i32 s64, s64, s65\n" "s_ashr_i32 s64, s64, 6\n" \
  "s_max_i32 s64, s64, 40\n" "s_min_i32 s64, s64, 100\n" "s_add_i32 s61, s61, s64\n" "s_add_i32 s61, s61, -3\n" "s_add_i32 s63, s61, s62\n" "s_add_i32 s57, s63, 3\n" "s_cmp_lt_i32 s61, -3\n" \
  "s_cselect_b32 s57, s62, s57\n" "s_cmp_lg_u32 s76, 0\n" "s_cbranch_scc1 ch_hb_an3\n" "s_mov_b32 s64, s63\n" "s_min_i32 s65, s61, 400\n" "ch_hb_n1:\n" "s_cmp_le_i32 s61, " XS(LIM2) "\n" \
  "s_cbranch_scc1 ch_hb_n1e\n" "s_sleep 1\n" "s_memrealtime s[62:63]\n" "s_sub_u32 s65, s65, 1\n" "s_cbranch_scc1 ch_hb_n1e\n" "s_waitcnt lgkmcnt(0)\n" "s_sub_i32 s61, s64, s62\n" \
  "s_branch ch_hb_n1\n" "ch_hb_n1e:\n" "s_waitcnt lgkmcnt(0)\n" "s_mov_b32 s63, s64\n" "buffer_load_dword v208, v189, s[40:43], s70 offen sc1\n" "ch_hb_w0:\n" \
  "ds_read_b32 v252, v254 offset:41216\n" "s_waitcnt lgkmcnt(0)\n" "v_readfirstlane_b32 s67, v252\n" "s_xor_b32 s61, s67, s56\n" "s_and_b32 s61, s61, 63\n" "s_cbranch_scc1 ch_hb_w0\n" \
  "s_or_b32 s60, s67, 63\n" "s_sub_i32 s60, s60, s55\n" "s_min_u32 s60, s60, 300*64\n" "s_sub_i32 s60, s60, s58\n" "s_ashr_i32 s60, s60, " XS(DSH) "\n" "s_cmp_eq_u32 s56, s68\n" \
  "s_cselect_b32 s60, 0, s60\n" "s_add_i32 s58, s58, s60\n" "s_mov_b32 s60, 0\n" "s_mov_b32 s76, 1\n" "s_branch ch_hb_anc\n" "ch_hb_an3:\n" "s_mov_b32 s64, s63\n" "s_min_i32 s65, s61, 400\n" \
  "ch_hb_n2:\n" "s_cmp_le_i32 s61, " XS(NAP) "\n" "s_cbranch_scc1 ch_hb_n2e\n" "s_sleep 1\n" "s_memrealtime s[62:63]\n" "s_sub_u32 s65, s65, 1\n" "s_cbranch_scc1 ch_hb_n2e\n" \
  "s_waitcnt lgkmcnt(0)\n" "s_sub_i32 s61, s64, s62\n" "s_branch ch_hb_n2\n" "ch_hb_n2e:\n" "s_waitcnt lgkmcnt(0)\n" "s_mov_b32 s63, s64\n" "s_setprio 3\n" "s_cmp_le_i32 s61, 0\n" \
  "s_cbranch_scc1 ch_hb_ane\n" "s_movk_i32 s60, 100\n" "s_mov_b32 vcc_lo, s62\n" "ch_hb_an1:\n" ".rept 8\n" "s_memrealtime vcc\n" "s_sub_u32 s61, vcc_lo, s63\n" "s_cbranch_scc0 ch_hb_an2\n" \
  ".endr\n" "s_sub_u32 s60, s60, 1\n" "s_cbranch_scc0 ch_hb_an1\n" "ch_hb_an2:\n" "s_min_u32 s61, s61, 3\n" "s_mul_i32 s61, s61, " XS(CPT) "\n" "s_and_b32 s61, s61, -4\n" \
  "s_waitcnt lgkmcnt(0)\n" "s_getpc_b64 vcc\n" "s_add_i32 s61, s61, 16\n" "s_add_u32 vcc_lo, vcc_lo, s61\n" "s_addc_u32 vcc_hi, vcc_hi, 0\n" "s_setpc_b64 vcc\n" ".rept 3*" XS(CPT) "/4\n" \
  "s_nop 0\n" ".endr\n" "ch_hb_ane:\n" ST12 "s_add_i32 m0, s77, 4608\n" "s_add_i32 s60, s69, 0x8000\n" "buffer_load_dwordx4 v189, s[40:43], s60 offen offset:0 sc1 lds\n" \
  "buffer_load_dwordx4 v189, s[40:43], s60 offen offset:1024 sc1 lds\n" "s_mov_b32 m0, s77\n" "s_add_i32 s61, s69, 0x10000\n" \
  "buffer_load_dwordx4 v189, s[40:43], s69 offen offset:2048 sc1 lds\n" "buffer_load_dwordx4 v189, s[40:43], s69 offen offset:3072 sc1 lds\n" \
  "buffer_load_dwordx4 v189, s[40:43], s69 offen offset:0 sc1 lds\n" "buffer_load_dwordx4 v189, s[40:43], s69 offen offset:1024 sc1 lds\n" "s_add_i32 m0, s77, 4608\n" "s_nop 0\n" \
  "buffer_load_dwordx4 v189, s[40:43], s60 offen offset:2048 sc1 lds\n" "buffer_load_dwordx4 v189, s[40:43], s60 offen offset:3072 sc1 lds\n" "s_add_i32 m0, s77, 4096\n" \
  "s_mov_b32 exec_hi, 0\n" "buffer_load_dwordx4 v190, s[40:43], s61 offen sc1 lds\n" "s_add_i32 m0, s77, 8192\n" "s_not_b64 exec, exec\n" \
  "buffer_load_dwordx4 v190, s[40:43], s61 offen sc1 lds\n" "s_mov_b32 exec_lo, -1\n" ST13 "ds_write_b16 v187, v255 offset:10254\n" "s_lshl_b32 s57, s57, 6\n" "s_andn2_b32 s60, s58, 63\n" \
  "s_add_i32 s60, s60, s57\n" "s_add_i32 s60, s60, s56\n" "s_add_i32 s60, s60, 1\n" "v_mov_b32 v252, s60\n" "buffer_store_dword v252, v191, s[40:43], 0 offen sc1\n" \
  "s_xor_b32 s70, s70, 0x400\n" "s_add_i32 s56, s56, 1\n" "s_setprio 1\n" "s_branch ch_hb_loop\n" "ch_hb_slow:\n" "s_cmp_eq_u32 s56, 0\n" "s_cbranch_scc1 ch_hb_back\n" \
  "v_max3_u32 v253, v208, v209, v210\n" "v_max3_u32 v253, v253, v211, v212\n" "v_max3_u32 v253, v253, v213, v214\n" "v_max3_u32 v253, v253, v215, v216\n" "v_max3_u32 v253, v253, v217, v218\n" \
  "v_max3_u32 v253, v253, v219, v220\n" "v_max3_u32 v253, v253, v221, v222\n" "v_max3_u32 v253, v253, v223, v224\n" "v_max3_u32 v253, v253, v225, v226\n" "v_max3_u32 v253, v253, v227, v228\n" \
  "v_max3_u32 v253, v253, v229, v230\n" "v_max3_u32 v253, v253, v231, v232\n" "v_max3_u32 v253, v253, v233, v234\n" "v_max3_u32 v253, v253, v235, v236\n" "v_max3_u32 v253, v253, v237, v238\n" \
  "v_max3_u32 v253, v253, v239, v240\n" "v_max3_u32 v253, v253, v241, v242\n" "v_max_u32 v253, v253, v243\n" "v_cmp_eq_u32 vcc, -1, v253\n" "s_cbranch_vccz ch_hb_back\n" \
  "s_sub_u32 s59, s59, 1\n" "s_cselect_b32 s59, 0, s59\n" "s_cbranch_scc1 ch_hb_back\n" "s_waitcnt lgkmcnt(0)\n" "s_nop 7\n" "ch_hb_redo:\n" CNTB "v_add_u32 v185, s71, v185\n" \
  "s_add_i32 s77, s77, s71\n" "s_sub_i32 s71, 0, s71\n" "s_cmp_lg_u32 s52, 7\n" "s_cbranch_scc1 ch_hb_rd2\n" "s_memrealtime s[62:63]\n" "s_add_i32 m0, s77, 4608\n" \
  "s_add_i32 s60, s69, 0x8000\n" "buffer_load_dwordx4 v189, s[40:43], s60 offen offset:0 sc1 lds\n" "buffer_load_dwordx4 v189, s[40:43], s60 offen offset:1024 sc1 lds\n" "s_mov_b32 m0, s77\n" \
  "s_add_i32 s61, s69, 0x10000\n" "buffer_load_dwordx4 v189, s[40:43], s69 offen offset:2048 sc1 lds\n" "buffer_load_dwordx4 v189, s[40:43], s69 offen offset:3072 sc1 lds\n" \
  "buffer_load_dwordx4 v189, s[40:43], s69 offen offset:0 sc1 lds\n" "buffer_load_dwordx4 v189, s[40:43], s69 offen offset:1024 sc1 lds\n" "s_add_i32 m0, s77, 4608\n" "s_nop 0\n" \
  "buffer_load_dwordx4 v189, s[40:43], s60 offen offset:2048 sc1 lds\n" "buffer_load_dwordx4 v189, s[40:43], s60 offen offset:3072 sc1 lds\n" "s_add_i32 m0, s77, 4096\n" \
  "s_mov_b32 exec_hi, 0\n" "buffer_load_dwordx4 v190, s[40:43], s61 offen sc1 lds\n" "s_add_i32 m0, s77, 8192\n" "s_not_b64 exec, exec\n" \
  "buffer_load_dwordx4 v190, s[40:43], s61 offen sc1 lds\n" "s_mov_b32 exec_lo, -1\n" "s_waitcnt lgkmcnt(0)\n" "s_mov_b32 s68, s56\n" "s_add_i32 s57, s62, " XS(GL1) "\n" \
  "s_add_i32 s54, s54, " XS(EXU) "*64\n" "s_lshl_b32 s57, s57, 6\n" "s_andn2_b32 s60, s58, 63\n" "s_add_i32 s60, s60, s57\n" "s_add_i32 s60, s60, s56\n" "v_mov_b32 v252, s60\n" \
  "buffer_store_dword v252, v191, s[40:43], 0 offen sc1\n" "ch_hb_rd2:\n" "s_add_i32 s67, s56, 1\n" "s_waitcnt lgkmcnt(0)\n" "s_nop 7\n" "ds_read_b128 v[176:179], v188 offset:0\n" \
  "ds_read_b128 v[180:183], v188 offset:1024\n" "ds_read_b128 v[192:195], v188 offset:24576\n" "ds_read_b128 v[196:199], v188 offset:25600\n" "ds_read_b128 v[200:203], v188 offset:49152\n" \
  "ds_read_b128 v[204:207], v188 offset:50176\n" "v_mov_b64 v[244:245], 0\n" "v_mov_b64 v[246:247], 0\n" "v_mov_b64 v[248:249], 0\n" "v_mov_b64 v[250:251], 0\n" "s_branch ch_hb_loop\n" \
  "ch_hb_miss:\n" "s_cmp_eq_u32 s56, 0\n" "s_cbranch_scc1 ch_hb_bk2\n" "s_sub_u32 s59, s59, 1\n" "s_cselect_b32 s59, 0, s59\n" "s_cbranch_scc0 ch_hb_redo\n" "s_branch ch_hb_bk2\n" \
  "ch_hb_fin:\n" "s_waitcnt vmcnt(0)\n"
#define CH_O5 \
  "s_mov_b32 s56, 0\n" "s_mov_b32 s59, " XS(SPIN_CAP) "\n" "s_mov_b32 s57, 0\n" "s_mov_b32 s68, 0\n" "s_mov_b32 s58, " XS(DUO0) "*64\n" "s_mov_b32 s69, 0x40000\n" "s_mov_b32 s70, 0x84000\n" \
  "ch_o5_loop:\n" "v_mov_b64 v[176:177], 0\n" "v_mov_b64 v[178:179], 0\n" "v_mov_b64 v[180:181], 0\n" "v_mov_b64 v[182:183], 0\n" "v_mov_b64 v[184:185], 0\n" "v_mov_b64 v[186:187], 0\n" \
  "v_mov_b64 v[188:189], 0\n" "v_mov_b64 v[190:191], 0\n" "v_mov_b64 v[192:193], 0\n" "v_mov_b64 v[194:195], 0\n" "s_cmp_lg_u32 s52, 7\n" "s_cbranch_scc1 ch_o5_go\n" "s_sleep " XS(RSL) "\n" \
  "v_mov_b32 v212, s58\n" "buffer_store_dword v212, v220, s[40:43], 0 offen sc1\n" "s_sleep " XS(OSL) "\n" "v_and_b32 v215, 0x1f0, v217\n" \
  "buffer_load_dword v214, v215, s[40:43], s54 offen sc1\n" "s_cmp_lt_u32 s56, 2\n" "s_cselect_b32 s66, " XS(KE1) ", 0\n" "s_add_i32 s66, s66, " XS(K1) "\n" "s_movk_i32 s60, 0x1000\n" \
  "s_mov_b32 s76, 0\n" "ch_o5_anc:\n" "s_waitcnt vmcnt(0)\n" "v_subrev_u32 v212, s56, v214\n" "v_and_b32 v212, 63, v212\n" "v_cmp_lt_u32 vcc, 1, v212\n" "s_cmp_eq_u32 vcc_lo, 0\n" \
  "s_cbranch_scc1 ch_o5_ano\n" "ch_o5_anr:\n" "s_sub_u32 s60, s60, 1\n" "s_cbranch_scc1 ch_o5_ane\n" "s_sleep 1\n" "buffer_load_dword v214, v215, s[40:43], s54 offen sc1\n" \
  "s_branch ch_o5_anc\n" "ch_o5_ano:\n" "s_memrealtime s[62:63]\n" "v_cmp_eq_u32 vcc, 0, v212\n" "v_bfrev_b32 v213, 1\n" "s_waitcnt lgkmcnt(0)\n" "s_lshl_b32 s61, s62, 6\n" \
  "v_subrev_u32 v212, s61, v214\n" "v_cndmask_b32 v212, v213, v212, vcc\n" "s_mov_b32 exec_lo, 0\n" "v_mov_b32 v212, v214\n" "s_mov_b32 exec_lo, -1\n" "s_nop 1\n" \
  "v_max_i32_dpp v212, v212, v212 row_ror:8 row_mask:0xf bank_mask:0xf\n" "s_nop 1\n" "v_max_i32_dpp v212, v212, v212 row_ror:4 row_mask:0xf bank_mask:0xf\n" "s_nop 1\n" \
  "v_max_i32_dpp v212, v212, v212 quad_perm:[2,3,0,1] row_mask:0xf bank_mask:0xf\n" "s_nop 1\n" "v_max_i32_dpp v212, v212, v212 quad_perm:[1,0,3,2] row_mask:0xf bank_mask:0xf\n" "s_nop 1\n" \
  "v_readlane_b32 s61, v212, 0\n" "v_readlane_b32 s63, v212, 16\n" "s_max_i32 s61, s61, s63\n" "s_ashr_i32 s61, s61, 6\n" "s_add_i32 s61, s61, s66\n" "s_add_i32 s61, s61, -3\n" \
  "s_add_i32 s63, s61, s62\n" "s_add_i32 s68, s63, 3\n" "s_cmp_lt_i32 s61, -3\n" "s_cselect_b32 s68, s62, s68\n" "s_cmp_gt_i32 s61, " XS(LIM1) "\n" "s_cbranch_scc1 ch_o5_anr\n" \
  "s_cmp_le_i32 s61, 0\n" "s_cbranch_scc1 ch_o5_ane\n" "s_movk_i32 s60, 100\n" "s_mov_b32 vcc_lo, s62\n" "ch_o5_an1:\n" ".rept 8\n" "s_memrealtime vcc\n" "s_sub_u32 s61, vcc_lo, s63\n" \
  "s_cbranch_scc0 ch_o5_an2\n" ".endr\n" "s_sub_u32 s60, s60, 1\n" "s_cbranch_scc0 ch_o5_an1\n" "ch_o5_an2:\n" "s_min_u32 s61, s61, 3\n" "s_mul_i32 s61, s61, " XS(CPT) "\n" \
  "s_and_b32 s61, s61, -4\n" "s_waitcnt lgkmcnt(0)\n" "s_getpc_b64 vcc\n" "s_add_i32 s61, s61, 16\n" "s_add_u32 vcc_lo, vcc_lo, s61\n" "s_addc_u32 vcc_hi, vcc_hi, 0\n" "s_setpc_b64 vcc\n" \
  ".rept 3*" XS(CPT) "/4\n" "s_nop 0\n" ".endr\n" "ch_o5_ane:\n" "s_xor_b32 s54, s54, 0x400\n" ".p2align 3\n" "ch_o5_go:\n" "s_nop 0\n" "s_cmp_lt_u32 s52, 4\n" "s_barrier\n" ST0 \
  "s_cbranch_scc0 ch_o5_l2\n" "ch_o5_l1:\n" "buffer_load_dwordx4 v[160:163], v217, s[40:43], s53 offen offset:0 sc1\n" "s_nop " XS(OG) "\n" "s_nop 0\n" \
  "buffer_load_dwordx4 v[164:167], v217, s[40:43], s53 offen offset:1024 sc1\n" "s_nop " XS(OG) "\n" "s_nop 0\n" "buffer_load_dwordx4 v[168:171], v217, s[40:43], s53 offen offset:2048 sc1\n" \
  "s_nop " XS(OG) "\n" "s_nop 0\n" "buffer_load_dwordx4 v[172:175], v217, s[40:43], s53 offen offset:3072 sc1\n" "ch_o5_mul:\n" "s_nop 0\n" "s_waitcnt vmcnt(3)\n" ST1 \
  "v_smfmac_f32_16x16x64_bf16 v[176:179], v[160:163], v[0:7], v216\n" "v_cmp_eq_u32_e64 vcc, -1, v160\n" "v_smfmac_f32_16x16x64_bf16 v[180:183], v[160:163], v[8:15], v216\n" \
  "v_smfmac_f32_16x16x64_bf16 v[184:187], v[160:163], v[16:23], v216\n" "s_nop 0\n" "s_cbranch_vccnz ch_o5_slow2\n" "v_smfmac_f32_16x16x64_bf16 v[188:191], v[160:163], v[24:31], v216\n" \
  "v_smfmac_f32_16x16x64_bf16 v[192:195], v[160:163], v[32:39], v216\n" "s_nop 0\n" "s_waitcnt vmcnt(2)\n" "v_smfmac_f32_16x16x64_bf16 v[176:179], v[164:167], v[40:47], v216\n" \
  "v_cmp_eq_u32_e64 vcc, -1, v164\n" "v_smfmac_f32_16x16x64_bf16 v[180:183], v[164:167], v[48:55], v216\n" "v_smfmac_f32_16x16x64_bf16 v[184:187], v[164:167], v[56:63], v216\n" "s_nop 0\n" \
  "s_cbranch_vccnz ch_o5_slow2\n" "v_smfmac_f32_16x16x64_bf16 v[188:191], v[164:167], v[64:71], v216\n" "v_smfmac_f32_16x16x64_bf16 v[192:195], v[164:167], v[72:79], v216\n" "s_nop 0\n" \
  "s_waitcnt vmcnt(1)\n" "v_smfmac_f32_16x16x64_bf16 v[176:179], v[168:171], v[80:87], v216\n" "v_smfmac_f32_16x16x64_bf16 v[180:183], v[168:171], v[88:95], v216\n" \
  "v_smfmac_f32_16x16x64_bf16 v[184:187], v[168:171], v[96:103], v216\n" "v_smfmac_f32_16x16x64_bf16 v[188:191], v[168:171], v[104:111], v216\n" \
  "v_smfmac_f32_16x16x64_bf16 v[192:195], v[168:171], v[112:119], v216\n" "s_nop 0\n" "s_waitcnt vmcnt(0)\n" ST2 "v_smfmac_f32_16x16x64_bf16 v[176:179], v[172:175], v[120:127], v216\n" \
  "v_smfmac_f32_16x16x64_bf16 v[180:183], v[172:175], v[128:135], v216\n" "v_smfmac_f32_16x16x64_bf16 v[184:187], v[172:175], v[136:143], v216\n" \
  "v_smfmac_f32_16x16x64_bf16 v[188:191], v[172:175], v[144:151], v216\n" "v_smfmac_f32_16x16x64_bf16 v[192:195], v[172:175], v[152:159], v216\n" ST3 "v_add_f32 v196, v176, v177\n" \
  "v_add_f32 v200, v178, v179\n" "v_add_f32_dpp v200, v180, v181 quad_perm:[0,1,2,3] row_mask:0xf bank_mask:0x3\n" \
  "v_add_f32_dpp v196, v182, v183 quad_perm:[0,1,2,3] row_mask:0xf bank_mask:0xc\n" "v_add_f32 v197, v184, v185\n" "v_add_f32 v201, v186, v187\n" \
  "v_add_f32_dpp v201, v188, v189 quad_perm:[0,1,2,3] row_mask:0xf bank_mask:0x3\n" "v_add_f32_dpp v197, v190, v191 quad_perm:[0,1,2,3] row_mask:0xf bank_mask:0xc\n" \
  "v_add_f32 v202, v194, v195\n" "v_add_f32 v198, v192, v193\n" "v_add_f32_dpp v196, v200, v196 row_ror:8 row_mask:0xf bank_mask:0xf\n" \
  "v_add_f32_dpp v197, v201, v197 row_ror:8 row_mask:0xf bank_mask:0xf\n" "v_add_f32_dpp v198, v202, v198 row_ror:8 row_mask:0xf bank_mask:0xf\n" \
  "ds_write2st64_b32 v218, v196, v197 offset1:8\n" "ds_write_b32 v218, v198 offset:4096\n" "v_cmp_u_f32 vcc, v196, v196\n" "s_cbranch_vccnz ch_o5_slow\n" "ch_o5_back:\n" "s_cmp_gt_u32 s52, 2\n" \
  "s_waitcnt lgkmcnt(0)\n" "s_barrier\n" "s_cbranch_scc1 ch_o5_rest\n" ST4 "ds_read_b128 v[204:207], v219\n" "ds_read_b128 v[208:211], v219 offset:1024\n" "s_add_i32 s60, s57, 6*0x8000\n" \
  "s_cmp_ge_u32 s60, 9*0x8000\n" "s_cselect_b32 s61, 9*0x8000, 0\n" "s_sub_i32 s60, s60, s61\n" "v_mov_b32 v213, -1\n" ST7 "s_waitcnt lgkmcnt(1)\n" "v_add_f32 v204, v204, v205\n" \
  "v_add_f32 v206, v206, v207\n" "s_waitcnt lgkmcnt(0)\n" "v_add_f32 v208, v208, v209\n" "v_add_f32 v210, v210, v211\n" "v_add_f32 v208, v208, v204\n" "v_add_f32 v210, v210, v206\n" \
  "v_add_f32 v204, v208, v210\n" "v_add_f32 v205, v208, v210\n" "s_nop 1\n" "v_permlane32_swap_b32 v204, v205\n" "v_cvt_pk_bf16_f32 v204, v204, v205\n" ST6 \
  "buffer_store_dword v204, v220, s[40:43], s57 offen sc1\n" ST5 "s_sleep " XS(RSL) "\n" "buffer_store_dword v213, v220, s[40:43], s60 offen sc1\n" "ch_o5_nxt:\n" PTS PTA \
  "s_add_i32 s57, s57, 3*0x8000\n" "s_cmp_eq_u32 s57, 9*0x8000\n" "s_cselect_b32 s57, 0, s57\n" "s_xor_b32 s53, s53, s69\n" "s_mov_b32 s60, s69\n" "s_xor_b32 s69, s69, s70\n" \
  "s_mov_b32 s70, s60\n" "s_add_i32 s56, s56, 1\n" "s_cmp_lt_u32 s56, 63\n" "s_cbranch_scc1 ch_o5_loop\n" "s_branch ch_o5_fin\n" "ch_o5_tim:\n" "s_memrealtime s[60:61]\n" \
  "s_waitcnt lgkmcnt(0)\n" "s_sub_i32 s60, s60, s68\n" "s_lshl_b32 s60, s60, 6\n" "s_sub_i32 s60, s60, s58\n" "s_ashr_i32 s60, s60, " XS(DSO) "\n" "s_min_i32 s60, s60, " XS(DUP) "\n" \
  "s_add_i32 s58, s58, s60\n" "s_branch ch_o5_nxt\n" "ch_o5_tch:\n" "s_add_i32 s60, s56, 2\n" "s_cmp_eq_u32 s60, 64\n" "s_cbranch_scc1 ch_o5_nxt\n" "s_lshl_b32 s61, s60, 19\n" \
  "s_lshl_b32 s62, s60, 11\n" "s_and_b32 s62, s62, 0x2000\n" "s_add_i32 s61, s61, s62\n" "s_sleep " XS(TSL) "\n" "s_cmp_gt_u32 s68, s60\n" "s_cbranch_scc1 ch_o5_tk\n" \
  "buffer_load_dword v214, v219, s[48:51], 0 offen sc1\n" "s_waitcnt vmcnt(0)\n" "v_cmp_gt_u32 vcc, 8, v214\n" "s_ff1_i32_b64 s68, vcc\n" "s_cmp_gt_u32 s68, s60\n" "s_cbranch_scc0 ch_o5_nxt\n" \
  "ch_o5_tk:\n" "buffer_load_dword v214, v220, s[44:47], s61 offen sc1\n" "s_branch ch_o5_nxt\n" "ch_o5_rest:\n" "s_cmp_eq_u32 s52, 7\n" "s_cbranch_scc1 ch_o5_tim\n" "s_cmp_lg_u32 s52, 6\n" \
  "s_cbranch_scc1 ch_o5_tch\n" "s_and_b32 s60, s56, 31\n" "s_cmp_lg_u32 s60, s55\n" "s_cbranch_scc1 ch_o5_nxt\n" "s_sub_u32 s60, s56, 1\n" "s_cbranch_scc1 ch_o5_nxt\n" \
  "s_lshl_b32 s61, s60, 7\n" "s_add_i32 s62, s61, " XS(SY_CS0) "*4\n" "s_lshl_b32 s61, s60, 10\n" "s_add_i32 s63, s61, " XS(SY_CS0) "*4-48*7*128\n" "s_cmp_lt_u32 s60, 48\n" \
  "s_cselect_b32 s62, s62, s63\n" "s_cselect_b32 exec_lo, 1, 255\n" "s_mov_b32 exec_hi, 0\n" "s_sleep " XS(RSL) "\n" "v_lshlrev_b32 v212, 3, v217\n" \
  "buffer_store_dword v216, v212, s[48:51], s62 offen sc1\n" "s_mov_b64 exec, -1\n" "s_branch ch_o5_nxt\n" "ch_o5_l2:\n" ".rept " XS(OD) "\n" "s_nop 3\n" ".endr\n" "s_branch ch_o5_l1\n" \
  "ch_o5_slow:\n" "v_max3_u32 v212, v160, v161, v162\n" "v_max3_u32 v212, v212, v163, v164\n" "v_max3_u32 v212, v212, v165, v166\n" "v_max3_u32 v212, v212, v167, v168\n" \
  "v_max3_u32 v212, v212, v169, v170\n" "v_max3_u32 v212, v212, v171, v172\n" "v_max3_u32 v212, v212, v173, v174\n" "v_max_u32 v212, v212, v175\n" "v_cmp_eq_u32 vcc, -1, v212\n" \
  "s_cbranch_vccz ch_o5_back\n" "s_sub_u32 s59, s59, 1\n" "s_cselect_b32 s59, 0, s59\n" "s_cbranch_scc1 ch_o5_back\n" "ch_o5_slow2:\n" CNTO "s_waitcnt vmcnt(0)\n" "ch_o5_wait:\n" \
  "v_max3_u32 v212, v160, v161, v162\n" "v_max_u32 v212, v212, v163\n" "v_cmp_eq_u32 vcc, -1, v212\n" "s_cbranch_vccz ch_o5_h0\n" \
  "buffer_load_dwordx4 v[160:163], v217, s[40:43], s53 offen offset:0 sc1\n" "ch_o5_h0:\n" "v_max3_u32 v212, v164, v165, v166\n" "v_max_u32 v212, v212, v167\n" "v_cmp_eq_u32 vcc, -1, v212\n" \
  "s_cbranch_vccz ch_o5_h1\n" "buffer_load_dwordx4 v[164:167], v217, s[40:43], s53 offen offset:1024 sc1\n" "ch_o5_h1:\n" "v_max3_u32 v212, v168, v169, v170\n" "v_max_u32 v212, v212, v171\n" \
  "v_cmp_eq_u32 vcc, -1, v212\n" "s_cbranch_vccz ch_o5_h2\n" "buffer_load_dwordx4 v[168:171], v217, s[40:43], s53 offen offset:2048 sc1\n" "ch_o5_h2:\n" "v_max3_u32 v212, v172, v173, v174\n" \
  "v_max_u32 v212, v212, v175\n" "v_cmp_eq_u32 vcc, -1, v212\n" "s_cbranch_vccz ch_o5_h3\n" "buffer_load_dwordx4 v[172:175], v217, s[40:43], s53 offen offset:3072 sc1\n" "ch_o5_h3:\n" \
  "v_mov_b64 v[176:177], 0\n" "v_mov_b64 v[178:179], 0\n" "v_mov_b64 v[180:181], 0\n" "v_mov_b64 v[182:183], 0\n" "v_mov_b64 v[184:185], 0\n" "v_mov_b64 v[186:187], 0\n" \
  "v_mov_b64 v[188:189], 0\n" "v_mov_b64 v[190:191], 0\n" "v_mov_b64 v[192:193], 0\n" "v_mov_b64 v[194:195], 0\n" "s_waitcnt vmcnt(0)\n" "v_max3_u32 v212, v160, v161, v162\n" \
  "v_max3_u32 v212, v212, v163, v164\n" "v_max3_u32 v212, v212, v165, v166\n" "v_max3_u32 v212, v212, v167, v168\n" "v_max3_u32 v212, v212, v169, v170\n" "v_max3_u32 v212, v212, v171, v172\n" \
  "v_max3_u32 v212, v212, v173, v174\n" "v_max_u32 v212, v212, v175\n" "v_cmp_eq_u32 vcc, -1, v212\n" "s_cbranch_vccz ch_o5_mul\n" "s_sub_u32 s59, s59, 1\n" "s_cselect_b32 s59, 0, s59\n" \
  "s_cbranch_scc0 ch_o5_wait\n" "v_mov_b32 v160, 0\n" "v_mov_b32 v164, 0\n" "v_mov_b32 v168, 0\n" "v_mov_b32 v172, 0\n" "s_mov_b32 s59, 0\n" "s_nop 0\n" "s_branch ch_o5_mul\n" "ch_o5_fin:\n" \
  "s_waitcnt vmcnt(0)\n"
#define CH_O4 \
  "s_mov_b32 s56, 0\n" "s_mov_b32 s59, " XS(SPIN_CAP) "\n" "s_mov_b32 s57, 0\n" "s_mov_b32 s68, 0\n" "s_mov_b32 s58, " XS(DUO0) "*64\n" "s_mov_b32 s69, 0x40000\n" "s_mov_b32 s70, 0x84000\n" \
  "ch_o4_loop:\n" "v_mov_b64 v[176:177], 0\n" "v_mov_b64 v[178:179], 0\n" "v_mov_b64 v[180:181], 0\n" "v_mov_b64 v[182:183], 0\n" "v_mov_b64 v[184:185], 0\n" "v_mov_b64 v[186:187], 0\n" \
  "v_mov_b64 v[188:189], 0\n" "v_mov_b64 v[190:191], 0\n" "s_cmp_lg_u32 s52, 7\n" "s_cbranch_scc1 ch_o4_go\n" "s_sleep " XS(RSL) "\n" "v_mov_b32 v212, s58\n" \
  "buffer_store_dword v212, v220, s[40:43], 0 offen sc1\n" "s_sleep " XS(OSL) "\n" "v_and_b32 v215, 0x1f0, v217\n" "buffer_load_dword v214, v215, s[40:43], s54 offen sc1\n" \
  "s_cmp_lt_u32 s56, 2\n" "s_cselect_b32 s66, " XS(KE1) ", 0\n" "s_add_i32 s66, s66, " XS(K1) "\n" "s_movk_i32 s60, 0x1000\n" "s_mov_b32 s76, 0\n" "ch_o4_anc:\n" "s_waitcnt vmcnt(0)\n" \
  "v_subrev_u32 v212, s56, v214\n" "v_and_b32 v212, 63, v212\n" "v_cmp_lt_u32 vcc, 1, v212\n" "s_cmp_eq_u32 vcc_lo, 0\n" "s_cbranch_scc1 ch_o4_ano\n" "ch_o4_anr:\n" "s_sub_u32 s60, s60, 1\n" \
  "s_cbranch_scc1 ch_o4_ane\n" "s_sleep 1\n" "buffer_load_dword v214, v215, s[40:43], s54 offen sc1\n" "s_branch ch_o4_anc\n" "ch_o4_ano:\n" "s_memrealtime s[62:63]\n" \
  "v_cmp_eq_u32 vcc, 0, v212\n" "v_bfrev_b32 v213, 1\n" "s_waitcnt lgkmcnt(0)\n" "s_lshl_b32 s61, s62, 6\n" "v_subrev_u32 v212, s61, v214\n" "v_cndmask_b32 v212, v213, v212, vcc\n" \
  "s_mov_b32 exec_lo, 0\n" "v_mov_b32 v212, v214\n" "s_mov_b32 exec_lo, -1\n" "s_nop 1\n" "v_max_i32_dpp v212, v212, v212 row_ror:8 row_mask:0xf bank_mask:0xf\n" "s_nop 1\n" \
  "v_max_i32_dpp v212, v212, v212 row_ror:4 row_mask:0xf bank_mask:0xf\n" "s_nop 1\n" "v_max_i32_dpp v212, v212, v212 quad_perm:[2,3,0,1] row_mask:0xf bank_mask:0xf\n" "s_nop 1\n" \
  "v_max_i32_dpp v212, v212, v212 quad_perm:[1,0,3,2] row_mask:0xf bank_mask:0xf\n" "s_nop 1\n" "v_readlane_b32 s61, v212, 0\n" "v_readlane_b32 s63, v212, 16\n" "s_max_i32 s61, s61, s63\n" \
  "s_ashr_i32 s61, s61, 6\n" "s_add_i32 s61, s61, s66\n" "s_add_i32 s61, s61, -3\n" "s_add_i32 s63, s61, s62\n" "s_add_i32 s68, s63, 3\n" "s_cmp_lt_i32 s61, -3\n" \
  "s_cselect_b32 s68, s62, s68\n" "s_cmp_gt_i32 s61, " XS(LIM1) "\n" "s_cbranch_scc1 ch_o4_anr\n" "s_cmp_le_i32 s61, 0\n" "s_cbranch_scc1 ch_o4_ane\n" "s_movk_i32 s60, 100\n" \
  "s_mov_b32 vcc_lo, s62\n" "ch_o4_an1:\n" ".rept 8\n" "s_memrealtime vcc\n" "s_sub_u32 s61, vcc_lo, s63\n" "s_cbranch_scc0 ch_o4_an2\n" ".endr\n" "s_sub_u32 s60, s60, 1\n" \
  "s_cbranch_scc0 ch_o4_an1\n" "ch_o4_an2:\n" "s_min_u32 s61, s61, 3\n" "s_mul_i32 s61, s61, " XS(CPT) "\n" "s_and_b32 s61, s61, -4\n" "s_waitcnt lgkmcnt(0)\n" "s_getpc_b64 vcc\n" \
  "s_add_i32 s61, s61, 16\n" "s_add_u32 vcc_lo, vcc_lo, s61\n" "s_addc_u32 vcc_hi, vcc_hi, 0\n" "s_setpc_b64 vcc\n" ".rept 3*" XS(CPT) "/4\n" "s_nop 0\n" ".endr\n" "ch_o4_ane:\n" \
  "s_xor_b32 s54, s54, 0x400\n" ".p2align 3\n" "ch_o4_go:\n" "s_nop 0\n" "s_cmp_lt_u32 s52, 4\n" "s_barrier\n" ST0 "s_cbranch_scc0 ch_o4_l2\n" "ch_o4_l1:\n" \
  "buffer_load_dwordx4 v[160:163], v217, s[40:43], s53 offen offset:0 sc1\n" "s_nop " XS(OG) "\n" "s_nop 0\n" "buffer_load_dwordx4 v[164:167], v217, s[40:43], s53 offen offset:1024 sc1\n" \
  "s_nop " XS(OG) "\n" "s_nop 0\n" "buffer_load_dwordx4 v[168:171], v217, s[40:43], s53 offen offset:2048 sc1\n" "s_nop " XS(OG) "\n" "s_nop 0\n" \
  "buffer_load_dwordx4 v[172:175], v217, s[40:43], s53 offen offset:3072 sc1\n" "ch_o4_mul:\n" "s_nop 0\n" "s_waitcnt vmcnt(3)\n" ST1 \
  "v_smfmac_f32_16x16x64_bf16 v[176:179], v[160:163], v[0:7], v216\n" "v_cmp_eq_u32_e64 vcc, -1, v160\n" "v_smfmac_f32_16x16x64_bf16 v[180:183], v[160:163], v[8:15], v216\n" \
  "v_smfmac_f32_16x16x64_bf16 v[184:187], v[160:163], v[16:23], v216\n" "s_nop 0\n" "s_cbranch_vccnz ch_o4_slow2\n" "v_smfmac_f32_16x16x64_bf16 v[188:191], v[160:163], v[24:31], v216\n" \
  "s_nop 0\n" "s_waitcnt vmcnt(2)\n" "v_smfmac_f32_16x16x64_bf16 v[176:179], v[164:167], v[40:47], v216\n" "v_cmp_eq_u32_e64 vcc, -1, v164\n" \
  "v_smfmac_f32_16x16x64_bf16 v[180:183], v[164:167], v[48:55], v216\n" "v_smfmac_f32_16x16x64_bf16 v[184:187], v[164:167], v[56:63], v216\n" "s_nop 0\n" "s_cbranch_vccnz ch_o4_slow2\n" \
  "v_smfmac_f32_16x16x64_bf16 v[188:191], v[164:167], v[64:71], v216\n" "s_nop 0\n" "s_waitcnt vmcnt(1)\n" "v_smfmac_f32_16x16x64_bf16 v[176:179], v[168:171], v[80:87], v216\n" \
  "v_smfmac_f32_16x16x64_bf16 v[180:183], v[168:171], v[88:95], v216\n" "v_smfmac_f32_16x16x64_bf16 v[184:187], v[168:171], v[96:103], v216\n" \
  "v_smfmac_f32_16x16x64_bf16 v[188:191], v[168:171], v[104:111], v216\n" "s_nop 0\n" "s_waitcnt vmcnt(0)\n" ST2 "v_smfmac_f32_16x16x64_bf16 v[176:179], v[172:175], v[120:127], v216\n" \
  "v_smfmac_f32_16x16x64_bf16 v[180:183], v[172:175], v[128:135], v216\n" "v_smfmac_f32_16x16x64_bf16 v[184:187], v[172:175], v[136:143], v216\n" \
  "v_smfmac_f32_16x16x64_bf16 v[188:191], v[172:175], v[144:151], v216\n" ST3 "v_add_f32 v196, v176, v177\n" "v_add_f32 v200, v178, v179\n" \
  "v_add_f32_dpp v200, v180, v181 quad_perm:[0,1,2,3] row_mask:0xf bank_mask:0x3\n" "v_add_f32_dpp v196, v182, v183 quad_perm:[0,1,2,3] row_mask:0xf bank_mask:0xc\n" \
  "v_add_f32_e64 v197, v184, v185\n" "v_add_f32 v201, v186, v187\n" "s_nop 1\n" "v_add_f32_dpp v201, v188, v189 quad_perm:[0,1,2,3] row_mask:0xf bank_mask:0x3\n" \
  "v_add_f32_dpp v197, v190, v191 quad_perm:[0,1,2,3] row_mask:0xf bank_mask:0xc\n" "v_add_f32_dpp v196, v200, v196 row_ror:8 row_mask:0xf bank_mask:0xf\n" \
  "v_add_f32_dpp v197, v201, v197 row_ror:8 row_mask:0xf bank_mask:0xf\n" "ds_write2st64_b32 v218, v196, v197 offset1:8\n" "v_cmp_u_f32 vcc, v196, v196\n" "s_cbranch_vccnz ch_o4_slow\n" \
  "ch_o4_back:\n" "s_cmp_gt_u32 s52, 2\n" "s_waitcnt lgkmcnt(0)\n" "s_barrier\n" "s_cbranch_scc1 ch_o4_rest\n" ST4 "ds_read_b128 v[204:207], v219\n" \
  "ds_read_b128 v[208:211], v219 offset:1024\n" "s_add_i32 s60, s57, 6*0x8000\n" "s_cmp_ge_u32 s60, 9*0x8000\n" "s_cselect_b32 s61, 9*0x8000, 0\n" "s_sub_i32 s60, s60, s61\n" \
  "v_mov_b32 v213, -1\n" ST7 "s_waitcnt lgkmcnt(1)\n" "v_add_f32 v204, v204, v205\n" "v_add_f32 v206, v206, v207\n" "s_waitcnt lgkmcnt(0)\n" "v_add_f32 v208, v208, v209\n" \
  "v_add_f32 v210, v210, v211\n" "v_add_f32 v208, v208, v204\n" "v_add_f32 v210, v210, v206\n" "v_add_f32 v204, v208, v210\n" "v_add_f32 v205, v208, v210\n" "s_nop 1\n" \
  "v_permlane32_swap_b32 v204, v205\n" "v_cvt_pk_bf16_f32 v204, v204, v205\n" ST6 "buffer_store_dword v204, v220, s[40:43], s57 offen sc1\n" ST5 "s_sleep " XS(RSL) "\n" \
  "buffer_store_dword v213, v220, s[40:43], s60 offen sc1\n" "ch_o4_nxt:\n" PTS PTA "s_add_i32 s57, s57, 3*0x8000\n" "s_cmp_eq_u32 s57, 9*0x8000\n" "s_cselect_b32 s57, 0, s57\n" \
  "s_xor_b32 s53, s53, s69\n" "s_mov_b32 s60, s69\n" "s_xor_b32 s69, s69, s70\n" "s_mov_b32 s70, s60\n" "s_add_i32 s56, s56, 1\n" "s_cmp_lt_u32 s56, 63\n" "s_cbranch_scc1 ch_o4_loop\n" \
  "s_branch ch_o4_fin\n" "ch_o4_tim:\n" "s_memrealtime s[60:61]\n" "s_waitcnt lgkmcnt(0)\n" "s_sub_i32 s60, s60, s68\n" "s_lshl_b32 s60, s60, 6\n" "s_sub_i32 s60, s60, s58\n" \
  "s_ashr_i32 s60, s60, " XS(DSO) "\n" "s_min_i32 s60, s60, " XS(DUP) "\n" "s_add_i32 s58, s58, s60\n" "s_branch ch_o4_nxt\n" "ch_o4_tch:\n" "s_add_i32 s60, s56, 2\n" "s_cmp_eq_u32 s60, 64\n" \
  "s_cbranch_scc1 ch_o4_nxt\n" "s_lshl_b32 s61, s60, 19\n" "s_lshl_b32 s62, s60, 11\n" "s_and_b32 s62, s62, 0x2000\n" "s_add_i32 s61, s61, s62\n" "s_sleep " XS(TSL) "\n" \
  "s_cmp_gt_u32 s68, s60\n" "s_cbranch_scc1 ch_o4_tk\n" "buffer_load_dword v214, v219, s[48:51], 0 offen sc1\n" "s_waitcnt vmcnt(0)\n" "v_cmp_gt_u32 vcc, 8, v214\n" "s_ff1_i32_b64 s68, vcc\n" \
  "s_cmp_gt_u32 s68, s60\n" "s_cbranch_scc0 ch_o4_nxt\n" "ch_o4_tk:\n" "buffer_load_dword v214, v220, s[44:47], s61 offen sc1\n" "s_branch ch_o4_nxt\n" "ch_o4_rest:\n" "s_cmp_eq_u32 s52, 7\n" \
  "s_cbranch_scc1 ch_o4_tim\n" "s_cmp_lg_u32 s52, 6\n" "s_cbranch_scc1 ch_o4_tch\n" "s_and_b32 s60, s56, 31\n" "s_cmp_lg_u32 s60, s55\n" "s_cbranch_scc1 ch_o4_nxt\n" "s_sub_u32 s60, s56, 1\n" \
  "s_cbranch_scc1 ch_o4_nxt\n" "s_lshl_b32 s61, s60, 7\n" "s_add_i32 s62, s61, " XS(SY_CS0) "*4\n" "s_lshl_b32 s61, s60, 10\n" "s_add_i32 s63, s61, " XS(SY_CS0) "*4-48*7*128\n" \
  "s_cmp_lt_u32 s60, 48\n" "s_cselect_b32 s62, s62, s63\n" "s_cselect_b32 exec_lo, 1, 255\n" "s_mov_b32 exec_hi, 0\n" "s_sleep " XS(RSL) "\n" "v_lshlrev_b32 v212, 3, v217\n" \
  "buffer_store_dword v216, v212, s[48:51], s62 offen sc1\n" "s_mov_b64 exec, -1\n" "s_branch ch_o4_nxt\n" "ch_o4_l2:\n" ".rept " XS(OD) "\n" "s_nop 3\n" ".endr\n" "s_branch ch_o4_l1\n" \
  "ch_o4_slow:\n" "v_max3_u32 v212, v160, v161, v162\n" "v_max3_u32 v212, v212, v163, v164\n" "v_max3_u32 v212, v212, v165, v166\n" "v_max3_u32 v212, v212, v167, v168\n" \
  "v_max3_u32 v212, v212, v169, v170\n" "v_max3_u32 v212, v212, v171, v172\n" "v_max3_u32 v212, v212, v173, v174\n" "v_max_u32 v212, v212, v175\n" "v_cmp_eq_u32 vcc, -1, v212\n" \
  "s_cbranch_vccz ch_o4_back\n" "s_sub_u32 s59, s59, 1\n" "s_cselect_b32 s59, 0, s59\n" "s_cbranch_scc1 ch_o4_back\n" "ch_o4_slow2:\n" CNTO "s_waitcnt vmcnt(0)\n" "ch_o4_wait:\n" \
  "v_max3_u32 v212, v160, v161, v162\n" "v_max_u32 v212, v212, v163\n" "v_cmp_eq_u32 vcc, -1, v212\n" "s_cbranch_vccz ch_o4_h0\n" \
  "buffer_load_dwordx4 v[160:163], v217, s[40:43], s53 offen offset:0 sc1\n" "ch_o4_h0:\n" "v_max3_u32 v212, v164, v165, v166\n" "v_max_u32 v212, v212, v167\n" "v_cmp_eq_u32 vcc, -1, v212\n" \
  "s_cbranch_vccz ch_o4_h1\n" "buffer_load_dwordx4 v[164:167], v217, s[40:43], s53 offen offset:1024 sc1\n" "ch_o4_h1:\n" "v_max3_u32 v212, v168, v169, v170\n" "v_max_u32 v212, v212, v171\n" \
  "v_cmp_eq_u32 vcc, -1, v212\n" "s_cbranch_vccz ch_o4_h2\n" "buffer_load_dwordx4 v[168:171], v217, s[40:43], s53 offen offset:2048 sc1\n" "ch_o4_h2:\n" "v_max3_u32 v212, v172, v173, v174\n" \
  "v_max_u32 v212, v212, v175\n" "v_cmp_eq_u32 vcc, -1, v212\n" "s_cbranch_vccz ch_o4_h3\n" "buffer_load_dwordx4 v[172:175], v217, s[40:43], s53 offen offset:3072 sc1\n" "ch_o4_h3:\n" \
  "v_mov_b64 v[176:177], 0\n" "v_mov_b64 v[178:179], 0\n" "v_mov_b64 v[180:181], 0\n" "v_mov_b64 v[182:183], 0\n" "v_mov_b64 v[184:185], 0\n" "v_mov_b64 v[186:187], 0\n" \
  "v_mov_b64 v[188:189], 0\n" "v_mov_b64 v[190:191], 0\n" "s_waitcnt vmcnt(0)\n" "v_max3_u32 v212, v160, v161, v162\n" "v_max3_u32 v212, v212, v163, v164\n" \
  "v_max3_u32 v212, v212, v165, v166\n" "v_max3_u32 v212, v212, v167, v168\n" "v_max3_u32 v212, v212, v169, v170\n" "v_max3_u32 v212, v212, v171, v172\n" "v_max3_u32 v212, v212, v173, v174\n" \
  "v_max_u32 v212, v212, v175\n" "v_cmp_eq_u32 vcc, -1, v212\n" "s_cbranch_vccz ch_o4_mul\n" "s_sub_u32 s59, s59, 1\n" "s_cselect_b32 s59, 0, s59\n" "s_cbranch_scc0 ch_o4_wait\n" \
  "v_mov_b32 v160, 0\n" "v_mov_b32 v164, 0\n" "v_mov_b32 v168, 0\n" "v_mov_b32 v172, 0\n" "s_mov_b32 s59, 0\n" "s_nop 0\n" "s_branch ch_o4_mul\n" "ch_o4_fin:\n" "s_waitcnt vmcnt(0)\n"
typedef __attribute__((ext_vector_type(16))) unsigned u32x16;
typedef __attribute__((ext_vector_type(32))) unsigned u32x32;
#define C_R0 114688
#define C_IMG (C_R0 + 37120)
__device__ __forceinline__ void count_votes(const u32 fv, int& xk, int& xb) {
  xk = 0; xb = X_W2;
#pragma unroll
  for (int i = 0; i < 3; i++) {
    const u32 m = (u32)__ballot((fv >> i) & 1u);
    const int a = i == 1 ? __popc(m & 0xF0000u) > 2 : __popc(m & 255u) > 4, b = i == 1 ? __popc(m & 0xF00000u) > 2 : __popc(m & 0xFF00u) > 4;
    xk |= a << (12 + i); xb |= (a ^ b) << (12 + i);
  }
}
#define TG_CLAIM2(n) do { SB0(); VMW(n); asm volatile("" : "+v"(st.ep)); const int t_ = TIDX; \
    st.l = ((u32)__builtin_amdgcn_readfirstlane((int)st.ep[0]) + 16u) & ~15u; st.h = (u32)__builtin_amdgcn_readfirstlane((int)st.ep[1]); \
    st.old = (u32x2){st.l, st.h}; \
    __builtin_amdgcn_raw_buffer_store_b64(st.old, RSS, (t_ & 63) ? PJ_OOB : 0, st.duty >= 0 ? TG_CF(st.duty) * 8 : PJ_OOB, 16); \
    asm volatile("s_nop 4\n\tbuffer_atomic_swap_x2 %0, %1, %2, 0 offen sc0" : "+{v[252:253]}"(st.old) : "v"(t_ == 448 ? TG_CL(st.slot) * 8 : PJ_OOB), "s"(RSS) : "memory"); SB0(); } while (0)
__device__ __forceinline__ u32x4 rs4(const void* q, u32 bytes) {
  return (u32x4){(u32)(uintptr_t)q, (u32)((uintptr_t)q >> 32) & 0xFFFFu, bytes, 0x00020000u};
}
__device__ __forceinline__ u32x16 sargs(u32x4 a, u32x4 b, u32x4 c, int d0, int d1, int d2, int d3) {
  u32x16 r;
#pragma unroll
  for (int i = 0; i < 4; i++) { r[i] = (u32)__builtin_amdgcn_readfirstlane((int)a[i]); r[4 + i] = (u32)__builtin_amdgcn_readfirstlane((int)b[i]); r[8 + i] = (u32)__builtin_amdgcn_readfirstlane((int)c[i]); }
  r[12] = (u32)__builtin_amdgcn_readfirstlane(d0); r[13] = (u32)__builtin_amdgcn_readfirstlane(d1); r[14] = (u32)__builtin_amdgcn_readfirstlane(d2); r[15] = (u32)__builtin_amdgcn_readfirstlane(d3);
  return r;
}
#define CV8(a) "v" #a "0", "v" #a "1", "v" #a "2", "v" #a "3", "v" #a "4", "v" #a "5", "v" #a "6", "v" #a "7"
#define CV10(a) CV8(a), "v" #a "8", "v" #a "9"
#define CS10(a) "s" #a "0", "s" #a "1", "s" #a "2", "s" #a "3", "s" #a "4", "s" #a "5", "s" #a "6", "s" #a "7", "s" #a "8", "s" #a "9"
#define CLS "s56", "s57", "s58", "s59", CS10(6), CS10(7), "vcc", "scc", "memory"
#define WIN() ((char*)(((uintptr_t)p.ws + O_XW + 0x1FFFFFu) & ~(uintptr_t)0x1FFFFFu))
#define AWAIT_ALL(fk) do { const int io_ = lane < 24 ? TG_VOTE(lane) : lane < 32 ? (fk) : TG_INIT(lane - 32); int it_ = 0; fv = TG_LD(io_); fp = TG_LD(lane); \
    __syncthreads(); TG_CHECK(151552); \
    while (!__all(TG_OK(fv)) && ++it_ < SPIN_CAP) { __builtin_amdgcn_s_sleep(1); fv = TG_LD(io_); } \
    count_votes(fv[0], xk, xb); TG_KPW(fp, 2); } while (0)
__device__ __forceinline__ void head_body(const Params& p, Start& st, char* smem, const int h, const int wv) {
  TG_BEGIN();
  const int wid = wv, w = wv & 3;
  u32x4 va;
  const __amdgpu_buffer_rsrc_t rq = RSRC(p.BWq, 576 * 8192), rk = RSRC(p.BWk, 576 * 8192), rv = RSRC(p.BWv, 576 * 8192);
  {
    const int tid = TIDX, lane = tid & 63, l15 = lane & 15, lq = lane >> 4;
    const int mo = (((lane >> 2) & 1) * 2 + (lane & 1)) * 1152 + (lane >> 3) * 32 + (lane & 2) * 8;
    DMA16(RSRC(p.Bm, 8 * 1152), C_IMG + wid * 1024, mo + (tid >= 288 ? 3 * 1152 : 0), wid * 256, 0);
    DMA16(RSRC(p.Bm, 8 * 1152), C_IMG + 8192, mo, 3 * 1152 + 2048, 0);
    va[0] = (wid * 4 + lq) * 8192 + h * 256 + (((l15 >> 1) ^ ((lq & 1) | (w << 1))) << 5) + (lane & 1) * 16;
    auto req = [&](const int r) {
#pragma unroll
      for (int i = 0; i < 6; i++) DMA16(i < 2 ? rq : i < 4 ? rk : rv, r * 49152 + i * 8192 + wid * 1024, va[0], (r * 64 + (i & 1) * 32) * 8192, HAUX);
      SB0();
    };
    req(0); req(1);
    const int r2 = l15 >> 2;
    va[1] = ((lq >> 1) * 16 + (lq & 1) * 4 + (r2 >> 1) * 8 + (r2 & 1)) * 256 + (((2 * w) ^ ((r2 & 1) | ((lq & 1) << 1) | ((r2 >> 1) << 2))) << 5) + (lane & 3) * 8;
    va[2] = w * 2048 + lane * 16; va[3] = C_R0 + tid * 16;
    TG_CLAIM2(14);
    req(2);
  }
  u32x32 w0, w1, w2, w3, w4; u32x16 w5; u32x8 w6;
  int t_;
#define W_OUT "={v[0:31]}"(w0), "={v[32:63]}"(w1), "={v[64:95]}"(w2), "={v[96:127]}"(w3), "={v[128:159]}"(w4), "={v[160:175]}"(w5)
#define W_IN "{v[0:31]}"(w0), "{v[32:63]}"(w1), "{v[64:95]}"(w2), "{v[96:127]}"(w3), "{v[128:159]}"(w4), "{v[160:175]}"(w5)
#define S_OPS "+{v[248:251]}"(va), "+{v[252:253]}"(st.old), [t] "=&s"(t_) : [lw] "s"(wid * 1024), [r0] "s"(rq), [r1] "s"(rk), [r2] "s"(rv)
  int xk, xb, kp;
  u32x2 fv, fp;
  PRDECL(h);
  asm volatile("s_cmp_lt_u32 %[lw], 0x1000\n s_cbranch_scc0 ch_s1\n" CH_SA "s_branch ch_s2\n ch_s1:\n" CH_SB "ch_s2:\n"
               : W_OUT, "={v[176:183]}"(w6), S_OPS : "v184", "v185", "v186", "v187", "v188", "v189", CV10(19), "v244", "v245", "scc", "memory");
  {
    const int tid = TIDX, lane = tid & 63, l15 = lane & 15, lq = lane >> 4;
    TG_NOTE(151552);
#pragma unroll
    for (int i = 0; i < 2; i++) *(u32x4*)(smem + C_R0 + 2048 + i * 8192 + tid * 16) = (u32x4){~0u, ~0u, ~0u, ~0u};
    if (wid < 4) *(u32x4*)(smem + C_R0 + 10240 + tid * 16) = (u32x4){(u32)(81920 + lq * 1024 + (w & 1) * 256 + l15 * 16),
        (u32)(65536 + lq * 1024 + (((l15 >> 2) & 1) | ((l15 & 3) << 1) | (l15 & 8)) * 16),
        (u32)(98304 + (lane & 2) * 256 + ((lq & 1) | ((l15 >> 2) << 1) | (((lq ^ lane) & 2) << 2)) * 16 + (lane & 1) * 8),
        (u32)(65536 + lq * 1024 + (lane & 2) * 256 + (((l15 >> 2) & 1) | (w << 1) | ((l15 ^ (l15 << 2)) & 8) | ((l15 & 1) << 4)) * 16)};
    *(u32*)(smem + C_R0 + 46336 + (tid & 255) * 4) = ~0u;
    GMAX(85); AWAIT_ALL(TG_VOTE(0)); GMAX(69);
    const int wa = C_R0 + (w >> 1) * 1024 + ((l15 >> 2) & 1) * 512 + (w & 1) * 256 + (l15 & 3) * 32;
    const bool lo = wid < 4;
    u32x8 vb = {(l15 & 2) ? 0xEEEEu : 0x4444u, (u32)(C_R0 + (l15 >> 3) * 4608 + lq * 128 + (lane & 7) * 16),
                (u32)(lo ? C_R0 + lane * 8 : wa + 2048 + lq * 8 + (l15 >> 3) * 4),
                (u32)(lo ? wa + (l15 >> 3) * 16 + lq * 4 : C_R0 + tid * 16),
                (u32)(lo ? C_R0 + tid * 4 : lane * 16 + w * 2048),
                (u32)(lo ? C_R0 + tid * 16 : w >= 2 ? lane * 16 : (lane & 32) * 16 + (((lane >> 4) & 1) | ((lane & 1) << 1) | ((((lane >> 3) ^ (lane >> 5)) & 1) << 2) | ((lane & 6) << 2)) * 16),
                (u32)(lo ? C_R0 + (lane < 32 ? 6144 + (lane & 16) * 64 + (lane & 8) * 36 : 3072 + 16 + (lane & 24) * 36) : ((lane >> 3) * 2 + ((lane >> 1) & 1)) * 128 + (((lane >> 2) & 1) * 2 + (lane & 1)) * 16),
                (u32)(lo ? (h >> 2) * 32768 + (XGEO((l15 >> 2) & 1) ^ xk) + (h & 3) * 1024 + w * 256 + (l15 & 3) * 64 + (l15 >> 3) * 32 + lq * 8
                         : lane < 8 ? ((41 * 32768 + 4096) ^ XGEO(lane >> 2) ^ xk) + h * 16 + (lane & 3) * 1024 : PJ_OOB)};
    static_assert(S_OB == S_QB, "one descriptor");
    u32x16 sa = sargs(rs4(WIN(), X_W2), rs4(BIGP(kp, S_QB), 3 * ACB + KVO), rs4(p.ws, O_XW), wid,
                      lo ? ((w >> 1) * 16 + (w & 1)) * RP + h * 256 : w == 3 ? (32 * 32768 + 4096) ^ XGEO(h >> 4) ^ xk : (w == 2 ? 0 : (w + 1) * ACB + KVO) + h * RP,
                      lo ? ((w & 1) * 16 + (h >> 1)) * RP + ((h & 1) * 4 + (w >> 1) * 2) * 1024 : w == 0 ? 65536 : w == 1 ? 98304 : 81920,
                      lo ? SY_SEG(62, h * 4 + w) * 4 : SY_BLK(w, h >> 1, 0) * 4);
    asm volatile(PRO "s_cmp_lt_u32 s52, 4\n s_cbranch_scc0 ch_h1\n" CH_HA "s_branch ch_hb_fin\n ch_h1:\n" CH_HB PRE
                 : "+{s[40:55]}"(sa), "+{v[184:191]}"(vb), "+{v[176:183]}"(w6) PROUT : W_IN
                 : "v192", "v193", "v194", "v195", "v196", "v197", "v198", "v199", CV10(20), CV10(21), CV10(22), CV10(23), CV10(24), "v250", "v251", "v252", "v253", CLS);
  }
  PRFIN(h);
}
__device__ __forceinline__ void stream_body(const Params& p, Start& st, char* smem, const int m, const int cb, int& kp, const int wv) {
  TG_BEGIN();
  const int wid = wv;
  u32x8 va;
  const char* const W = (const char*)(__attribute__((address_space(1))) const char*)((const __attribute__((address_space(4))) u64*)__builtin_amdgcn_kernarg_segment_ptr())[(0x8A9 >> (m * 4)) & 15];
  const __amdgpu_buffer_rsrc_t rx = RSRC(p.Bx, 2048 * 1152), rw = RSRC(W, 576 * 8192);
  {
    const int tid = TIDX, lane = tid & 63, l15 = lane & 15, lq = lane >> 4;
    va[1] = ((wid & 3) * 8 + (lane >> 3)) * 1152 + (((lane & 7) ^ (lane >> 3)) << 4);
    va[0] = va[1] + (wid >> 2) * 128;
    va[2] = (wid * 2 + (lane >> 5)) * 8192 + cb * 512 + ((((lane >> 1) & 15) ^ (((wid & 1) * 2 + (lane >> 5)) | (wid & 4))) << 5) + (lane & 1) * 16;
#pragma unroll
    for (int t = 0; t < 5; t++) DMA16(rx, t * 8192 + (t == 4 ? wid & 3 : wid) * 1024, va[t == 4], t * 256, 0);
    auto req = [&](const int r) {
#pragma unroll
      for (int t = 0; t < 4; t++) DMA16(rw, (r + 1) * 36864 + t * 8192 + wid * 1024, va[2], ((8 - r) * 64 + t * 16) * 8192, SAUX);
      SB0();
    };
    req(0); req(1);
    va[3] = (lq * 8 + (l15 >> 2)) * 512 + (((wid * 2) ^ ((l15 >> 2) | ((lq & 1) << 2))) << 5) + (lane & 3) * 8;
    va[4] = l15 * 128 + ((lq ^ (l15 & 7)) << 4);
    va[5] = lane * 8;
    va[6] = (cb * 2 + (wid >> 2)) * RP + ((wid & 3) * 4 + (lq & 1) * 2 + (lq >> 1)) * 512 + l15 * 16;
    va[7] = lane ? PJ_OOB : 0;
    TG_CLAIM2(13);
    req(2);
  }
  u32x32 w0, w1, w2, w3; u32x16 w4, acc; u32x2 kv;
  u32x16 sa = sargs(rs4(W, 576 * 8192), rs4(p.Bx, 2048 * 1152), rs4(p.ws, O_XW), wid * 1024, SY_BLK(m, cb, 0) * 4, 32768 - (wid >> 2) * 4096, 0);
#define ST_CL "v176", "v177", "v178", "v179", CV10(18), CV10(19), CV8(20), "v216", "v217", CLS
  asm volatile(CH_ST1 : "={v[0:31]}"(w0), "={v[32:63]}"(w1), "={v[64:95]}"(w2), "={v[96:127]}"(w3), "={v[128:143]}"(w4), "={v[144:159]}"(acc), "={v[250:251]}"(kv),
               "+{v[252:253]}"(st.old) : "{v[208:215]}"(va), "{s[40:55]}"(sa) : ST_CL);
  TG_NOTE(151552);
  CFENCE(); __builtin_amdgcn_s_barrier(); CFENCE();
  TG_CHECK(151552);
  TG_KPW(kv, 2);
  GMAX(86);
  const u32x4 ro = rs4(BIGP(kp, m == 2 ? S_QB : S_KB + m * ACB), ACB);
#pragma unroll
  for (int i = 0; i < 4; i++) sa[i] = (u32)__builtin_amdgcn_readfirstlane((int)ro[i]);
  asm volatile(CH_ST2 : "+{v[144:159]}"(acc), "+{v[208:215]}"(va), "+{s[40:55]}"(sa)
               : "{v[0:31]}"(w0), "{v[32:63]}"(w1), "{v[64:95]}"(w2), "{v[96:127]}"(w3), "{v[128:143]}"(w4) : CV10(16), "v170", "v171", "v172", "v173", "v174", "v175", ST_CL);
}
__device__ __forceinline__ void outp_body(const Params& p, Start& st, char* smem, const int x, const int i, const int wv) {
  TG_BEGIN();
  const int tg = x ^ (i >> 3), j = x * 8 + (i & 7), me = x * 16 + i;
  const int wid = wv, hw = wv ^ (x * OHX) ^ (((i & 1) * 2 + (i >> 3)) & OHR);
  u32x4 va; int vt;
  const __amdgpu_buffer_rsrc_t ro = RSRC(p.BWo, 4096 * 1152);
  {
    const int tid = TIDX, lane = tid & 63, l15 = lane & 15, lq = lane >> 4, r2 = l15 >> 2;
    va[0] = (hw * 512 + (lane >> 2)) * 1152 + j * 64 + (((lane & 3) ^ lq) << 4);
    for (int u = 0; u < ODL; u++) __builtin_amdgcn_s_sleep(16);
#pragma unroll
    for (int u = 0; u < 16; u++) DMA16(ro, wid * 16384 + u * 1024, va[0], u * 16 * 1152, 0);
    SB0();
    va[1] = i < 8 ? (hw * 512 + lane) * 1152 + 1024 + i * 16 : PJ_OOB;
    const int dim = (lq >> 1) * 32 + (lane & 2) * 8 + (r2 >> 1) * 8 + (lq & 1) * 4 + (r2 & 1);
    va[2] = wid * 16384 + dim * 64 + (((lq & 1) | (r2 & 2)) << 4) + (lane & 1) * 8;
    va[3] = wid * 16384 + dim * 16 + (lane & 1) * 8;
    vt = i < 8 ? PJ_OOB : (hw * 512 + (i & 7) * 64 + lane) * 1152 + 1024;
    TG_CLAIM2(16);
  }
  u32x32 w0, w1, w2, w3, w4;
  int t_, xk, xb, kp;
  u32x2 fv, fp;
  PRDECL(32 + me);
  asm volatile(CH_SO : "={v[0:31]}"(w0), "={v[32:63]}"(w1), "={v[64:95]}"(w2), "={v[96:127]}"(w3), "={v[128:159]}"(w4), "+{v[216:219]}"(va), "+{v[252:253]}"(st.old), [t] "=&s"(t_)
               : [lw] "s"(wid * 16384), [r] "s"(ro), "{v220}"(vt) : "v200", "v221", "scc", "memory");
  const int tid = TIDX, lane = tid & 63, l = lane & 31;
  TG_NOTE(151552);
  AWAIT_ALL(TG_VOTE(0));
  GMAX(84);
  const int cp = (32 * 32768 + 4096) ^ XGEO(lane >> 5) ^ xk;
  u32x8 vb = {(lane & 1) ? 0xEEEEu : 0x4444u, (u32)(lane * 16), (u32)((wid >> 2) * 1024 + ((lane & 1) * 32 + (lane >> 5) * 16 + (lane & 8) + ((lane >> 2) & 4) + ((lane >> 1) & 3)) * 16 + ((wid + ((lane >> 2) & 2) + (lane & 1)) & 3) * 4),
              (u32)(wid > 2 ? SY_BLK(wid - 3, me >> 1, lane) * 4 : wid * 2048 + (wid < 2 ? lane : (lane & 0x27) + (lane & 8) * 2) * 16),
              (u32)(wid == 7 ? (lane < 4 ? ((41 * 32768 + 4096) ^ XGEO(lane >> 1) ^ xk) + (32 + me) * 16 + (lane & 1) * 1024 : PJ_OOB) : wid > 2 ? (wid == 5 ? 0 : (wid - 2) * ACB + KVO) + me * RP + lane * 128
                    : wid < 2 ? cp + tg * 32768 + j * 256 + wid * 128 + l * 4 : i < 8 ? cp + 65536 + j * 128 + l * 4 : PJ_OOB), 0u, 0u, 0u};
  u32x16 sa = sargs(rs4(WIN(), X_W2), rs4(BIGP(kp, S_QB), 3 * ACB + KVO), rs4(p.ws, O_XW), wid, hw * 32768 + (XGEO(tg) ^ xk), ((41 * 32768 + 4096) ^ XGEO(x) ^ xk) + 2048, me);
  asm volatile(PRO "s_bitcmp1_b32 s55, 3\n s_cbranch_scc1 ch_o1\n" CH_O5 "s_branch ch_o4_fin\n ch_o1:\n" CH_O4 PRE : "+{s[40:55]}"(sa), "+{v[216:223]}"(vb) PROUT : "{v[0:31]}"(w0), "{v[32:63]}"(w1), "{v[64:95]}"(w2), "{v[96:127]}"(w3), "{v[128:159]}"(w4)
               : CV10(16), CV10(17), CV10(18), CV10(19), CV10(20), "v210", "v211", "v212", "v213", "v214", "v215", "v224", "v225", CLS);
  PRFIN(32 + me);
  if (me + wv == 0) {
    int it = 0; bool ok;
    do { ok = true; _Pragma("unroll") for (int u = 0; u < 4; u++) { const u32x2 y = TG_LD(TG_CL(u * 64 + TIDX)); ok = ok && TG_OK(y); } } while (!__all(ok) && ++it < SPIN_CAP);
    if (TIDX == 0) st4_sc1(WSP(u32, 0) + TG_EP * 2, st.l);
  }
}

__device__ __forceinline__ void wait_ge(const u32* w, u32 val, const int wv) {
  if (TIDX == 0) {
    int it = 0;
    while (ld4_sc1(w) < val && ++it < SPIN_CAP) __builtin_amdgcn_s_sleep(WPS);
  }
}
__device__ __forceinline__ void acquire_all(const int wv) {
  if (TIDX == 0) {
    __builtin_amdgcn_fence(__ATOMIC_ACQUIRE, "agent");
    VMW0();
  }
  __syncthreads();
}

#define KROT 0
#define OPD 4
#define PPD 2
#define WWM 2
#define WWN 2
#define NWM 4
#define NWN 1
#define WPD 2
#define WAD false
#define NAD false
#define KPX 0
#define NAW 0
#define NBW 10
#define NAM 4
#define NBM 2
#define NKV 8
#define CFG 1
#define TMASK 0x71
#define SBM 256
#define SPD 4
#define SMASK 0x00
#define TU 10
#define PORD 0x354
#define SDL 0
#define PWV 1
#define NPF 2
#define NPS 4
#define BST 0
#define NPR 3
#define QUIET 0
#define WDL 0
#define TGAP 6
#define TOUCH 1
__host__ __device__ __forceinline__ int decode_task(int q, int u, int& a, int& b) {
  if (q < 5) {
    const int nw = q < 4 ? NAW : NBW, nm = q < 4 ? NAM : NBM, n = q < 4 ? 4 : SM0 / 4;
    if (u < nw * 3) { a = u / 3; b = u % 3; }
    else if ((u -= nw * 3) < nm * 6) { a = nw + u / 6; b = 3 + u % 6; }
    else { u -= nm * 6; a = nw + nm + u / 9; b = 9 + u % 9; }
    if (a >= n) return -1;
    if (q < 4) a += q * 4;
    return q < 4 ? 2 : 3;
  }
  if (u >= 108) return -1;
  a = (u / 36 + SM0) % 3; b = u % 36; return 4;
}

#define TP(code)
#define TPC(ty, a, b) TP(((ty) << 16) | ((a) << 8) | (b))
#if defined(__HIP_DEVICE_COMPILE__) && !defined(KEEP_PK)
#define NOPK __attribute__((target("no-packed-fp32-ops")))
#else
#define NOPK
#endif
#define P0V 2
__device__ __forceinline__ Params prm() {
  const __attribute__((address_space(4))) u64* a = (const __attribute__((address_space(4))) u64*)__builtin_amdgcn_kernarg_segment_ptr();
  asm volatile("" : "+s"(a));
#define GP(T, x) ((T*)(__attribute__((address_space(1))) T*)(x))
  Params p;
  p.Ax = GP(const u16, a[0]); p.Ac = GP(const u16, a[1]); p.Awq = GP(const u16, a[2]); p.Awk = GP(const u16, a[3]); p.Awv = GP(const u16, a[4]); p.Awo = GP(const u16, a[5]);
  p.Bx = GP(const u16, a[6]); p.Bm = GP(const u16, a[7]); p.BWq = GP(const u16, a[8]); p.BWk = GP(const u16, a[9]); p.BWv = GP(const u16, a[10]); p.BWo = GP(const u16, a[11]);
  p.out = GP(u16, a[12]); p.ws = GP(char, a[13]); p.big = GP(char, a[14]);
  return p;
}
#define p prm()
__device__ __forceinline__ void mega_body(char* smem) {
  int* const s_task = (int*)(smem + 151552);
  const int wv = __builtin_amdgcn_readfirstlane(threadIdx.x >> 6);
  if (blockIdx.x == 0 && TIDX == 0) asm volatile("s_nop 0" ::: "memory");
  STS(0);
#define tid TIDX
  const int xcc = (int)(__builtin_amdgcn_s_getreg(20 | (3 << 11)) & 7u);
  Start st;
  const int bid = blockIdx.x;
  const int slot = bid >= 256 ? bid & 255 : xcc * 32 + (bid >> 3);
  const int x = (slot >> 5) & 3, j = slot & 31;
  st.slot = slot; st.duty = bid >= 200 && bid < 256 && (bid & 7) == 2 ? bid - 202 + wv : -1;
  const int wk = slot >> 7;
  const int role = x >= 2 ? (j < 24 ? 16 + x * 24 + j : 72 + x * 8 + j) : (j & 16) * 2 + x * 16 + (j & 15);
  int kp = 0, pre = 0, q = wk ? x : 4;
  u32* qh = WSP(u32, 0) + SY_QH(q);
  if (!wk && role < 64) {
    if (role < 32) head_body(p, st, smem, role ^ HPX, wv); else outp_body(p, st, smem, (role >> 4) & 1, role & 15, wv);
    GMAX(65); GMAX(68);
    return;
  } else {
    TPC(10, 0, 0);
    auto touch = [&](const int mask, const int b, const int rot) {
#pragma unroll
      for (int m = 0; m < 7; m++) {
        if (!((mask >> m) & 1)) continue;
        const int bytes = m == 2 ? 2048 * 1024 : (m == 3 || m == 4 ? 512 * 8192 : (m == 0 || m == 5 ? 2048 * 1152 : 576 * 8192));
        __builtin_amdgcn_raw_ptr_buffer_load_lds(RSRC(m == 0 ? p.Bx : m == 1 ? p.BWq : m == 2 ? p.Ac : m == 3 ? p.Awk : m == 4 ? p.Awv : m == 5 ? p.Ax : p.Awq, bytes),
                                                 (LDSP void*)(smem + C_BUFB + wv * 256), 4, (tid & 63) * 128, (rot * ((wv + m * 5) & 7) * 128 + b) * 8192, 0, 0);
      }
    };
    if (!wk && role >= 112) {
      const Params p0 = p;
#undef p
#define p p0
      TG_BEGIN();
      const int v = role & 15;
      const __amdgpu_buffer_rsrc_t rsX = RSRC((((uintptr_t)p.ws + O_XW + 0x1FFFFFu) & ~(uintptr_t)0x1FFFFFu), X_END);
      const int ty = v & 3, vi = v >> 2;
      {
        const __amdgpu_buffer_rsrc_t r = ty ? rsX : RSRC(BIGP(0, vi * 0x3000000), 32768);
        const int o = (ty ? 63 * 32768 : 0) + v * 256 + (wv & 1) * 128;
        u32* const bs = (u32*)smem;
        if (tid < 8) bs[tid] = ~0u;
        auto probe = [&](const int k) {
          __syncthreads();
          if (wv < PWV) {
            const u32 t0 = (u32)__builtin_readcyclecounter();
            __builtin_amdgcn_raw_buffer_store_b32(0u, r, k * 4096, o, 16);
            VMW0();
            const u32 t = (u32)__builtin_readcyclecounter() - t0;
            if ((tid & 63) == 0) __hip_atomic_fetch_min(bs + k, t, __ATOMIC_RELAXED, __HIP_MEMORY_SCOPE_WORKGROUP);
          }
        };
        if (ty) probe(0);
        TG_CLAIM(0);
        if (ty) probe(2);
        VMW0(); TG_NOTE(151552);
        __syncthreads();
        TG_CHECK(151552);
#pragma unroll 1
        for (int n = ty ? 2 : 0; n < (ty < 2 ? 2 * NPR : NPS); n++) probe((n & 1) * 2);
        __syncthreads();
        const int sd = __builtin_amdgcn_readfirstlane(bs[2] < bs[0] ? 2 : 0);
        int kb = sd;
        if (ty >= 2) {
#pragma unroll 1
          for (int n = 1; n < 4 * NPF; n++) probe(sd | (n & 1) | ((n & 2) << 1));
          __syncthreads();
          const u32 l0 = bs[sd], l1 = bs[sd | 1], l4 = bs[sd | 4], l5 = bs[sd | 5];
          kb = sd | (l1 + l5 < l0 + l4 ? 1 : 0) | (l4 + l5 < l0 + l1 ? 4 : 0);
        }
        kb ^= 5 ^ (XGEO(xcc) >> 12) ^ ((xcc >> 2) << 1);
        if (tid < 2) TG_ST(ty == 0 ? TG_KP(vi * 2 + tid) : ty == 1 ? TG_VOTE(16 + tid * 4 + vi) : TG_VOTE(tid * 8 + vi * 2 + (ty & 1)),
                           ty ? (u32)kb : (u32)(((kb >> 1) ^ KPX ^ 1) & 1));
      }
      TPC(14, 0, 0);
      {
        u32x2 fv; int it = 0;
        while (fv = TG_LD(TG_VOTE(16 + (tid & 7))), !__all(TG_OK(fv)) && ++it < SPIN_CAP) __builtin_amdgcn_s_sleep(4);
        TPC(15, 0, 0);
        int xk, xb;
        count_votes(fv[0], xk, xb);
        const int cv = (xk & 8192) + tid * 16;
#pragma unroll
        for (int i = 0; i < 5; i++) {
          const int n = i * 16 + v;
          u32 z0 = ~0u; asm volatile("" : "+v"(z0));
          __builtin_amdgcn_raw_buffer_store_b128((u32x4){z0, z0, z0, z0}, rsX, n < 68 ? cv + (n < 48 ? n : n + 16) * 16384 : PJ_OOB, 0, 16);
        }
        VMW0();
        __syncthreads();
        if (tid < 2) TG_ST(TG_INIT(v + tid * 16), 0u);
        u32x2 kv = TG_LD(tid & 63);
        TG_KPW(kv, 4);
      }
#undef p
#define p prm()
      TPC(11, 0, 0);
      __syncthreads();
    }
    TPC(12, wk, 0);
    if (wk) {
      kp = -1;
      TG_BEGIN();
      __builtin_amdgcn_raw_ptr_buffer_load_lds(RSRC(p.BWo, 576 * 8192), (LDSP void*)(smem + C_BUFB + wv * 256), 4, (tid & 63) * 128, (wv * 128 + (slot & 127)) * 8192, 0, 0);
      for (int i = 0; i < WDL; i++) __builtin_amdgcn_s_sleep(32);
      const int hd = x * 8 + (j >> 3) * 2 + ((j & 4) == 0);
      bf8 qr[8]; u32 vfl;
      proj_slot([&]() __attribute__((always_inline)) { touch(TMASK, slot & 127, 1); }, p, st, 3, PORD, (j & 7) * 2, 0, x * 4 + (j >> 3), kp, hd, qr, vfl, smem, wv);
      TPC(0, j & 7, hd);
      fa::attn_pair(p, BIGP(kp, S_KA + hd * 256), BIGP(kp, S_VA + hd * 256), hd, j & 7, kp, qr, vfl, smem, wv, qh, pre);
      if (tid < 2) __hip_atomic_fetch_add(WSP(u32, 0) + SY_CC((j & 7) ^ (tid * 7)), 1u, __ATOMIC_RELAXED, __HIP_MEMORY_SCOPE_AGENT);
      GMAX(80);
    } else if (role < 112) {
      stream_body(p, st, smem, (role >> 4) - 4, role & 15, kp, wv);
      GMAX(82);
    } else {
      kp = __builtin_amdgcn_readfirstlane(kp);
      for (int i = 0; i < TSD; i++) __builtin_amdgcn_s_sleep(32);
#pragma unroll
      for (int i = 0; i < 5; i++) touch(SMASK, (i * 8 + wv) * 16 + (role & 15), 0);
#pragma unroll 1
      for (int u = role & 15; u < 128; u += 16) {
        TPC(13, 0, 0);
        u32x4 sv[9];
        const __amdgpu_buffer_rsrc_t r = RSRC((u & 64) ? p.Awo : p.BWo, 4096 * 1152);
#pragma unroll
        for (int i = 0; i < 9; i++) { sv[i] = __builtin_amdgcn_raw_buffer_load_b128(r, tid * 16, (u & 63) * 64 * 1152 + i * 8192, 2); if (WOP) { SB0(); __builtin_amdgcn_s_sleep(WOP); SB0(); } }
        wof_task(p, sv, (u >> 6) ^ 1, u & 63, kp, smem, wv);
      }
      VMW0();
      __syncthreads();
      if (tid == 0) __hip_atomic_fetch_add(WSP(u32, 0) + SY_CW, 8u, __ATOMIC_RELAXED, __HIP_MEMORY_SCOPE_AGENT);
      GMAX(83);
    }
    {
      if (!wk) {
        for (int i = 0; i < BST * (role & 31); i++) __builtin_amdgcn_s_sleep(4);
        CLAIM();
      }
    }
  }
  while (true) {
    if (tid == 0) *s_task = pre;
    __syncthreads();
    const int t = __builtin_amdgcn_readfirstlane(*s_task);
    __syncthreads();
    int a, b;
    const int ty = decode_task(q, t, a, b);
    if (ty < 0) {
      if (q == 5) break;
      if (!wk && QUIET) break;
      q = q < 4 ? 4 : 5; qh = WSP(u32, 0) + SY_QH(q);
      CLAIM();
      continue;
    }
    TPC(ty, a, b);
    if (ty < 4) {
      const int nt0 = b < 3 ? b * 12 : b < 9 ? (b - 3) * 6 : (b - 9) * 4;
      wait_ge(WSP(u32, 0) + SY_CW, 128u, wv);
      if (ty == 2) wait_ge(WSP(u32, 0) + SY_CC(a >> 1), 32u, wv);
      else for (int i = 0; i < 4; i++) wait_ge(WSP(u32, 0) + SY_CS(a * 4 + i), 4u, wv);
      __syncthreads();
      TPC(ty + 20, a, b);
      const char* A = BIGP(kp, ty == 2 ? S_CA + ROWO(a * 128) : S_OB + OROW(a * 128));
      u16* C = p.out + (size_t)a * 128 * 1152 + (ty - 2) * 576 + nt0 * 16;
      if (b < 3) out_tile<4, 6, 2, 4>(A, BIGP(kp, S_WF(ty - 2)), nt0, C, smem, wv, qh, pre);
      else if (b < 9) out_tile<4, 3, 2, 5>(A, BIGP(kp, S_WF(ty - 2)), nt0, C, smem, wv, qh, pre);
      else out_tile<4, 2, 2, 6>(A, BIGP(kp, S_WF(ty - 2)), nt0, C, smem, wv, qh, pre);
      GMAX(64 + ty);
    } else {
      GMAX(81);
      small_out_task(p, a, b & 1, b >> 1, kp, smem, wv, qh, pre);
    }
  }
  TPC(99, 0, 0);
}

#undef tid
#undef p
__global__ NOPK void __launch_bounds__(512, 2) mega(Params p_) {
  __shared__ __attribute__((aligned(16))) char smem[LDS_ALL];
#define CODE_KB 76
#define CPM 8
  int t;
  asm volatile("s_load_dword s8, %1, 0\n\ts_load_dword s9, %1, 0x40\n\t"
               "s_getpc_b64 s[4:5]\n\ts_and_b32 s4, s4, 0xffffff00\n\ts_mov_b32 s6, %4\n\ts_mov_b32 s7, 0x20000\n\ts_mov_b32 m0, %5\n\ts_and_b32 s3, %2, %6\n\ts_lshl_b32 s3, s3, 13\n\t"
               "v_lshlrev_b32 %0, 7, %3\n\tbuffer_load_dword %0, s[4:7], s3 offen lds\n\ts_waitcnt lgkmcnt(0)"
               : "=&v"(t) : "s"(__builtin_amdgcn_kernarg_segment_ptr()), "s"(blockIdx.x), "v"(threadIdx.x), "n"(CODE_KB * 1024), "n"(150528), "n"(CPM)
               : "s3", "s4", "s5", "s6", "s7", "s8", "s9", "scc", "memory");
  mega_body(smem);
}
extern "C" void kernel_launch(void* const* d_in, const int* in_sizes, int n_in, void* d_out, int out_size, void* d_ws,
                              size_t ws_size, hipStream_t stream) {
  Params p;
  p.Ax = (const u16*)d_in[0]; p.Ac = (const u16*)d_in[1]; p.Awq = (const u16*)d_in[2]; p.Awk = (const u16*)d_in[3];
  p.Awv = (const u16*)d_in[4]; p.Awo = (const u16*)d_in[5]; p.Bx = (const u16*)d_in[6]; p.Bm = (const u16*)d_in[7];
  p.BWq = (const u16*)d_in[8]; p.BWk = (const u16*)d_in[9]; p.BWv = (const u16*)d_in[10]; p.BWo = (const u16*)d_in[11];
  p.out = (u16*)d_out;
  p.ws = (char*)d_ws;
  p.big = (char*)d_ws + O_BIG + 0x200000;
  mega<<<256, 512, 0, stream>>>(p);
}
```
